```python
import torch

def _softmax(x, axis=-1):
    return torch.softmax(x, dim=axis)
def _gelu(x):
    return torch.nn.functional.gelu(x, approximate="tanh")
def _silu(x):
    return torch.nn.functional.silu(x)
def _sigmoid(x):
    return torch.sigmoid(x)
def _layernorm(x, axis=-1, eps=1e-5):
    m = x.mean(dim=axis, keepdim=True)
    v = x.var(dim=axis, keepdim=True, unbiased=False)
    return (x - m) / torch.sqrt(v + eps)
def _scatter_add(tgt, idx, src, axis=0):
    return tgt.clone().index_add_(axis, idx, src)
def _segment_sum(src, seg, n):
    out = torch.zeros((n,) + src.shape[1:], dtype=src.dtype)
    return out.index_add_(0, seg, src)
def _pad(x, pw):
    flat = tuple(v for pair in reversed(pw) for v in pair)
    return torch.nn.functional.pad(x, flat)

kernel_name = 'gated_mix__ssm_conv_s5__mixer_channel_mlp__prompt'

def setup_inputs():

    _w = (lambda a: (lambda t: t.to(torch.bfloat16) if t.is_floating_point() else t)(torch.as_tensor(a)))
    def _A_setup():
        B, S, D = 1, 2048, 1024
        P = D
        Lambda_re = -0.5 * (lambda _x: torch.exp(torch.as_tensor(_x)))((lambda *s: torch.randn(*s, dtype=torch.bfloat16) * 0.4)(P))
        Lambda_im = (lambda *s: torch.randn(*s, dtype=torch.bfloat16) * 0.8)(P) * 1.0
        B_re = (lambda *s: torch.randn(*s, dtype=torch.bfloat16) * 0.8)(P, D) * (D ** -0.5)
        B_im = (lambda *s: torch.randn(*s, dtype=torch.bfloat16) * 0.4)(P, D) * (D ** -0.5)
        C_re = (lambda *s: torch.randn(*s, dtype=torch.bfloat16) * 0.8)(D, P) * (P ** -0.5)
        C_im = (lambda *s: torch.randn(*s, dtype=torch.bfloat16) * 0.8)(D, P) * (P ** -0.5)
        log_dt = (lambda *s: torch.randn(*s, dtype=torch.bfloat16) * 0.4)(P) * 0.3 - 2.0
        Dskip = (lambda *s: torch.randn(*s, dtype=torch.bfloat16) * 0.1)(D)
        return {
            "x": _w((lambda *s: torch.randn(*s, dtype=torch.bfloat16) * 0.05)(B, S, D)),
            "Lambda_re": _w(Lambda_re),
            "Lambda_im": _w(Lambda_im),
            "B_re": _w(B_re),
            "B_im": _w(B_im),
            "C_re": _w(C_re),
            "C_im": _w(C_im),
            "log_dt": _w(log_dt),
            "Dskip": _w(Dskip),
        }
    _A_in = _A_setup()

    def _B_setup():
        B, S, D, F = 1, 2048, 1024, 3584
        return {
            "x": _w((lambda *s: torch.randn(*s, dtype=torch.bfloat16) * 0.05)(B, S, D)),
            "w1": _w((lambda *s: torch.randn(*s, dtype=torch.bfloat16) * 0.8)(D, F) * (D ** -0.5)),
            "b1": _w((lambda *s: torch.randn(*s, dtype=torch.bfloat16) * 0.2)(F)),
            "w2": _w((lambda *s: torch.randn(*s, dtype=torch.bfloat16) * 0.05)(F, D) * (F ** -0.5)),
            "b2": _w((lambda *s: torch.randn(*s, dtype=torch.bfloat16) * 0.05)(D)),
        }
    _B_in = _B_setup()

    return {
        "A_x": _A_in["x"],
        "A_Lambda_re": _A_in["Lambda_re"],
        "A_Lambda_im": _A_in["Lambda_im"],
        "A_B_re": _A_in["B_re"],
        "A_B_im": _A_in["B_im"],
        "A_C_re": _A_in["C_re"],
        "A_C_im": _A_in["C_im"],
        "A_log_dt": _A_in["log_dt"],
        "A_Dskip": _A_in["Dskip"],
        "B_x": _B_in["x"],
        "B_w1": _B_in["w1"],
        "B_b1": _B_in["b1"],
        "B_w2": _B_in["w2"],
        "B_b2": _B_in["b2"],
        "gate_w": _w((lambda *s: torch.randn(*s, dtype=torch.bfloat16) * 0.05)(1024) * 16.0),
    }

def reference(A_x, A_Lambda_re, A_Lambda_im, A_B_re, A_B_im, A_C_re, A_C_im, A_log_dt, A_Dskip, B_x, B_w1, B_b1, B_w2, B_b2, gate_w):
    def _A_ref(x, Lambda_re, Lambda_im, B_re, B_im, C_re, C_im, log_dt, Dskip):
        Bsz, L, H = x.shape
        Lam = Lambda_re + 1j * Lambda_im
        Bmat = B_re + 1j * B_im
        Cmat = C_re + 1j * C_im
        dt = torch.exp(log_dt)
        Lam_bar = torch.exp(dt * Lam)
        B_bar = ((Lam_bar - 1.0) / Lam)[:, None] * Bmat
        Bu = torch.einsum("bld,pd->blp", x + 0j, B_bar)
        h = torch.zeros_like(Bu[:, 0, :])
        hs = []
        for t in range(L):
            h = Lam_bar[None, :] * h + Bu[:, t, :]
            hs.append(h)
        hseq = torch.stack(hs, 1)
        y = 2.0 * torch.einsum("blp,dp->bld", hseq, Cmat).real + x * Dskip[None, None, :]
        return y

    def _B_ref(x, w1, b1, w2, b2):
        y = _layernorm(x, axis=-1)
        h = _gelu(torch.einsum("bsd,df->bsf", y, w1) + b1)
        return torch.einsum("bsf,fd->bsd", h, w2) + b2

    x = A_x
    g = _sigmoid((x * gate_w).sum(-1, keepdims=True))
    a = _A_ref(x, A_Lambda_re, A_Lambda_im, A_B_re, A_B_im, A_C_re, A_C_im, A_log_dt, A_Dskip)
    b = _B_ref(x, B_w1, B_b1, B_w2, B_b2)
    return g * a + (1 - g) * b
```

```cpp
#include <hip/hip_runtime.h>
#include <hip/hip_bf16.h>
typedef unsigned short u16;
__device__ __forceinline__ float bf2f(u16 v){ return __uint_as_float(((unsigned)v)<<16); }
__device__ __forceinline__ float rbf(float f){ return __bfloat162float(__float2bfloat16(f)); }
constexpr int L=2048,D=1024,P=1024,F=3584;

__global__ void k_stats(const u16* x,const u16* gw,float* mean,float* rstd,float* g){
  int t=blockIdx.x; __shared__ float s[3][256];
  float a=0,b=0,c=0;
  for(int d=threadIdx.x;d<D;d+=256){float v=bf2f(x[t*D+d]);a+=v;b+=v*v;c+=v*bf2f(gw[d]);}
  s[0][threadIdx.x]=a;s[1][threadIdx.x]=b;s[2][threadIdx.x]=c;__syncthreads();
  for(int o=128;o>0;o>>=1){if(threadIdx.x<o){for(int k=0;k<3;++k)s[k][threadIdx.x]+=s[k][threadIdx.x+o];}__syncthreads();}
  if(threadIdx.x==0){float m=s[0][0]/D;float v=s[1][0]/D-m*m;mean[t]=m;rstd[t]=rsqrtf(v+1e-5f);g[t]=1.f/(1.f+__expf(-s[2][0]));}
}
__global__ void k_bu(const u16* x,const u16* Bre,const u16* Bim,float* ure,float* uim){
  int t=blockIdx.y,p=blockIdx.x*256+threadIdx.x; float a=0,b=0;
  for(int d=0;d<D;++d){float v=bf2f(x[t*D+d]);a+=v*bf2f(Bre[p*D+d]);b+=v*bf2f(Bim[p*D+d]);}
  ure[t*P+p]=a;uim[t*P+p]=b;
}
__global__ void k_scan(const u16* lre,const u16* lim,const u16* ldt,float* ure,float* uim){
  int p=blockIdx.x*64+threadIdx.x;
  float dt=rbf(expf(bf2f(ldt[p]))); float lr=bf2f(lre[p]),li=bf2f(lim[p]);
  float e=expf(dt*lr); float br=e*cosf(dt*li), bi=e*sinf(dt*li);
  float nr=br-1.f,ni=bi; float den=lr*lr+li*li; float cr=(nr*lr+ni*li)/den, ci=(ni*lr-nr*li)/den;
  float hr=0,hi=0;
  for(int t=0;t<L;++t){float ur=ure[t*P+p],ui=uim[t*P+p];
    float vr=cr*ur-ci*ui, vi=cr*ui+ci*ur;
    float a=br*hr-bi*hi+vr, b=br*hi+bi*hr+vi; hr=a;hi=b; ure[t*P+p]=rbf(hr);uim[t*P+p]=rbf(hi);}
}
__global__ void k_mlp1(const u16* x,const u16* w1,const u16* b1,const float* mean,const float* rstd,float* hid){
  int t=blockIdx.y,f=blockIdx.x*256+threadIdx.x; float a=0,cs=0;
  for(int d=0;d<D;++d){float w=bf2f(w1[d*F+f]);a+=bf2f(x[t*D+d])*w;cs+=w;}
  float u=rstd[t]*(a-mean[t]*cs)+bf2f(b1[f]);
  float z=0.7978845608f*(u+0.044715f*u*u*u);
  hid[t*F+f]=rbf(0.5f*u*(1.f+tanhf(z)));
}
__global__ void k_out(const u16* x,const float* hr,const float* hi,const u16* Cre,const u16* Cim,const u16* Dsk,const float* hid,const u16* w2,const u16* b2,const float* g,float* out){
  int t=blockIdx.y,d=blockIdx.x*256+threadIdx.x; float a=0,b=0;
  for(int p=0;p<P;++p) a+=hr[t*P+p]*bf2f(Cre[d*P+p])-hi[t*P+p]*bf2f(Cim[d*P+p]);
  for(int f=0;f<F;++f) b+=hid[t*F+f]*bf2f(w2[f*D+d]);
  a=2.f*a+rbf(bf2f(x[t*D+d])*bf2f(Dsk[d])); b+=bf2f(b2[d]);
  out[t*D+d]=g[t]*a+(1.f-g[t])*b;
}
extern "C" void kernel_launch(void* const* d_in,const int* in_sizes,int n_in,void* d_out,int out_size,void* d_ws,size_t ws_size,hipStream_t s){
  const u16* x=(const u16*)d_in[0];
  float* ws=(float*)d_ws; float* mean=ws,*rstd=ws+L,*g=ws+2*L,*ure=ws+4*L,*uim=ure+L*P,*hid=uim+L*P;
  k_stats<<<L,256,0,s>>>(x,(const u16*)d_in[14],mean,rstd,g);
  k_bu<<<dim3(P/256,L),256,0,s>>>(x,(const u16*)d_in[3],(const u16*)d_in[4],ure,uim);
  k_scan<<<P/64,64,0,s>>>((const u16*)d_in[1],(const u16*)d_in[2],(const u16*)d_in[7],ure,uim);
  k_mlp1<<<dim3(F/256,L),256,0,s>>>(x,(const u16*)d_in[10],(const u16*)d_in[11],mean,rstd,hid);
  k_out<<<dim3(D/256,L),256,0,s>>>(x,ure,uim,(const u16*)d_in[5],(const u16*)d_in[6],(const u16*)d_in[8],hid,(const u16*)d_in[12],(const u16*)d_in[13],g,(float*)d_out);
}
```

```cpp
#include <hip/hip_runtime.h>
#include <utility>

typedef unsigned short u16;
typedef __bf16 bf16x2 __attribute__((ext_vector_type(2)));
typedef __bf16 bf16x4 __attribute__((ext_vector_type(4)));
typedef __bf16 bf16x8 __attribute__((ext_vector_type(8)));
typedef float f32x2 __attribute__((ext_vector_type(2)));
typedef float f32x4 __attribute__((ext_vector_type(4)));
typedef unsigned u32x2 __attribute__((ext_vector_type(2)));
typedef unsigned u32x4 __attribute__((ext_vector_type(4)));
typedef __amdgpu_buffer_rsrc_t rsrc_t;
#define LAS __attribute__((address_space(3)))
#define DI __device__ __forceinline__
#define AI __attribute__((always_inline))
#define SB() __builtin_amdgcn_sched_barrier(0)
#define PIN(x) asm volatile("" : "+v"(x))
#define MFMA(a, b, c) __builtin_amdgcn_mfma_f32_16x16x32_bf16(a, b, c, 0, 0, 0)
template <int V> using IC = std::integral_constant<int, V>;

constexpr unsigned HID_PANEL = 112 * 8192, H_OFF = 16 * HID_PANEL, H_PANEL = 32 * 16384;
constexpr unsigned PART_OFF = H_OFF + 16 * H_PANEL;
constexpr unsigned GRAN_OFF = PART_OFF + 128 * 65536;
constexpr unsigned FLAG_OFF = GRAN_OFF + 256 * 1024;
constexpr unsigned EPC_OFF = FLAG_OFF + 16 * 1024;
constexpr unsigned DUM_OFF = EPC_OFF + 16 * 128;
constexpr unsigned WS_BYTES = DUM_OFF + 256 * 128;
constexpr int FK_M1 = 0, FK_M2 = 1, FK_H = 2, FK_P = 3, FK_EP = 4;
constexpr unsigned TAB = 147456, T_GW = TAB, T_COL = TAB + 2048, T_AGG = TAB, T_ROW = TAB + 4096, T_OUT = TAB + 6144, T_CON = TAB + 7168;
constexpr unsigned LDS_BYTES = 163840;
#define N4A 11
constexpr int LAG = 2, LASTR = 25;
constexpr unsigned SPIN_CAP = 1u << 16;

DI rsrc_t mk(const void* p, unsigned bytes) { return __builtin_amdgcn_make_buffer_rsrc((void*)p, 0, bytes, 0x00020000); }
DI void glds(rsrc_t r, unsigned voff, unsigned soff, unsigned lds) {
  asm volatile("s_mov_b32 m0, %0\n\ts_nop 3\n\tbuffer_load_dwordx4 %1, %2, %3 offen lds" ::"s"(lds), "v"(voff), "s"(r), "s"(soff) : "memory");
}
template <int N> DI void waitv() { asm volatile("s_waitcnt vmcnt(%0)" ::"i"(N) : "memory"); }
DI void waitl0() { SB(); __builtin_amdgcn_s_waitcnt(0xC07F); asm volatile("" ::: "memory"); }
DI void bar() { asm volatile("s_barrier" ::: "memory"); SB(); }
DI void acquire() { asm volatile("buffer_inv sc1" ::: "memory"); }
DI bf16x8 rd128(unsigned a) { return *(LAS bf16x8*)(size_t)a; }
DI u32x4 rdu4(unsigned a) { return *(LAS u32x4*)(size_t)a; }
DI f32x4 rdf4(unsigned a) { return *(LAS f32x4*)(size_t)a; }
DI f32x2 rdf2(unsigned a) { return *(LAS f32x2*)(size_t)a; }
DI float rdf(unsigned a) { return *(LAS float*)(size_t)a; }
DI void wrf(unsigned a, float v) { *(LAS float*)(size_t)a = v; }
DI void wrf2(unsigned a, f32x2 v) { *(LAS f32x2*)(size_t)a = v; }
DI bf16x4 rtr(unsigned a) { return __builtin_amdgcn_ds_read_tr16_b64_v4bf16((LAS bf16x4*)(size_t)a); }
DI void sethalf(bf16x8& f, int h, bf16x4 v) {
  if (h == 0) f = __builtin_shufflevector(v, v, 0, 1, 2, 3, -1, -1, -1, -1);
  else { bf16x8 t = __builtin_shufflevector(v, v, -1, -1, -1, -1, 0, 1, 2, 3); f = __builtin_shufflevector(f, t, 0, 1, 2, 3, 12, 13, 14, 15); }
}
DI float bf2f(unsigned v) { return __uint_as_float(v << 16); }
DI unsigned pk2(float lo, float hi) { f32x2 v = {lo, hi}; return __builtin_bit_cast(unsigned, __builtin_convertvector(v, bf16x2)); }
DI float rbf(float f) { return __uint_as_float(pk2(f, 0.f) << 16); }
DI float dot2(unsigned a, unsigned b, float c) { return __builtin_amdgcn_fdot2_f32_bf16(__builtin_bit_cast(bf16x2, a), __builtin_bit_cast(bf16x2, b), c, false); }
template <int C> DI float dppadd(float v) { return v + __int_as_float(__builtin_amdgcn_update_dpp(0, __float_as_int(v), C, 0xf, 0xf, true)); }
DI f32x2 cmul(f32x2 a, f32x2 b) { return (f32x2){a.x * b.x - a.y * b.y, a.x * b.y + a.y * b.x}; }
DI f32x2 cfma(f32x2 a, f32x2 b, f32x2 c) { return (f32x2){__builtin_fmaf(a.x, b.x, __builtin_fmaf(-a.y, b.y, c.x)), __builtin_fmaf(a.x, b.y, __builtin_fmaf(a.y, b.x, c.y))}; }
DI void st128(rsrc_t r, u32x4 v, unsigned voff, unsigned soff) { __builtin_amdgcn_raw_buffer_store_b128(v, r, voff, soff, 16); }
DI void st64(rsrc_t r, u32x2 v, unsigned voff, unsigned soff) { __builtin_amdgcn_raw_buffer_store_b64(v, r, voff, soff, 16); }
DI void st32(rsrc_t r, unsigned v, unsigned voff, unsigned soff) { __builtin_amdgcn_raw_buffer_store_b32(v, r, voff, soff, 16); }
DI void stflag(rsrc_t r, unsigned v, int lane, unsigned soff) { if (lane == 0) st32(r, v, 0u, soff); }
DI void ld32a(unsigned& d, rsrc_t r, unsigned voff, unsigned soff) { asm volatile("s_nop 4\n\tbuffer_load_dword %0, %1, %2, %3 offen sc1" : "+v"(d) : "v"(voff), "s"(r), "s"(soff) : "memory"); }
DI void ld128a(u32x4& d, rsrc_t r, unsigned voff, unsigned soff) { asm volatile("s_nop 4\n\tbuffer_load_dwordx4 %0, %1, %2, %3 offen sc1" : "+v"(d) : "v"(voff), "s"(r), "s"(soff) : "memory"); }

DI unsigned x1off(int a) { return a == 0 ? 131072u : (unsigned)(81920 + a * 16384); }
DI unsigned nq(unsigned s) { return (s + 32768u) & 131071u; }

struct Ld {
  rsrc_t x, w1, bre, bim, ws, w2, cre, cim;
  unsigned vnat, vperm, vlin, vw2, vw1f, vw1h;
  int wq, i, j, nt, half, rot, n4, k4;
};
DI int k1of(const Ld& c, int s) { int m = s >= 8 ? 7 : (s >= 4 ? 3 : (s >= 2 ? 1 : 0)); return s ^ (c.rot & m); }
DI int k2of(const Ld& c, int s) { int m = s < 8 ? 7 : (s < 12 ? 3 : 0); return (s ^ (c.i & m)) ^ (2 * c.nt) ^ 12; }
DI void gl1(const Ld& c, int s, int a, int u) {
  const int k = k1of(c, s);
  if (u < 4) glds(c.x, c.vnat, (c.i * 128 + u * 32) * 2048 + k * 128, x1off(a) + (u * 4 + c.wq) * 1024);
  else if (u < 10) { const int lc = (u - 4) >> 1, h = (u - 4) & 1;
    glds(c.w1, c.vw1f, (k * 64 + h * 32) * 7168 + c.j * 448 + ((c.j & 1) ? 64 : 0) + lc * 128, a * 32768 + lc * 8192 + (h * 4 + c.wq) * 1024); }
  else glds(c.w1, c.vw1h, k * 64 * 7168 + c.j * 448 + ((c.j & 1) ? 0 : 384), a * 32768 + 24576 + c.wq * 1024);
}
DI void gl2(const Ld& c, int s, unsigned sl, int u) {
  const int k = k2of(c, s);
  if (u < 4) glds(c.x, c.vperm, (c.i * 128 + 64 * (u >> 1) + 8 * (u & 1)) * 2048 + k * 128, sl + (u * 4 + c.wq) * 1024);
  else glds((u & 1) ? c.bim : c.bre, c.vnat, (c.j * 64 + 32 * ((u - 4) >> 1)) * 2048 + k * 128, sl + 16384 + ((u - 4) * 4 + c.wq) * 1024);
}
DI int kb_of(int j, int bl) { return 7 * j + ((j & 1) ? (bl == 6 ? 0 : bl + 1) : bl); }
DI void gl3(const Ld& c, int s, unsigned sl, int u) {
  const int h = u >> 2, n = 2 * s + h, kb = kb_of(c.half * 8 + (((n & 7) ^ c.rot) & 7), n >> 3), v = u & 1;
  if ((u & 2) == 0) glds(c.ws, c.vlin, c.i * HID_PANEL + kb * 8192 + v * 4096, sl + h * 8192 + v * 4096 + c.wq * 1024);
  else glds(c.w2, c.vw2, (kb * 32 + 16 * v) * 2048 + c.nt * 256, sl + 16384 + h * 8192 + v * 4096 + c.wq * 1024);
}
DI void gl4(const Ld& c, int s, unsigned sl, int u) {
  int ks = s + c.rot; if (ks >= c.n4) ks -= c.n4; ks += c.k4;
  if (u < 4) glds(c.ws, c.vlin, H_OFF + c.i * H_PANEL + ks * 16384 + u * 4096, sl + u * 4096 + c.wq * 1024);
  else glds((ks & 1) ? c.cim : c.cre, c.vnat, (c.nt * 128 + (u - 4) * 32) * 2048 + (ks >> 1) * 128, sl + 16384 + ((u - 4) * 4 + c.wq) * 1024);
}
#define ISSUE(fn, n, ...) do { _Pragma("unroll") for (int _u = 0; _u < n; ++_u) fn(__VA_ARGS__, _u); } while (0)

template <int TA, int TB, bool FIRST, int NR, int LAST, int NB, int SL, class RD, class MM, class UP>
DI void slot(RD& rd, MM& mm, UP& upd) {
  constexpr int NM = TA * TB, q = SL + LAG;
  if constexpr (!(FIRST && q < NM)) { constexpr int ss = (q < NM) ? 1 : ((q < 2 * NM) ? 0 : 1); constexpr int m = q % NM; mm(ss, m / TB, m % TB); }
  SB();
  constexpr int r = (SL * (NR - 1) + LAST - 1) / LAST;
  if constexpr (r < NR && (r * LAST) / (NR - 1) == SL) rd(r);
  if constexpr (SL > LAST && SL - LAST - 1 < NB) upd(SL - LAST - 1);
  SB();
}
template <int TA, int TB, bool FIRST, int NR, int LAST, int NB, class RD, class MM, class UP, int... I>
DI void period_(RD& rd, MM& mm, UP& upd, std::integer_sequence<int, I...>) { (slot<TA, TB, FIRST, NR, LAST, NB, I>(rd, mm, upd), ...); }
template <int TA, int TB, bool FIRST, int NR, int LAST, int NB, class RD, class MM, class UP>
DI void period(RD& rd, MM& mm, UP& upd) {
  static_assert(NR - 1 <= LAST && LAST + NB < 2 * TA * TB, "");
  period_<TA, TB, FIRST, NR, LAST, NB>(rd, mm, upd, std::make_integer_sequence<int, 2 * TA * TB>{});
}
template <int TA, int TB, class MM> DI void drain(MM& mm) {
#pragma unroll
  for (int m = LAG; m < TA * TB; ++m) mm(1, m / TB, m % TB);
}
template <class G, class W, int... U> DI void ival_(G& g, W& wk, std::integer_sequence<int, U...>) { ((g(U), SB(), wk(IC<U>{}), SB()), ...); }
template <class G, class W> DI void ival(G&& g, W&& wk) { ival_(g, wk, std::make_integer_sequence<int, 8>{}); }
template <int N, class F, int... I> DI void sfor_(F& f, std::integer_sequence<int, I...>) { (f(IC<I>{}), ...); }
template <int N, class F> DI void sfor(F&& f) { sfor_<N>(f, std::make_integer_sequence<int, N>{}); }

DI void check_flags(unsigned& pv, rsrc_t ws, unsigned voff, unsigned soff, int lane, int lo, int hi, unsigned ep) {
  PIN(pv);
  const bool need = lane >= lo && lane < hi;
  if (__builtin_expect(!__all(!need || pv == ep), 0)) {
    for (unsigned sp = 0; sp < SPIN_CAP; ++sp) {
      __builtin_amdgcn_s_sleep(2);
      ld32a(pv, ws, voff, soff); asm volatile("s_waitcnt vmcnt(0)" : "+v"(pv)::"memory");
      if (__all(!need || pv == ep)) break;
    }
  }
  acquire();
}

__global__ __launch_bounds__(512, 2) void fused_kernel(const u16* __restrict__ x, const u16* __restrict__ lre, const u16* __restrict__ lim,
    const u16* __restrict__ bre, const u16* __restrict__ bim, const u16* __restrict__ cre, const u16* __restrict__ cim,
    const u16* __restrict__ ldt, const u16* __restrict__ dsk, const u16* __restrict__ w1, const u16* __restrict__ b1,
    const u16* __restrict__ w2, const u16* __restrict__ b2, const u16* __restrict__ gw, char* ws, float* out) {
  extern __shared__ __attribute__((aligned(16))) char shm[];
  const int lane = threadIdx.x & 63, w = __builtin_amdgcn_readfirstlane(threadIdx.x >> 6);
  const int bid = blockIdx.x, xcd = bid & 7, loc = bid >> 3;
  Ld c;
  c.i = (xcd & 1) * 8 + (loc & 7); c.j = (xcd >> 1) * 4 + (loc >> 3); c.nt = c.j & 7; c.half = c.j >> 3; c.wq = w & 3; c.rot = (c.i + 2 * c.j) & 7;
  c.n4 = c.half ? 32 - N4A : N4A; c.k4 = c.half ? N4A : 0;
  c.x = mk(x, 2048 * 2048); c.w1 = mk(w1, 1024 * 7168); c.bre = mk(bre, 1024 * 2048); c.bim = mk(bim, 1024 * 2048); c.ws = mk(ws, WS_BYTES);
  c.w2 = mk(w2, 3584 * 2048); c.cre = mk(cre, 1024 * 2048); c.cim = mk(cim, 1024 * 2048);
  {
    const int r = 8 * c.wq + (lane >> 3), sw = ((lane & 7) ^ ((r >> 1) & 7)) * 16;
    c.vnat = r * 2048 + sw;
    const int rho = 8 * (c.wq & 1) + (lane >> 3); c.vperm = (4 * (c.wq >> 1) + 16 * (rho >> 2) + (rho & 3)) * 2048 + sw;
    c.vlin = c.wq * 1024 + lane * 16;
    const int kk = 4 * c.wq + (lane >> 4), s16 = lane & 15; c.vw2 = kk * 2048 + ((((s16 >> 1) ^ (kk & 7)) * 2) + (s16 & 1)) * 16;
    const int f = ((r >> 1) & 1) | (((r >> 3) & 1) << 1); c.vw1f = r * 7168 + (((((lane & 7) >> 1) ^ f) * 2) + (lane & 1)) * 16;
    const int kh = 16 * c.wq + (lane >> 2); c.vw1h = kh * 7168 + (((((lane & 3) >> 1) ^ ((kh >> 3) & 1)) * 2) + (lane & 1)) * 16;
  }
  const int wm = (w >> 1) & 1, wn = w & 1, g = lane >> 4, r15 = lane & 15, q2 = (lane >> 2) & 3, p = lane & 3;
  const unsigned nb0 = r15 * 128 + ((g ^ (r15 >> 1)) * 16), nb1 = r15 * 128 + (((4 + g) ^ (r15 >> 1)) * 16);
  const unsigned flagb = FLAG_OFF + c.i * 1024, fvo = (lane & 15) * 4;
  auto nowork = [&](auto) AI {};

  if (w < 4) {
    unsigned epr = 0, epc = 0;
    ld32a(epr, c.ws, 0u, flagb + FK_EP * 128); ld32a(epc, c.ws, 0u, EPC_OFF + c.j * 128);
    ISSUE(gl1, 11, c, 0, 0); ISSUE(gl1, 11, c, 1, 1); ISSUE(gl1, 11, c, 2, 2);
    waitv<22>(); PIN(epr); PIN(epc);
    epr = __builtin_amdgcn_readfirstlane(epr) + 1u; epc = __builtin_amdgcn_readfirstlane(epc) + 1u;
    bar();
    {
      float s1[4] = {0, 0, 0, 0}, s2[4] = {0, 0, 0, 0}, s3[4] = {0, 0, 0, 0}, cs[4] = {0, 0, 0, 0};
      const unsigned xr0 = (32 * c.wq + (lane >> 3)) * 128 + (((lane & 7) ^ g) * 16), xr1 = (32 * c.wq + (lane >> 3)) * 128 + (((lane & 7) ^ (4 + g)) * 16);
      const int f = ((q2 >> 1) & 1) | ((g & 1) << 1);
      const unsigned cf = (8 * g + q2) * 128 + ((c.wq ^ f) * 32) + p * 8, ch = 24576 + (8 * g + q2) * 64 + (((c.wq & 1) ^ (g & 1)) * 32) + p * 8;
      const unsigned ONE = 0x3F803F80u;
      auto stats = [&](int s, int a, auto U) AI {
        constexpr int u = U.value;
        if constexpr (u < 4) {
          const u32x4 v = rdu4(x1off(a) + ((u & 1) ? xr1 : xr0) + u * 1024), gv = rdu4(T_GW + k1of(c, s) * 128 + (lane & 7) * 16);
#pragma unroll
          for (int e = 0; e < 4; ++e) { s1[u] = dot2(v[e], ONE, s1[u]); s2[u] = dot2(v[e], v[e], s2[u]); s3[u] = dot2(v[e], gv[e], s3[u]); }
        } else if constexpr (u < 8) {
          constexpr int t = u - 4;
#pragma unroll
          for (int e = 0; e < 4; ++e) {
            const u32x2 v = __builtin_bit_cast(u32x2, rtr(a * 32768 + (t < 3 ? cf + t * 8192 + (e >> 1) * 4096 + (e & 1) * 512 : ch + (e >> 1) * 2048 + (e & 1) * 256)));
            cs[t] = dot2(v[0], ONE, cs[t]); cs[t] = dot2(v[1], ONE, cs[t]);
          }
        }
      };
      sfor<8>([&](auto U) AI { stats(0, 0, U); });
      int a = 0;
      for (int s = 0; s < 13; ++s) {
        waitl0(); waitv<11>(); bar();
        const int an = (a == 2) ? 0 : a + 1;
        sfor<11>([&](auto U) AI { gl1(c, s + 3, a, U.value); SB(); stats(s + 1, an, U); SB(); });
        a = an;
      }
      waitl0(); waitv<11>(); bar();
      ival([&](int u) AI { gl2(c, 0, 32768, u); }, [&](auto U) AI { stats(14, 2, U); });
      waitl0(); waitv<8>(); bar();
      ival([&](int u) AI { gl2(c, 1, 65536, u); }, [&](auto U) AI { stats(15, 0, U); });
      ISSUE(gl2, 8, c, 2, 98304);
#pragma unroll
      for (int u = 0; u < 4; ++u) {
        s1[u] = dppadd<0x141>(dppadd<0x4E>(dppadd<0xB1>(s1[u])));
        s2[u] = dppadd<0x141>(dppadd<0x4E>(dppadd<0xB1>(s2[u])));
        s3[u] = dppadd<0x141>(dppadd<0x4E>(dppadd<0xB1>(s3[u])));
        cs[u] += __shfl_xor(cs[u], 16); cs[u] += __shfl_xor(cs[u], 32);
      }
      const int us = lane & 3;
      const float a1 = us == 0 ? s1[0] : us == 1 ? s1[1] : us == 2 ? s1[2] : s1[3];
      const float a2 = us == 0 ? s2[0] : us == 1 ? s2[1] : us == 2 ? s2[2] : s2[3];
      const float a3 = us == 0 ? s3[0] : us == 1 ? s3[1] : us == 2 ? s3[2] : s3[3];
      const float mean = a1 * (1.f / 1024.f), var = fmaxf(a2 * (1.f / 1024.f) - mean * mean, 0.f), rstd = __builtin_amdgcn_rsqf(var + 1e-5f);
      const float qv = 1.f + __expf(fminf(a3, 80.f)), gv = __builtin_amdgcn_rcpf(1.f + __expf(fminf(-a3, 80.f)));
      if ((lane & 4) == 0) {
        const unsigned ra = T_ROW + (32 * c.wq + 8 * us + (lane >> 3)) * 4;
        wrf(ra, rstd); wrf(ra + 512, mean * rstd); wrf(ra + 1024, qv); wrf(ra + 1536, gv);
      }
      const float cv = g == 0 ? cs[0] : g == 1 ? cs[1] : g == 2 ? cs[2] : cs[3];
      wrf(T_COL + (g < 3 ? g * 64 + c.wq * 16 + r15 : 192 + (c.wq & 1) * 16 + r15) * 4, cv);
      waitl0(); waitv<16>(); bar();
    }
    unsigned sl = 32768;
    f32x4 acc[4][4];
#pragma unroll
    for (int a = 0; a < 4; ++a)
#pragma unroll
      for (int b = 0; b < 4; ++b) acc[a][b] = (f32x4){0, 0, 0, 0};
    {
      bf16x8 fa[2][4], fb[2][4];
      const unsigned cb[4] = {wm * 8192u + nb0, wm * 8192u + nb1, 16384 + wn * 8192u + nb0, 16384 + wn * 8192u + nb1};
      unsigned ba[4];
#pragma unroll
      for (int b = 0; b < 4; ++b) { ba[b] = sl + cb[b]; PIN(ba[b]); }
      auto rd = [&](int r) AI { const int ks = r >> 3, xx = r & 7;
        if (xx == 0) fa[ks][0] = rd128(ba[ks]); else if (xx <= 4) fb[ks][xx - 1] = rd128(ba[2 + ks] + (xx - 1) * 2048); else fa[ks][xx - 4] = rd128(ba[ks] + (xx - 4) * 2048); };
      auto mm = [&](int ks, int a, int b) AI { acc[a][b] = MFMA(fa[ks][a], fb[ks][b], acc[a][b]); };
      auto up = [&](int b) AI { ba[b] = nq(sl) + cb[b]; PIN(ba[b]); };
      period<4, 4, true, 16, LASTR, 4>(rd, mm, up); waitl0(); waitv<8>(); bar(); sl = nq(sl);
      for (int s = 1; s < 16; ++s) { period<4, 4, false, 16, LASTR, 4>(rd, mm, up); waitl0(); waitv<0>(); bar(); sl = nq(sl); }
      acquire();
      ISSUE(gl3, 8, c, 3, (sl + 98304u) & 131071u);
      drain<4, 4>(mm);
    }
    {
      const int seg = 4 * wm + g;
      const unsigned ca = (32 * wn + r15) * 8;
      f32x2 lam[2], st[2], Pf[2], Gt[2];
      unsigned pv = epr - 1u;
      int s3 = 4;
      auto G3 = [&](int u) AI { gl3(c, s3, sl, u); };
#define NEXT3() do { sl = nq(sl); ++s3; } while (0)
      bar();
      lam[0] = rdf2(T_CON + ca); lam[1] = rdf2(T_CON + ca + 128);
      st[0] = (f32x2){0, 0}; st[1] = (f32x2){0, 0};
      ival(G3, [&](auto U) AI { constexpr int u = U.value;
#pragma unroll
        for (int t = 2 * u; t < 2 * u + 2; ++t)
#pragma unroll
          for (int ch = 0; ch < 2; ++ch) st[ch] = cfma(lam[ch], st[ch], (f32x2){acc[t >> 2][ch][t & 3], acc[t >> 2][2 + ch][t & 3]});
      });
      wrf2(T_AGG + seg * 512 + ca, st[0]); wrf2(T_AGG + seg * 512 + ca + 128, st[1]);
      NEXT3(); waitl0(); bar();
      {
        const f32x2 l16[2] = {rdf2(T_CON + 3 * 512 + ca), rdf2(T_CON + 3 * 512 + ca + 128)};
        Gt[0] = (f32x2){0, 0}; Gt[1] = (f32x2){0, 0}; Pf[0] = (f32x2){0, 0}; Pf[1] = (f32x2){0, 0};
        ld32a(pv, c.ws, fvo, flagb + FK_M2 * 128);
        ival(G3, [&](auto U) AI { constexpr int u = U.value;
          if constexpr (u < 4) {
#pragma unroll
            for (int sg = 2 * u; sg < 2 * u + 2; ++sg)
#pragma unroll
              for (int ch = 0; ch < 2; ++ch) { if (sg == seg) Pf[ch] = Gt[ch]; Gt[ch] = cfma(l16[ch], Gt[ch], rdf2(T_AGG + sg * 512 + ca + ch * 128)); }
          } else if constexpr (u == 4) {
            const float val = g == 0 ? Gt[0].x : g == 1 ? Gt[0].y : g == 2 ? Gt[1].x : Gt[1].y;
            st64(c.ws, (u32x2){__float_as_uint(val), epc}, (32 * wn + 16 * (g >> 1) + r15) * 16 + (g & 1) * 8, GRAN_OFF + (c.j * 16 + c.i) * 1024);
          }
        });
      }
      NEXT3(); waitl0(); waitv<16 + 2>(); bar();
      ival(G3, nowork); NEXT3(); waitv<16 + 2>(); bar();
      ival(G3, nowork); NEXT3(); waitv<16>(); bar();
      check_flags(pv, c.ws, fvo, flagb + FK_M2 * 128, lane, 8 * c.half, 8 * c.half + 8, epr);
      ival(G3, nowork); NEXT3(); waitv<16>(); bar();
      ival(G3, nowork); NEXT3(); waitv<16>(); bar();
      u32x4 lb[15];
#pragma unroll
      for (int q = 0; q < 15; ++q) lb[q] = (u32x4){0, epc - 1u, 0, epc - 1u};
      const unsigned lbo = (32 * wn + 16 * (g & 1) + r15) * 16;
      ival(G3, [&](auto U) AI { constexpr int u = U.value;
        if constexpr (u < 5) {
#pragma unroll
          for (int q = 3 * u; q < 3 * u + 3; ++q) ld128a(lb[q], c.ws, lbo, GRAN_OFF + (c.j * 16 + q) * 1024);
        } });
      NEXT3(); waitv<16 + 15>(); bar();
      ival(G3, nowork); NEXT3(); waitv<16 + 15>(); bar();
      ival(G3, nowork); NEXT3(); waitv<16>(); bar();
      {
        bool ok = true;
#pragma unroll
        for (int q = 0; q < 15; ++q) { PIN(lb[q]); ok = ok && (q >= c.i || (lb[q][1] == epc && lb[q][3] == epc)); }
        if (__builtin_expect(!__all(ok), 0)) {
          for (unsigned sp = 0; sp < SPIN_CAP; ++sp) {
            __builtin_amdgcn_s_sleep(2);
#pragma unroll
            for (int q = 0; q < 15; ++q) ld128a(lb[q], c.ws, lbo, GRAN_OFF + (c.j * 16 + q) * 1024);
            waitv<0>(); ok = true;
#pragma unroll
            for (int q = 0; q < 15; ++q) { PIN(lb[q]); ok = ok && (q >= c.i || (lb[q][1] == epc && lb[q][3] == epc)); }
            if (__all(ok)) break;
          }
        }
      }
      f32x2 cof[2];
      {
        const f32x2 l128 = rdf2(T_CON + 10 * 512 + ca + (g & 1) * 128);
        f32x2 cr = {0, 0};
        ival(G3, [&](auto U) AI { constexpr int u = U.value;
          if constexpr (u < 5) {
#pragma unroll
            for (int q = 3 * u; q < 3 * u + 3; ++q) { const f32x2 nx = cfma(l128, cr, (f32x2){__uint_as_float(lb[q][0]), __uint_as_float(lb[q][2])}); if (q < c.i) cr = nx; }
          } else if constexpr (u == 5) {
            const f32x2 ot = {__shfl_xor(cr.x, 16), __shfl_xor(cr.y, 16)};
            const f32x2 c0 = (g & 1) ? ot : cr, c1 = (g & 1) ? cr : ot;
            st[0] = cfma(rdf2(T_CON + (2 + seg) * 512 + ca), c0, Pf[0]);
            st[1] = cfma(rdf2(T_CON + (2 + seg) * 512 + ca + 128), c1, Pf[1]);
            cof[0] = rdf2(T_CON + 512 + ca); cof[1] = rdf2(T_CON + 512 + ca + 128);
          } else if constexpr (u == 6) {
            stflag(c.ws, epc, lane, c.i == 15 ? EPC_OFF + c.j * 128 : DUM_OFF + bid * 128);
          }
        });
      }
      NEXT3(); waitl0(); waitv<16 + 1>(); bar();
      {
        unsigned ob[2][2][4];
        float o[2][2][2];
        f32x4 g2;
        sfor<4>([&](auto A) AI { constexpr int a = A.value;
          g2 = rdf4(T_ROW + 1536 + (64 * wm + 16 * g + 4 * a) * 4);
          ival(G3, [&](auto U) AI { constexpr int u = U.value;
            if constexpr (u < 4) {
              const float gg = 2.f * g2[u];
#pragma unroll
              for (int ch = 0; ch < 2; ++ch) {
                st[ch] = cfma(lam[ch], st[ch], (f32x2){acc[a][ch][u], acc[a][2 + ch][u]});
                const f32x2 h = cmul(cof[ch], st[ch]);
                o[ch][0][u & 1] = gg * h.x; o[ch][1][u & 1] = -gg * h.y;
                if constexpr (u & 1) { ob[ch][0][(a & 1) * 2 + (u >> 1)] = pk2(o[ch][0][0], o[ch][0][1]); ob[ch][1][(a & 1) * 2 + (u >> 1)] = pk2(o[ch][1][0], o[ch][1][1]); }
              }
            } else if constexpr ((a & 1) && u < 6) {
              constexpr int hf = a >> 1, ch = u - 4;
#pragma unroll
              for (int pt = 0; pt < 2; ++pt)
                st128(c.ws, (u32x4){ob[ch][pt][0], ob[ch][pt][1], ob[ch][pt][2], ob[ch][pt][3]}, (2 * wn + ch) * 4096 + wm * 2048 + hf * 1024 + g * 256 + ((r15 ^ (hf << 2)) * 16),
                      H_OFF + c.i * H_PANEL + (2 * c.j + pt) * 16384);
            }
          });
          NEXT3(); waitl0();
          if constexpr (a == 0) waitv<16 + 1>(); else waitv<16 + 4>();
          bar();
        });
      }
      ival(G3, nowork); NEXT3(); waitv<16 + 4>(); bar();
      ival(G3, nowork); NEXT3(); waitv<16>(); bar();
      stflag(c.ws, epr, lane, flagb + FK_H * 128 + c.j * 4);
      ival(G3, nowork); NEXT3(); waitv<16 + 1>(); bar();
      ival(G3, nowork); NEXT3(); waitv<16 + 1>(); bar();
      for (int s = 18; s < 21; ++s) { ival(G3, nowork); NEXT3(); waitv<16>(); bar(); }
      pv = epr - 1u; ld32a(pv, c.ws, fvo, flagb + FK_H * 128);
      ival(G3, nowork); NEXT3(); waitv<16 + 1>(); bar();
      ival(G3, nowork); NEXT3(); waitv<16 + 1>(); bar();
      ival(G3, nowork); NEXT3(); waitv<16>(); bar();
      check_flags(pv, c.ws, fvo, flagb + FK_H * 128, lane, c.k4 >> 1, (c.k4 + c.n4 + 1) >> 1, epr);
      for (int s = 0; s < c.n4; ++s) { ISSUE(gl4, 8, c, s, sl); sl = nq(sl); waitv<16>(); bar(); }
      pv = epr - 1u; ld32a(pv, c.ws, fvo, flagb + FK_P * 128);
      waitv<8 + 1>(); bar();
      waitv<0>();
      if (c.half) {
        check_flags(pv, c.ws, fvo, flagb + FK_P * 128, lane, c.nt, c.nt + 1, epr);
#pragma unroll
        for (int u = 0; u < 16; ++u) glds(c.ws, lane * 16u, PART_OFF + (c.i * 8 + c.nt) * 65536 + c.wq * 16384 + u * 1024, ((sl + (c.wq >> 1) * 32768u) & 131071u) + (c.wq & 1) * 16384 + u * 1024);
        if (c.nt == 0 && w == 0) stflag(c.ws, epr, lane, flagb + FK_EP * 128);
      }
      bar();
      waitv<0>(); bar();
      if (!c.half) { bar(); if (w == 0) stflag(c.ws, epr, lane, flagb + FK_P * 128 + c.nt * 4); }
      waitv<0>();
    }
  } else {
    const int ty = threadIdx.x - 256;
    unsigned epr = __hip_atomic_load((const unsigned*)(ws + flagb + FK_EP * 128), __ATOMIC_RELAXED, __HIP_MEMORY_SCOPE_AGENT);
    {
      *(LAS u32x2*)(size_t)(T_GW + ty * 8) = *(const u32x2*)(gw + ty * 4);
      if (ty < 224) { const int col = 224 * c.j + (ty < 192 ? ((c.j & 1) ? 32 : 0) + ty : ((c.j & 1) ? -192 : 0) + ty); wrf(T_COL + 1024 + ty * 4, bf2f(b1[col])); }
      wrf(T_OUT + ty * 4, bf2f(ty < 128 ? b2[c.nt * 128 + ty] : dsk[c.nt * 128 + ty - 128]));
      if (w == 4) {
        const int pch = 64 * c.j + lane;
        const float dt = rbf(expf(bf2f(ldt[pch]))), lr = bf2f(lre[pch]), li = bf2f(lim[pch]);
        const float e = expf(dt * lr); float sn, cs; sincosf(dt * li, &sn, &cs);
        const f32x2 lam = {e * cs, e * sn};
        const float nr = lam.x - 1.f, ni = lam.y, den = 1.f / (lr * lr + li * li);
        wrf2(T_CON + lane * 8, lam); wrf2(T_CON + 512 + lane * 8, (f32x2){(nr * lr + ni * li) * den, (ni * lr - nr * li) * den});
        f32x2 l16 = lam;
#pragma unroll
        for (int q = 0; q < 4; ++q) l16 = cmul(l16, l16);
        f32x2 pw = {1.f, 0.f};
#pragma unroll
        for (int q = 0; q < 9; ++q) { wrf2(T_CON + (2 + q) * 512 + lane * 8, pw); pw = cmul(pw, l16); }
      }
    }
    epr = __builtin_amdgcn_readfirstlane(epr) + 1u;
    unsigned sl = 32768;
    f32x4 acc[4][4];
    {
      f32x4 ac1[4][7];
#pragma unroll
      for (int a = 0; a < 4; ++a)
#pragma unroll
        for (int b = 0; b < 7; ++b) ac1[a][b] = (f32x4){0, 0, 0, 0};
      {
        bf16x8 fa[2][4], fb[2][7];
        const int f = ((q2 >> 1) & 1) | ((g & 1) << 1);
        const unsigned cb[5] = {wm * 8192u + nb0, wm * 8192u + nb1, (8u * g + q2) * 128 + (((2 * wn) ^ f) * 32) + p * 8, (8u * g + q2) * 128 + (((2 * wn + 1) ^ f) * 32) + p * 8,
                                24576 + (8u * g + q2) * 64 + ((wn ^ (g & 1)) * 32) + p * 8};
        unsigned ba[5]; unsigned xn = x1off(1), wnx = 32768;
#pragma unroll
        for (int b = 0; b < 5; ++b) { ba[b] = cb[b] + (b < 2 ? x1off(0) : 0u); PIN(ba[b]); }
        auto rd = [&](int r) AI { const int ks = r / 18, xx = r % 18;
          if (xx == 0) fa[ks][0] = rd128(ba[ks]);
          else if (xx <= 12) { const int b = (xx - 1) >> 1, h = (xx - 1) & 1; sethalf(fb[ks][b], h, rtr(ba[2 + (b & 1)] + (b >> 1) * 8192 + ks * 4096 + h * 512)); }
          else if (xx <= 14) { const int h = xx - 13; sethalf(fb[ks][6], h, rtr(ba[4] + ks * 2048 + h * 256)); }
          else fa[ks][xx - 14] = rd128(ba[ks] + (xx - 14) * 2048); };
        auto mm = [&](int ks, int a, int b) AI { ac1[a][b] = MFMA(fb[ks][b], fa[ks][a], ac1[a][b]); };
        auto up = [&](int b) AI { if (b < 5) { ba[b] = cb[b] + (b < 2 ? xn : wnx); PIN(ba[b]); }
                                  else { wnx = (wnx == 65536u) ? 0u : wnx + 32768u; xn = (xn == 131072u) ? 98304u : xn + 16384u; } };
        waitl0(); bar();
        period<4, 7, true, 36, LASTR + 24, 6>(rd, mm, up); waitl0(); bar();
        for (int s = 1; s < 16; ++s) { period<4, 7, false, 36, LASTR + 24, 6>(rd, mm, up); waitl0(); bar(); }
        ISSUE(gl2, 8, c, 3, 0);
        drain<4, 7>(mm);
      }
      float rs[4], mr[4], qq[4];
#pragma unroll
      for (int a = 0; a < 4; ++a) { const unsigned ra = T_ROW + (64 * wm + 16 * a + r15) * 4; rs[a] = rdf(ra); mr[a] = rdf(ra + 512); qq[a] = rdf(ra + 1024); }
      f32x4 csv[2], b1v[2];
      unsigned hw[4];
      unsigned pv = epr - 1u;
      u32x2 xv[4][4];
      auto gelu = [&](auto I) AI {
        constexpr int idx = I.value;
        if constexpr (idx < 56) {
          constexpr int lc = idx < 48 ? idx >> 4 : 3, a = idx < 48 ? (idx >> 2) & 3 : (idx - 48) >> 1, bb = idx < 48 ? (idx >> 1) & 1 : 0, hh = idx & 1, b = 2 * lc + bb;
          if constexpr ((idx & 15) == 0) {
            const unsigned co = T_COL + (lc < 3 ? lc * 64 + wn * 32 + 4 * g : 192 + wn * 16 + 4 * g) * 4;
            csv[0] = rdf4(co); b1v[0] = rdf4(co + 1024);
            if constexpr (lc < 3) { csv[1] = rdf4(co + 64); b1v[1] = rdf4(co + 64 + 1024); }
          }
          float y[2];
#pragma unroll
          for (int e = 0; e < 2; ++e) {
            const int jj = 2 * hh + e;
            const float u = __builtin_fmaf(ac1[a][b][jj], rs[a], __builtin_fmaf(-mr[a], csv[bb][jj], b1v[bb][jj]));
            const float z = u * __builtin_fmaf(u * u, -0.1029432397f, -2.302208198f);
            const float d = __builtin_fmaf(__builtin_amdgcn_exp2f(z), qq[a], qq[a]);
            y[e] = u * __builtin_amdgcn_rcpf(d);
          }
          hw[bb * 2 + hh] = pk2(y[0], y[1]);
          if constexpr (lc < 3 && (idx & 3) == 3)
            st128(c.ws, (u32x4){hw[0], hw[1], hw[2], hw[3]}, lane * 16u + (4 * wm + a) * 1024, c.i * HID_PANEL + kb_of(c.j, 2 * lc + wn) * 8192);
          if constexpr (lc == 3 && hh == 1)
            st64(c.ws, (u32x2){hw[0], hw[1]}, lane * 16u + (4 * wm + a) * 1024 + wn * 8, c.i * HID_PANEL + kb_of(c.j, 6) * 8192);
        }
      };
      auto E = [](int s) constexpr { int n = 0; if (s >= 0 && s < 10) for (int idx = 6 * s; idx < 6 * s + 6 && idx < 56; ++idx) n += idx < 48 ? ((idx & 3) == 3) : (idx & 1);
                                     if (s == 5 || s == 9 || s == 12) n += 1; return n; };
      sfor<15>([&](auto S) AI { constexpr int s = S.value;
        if constexpr (s >= 2) waitv<16 + E(s - 2) + E(s - 1)>();
        waitl0(); bar();
        if constexpr (s == 5) stflag(c.ws, epr, lane, flagb + FK_M1 * 128 + c.j * 4);
        if constexpr (s == 9) ld32a(pv, c.ws, fvo, flagb + FK_M1 * 128);
        if constexpr (s == 12) {
          stflag(c.ws, epr, lane, flagb + FK_M2 * 128 + c.j * 4);
          check_flags(pv, c.ws, fvo, flagb + FK_M1 * 128, lane, 8 * c.half, 8 * c.half + 8, epr);
        }
        if constexpr (s == 11 || s == 12) {
          if (wn == s - 11) {
            const unsigned xs = nq(sl);
#pragma unroll
            for (int a = 0; a < 4; ++a) {
              const int ir = 64 * wm + 16 * (r15 >> 2) + 4 * a + (r15 & 3);
#pragma unroll
              for (int b = 0; b < 4; ++b) xv[a][b] = *(LAS u32x2*)(size_t)(xs + ir * 128 + (((2 * b + (g >> 1)) ^ ((ir >> 1) & 7)) * 16) + (g & 1) * 8);
            }
          }
        }
        ival([&](int u) AI { if (s < 12) gl2(c, s + 4, sl, u); else gl3(c, s - 12, sl, u); },
             [&](auto U) AI { constexpr int u = U.value;
               if constexpr (s < 10 && u < 6) gelu(IC<6 * s + u>{});
               if constexpr (s >= 13) {
                 constexpr int a = (s - 13) * 2 + (u >> 2), b = u & 3;
                 const float gv = c.half ? 0.f : rdf(T_ROW + 1536 + (64 * wm + 16 * a + r15) * 4), og = c.half ? 0.f : 1.f - gv;
                 const f32x4 bv = rdf4(T_OUT + (64 * wn + 16 * b + 4 * g) * 4), dv = rdf4(T_OUT + 512 + (64 * wn + 16 * b + 4 * g) * 4);
                 const float xe[4] = {bf2f(xv[a][b][0] & 0xffffu), __uint_as_float(xv[a][b][0] & 0xffff0000u), bf2f(xv[a][b][1] & 0xffffu), __uint_as_float(xv[a][b][1] & 0xffff0000u)};
#pragma unroll
                 for (int e = 0; e < 4; ++e) acc[a][b][e] = __builtin_fmaf(gv, rbf(xe[e] * dv[e]), og * bv[e]);
               }
             });
        sl = nq(sl);
      });
      waitv<16>(); waitl0(); bar(); sl = nq(sl);
    }
    bf16x8 fa[2][4], fb[2][4];
    const int kap = 4 * (g & 1) + q2, hf = p >> 1;
    const unsigned wb = 16384 + (4 * g + q2) * 256 + p * 8, hb = (g >> 1) * 4096 + wm * 2048 + hf * 1024 + (8 * (g & 1) + q2) * 16 + (p & 1) * 8;
    const unsigned cb3[5] = {wm * 4096u + lane * 16, wb + (((4 * wn + 0) ^ kap) * 32), wb + (((4 * wn + 1) ^ kap) * 32), wb + (((4 * wn + 2) ^ kap) * 32), wb + (((4 * wn + 3) ^ kap) * 32)};
    const unsigned cb4[4] = {hb + 64u * hf, hb + 64u * (1 ^ hf), 16384 + wn * 8192u + nb0, 16384 + wn * 8192u + nb1};
    unsigned ba[5];
#pragma unroll
    for (int b = 0; b < 5; ++b) { ba[b] = sl + cb3[b]; PIN(ba[b]); }
    auto rd3 = [&](int r) AI { const int ks = r / 12, xx = r % 12;
      if (xx == 0) fa[ks][0] = rd128(ba[0] + ks * 8192);
      else if (xx <= 8) { const int b = (xx - 1) >> 1, h = (xx - 1) & 1; sethalf(fb[ks][b], h, rtr(ba[1 + b] + ks * 8192 + h * 4096)); }
      else fa[ks][xx - 8] = rd128(ba[0] + ks * 8192 + (xx - 8) * 1024); };
    auto rd4 = [&](int r) AI { const int ks = r / 12, xx = r % 12;
      if (xx < 2) sethalf(fa[ks][0], xx, rtr(ba[xx] + ks * 8192));
      else if (xx < 6) fb[ks][xx - 2] = rd128(ba[2 + ks] + (xx - 2) * 2048);
      else { const int a = (xx - 4) >> 1, h = xx & 1; sethalf(fa[ks][a], h, rtr(ba[h] + ks * 8192 + a * 256)); } };
    auto mm = [&](int ks, int a, int b) AI { acc[a][b] = MFMA(fb[ks][b], fa[ks][a], acc[a][b]); };
    auto up3 = [&](int b) AI { ba[b] = nq(sl) + cb3[b]; PIN(ba[b]); };
    auto up4 = [&](int b) AI { ba[b] = nq(sl) + cb4[b]; PIN(ba[b]); };
    period<4, 4, true, 24, LASTR, 5>(rd3, mm, up3); waitl0(); waitv<8>(); bar(); sl = nq(sl);
    for (int s = 1; s < 27; ++s) { period<4, 4, false, 24, LASTR, 5>(rd3, mm, up3); waitl0(); waitv<0>(); bar(); sl = nq(sl); }
    period<4, 4, false, 24, LASTR, 4>(rd3, mm, up4); waitl0(); bar(); sl = nq(sl);
    for (int s = 0; s < c.n4 - 1; ++s) { period<4, 4, false, 24, LASTR, 4>(rd4, mm, up4); waitl0(); bar(); sl = nq(sl); }
    period<4, 4, false, 24, LASTR, 4>(rd4, mm, up4);
    drain<4, 4>(mm);
    bar();
    if (c.half) {
      const unsigned pb = ((sl + 32768u + (c.wq >> 1) * 32768u) & 131071u) + (c.wq & 1) * 16384 + lane * 16;
      float* op = out + (size_t)(c.i * 128 + 64 * wm + r15) * 1024 + c.nt * 128 + 64 * wn + 4 * g;
#pragma unroll
      for (int a = 0; a < 4; ++a)
#pragma unroll
        for (int b = 0; b < 4; ++b) *(f32x4*)(op + a * 16384 + b * 16) = acc[a][b] + rdf4(pb + (a * 4 + b) * 1024);
    } else {
#pragma unroll
      for (int a = 0; a < 4; ++a)
#pragma unroll
        for (int b = 0; b < 4; ++b) st128(c.ws, __builtin_bit_cast(u32x4, acc[a][b]), lane * 16u + (a * 4 + b) * 1024, PART_OFF + (c.i * 8 + c.nt) * 65536 + c.wq * 16384);
      waitv<0>(); bar();
    }
  }
}

extern "C" void kernel_launch(void* const* d_in, const int* in_sizes, int n_in, void* d_out, int out_size, void* d_ws, size_t ws_size, hipStream_t stream) {
  (void)in_sizes; (void)n_in; (void)out_size; (void)ws_size;
  (void)hipFuncSetAttribute((const void*)fused_kernel, hipFuncAttributeMaxDynamicSharedMemorySize, LDS_BYTES);
  fused_kernel<<<256, 512, LDS_BYTES, stream>>>((const u16*)d_in[0], (const u16*)d_in[1], (const u16*)d_in[2], (const u16*)d_in[3], (const u16*)d_in[4],
      (const u16*)d_in[5], (const u16*)d_in[6], (const u16*)d_in[7], (const u16*)d_in[8], (const u16*)d_in[10], (const u16*)d_in[11], (const u16*)d_in[12],
      (const u16*)d_in[13], (const u16*)d_in[14], (char*)d_ws, (float*)d_out);
}
```

```cpp
#include <hip/hip_runtime.h>
#include <utility>

typedef unsigned short u16;
typedef __bf16 bf16x2 __attribute__((ext_vector_type(2)));
typedef __bf16 bf16x4 __attribute__((ext_vector_type(4)));
typedef __bf16 bf16x8 __attribute__((ext_vector_type(8)));
typedef float f32x2 __attribute__((ext_vector_type(2)));
typedef float f32x4 __attribute__((ext_vector_type(4)));
typedef float f32x16 __attribute__((ext_vector_type(16)));
typedef unsigned u32x2 __attribute__((ext_vector_type(2)));
typedef unsigned u32x4 __attribute__((ext_vector_type(4)));
typedef __amdgpu_buffer_rsrc_t rsrc_t;
#define LAS __attribute__((address_space(3)))
#define DI __device__ __forceinline__
#define AI __attribute__((always_inline))
#define SB() __builtin_amdgcn_sched_barrier(0)
#define PIN(x) asm volatile("" : "+v"(x))
#define MFMA(a, b, c) __builtin_amdgcn_mfma_f32_16x16x32_bf16(a, b, c, 0, 0, 0)
#define MFMA32(a, b, c) __builtin_amdgcn_mfma_f32_32x32x16_bf16(a, b, c, 0, 0, 0)
template <int V> using IC = std::integral_constant<int, V>;

constexpr unsigned HID_PANEL = 112 * 8192, H_OFF = 16 * HID_PANEL, H_PANEL = 32 * 16384;
constexpr unsigned PART_OFF = H_OFF + 16 * H_PANEL;
constexpr unsigned GRAN_OFF = PART_OFF + 128 * 65536;
constexpr unsigned RG_OFF = GRAN_OFF + 256 * 1024;
constexpr unsigned CG_OFF = RG_OFF + 16 * 4096;
constexpr unsigned FLAG_OFF = CG_OFF + 256 * 2048;
constexpr unsigned EPC_OFF = FLAG_OFF + 16 * 1024;
constexpr unsigned DUM_OFF = EPC_OFF + 16 * 128;
constexpr unsigned WS_BYTES = DUM_OFF + 256 * 128;
constexpr int FK_M1 = 0, FK_M2 = 1, FK_M3 = 2, FK_H = 3, FK_P = 4, FK_EP = 5;
constexpr unsigned TAB = 147456, T_COL = TAB, T_ROW = TAB + 2048, T_OUT = TAB + 4096, T_CON = TAB + 5120, T_CSP = TAB + 10752, T_AGG = 131072;
constexpr unsigned LDS_BYTES = 163840;
#define N4A 11
constexpr int LAG = 2, LASTR = 25;
constexpr unsigned SPIN_CAP = 1u << 16;

DI rsrc_t mk(const void* p, unsigned bytes) { return __builtin_amdgcn_make_buffer_rsrc((void*)p, 0, bytes, 0x00020000); }
DI void glds(rsrc_t r, unsigned voff, unsigned soff, unsigned lds) {
  asm volatile("s_mov_b32 m0, %0\n\ts_nop 3\n\tbuffer_load_dwordx4 %1, %2, %3 offen lds" ::"s"(lds), "v"(voff), "s"(r), "s"(soff) : "memory");
}
DI void glds1(rsrc_t r, unsigned voff, unsigned soff, unsigned lds) {
  asm volatile("s_mov_b32 m0, %0\n\ts_nop 3\n\tbuffer_load_dwordx4 %1, %2, %3 offen sc1 lds" ::"s"(lds), "v"(voff), "s"(r), "s"(soff) : "memory");
}
template <int N> DI void waitv() { asm volatile("s_waitcnt vmcnt(%0)" ::"i"(N) : "memory"); }
DI void waitl0() { SB(); __builtin_amdgcn_s_waitcnt(0xC07F); asm volatile("" ::: "memory"); }
#define FST(gp, k)
DI void bar() { asm volatile("s_barrier" ::: "memory"); SB(); }
DI bf16x8 rd128(unsigned a) { return *(LAS bf16x8*)(size_t)a; }
DI u32x4 rdu4(unsigned a) { return *(LAS u32x4*)(size_t)a; }
DI f32x4 rdf4(unsigned a) { return *(LAS f32x4*)(size_t)a; }
DI f32x2 rdf2(unsigned a) { return *(LAS f32x2*)(size_t)a; }
DI float rdf(unsigned a) { return *(LAS float*)(size_t)a; }
DI void wrf(unsigned a, float v) { *(LAS float*)(size_t)a = v; }
DI void wrf2(unsigned a, f32x2 v) { *(LAS f32x2*)(size_t)a = v; }
DI bf16x4 rtr(unsigned a) { return __builtin_amdgcn_ds_read_tr16_b64_v4bf16((LAS bf16x4*)(size_t)a); }
DI void sethalf(bf16x8& f, int h, bf16x4 v) {
  if (h == 0) f = __builtin_shufflevector(v, v, 0, 1, 2, 3, -1, -1, -1, -1);
  else { bf16x8 t = __builtin_shufflevector(v, v, -1, -1, -1, -1, 0, 1, 2, 3); f = __builtin_shufflevector(f, t, 0, 1, 2, 3, 12, 13, 14, 15); }
}
DI unsigned f2u(float f) { return __builtin_bit_cast(unsigned, f); }
DI float u2f(unsigned u) { return __builtin_bit_cast(float, u); }
DI float shx(int lane, float v, int m) { return u2f(__builtin_amdgcn_ds_bpermute((lane ^ m) * 4, (int)f2u(v))); }
DI float fexp(float v) { return __builtin_amdgcn_exp2f(v * 1.4426950408889634f); }
DI float bf2f(unsigned v) { return u2f(v << 16); }
DI unsigned pk2(float lo, float hi) { f32x2 v = {lo, hi}; return __builtin_bit_cast(unsigned, __builtin_convertvector(v, bf16x2)); }
DI float rbf(float f) { return u2f(pk2(f, 0.f) << 16); }
DI float dot2(unsigned a, unsigned b, float c) { return __builtin_amdgcn_fdot2_f32_bf16(__builtin_bit_cast(bf16x2, a), __builtin_bit_cast(bf16x2, b), c, false); }
template <int C> DI float dppadd(float v) { return v + u2f(__builtin_amdgcn_update_dpp(0, (int)f2u(v), C, 0xf, 0xf, true)); }
DI float sel4(bool b0, bool b1, float a, float b, float c, float d) { PIN(a); PIN(b); PIN(c); PIN(d); const float lo = b0 ? b : a, hi = b0 ? d : c; return b1 ? hi : lo; }
DI f32x2 cmul(f32x2 a, f32x2 b) { return (f32x2){a.x * b.x - a.y * b.y, a.x * b.y + a.y * b.x}; }
DI f32x2 cfma(f32x2 a, f32x2 b, f32x2 c) { return (f32x2){__builtin_fmaf(a.x, b.x, __builtin_fmaf(-a.y, b.y, c.x)), __builtin_fmaf(a.x, b.y, __builtin_fmaf(a.y, b.x, c.y))}; }
DI void st128(rsrc_t r, u32x4 v, unsigned voff, unsigned soff) { __builtin_amdgcn_raw_buffer_store_b128(v, r, voff, soff, 16); }
DI void st64(rsrc_t r, u32x2 v, unsigned voff, unsigned soff) { __builtin_amdgcn_raw_buffer_store_b64(v, r, voff, soff, 16); }
DI void st32(rsrc_t r, unsigned v, unsigned voff, unsigned soff) { __builtin_amdgcn_raw_buffer_store_b32(v, r, voff, soff, 16); }
DI void stflag(rsrc_t r, unsigned v, int lane, unsigned soff) { if (lane == 0) st32(r, v, 0u, soff); }
DI void ld32a(unsigned& d, rsrc_t r, unsigned voff, unsigned soff) { asm volatile("s_nop 4\n\tbuffer_load_dword %0, %1, %2, %3 offen sc1" : "+v"(d) : "v"(voff), "s"(r), "s"(soff) : "memory"); }
DI void ld16p(unsigned& d, const u16* p, unsigned voff) { asm volatile("s_nop 4\n\tglobal_load_ushort %0, %1, %2" : "+v"(d) : "v"(voff), "s"(p) : "memory"); }
DI void ld64p(u32x2& d, rsrc_t r, unsigned voff, unsigned soff) { asm volatile("s_nop 4\n\tbuffer_load_dwordx2 %0, %1, %2, %3 offen" : "+v"(d) : "v"(voff), "s"(r), "s"(soff) : "memory"); }
DI void ld64a(u32x2& d, rsrc_t r, unsigned voff, unsigned soff) { asm volatile("s_nop 4\n\tbuffer_load_dwordx2 %0, %1, %2, %3 offen sc1" : "+v"(d) : "v"(voff), "s"(r), "s"(soff) : "memory"); }
DI void ld128g(u32x4& d, const u16* p, unsigned voff) { asm volatile("s_nop 4\n\tglobal_load_dwordx4 %0, %1, %2" : "+v"(d) : "v"(voff), "s"(p) : "memory"); }
DI bool anyl(bool b) { return __builtin_amdgcn_ballot_w64(b) != 0; }
DI void ld128p(u32x4& d, rsrc_t r, unsigned voff, unsigned soff) { asm volatile("s_nop 4\n\tbuffer_load_dwordx4 %0, %1, %2, %3 offen" : "+v"(d) : "v"(voff), "s"(r), "s"(soff) : "memory"); }
DI void sincos_f(float t, float& sn, float& cs) {
  const float k = __builtin_rintf(t * 0.63661977236f);
  float r = __builtin_fmaf(k, -1.5707962513f, t); r = __builtin_fmaf(k, -7.5497894159e-08f, r); r = __builtin_fmaf(k, -5.3903029534e-15f, r);
  const float z = r * r;
  const float sp = __builtin_fmaf(r * z, __builtin_fmaf(z, __builtin_fmaf(z, __builtin_fmaf(z, 2.7183114939e-06f, -1.9839334836e-04f), 8.3333293858e-03f), -1.6666666642e-01f), r);
  const float cp = __builtin_fmaf(z, __builtin_fmaf(z, __builtin_fmaf(z, __builtin_fmaf(z, 2.4390448796e-05f, -1.3886763775e-03f), 4.1666623324e-02f), -4.9999999726e-01f), 1.f);
  const int q = (int)k;
  const float a = (q & 1) ? cp : sp, b = (q & 1) ? sp : cp;
  sn = (q & 2) ? -a : a; cs = ((q + 1) & 2) ? -b : b;
}
DI void ld128a(u32x4& d, rsrc_t r, unsigned voff, unsigned soff) { asm volatile("s_nop 4\n\tbuffer_load_dwordx4 %0, %1, %2, %3 offen sc1" : "+v"(d) : "v"(voff), "s"(r), "s"(soff) : "memory"); }

DI unsigned x1off(int a) { return a == 0 ? 131072u : (unsigned)(81920 + a * 16384); }
DI unsigned nq(unsigned s) { return (s + 32768u) & 131071u; }

struct Ld {
  rsrc_t x, w1, bre, bim, ws, w2, cre, cim;
  unsigned vnat, vperm, vlin, vw2, vw1f, vw1h;
  int wq, i, j, nt, half, rot, n4, k4;
};
DI int k1of(const Ld& c, int s) { int m = s >= 8 ? 7 : (s >= 4 ? 3 : (s >= 2 ? 1 : 0)); return s ^ (c.rot & m); }
DI int k2of(const Ld& c, int s) { return s ^ c.rot; }
DI void gl1(const Ld& c, int s, int a, int u) {
  const int k = k1of(c, s);
  if (u < 4) glds(c.x, c.vnat, (c.i * 128 + u * 32) * 2048 + k * 128, x1off(a) + (u * 4 + c.wq) * 1024);
  else if (u < 10) { const int lc = (u - 4) >> 1, h = (u - 4) & 1;
    glds(c.w1, c.vw1f, (k * 64 + h * 32) * 7168 + c.j * 448 + ((c.j & 1) ? 64 : 0) + lc * 128, a * 32768 + lc * 8192 + (h * 4 + c.wq) * 1024); }
  else glds(c.w1, c.vw1h, k * 64 * 7168 + c.j * 448 + ((c.j & 1) ? 0 : 384), a * 32768 + 24576 + c.wq * 1024);
}
DI void gl2(const Ld& c, int s, unsigned sl, int u) {
  const int k = k2of(c, s);
  if (u < 4) glds(c.x, c.vperm, (c.i * 128 + 32 * u) * 2048 + k * 128, sl + (u * 4 + c.wq) * 1024);
  else glds((u & 1) ? c.bim : c.bre, c.vnat, (c.j * 64 + 32 * ((u - 4) >> 1)) * 2048 + k * 128, sl + 16384 + ((u - 4) * 4 + c.wq) * 1024);
}
DI int kb_of(int j, int bl) { return 7 * j + ((j & 1) ? (bl == 6 ? 0 : bl + 1) : bl); }
DI void gl3(const Ld& c, int s, unsigned sl, int u) {
  const int h = u >> 2, n = 2 * s + h, kb = kb_of(c.half * 8 + (((n & 7) ^ c.rot) & 7), n >> 3), v = u & 1;
  if ((u & 2) == 0) glds1(c.ws, c.vlin, c.i * HID_PANEL + kb * 8192 + v * 4096, sl + h * 8192 + v * 4096 + c.wq * 1024);
  else glds(c.w2, c.vw2, (kb * 32 + 16 * v) * 2048 + c.nt * 256, sl + 16384 + h * 8192 + v * 4096 + c.wq * 1024);
}
DI void gl4(const Ld& c, int s, unsigned sl, int u) {
  int ks = s + c.rot; if (ks >= c.n4) ks -= c.n4; ks += c.k4;
  if (u < 4) glds1(c.ws, c.vlin, H_OFF + c.i * H_PANEL + ks * 16384 + u * 4096, sl + u * 4096 + c.wq * 1024);
  else glds((ks & 1) ? c.cim : c.cre, c.vnat, (c.nt * 128 + (u - 4) * 32) * 2048 + (ks >> 1) * 128, sl + 16384 + ((u - 4) * 4 + c.wq) * 1024);
}
#define ISSUE(fn, n, ...) do { _Pragma("unroll") for (int _u = 0; _u < n; ++_u) fn(__VA_ARGS__, _u); } while (0)

template <int TA, int TB, bool FIRST, int NR, int LAST, int NB, int SL, class RD, class MM, class UP>
DI void slot(RD& rd, MM& mm, UP& upd) {
  constexpr int NM = TA * TB, q = SL + LAG;
  if constexpr (!(FIRST && q < NM)) { constexpr int ss = (q < NM) ? 1 : ((q < 2 * NM) ? 0 : 1); constexpr int m = q % NM; mm(ss, m / TB, m % TB); }
  SB();
  constexpr int r = (SL * (NR - 1) + LAST - 1) / LAST;
  if constexpr (r < NR && (r * LAST) / (NR - 1) == SL) rd(r);
  if constexpr (SL > LAST && SL - LAST - 1 < NB) upd(SL - LAST - 1);
  SB();
}
template <int TA, int TB, bool FIRST, int NR, int LAST, int NB, class RD, class MM, class UP, int... I>
DI void period_(RD& rd, MM& mm, UP& upd, std::integer_sequence<int, I...>) { (slot<TA, TB, FIRST, NR, LAST, NB, I>(rd, mm, upd), ...); }
template <int TA, int TB, bool FIRST, int NR, int LAST, int NB, class RD, class MM, class UP>
DI void period(RD& rd, MM& mm, UP& upd) {
  static_assert(NR - 1 <= LAST && LAST + NB < 2 * TA * TB, "");
  period_<TA, TB, FIRST, NR, LAST, NB>(rd, mm, upd, std::make_integer_sequence<int, 2 * TA * TB>{});
}
template <int TA, int TB, class MM> DI void drain(MM& mm) {
#pragma unroll
  for (int m = LAG; m < TA * TB; ++m) mm(1, m / TB, m % TB);
}
template <bool FIRST, int SL, class RD, class MM, class UP> DI void slot32(RD& rd, MM& mm, UP& upd) {
  constexpr int q = SL + 1;
  if constexpr (!(FIRST && q < 8)) mm((q < 8) ? 1 : ((q < 16) ? 0 : 1), q & 7);
  SB();
  if constexpr (SL < 12) { constexpr int r0 = (SL * 4 + 2) / 3, r1 = ((SL + 1) * 4 + 2) / 3; rd(r0); if constexpr (r0 + 1 < r1) rd(r0 + 1); }
  else { upd(2 * (SL - 12)); upd(2 * (SL - 12) + 1); }
  SB();
}
template <bool FIRST, class RD, class MM, class UP, int... I> DI void period32_(RD& rd, MM& mm, UP& upd, std::integer_sequence<int, I...>) { (slot32<FIRST, I>(rd, mm, upd), ...); }
template <bool FIRST, class RD, class MM, class UP> DI void period32(RD& rd, MM& mm, UP& upd) { period32_<FIRST>(rd, mm, upd, std::make_integer_sequence<int, 16>{}); }
template <class G, class W, int... U> DI void ival_(G& g, W& wk, std::integer_sequence<int, U...>) { ((g(U), SB(), wk(IC<U>{}), SB()), ...); }
template <class G, class W> DI void ival(G&& g, W&& wk) { ival_(g, wk, std::make_integer_sequence<int, 8>{}); }
template <int N, class F, int... I> DI void sfor_(F& f, std::integer_sequence<int, I...>) { (f(IC<I>{}), ...); }
template <int N, class F> DI void sfor(F&& f) { sfor_<N>(f, std::make_integer_sequence<int, N>{}); }

DI void check_flags(unsigned& pv, rsrc_t ws, unsigned voff, unsigned soff, int lane, int lo, int hi, unsigned ep) {
  PIN(pv);
  const bool need = lane >= lo && lane < hi;
  if (__builtin_expect(anyl(need && pv != ep), 0)) {
    for (unsigned sp = 0; sp < SPIN_CAP; ++sp) {
      __builtin_amdgcn_s_sleep(2);
      ld32a(pv, ws, voff, soff); asm volatile("s_waitcnt vmcnt(0)" : "+v"(pv)::"memory");
      if (!anyl(need && pv != ep)) break;
    }
  }
}

#if defined(__HIP_DEVICE_COMPILE__) && __HIP_DEVICE_COMPILE__
__attribute__((target("no-packed-fp32-ops")))
#endif
__global__ __launch_bounds__(512, 2) void fused_kernel(const u16* __restrict__ x, const u16* __restrict__ lre, const u16* __restrict__ lim,
    const u16* __restrict__ bre, const u16* __restrict__ bim, const u16* __restrict__ cre, const u16* __restrict__ cim,
    const u16* __restrict__ ldt, const u16* __restrict__ dsk, const u16* __restrict__ w1, const u16* __restrict__ b1,
    const u16* __restrict__ w2, const u16* __restrict__ b2, const u16* __restrict__ gw, char* ws, float* out) {
  extern __shared__ __attribute__((aligned(16))) char shm[];
  const int lane = threadIdx.x & 63, w = __builtin_amdgcn_readfirstlane(threadIdx.x >> 6);
  const int bid = blockIdx.x, xcd = bid & 7, loc = bid >> 3;
  Ld c;
  c.i = (xcd & 1) * 8 + (loc & 7); c.j = (xcd >> 1) * 4 + (loc >> 3); c.nt = c.j & 7; c.half = c.j >> 3; c.wq = w & 3; c.rot = (c.i + 2 * c.j) & 7;
  c.n4 = c.half ? 32 - N4A : N4A; c.k4 = c.half ? N4A : 0;
  c.x = mk(x, 2048 * 2048); c.w1 = mk(w1, 1024 * 7168); c.bre = mk(bre, 1024 * 2048); c.bim = mk(bim, 1024 * 2048); c.ws = mk(ws, WS_BYTES);
  c.w2 = mk(w2, 3584 * 2048); c.cre = mk(cre, 1024 * 2048); c.cim = mk(cim, 1024 * 2048);
  {
    const int r = 8 * c.wq + (lane >> 3), sw = ((lane & 7) ^ ((r >> 1) & 7)) * 16;
    c.vnat = r * 2048 + sw;
    c.vperm = (16 * (lane >> 5) + 4 * c.wq + ((lane >> 3) & 3)) * 2048 + sw;
    c.vlin = c.wq * 1024 + lane * 16;
    const int kk = 4 * c.wq + (lane >> 4), s16 = lane & 15; c.vw2 = kk * 2048 + ((((s16 >> 1) ^ (kk & 7)) * 2) + (s16 & 1)) * 16;
    const int f = ((r >> 1) & 1) | (((r >> 3) & 1) << 1); c.vw1f = r * 7168 + (((((lane & 7) >> 1) ^ f) * 2) + (lane & 1)) * 16;
    const int kh = 16 * c.wq + (lane >> 2); c.vw1h = kh * 7168 + (((((lane & 3) >> 1) ^ ((kh >> 3) & 1)) * 2) + (lane & 1)) * 16;
  }
  const int wm = (w >> 1) & 1, wn = w & 1, g = lane >> 4, r15 = lane & 15, q2 = (lane >> 2) & 3, p = lane & 3;
  const unsigned nb0 = r15 * 128 + ((g ^ (r15 >> 1)) * 16), nb1 = r15 * 128 + (((4 + g) ^ (r15 >> 1)) * 16);
  const unsigned flagb = FLAG_OFF + c.i * 1024, fvo = (lane & 15) * 4;
  auto nowork = [&](auto) AI {};

  if (w < 4) {
    unsigned epr = 0, epc = 0, pdt = 0, plr = 0, pli = 0;
    const int tx = threadIdx.x;
    {
      u32x4 xr[2][2], gq[2]; u32x2 wv[16];
#pragma unroll
      for (int q = 0; q < 2; ++q) { xr[q][0] = (u32x4){0, 0, 0, 0}; xr[q][1] = (u32x4){0, 0, 0, 0}; gq[q] = (u32x4){0, 0, 0, 0}; }
#pragma unroll
      for (int q = 0; q < 16; ++q) wv[q] = (u32x2){0, 0};
      ld32a(epr, c.ws, 0u, flagb + FK_EP * 128); ld32a(epc, c.ws, 0u, EPC_OFF + c.j * 128);
#pragma unroll
      for (int q = 0; q < 4; ++q) ld128p(xr[q >> 1][q & 1], c.x, lane * 16u, (c.i * 128 + 8 * c.j + 2 * c.wq + (q >> 1)) * 2048 + (q & 1) * 1024);
      ld128g(gq[0], gw, lane * 16u); ld128g(gq[1], gw + 512, lane * 16u);
      ld16p(pdt, ldt + 64 * c.j, lane * 2u); ld16p(plr, lre + 64 * c.j, lane * 2u); ld16p(pli, lim + 64 * c.j, lane * 2u);
      ISSUE(gl1, 11, c, 0, 0); ISSUE(gl1, 11, c, 1, 1); ISSUE(gl1, 11, c, 2, 2);
      const unsigned wvo = (lane < 56 ? lane : 55) * 8u;
#pragma unroll
      for (int q = 0; q < 16; ++q) ld64p(wv[q], c.w1, wvo, (64 * c.i + 16 * c.wq + q) * 7168 + c.j * 448);
      waitv<33 + 16>(); PIN(epr); PIN(epc); PIN(pdt); PIN(plr); PIN(pli); PIN(gq[0]); PIN(gq[1]);
      epr = __builtin_amdgcn_readfirstlane(epr) + 1u; epc = __builtin_amdgcn_readfirstlane(epc) + 1u;
      {
        float sv[6];
#pragma unroll
        for (int r = 0; r < 2; ++r) {
          PIN(xr[r][0]); PIN(xr[r][1]);
          float a1 = 0.f, a2 = 0.f, a3 = 0.f;
#pragma unroll
          for (int e = 0; e < 8; ++e) { const unsigned v = xr[r][e >> 2][e & 3]; a1 = dot2(v, 0x3F803F80u, a1); a2 = dot2(v, v, a2); a3 = dot2(v, gq[e >> 2][e & 3], a3); }
          sv[r] = a1; sv[2 + r] = a2; sv[4 + r] = a3;
        }
#pragma unroll
        for (int q = 0; q < 6; ++q) sv[q] = dppadd<0x140>(dppadd<0x141>(dppadd<0x4E>(dppadd<0xB1>(sv[q]))));
#pragma unroll
        for (int q = 0; q < 6; ++q) PIN(sv[q]);
        const bool r1 = lane & 4;
        float a1 = r1 ? sv[1] : sv[0], a2 = r1 ? sv[3] : sv[2], a3 = r1 ? sv[5] : sv[4];
        a1 += shx(lane, a1, 16); a2 += shx(lane, a2, 16); a3 += shx(lane, a3, 16);
        a1 += shx(lane, a1, 32); a2 += shx(lane, a2, 32); a3 += shx(lane, a3, 32);
        const float mean = a1 * (1.f / 1024.f), var = __builtin_fmaxf(a2 * (1.f / 1024.f) - mean * mean, 0.f), rstd = __builtin_amdgcn_rsqf(var + 1e-5f);
        const float qv = 1.f + fexp(__builtin_fminf(a3, 80.f)), gg = __builtin_amdgcn_rcpf(1.f + fexp(__builtin_fminf(-a3, 80.f)));
        const float val = sel4(lane & 1, lane & 2, rstd, mean * rstd, qv, gg);
        if (lane < 8) st64(c.ws, (u32x2){f2u(val), epr}, lane * 8u, RG_OFF + c.i * 4096 + (8 * c.j + 2 * c.wq) * 32);
      }
      waitv<22 + 16 + 1>();
      bar();
      if (w == 0) {
        const float dt = rbf(fexp(bf2f(pdt))), lr = bf2f(plr), li = bf2f(pli);
        const float e = fexp(dt * lr); float sn, cs; sincos_f(dt * li, sn, cs);
        const f32x2 lam = {e * cs, e * sn};
        const float nr = lam.x - 1.f, ni = lam.y, den = 1.f / (lr * lr + li * li);
        wrf2(T_CON + lane * 8, lam); wrf2(T_CON + 512 + lane * 8, (f32x2){(nr * lr + ni * li) * den, (ni * lr - nr * li) * den});
        f32x2 l16 = lam;
#pragma unroll
        for (int q = 0; q < 4; ++q) l16 = cmul(l16, l16);
        f32x2 pw = {1.f, 0.f};
#pragma unroll
        for (int q = 0; q < 9; ++q) { wrf2(T_CON + (2 + q) * 512 + lane * 8, pw); pw = cmul(pw, l16); }
      }
      f32x4 cs4 = {0, 0, 0, 0};
      u32x4 rgv = {0, epr - 1u, 0, epr - 1u};
      const int tc = tx < 224 ? tx : 223;
      const int lcn = (c.j & 1) ? (tc < 32 ? 192 + tc : tc - 32) : tc;
      auto gather = [&]() AI {
#pragma unroll
        for (int q = 0; q < 16; ++q) ld64a(wv[q], c.ws, tc * 8u, CG_OFF + (c.j * 16 + q) * 2048);
        ld128a(rgv, c.ws, tx * 16u, RG_OFF + c.i * 4096);
      };
      auto gbad = [&]() AI { PIN(rgv); unsigned d = (rgv[1] ^ epr) | (rgv[3] ^ epr);
#pragma unroll
        for (int q = 0; q < 16; ++q) { PIN(wv[q]); d |= wv[q][1] ^ epc; }
        return d != 0; };
      auto E1 = [](int s) constexpr { return s == 4 ? 1 : (s == 9 ? 17 : 0); };
      sfor<15>([&](auto S) AI { constexpr int s = S.value;
        if constexpr (s == 0) waitv<11 + 16 + 1>(); else if constexpr (s == 1) waitv<16 + 1 + 11>(); else if constexpr (s < 14) waitv<11 + E1(s - 1)>(); else waitv<8>();
        waitl0(); bar();
        if constexpr (s == 9) {
#pragma unroll
          for (int q = 0; q < 16; ++q) wv[q] = (u32x2){0, epc - 1u};
          gather();
        }
        if constexpr (s == 11) {
          if (__builtin_expect(anyl(gbad()), 0))
            for (unsigned sp = 0; sp < SPIN_CAP; ++sp) { __builtin_amdgcn_s_sleep(2); gather(); waitv<0>(); if (!anyl(gbad())) break; }
        }
        sfor<(s < 13 ? 11 : (s == 13 ? 8 : 16))>([&](auto U) AI { constexpr int u = U.value;
          if constexpr (s < 13) gl1(c, s + 3, s % 3, u); else if constexpr (s == 13) gl2(c, 0, 32768, u); else gl2(c, 1 + (u >> 3), 65536 + (u >> 3) * 32768, u & 7);
          SB();
          if constexpr ((s == 2 || s == 3) && u < 8) {
            constexpr int q = (s - 2) * 8 + u;
            PIN(wv[q]);
            cs4[0] += bf2f(wv[q][0] & 0xffffu); cs4[1] += u2f(wv[q][0] & 0xffff0000u); cs4[2] += bf2f(wv[q][1] & 0xffffu); cs4[3] += u2f(wv[q][1] & 0xffff0000u);
            PIN(cs4);
          }
          if constexpr (s == 3 && u == 8) { if (lane < 56) *(LAS f32x4*)(size_t)(T_CSP + c.wq * 896 + lane * 16) = cs4; }
          if constexpr (s == 4 && u == 2) {
            const float v = (rdf(T_CSP + tc * 4) + rdf(T_CSP + 896 + tc * 4)) + (rdf(T_CSP + 1792 + tc * 4) + rdf(T_CSP + 2688 + tc * 4));
            st64(c.ws, (u32x2){f2u(v), epc}, tc * 8u, CG_OFF + (c.j * 16 + c.i) * 2048);
          }
          if constexpr (s == 11 && u == 1) {
            float v = 0.f;
#pragma unroll
            for (int q = 0; q < 16; ++q) v += u2f(wv[q][0]);
            wrf(T_COL + lcn * 4, v);
            wrf(T_ROW + (tx & 1) * 1024 + (tx >> 1) * 4, u2f(rgv[0])); wrf(T_ROW + (tx & 1) * 1024 + 512 + (tx >> 1) * 4, u2f(rgv[2]));
          }
          SB();
        });
      });
      waitv<16>(); bar();
    }
    unsigned sl = 32768;
    f32x16 acc[2][2];
#pragma unroll
    for (int a = 0; a < 4; ++a)
#pragma unroll
      for (int e = 0; e < 16; ++e) acc[a >> 1][a & 1][e] = 0.f;
    const int hl = lane >> 5, r31 = lane & 31;
    {
      bf16x8 fa[2][4], fb[2][4];
      unsigned cb[8], ba[8];
#pragma unroll
      for (int b = 0; b < 8; ++b) { cb[b] = (b < 4 ? wm * 8192u : 16384 + wn * 8192u) + r31 * 128 + (((2 * (b & 3) + hl) ^ ((r31 >> 1) & 7)) * 16); ba[b] = sl + cb[b]; PIN(ba[b]); }
      auto rd = [&](int r) AI { const int k16 = r >> 2, ks = r >> 3, kk = k16 & 1, xx = r & 3;
        if (xx == 0) fa[ks][kk * 2] = rd128(ba[k16]); else if (xx == 3) fa[ks][kk * 2 + 1] = rd128(ba[k16] + 4096); else fb[ks][kk * 2 + xx - 1] = rd128(ba[4 + k16] + (xx - 1) * 4096); };
      auto mm = [&](int ks, int m) AI { const int kk = m >> 2, mt = (m >> 1) & 1, nt = m & 1; acc[mt][nt] = MFMA32(fa[ks][kk * 2 + mt], fb[ks][kk * 2 + nt], acc[mt][nt]); };
      auto up = [&](int b) AI { ba[b] = nq(sl) + cb[b]; PIN(ba[b]); };
      period32<true>(rd, mm, up); waitl0(); waitv<8>(); bar(); sl = nq(sl);
      for (int s = 1; s < 16; ++s) { period32<false>(rd, mm, up); waitl0(); waitv<0>(); bar(); sl = nq(sl); }
#pragma unroll
      for (int m = 1; m < 8; ++m) { gl3(c, 3, (sl + 98304u) & 131071u, m - 1); SB(); mm(1, m); SB(); }
      gl3(c, 3, (sl + 98304u) & 131071u, 7);
    }
    {
      const int seg0 = 4 * wm + hl;
      const unsigned ca = (32 * wn + r31) * 8;
      f32x2 st[2], Pf[2], Gt;
      unsigned pv = epr - 1u;
      int s3 = 4;
      auto G3 = [&](int u) AI { gl3(c, s3, sl, u); };
#define NEXT3() do { sl = nq(sl); ++s3; } while (0)
      const f32x2 lam = rdf2(T_CON + ca);
      st[0] = (f32x2){0, 0}; st[1] = (f32x2){0, 0};
      sfor<2>([&](auto H) AI { constexpr int h2 = H.value;
        bar();
        if constexpr (h2 == 1) { stflag(c.ws, epr, lane, flagb + FK_M3 * 128 + c.j * 4); ld32a(pv, c.ws, fvo, flagb + FK_M2 * 128); }
        ival(G3, [&](auto U) AI { constexpr int t = 8 * h2 + U.value;
#pragma unroll
          for (int mt = 0; mt < 2; ++mt) { st[mt] = cfma(lam, st[mt], (f32x2){acc[mt][0][t], acc[mt][1][t]}); PIN(st[mt]); }
        });
        NEXT3();
      });
      wrf2(T_AGG + seg0 * 512 + ca, st[0]); wrf2(T_AGG + (seg0 + 2) * 512 + ca, st[1]);
      waitl0(); waitv<16 + 2>(); bar();
      {
        const f32x2 l16 = rdf2(T_CON + 3 * 512 + ca);
        f32x2 ag[8];
#pragma unroll
        for (int sg = 0; sg < 8; ++sg) ag[sg] = rdf2(T_AGG + sg * 512 + ca);
        Gt = (f32x2){0, 0}; Pf[0] = (f32x2){0, 0}; Pf[1] = (f32x2){0, 0};
        SB();
        ival(G3, [&](auto U) AI { constexpr int u = U.value;
          if constexpr (u >= 1 && u < 5) {
#pragma unroll
            for (int sg = 2 * u - 2; sg < 2 * u; ++sg) {
              const int mt = (sg >> 1) & 1; const bool me = sg == seg0 + 2 * mt;
              Pf[mt].x = me ? Gt.x : Pf[mt].x; Pf[mt].y = me ? Gt.y : Pf[mt].y; Gt = cfma(l16, Gt, ag[sg]);
            }
            PIN(Gt); PIN(Pf[0]); PIN(Pf[1]);
          } else if constexpr (u == 5) {
            st64(c.ws, (u32x2){f2u(hl ? Gt.y : Gt.x), epc}, ca * 2 + hl * 8, GRAN_OFF + (c.j * 16 + c.i) * 1024);
          }
        });
      }
      NEXT3(); waitv<16 + 2 + 1>(); bar();
      ival(G3, nowork); NEXT3(); waitv<16 + 1>(); bar();
      check_flags(pv, c.ws, fvo, flagb + FK_M2 * 128, lane, 8 * c.half, 8 * c.half + 8, epr);
      ival(G3, nowork); NEXT3(); waitv<16>(); bar();
      ival(G3, nowork); NEXT3(); waitv<16>(); bar();
      u32x4 lb[15];
#pragma unroll
      for (int q = 0; q < 15; ++q) lb[q] = (u32x4){0, epc - 1u, 0, epc - 1u};
      ival(G3, [&](auto U) AI { constexpr int u = U.value;
        if constexpr (u < 5) {
#pragma unroll
          for (int q = 3 * u; q < 3 * u + 3; ++q) ld128a(lb[q], c.ws, ca * 2, GRAN_OFF + (c.j * 16 + q) * 1024);
        } });
      NEXT3(); waitv<16 + 15>(); bar();
      ival(G3, nowork); NEXT3(); waitv<16 + 15>(); bar();
      ival(G3, nowork); NEXT3(); waitv<16>(); bar();
      pv = epr - 1u; ld32a(pv, c.ws, fvo, flagb + FK_M3 * 128);
      auto bad = [&]() AI { unsigned d = 0;
#pragma unroll
        for (int q = 0; q < 15; ++q) { PIN(lb[q]); const unsigned m = q < c.i ? 0xFFFFFFFFu : 0u; d |= ((lb[q][1] ^ epc) | (lb[q][3] ^ epc)) & m; }
        return d != 0; };
      ival(G3, [&](auto U) AI { constexpr int u = U.value;
        if constexpr (u == 3) {
          if (__builtin_expect(anyl(bad()), 0)) {
            for (unsigned sp = 0; sp < SPIN_CAP; ++sp) {
              __builtin_amdgcn_s_sleep(2);
#pragma unroll
              for (int q = 0; q < 15; ++q) ld128a(lb[q], c.ws, ca * 2, GRAN_OFF + (c.j * 16 + q) * 1024);
              waitv<0>();
              if (!anyl(bad())) break;
            }
          }
        } else if constexpr (u == 6) {
          stflag(c.ws, epc, lane, c.i == 15 ? EPC_OFF + c.j * 128 : DUM_OFF + bid * 128);
        } });
      NEXT3(); waitv<16 + 2>(); bar();
      const f32x2 cof = rdf2(T_CON + 512 + ca);
      {
        const f32x2 l128 = rdf2(T_CON + 10 * 512 + ca);
        const f32x2 pw0 = rdf2(T_CON + (2 + seg0) * 512 + ca), pw1 = rdf2(T_CON + (4 + seg0) * 512 + ca);
        f32x2 cr = {0, 0};
        ival(G3, [&](auto U) AI { constexpr int u = U.value;
#pragma unroll
          for (int q = 2 * u; q < 2 * u + 2; ++q)
            if (q < 15) { const float m = q < c.i ? 1.f : 0.f;
              if (m != 0.f) cr = cfma(l128, cr, (f32x2){u2f(lb[q][0]), u2f(lb[q][2])}); }
          if constexpr (u == 7) { st[0] = cfma(pw0, cr, Pf[0]); st[1] = cfma(pw1, cr, Pf[1]); PIN(st[0]); PIN(st[1]); }
          PIN(cr);
        });
      }
      NEXT3(); waitv<16 + 2>(); bar();
      {
        unsigned ob[2][2][4];
        sfor<4>([&](auto Q) AI { constexpr int q4 = Q.value, hf = q4 >> 1;
          if constexpr (q4 == 1) check_flags(pv, c.ws, fvo, flagb + FK_M3 * 128, lane, 8 * c.half, 8 * c.half + 8, epr);
          const f32x4 g2[2] = {rdf4(T_ROW + 1536 + (64 * wm + 16 * hl + 4 * q4) * 4), rdf4(T_ROW + 1536 + (64 * wm + 32 + 16 * hl + 4 * q4) * 4)};
          float o[2][2][2];
          ival(G3, [&](auto U) AI { constexpr int u = U.value;
            if constexpr (u >= 1 && u < 5) {
              constexpr int e = u - 1, t = 4 * q4 + e;
#pragma unroll
              for (int mt = 0; mt < 2; ++mt) {
                const float gg = 2.f * g2[mt][e];
                st[mt] = cfma(lam, st[mt], (f32x2){acc[mt][0][t], acc[mt][1][t]});
                const f32x2 h = cmul(cof, st[mt]);
                o[mt][0][e & 1] = gg * h.x; o[mt][1][e & 1] = -gg * h.y;
                PIN(st[mt]); PIN(o[mt][0][e & 1]); PIN(o[mt][1][e & 1]);
                if constexpr (e & 1) { ob[mt][0][(t >> 1) & 3] = pk2(o[mt][0][0], o[mt][0][1]); ob[mt][1][(t >> 1) & 3] = pk2(o[mt][1][0], o[mt][1][1]); PIN(ob[mt][0][(t >> 1) & 3]); PIN(ob[mt][1][(t >> 1) & 3]); }
              }
            } else if constexpr ((q4 & 1) && (u == 5 || u == 6)) {
              constexpr int mt = u - 5;
#pragma unroll
              for (int pt = 0; pt < 2; ++pt)
                st128(c.ws, (u32x4){ob[mt][pt][0], ob[mt][pt][1], ob[mt][pt][2], ob[mt][pt][3]},
                      (2 * wn + (r31 >> 4)) * 4096 + wm * 2048 + hf * 1024 + (2 * mt + hl) * 256 + ((r15 ^ (hf << 2)) * 16), H_OFF + c.i * H_PANEL + (2 * c.j + pt) * 16384);
            }
          });
          NEXT3();
          if constexpr (q4 == 0) waitv<16>(); else waitv<16 + 4>();
          bar();
        });
      }
      u32x4 xe[4][2];
#pragma unroll
      for (int q = 0; q < 8; ++q) xe[q >> 1][q & 1] = (u32x4){0, 0, 0, 0};
      ival(G3, [&](auto U) AI { constexpr int u = U.value;
        if constexpr (u < 4) {
#pragma unroll
          for (int hh = 0; hh < 2; ++hh) ld128p(xe[u][hh], c.x, (64u * wm + r15) * 2048 + wn * 128 + g * 16, (c.i * 128 + 16 * u) * 2048 + c.nt * 256 + hh * 64);
        } });
      NEXT3(); waitv<16 + 4 + 8>(); bar();
      ival(G3, nowork); NEXT3(); waitv<16 + 8>(); bar();
      stflag(c.ws, epr, lane, flagb + FK_H * 128 + c.j * 4);
      ival(G3, nowork); NEXT3(); waitv<16 + 1>(); bar();
      f32x4 ev[4][2][2];
      {
        f32x4 bv[2][2], dv[2][2]; float gr[4];
#pragma unroll
        for (int q = 0; q < 4; ++q) { const unsigned oa = T_OUT + (64 * wn + 32 * (q >> 1) + 8 * g + 4 * (q & 1)) * 4; bv[q >> 1][q & 1] = rdf4(oa); dv[q >> 1][q & 1] = rdf4(oa + 512);
                                      gr[q] = rdf(T_ROW + 1536 + (64 * wm + 16 * q + r15) * 4); }
        sfor<6>([&](auto A) AI { constexpr int a6 = A.value;
          if constexpr (a6 == 3) { pv = epr - 1u; ld32a(pv, c.ws, fvo, flagb + FK_H * 128); }
          ival(G3, [&](auto U) AI { constexpr int u = U.value;
            if constexpr ((u & 1) && u < 7 && 3 * a6 + (u >> 1) < 16) {
              constexpr int id = 3 * a6 + (u >> 1), a = id >> 2, hh = (id >> 1) & 1, q = id & 1;
              PIN(xe[a][hh]);
#pragma unroll
              for (int e = 0; e < 4; ++e) {
                const unsigned xw = xe[a][hh][2 * q + (e >> 1)];
                const float xf = (e & 1) ? u2f(xw & 0xffff0000u) : bf2f(xw & 0xffffu);
                ev[a][hh][q][e] = __builtin_fmaf(gr[a], rbf(xf * dv[hh][q][e]) - bv[hh][q][e], bv[hh][q][e]);
              }
              PIN(ev[a][hh][q]);
            } });
          NEXT3();
          if constexpr (a6 == 0 || a6 == 3 || a6 == 4) waitv<16 + 1>(); else waitv<16>();
          bar();
        });
      }
      check_flags(pv, c.ws, fvo, flagb + FK_H * 128, lane, c.k4 >> 1, (c.k4 + c.n4 + 1) >> 1, epr);
      for (int s = 0; s < c.n4; ++s) { ISSUE(gl4, 8, c, s, sl); sl = nq(sl); waitv<16>(); bar(); }
      pv = epr - 1u; ld32a(pv, c.ws, fvo, flagb + FK_P * 128);
      waitv<8 + 1>(); bar();
      waitv<0>();
      const unsigned pb = ((sl + (c.wq >> 1) * 32768u) & 131071u) + (c.wq & 1) * 16384;
      if (c.half) {
        check_flags(pv, c.ws, fvo, flagb + FK_P * 128, lane, c.nt, c.nt + 1, epr);
#pragma unroll
        for (int u = 0; u < 16; ++u) glds1(c.ws, lane * 16u, PART_OFF + (c.i * 8 + c.nt) * 65536 + c.wq * 16384 + u * 1024, pb + u * 1024);
        if (c.nt == 0 && w == 0) stflag(c.ws, epr, lane, flagb + FK_EP * 128);
      } else {
#pragma unroll
        for (int a = 0; a < 4; ++a)
#pragma unroll
          for (int q = 0; q < 4; ++q) *(LAS f32x4*)(size_t)(pb + (a * 4 + 2 * (q >> 1) + (g >> 1)) * 1024 + ((2 * (g & 1) + (q & 1)) * 16 + r15) * 16) = ev[a][q >> 1][q & 1];
      }
      bar();
      waitl0(); waitv<0>(); bar();
      if (!c.half) { bar(); if (w == 0) stflag(c.ws, epr, lane, flagb + FK_P * 128 + c.nt * 4); }
      waitv<0>();
    }
  } else {
    const int ty = threadIdx.x - 256;
    unsigned epr = __hip_atomic_load((const unsigned*)(ws + flagb + FK_EP * 128), __ATOMIC_RELAXED, __HIP_MEMORY_SCOPE_AGENT);
    {
      const int tc = ty < 224 ? ty : 223;
      const unsigned bq = b1[224 * c.j + (tc < 192 ? ((c.j & 1) ? 32 : 0) + tc : ((c.j & 1) ? -192 : 0) + tc)];
      const unsigned oq = (ty < 128 ? b2 : dsk)[c.nt * 128 + (ty & 127)];
      wrf(T_COL + 1024 + ty * 4, bf2f(bq));
      wrf(T_OUT + ty * 4, bf2f(oq));
    }
    epr = __builtin_amdgcn_readfirstlane(epr) + 1u;
    unsigned sl = 32768;
    f32x4 acc[4][4];
    {
      f32x4 ac1[4][7];
#pragma unroll
      for (int a = 0; a < 4; ++a)
#pragma unroll
        for (int b = 0; b < 7; ++b) ac1[a][b] = (f32x4){0, 0, 0, 0};
      {
        bf16x8 fa[2][4], fb[2][7];
        const int f = ((q2 >> 1) & 1) | ((g & 1) << 1);
        const unsigned cb[5] = {wm * 8192u + nb0, wm * 8192u + nb1, (8u * g + q2) * 128 + (((2 * wn) ^ f) * 32) + p * 8, (8u * g + q2) * 128 + (((2 * wn + 1) ^ f) * 32) + p * 8,
                                24576 + (8u * g + q2) * 64 + ((wn ^ (g & 1)) * 32) + p * 8};
        unsigned ba[5]; unsigned xn = x1off(1), wnx = 32768;
#pragma unroll
        for (int b = 0; b < 5; ++b) { ba[b] = cb[b] + (b < 2 ? x1off(0) : 0u); PIN(ba[b]); }
        auto rd = [&](int r) AI { const int ks = r / 18, xx = r % 18;
          if (xx == 0) fa[ks][0] = rd128(ba[ks]);
          else if (xx <= 12) { const int b = (xx - 1) >> 1, h = (xx - 1) & 1; sethalf(fb[ks][b], h, rtr(ba[2 + (b & 1)] + (b >> 1) * 8192 + ks * 4096 + h * 512)); }
          else if (xx <= 14) { const int h = xx - 13; sethalf(fb[ks][6], h, rtr(ba[4] + ks * 2048 + h * 256)); }
          else fa[ks][xx - 14] = rd128(ba[ks] + (xx - 14) * 2048); };
        auto mm = [&](int ks, int a, int b) AI { ac1[a][b] = MFMA(fb[ks][b], fa[ks][a], ac1[a][b]); };
        auto up = [&](int b) AI { if (b < 5) { ba[b] = cb[b] + (b < 2 ? xn : wnx); PIN(ba[b]); }
                                  else { wnx = (wnx == 65536u) ? 0u : wnx + 32768u; xn = (xn == 131072u) ? 98304u : xn + 16384u; } };
        waitl0(); bar();
        period<4, 7, true, 36, LASTR + 24, 6>(rd, mm, up); waitl0(); bar();
        for (int s = 1; s < 16; ++s) { period<4, 7, false, 36, LASTR + 24, 6>(rd, mm, up); waitl0(); bar(); }
        ISSUE(gl2, 8, c, 3, 0);
        drain<4, 7>(mm);
      }
      float rs[4], mr[4], qq[4];
#pragma unroll
      for (int a = 0; a < 4; ++a) { const unsigned ra = T_ROW + (64 * wm + 16 * a + r15) * 4; rs[a] = rdf(ra); mr[a] = rdf(ra + 512); qq[a] = rdf(ra + 1024); }
      f32x4 csv[2], b1v[2];
      unsigned hw[4];
      unsigned pv = epr - 1u;
      auto gelu = [&](auto I) AI {
        constexpr int idx = I.value;
        if constexpr (idx < 28) {
          constexpr int lc = idx >> 3, a = idx < 24 ? (idx >> 1) & 3 : idx - 24, bb = idx < 24 ? idx & 1 : 0, b = 2 * lc + bb;
          float y[4];
#pragma unroll
          for (int e = 0; e < 4; ++e) {
            const float u = __builtin_fmaf(ac1[a][b][e], rs[a], __builtin_fmaf(-mr[a], csv[bb][e], b1v[bb][e]));
            const float z = u * __builtin_fmaf(u * u, -0.1029432397f, -2.302208198f);
            const float d = __builtin_fmaf(__builtin_amdgcn_exp2f(z), qq[a], qq[a]);
            y[e] = u * __builtin_amdgcn_rcpf(d);
          }
          hw[bb * 2] = pk2(y[0], y[1]); hw[bb * 2 + 1] = pk2(y[2], y[3]); PIN(hw[bb * 2]); PIN(hw[bb * 2 + 1]);
          if constexpr (lc < 3 && bb == 1)
            st128(c.ws, (u32x4){hw[0], hw[1], hw[2], hw[3]}, lane * 16u + (4 * wm + a) * 1024, c.i * HID_PANEL + kb_of(c.j, 2 * lc + wn) * 8192);
          if constexpr (lc == 3)
            st64(c.ws, (u32x2){hw[0], hw[1]}, lane * 16u + (4 * wm + a) * 1024 + wn * 8, c.i * HID_PANEL + kb_of(c.j, 6) * 8192);
          if constexpr ((idx & 7) == 7) {
            SB();
            const unsigned co = T_COL + (lc < 2 ? (lc + 1) * 64 + wn * 32 + 4 * g : 192 + wn * 16 + 4 * g) * 4;
            csv[0] = rdf4(co); b1v[0] = rdf4(co + 1024); csv[1] = rdf4(co + 64); b1v[1] = rdf4(co + 64 + 1024);
          }
        }
      };
      { const unsigned co = T_COL + (wn * 32 + 4 * g) * 4; csv[0] = rdf4(co); b1v[0] = rdf4(co + 1024); csv[1] = rdf4(co + 64); b1v[1] = rdf4(co + 64 + 1024); }
      auto E = [](int s) constexpr { int n = 0; if (s >= 0 && s < 12) n = 1; else if (s < 14) n = 2;
                                     if (s == 6 || s == 9 || s == 10) n += 1; return n; };
      sfor<15>([&](auto S) AI { constexpr int s = S.value;
        if constexpr (s >= 2) waitv<16 + E(s - 2) + E(s - 1)>();
        bar();
        if constexpr (s == 6) stflag(c.ws, epr, lane, flagb + FK_M1 * 128 + c.j * 4);
        if constexpr (s == 9) ld32a(pv, c.ws, fvo, flagb + FK_M1 * 128);
        if constexpr (s == 10) stflag(c.ws, epr, lane, flagb + FK_M2 * 128 + c.j * 4);
        if constexpr (s == 12) check_flags(pv, c.ws, fvo, flagb + FK_M1 * 128, lane, 8 * c.half, 8 * c.half + 8, epr);
        ival([&](int u) AI { if (s < 12) gl2(c, s + 4, sl, u); else gl3(c, s - 12, sl, u); },
             [&](auto U) AI { constexpr int u = U.value;
               if constexpr (u == 1) gelu(IC<2 * s>{});
               if constexpr (u == 4) gelu(IC<2 * s + 1>{});
             });
        sl = nq(sl);
      });
      waitv<16 + E(13)>(); bar(); sl = nq(sl);
    }
#pragma unroll
    for (int a = 0; a < 4; ++a)
#pragma unroll
      for (int b = 0; b < 4; ++b) acc[a][b] = (f32x4){0, 0, 0, 0};
    bf16x8 fa[2][4], fb[2][4];
    const int kap = 4 * (g & 1) + q2, hf = p >> 1;
    const unsigned wb = 16384 + (4 * g + q2) * 256 + p * 8, hb = (g >> 1) * 4096 + wm * 2048 + hf * 1024 + (8 * (g & 1) + q2) * 16 + (p & 1) * 8;
    const unsigned cb3[5] = {wm * 4096u + lane * 16, wb + (((4 * wn + 0) ^ kap) * 32), wb + (((4 * wn + 1) ^ kap) * 32), wb + (((4 * wn + 2) ^ kap) * 32), wb + (((4 * wn + 3) ^ kap) * 32)};
    const unsigned cb4[4] = {hb + 64u * hf, hb + 64u * (1 ^ hf), 16384 + wn * 8192u + nb0, 16384 + wn * 8192u + nb1};
    unsigned ba[5];
#pragma unroll
    for (int b = 0; b < 5; ++b) { ba[b] = sl + cb3[b]; PIN(ba[b]); }
    auto rd3 = [&](int r) AI { const int ks = r / 12, xx = r % 12;
      if (xx == 0) fa[ks][0] = rd128(ba[0] + ks * 8192);
      else if (xx <= 8) { const int b = (xx - 1) >> 1, h = (xx - 1) & 1; sethalf(fb[ks][b], h, rtr(ba[1 + b] + ks * 8192 + h * 4096)); }
      else fa[ks][xx - 8] = rd128(ba[0] + ks * 8192 + (xx - 8) * 1024); };
    auto rd4 = [&](int r) AI { const int ks = r / 12, xx = r % 12;
      if (xx < 2) sethalf(fa[ks][0], xx, rtr(ba[xx] + ks * 8192));
      else if (xx < 6) fb[ks][xx - 2] = rd128(ba[2 + ks] + (xx - 2) * 2048);
      else { const int a = (xx - 4) >> 1, h = xx & 1; sethalf(fa[ks][a], h, rtr(ba[h] + ks * 8192 + a * 256)); } };
    auto mm = [&](int ks, int a, int b) AI { acc[a][b] = MFMA(fb[ks][b], fa[ks][a], acc[a][b]); };
    auto up3 = [&](int b) AI { ba[b] = nq(sl) + cb3[b]; PIN(ba[b]); };
    auto up4 = [&](int b) AI { ba[b] = nq(sl) + cb4[b]; PIN(ba[b]); };
    period<4, 4, true, 24, LASTR, 5>(rd3, mm, up3); waitl0(); waitv<8>(); bar(); sl = nq(sl);
    for (int s = 1; s < 27; ++s) { period<4, 4, false, 24, LASTR, 5>(rd3, mm, up3); waitl0(); waitv<0>(); bar(); sl = nq(sl); }
    period<4, 4, false, 24, LASTR, 4>(rd3, mm, up4); waitl0(); bar(); sl = nq(sl);
    for (int s = 0; s < c.n4 - 1; ++s) { period<4, 4, false, 24, LASTR, 4>(rd4, mm, up4); waitl0(); bar(); sl = nq(sl); }
    period<4, 4, false, 24, LASTR, 4>(rd4, mm, up4);
    drain<4, 4>(mm);
    SB(); bar();
    const unsigned pb = ((sl + 32768u + (c.wq >> 1) * 32768u) & 131071u) + (c.wq & 1) * 16384 + lane * 16;
    if (c.half) {
      float* op = out + (size_t)(c.i * 128 + 64 * wm + r15) * 1024 + c.nt * 128 + 64 * wn + 4 * g;
#pragma unroll
      for (int a = 0; a < 4; ++a)
#pragma unroll
        for (int b = 0; b < 4; ++b) *(f32x4*)(op + a * 16384 + b * 16) = acc[a][b] + rdf4(pb + (a * 4 + b) * 1024);
    } else {
#pragma unroll
      for (int a = 0; a < 4; ++a)
#pragma unroll
        for (int b = 0; b < 4; ++b) st128(c.ws, __builtin_bit_cast(u32x4, acc[a][b] + rdf4(pb + (a * 4 + b) * 1024)), lane * 16u + (a * 4 + b) * 1024, PART_OFF + (c.i * 8 + c.nt) * 65536 + c.wq * 16384);
      waitv<0>(); bar();
    }
  }
}

extern "C" void kernel_launch(void* const* d_in, const int* in_sizes, int n_in, void* d_out, int out_size, void* d_ws, size_t ws_size, hipStream_t stream) {
  (void)in_sizes; (void)n_in; (void)out_size; (void)ws_size;
  (void)hipFuncSetAttribute((const void*)fused_kernel, hipFuncAttributeMaxDynamicSharedMemorySize, LDS_BYTES);
  fused_kernel<<<256, 512, LDS_BYTES, stream>>>((const u16*)d_in[0], (const u16*)d_in[1], (const u16*)d_in[2], (const u16*)d_in[3], (const u16*)d_in[4],
      (const u16*)d_in[5], (const u16*)d_in[6], (const u16*)d_in[7], (const u16*)d_in[8], (const u16*)d_in[10], (const u16*)d_in[11], (const u16*)d_in[12],
      (const u16*)d_in[13], (const u16*)d_in[14], (char*)d_ws, (float*)d_out);
}
```

```cpp
#include <hip/hip_runtime.h>
#include <utility>

typedef unsigned short u16;
typedef __bf16 bf16x2 __attribute__((ext_vector_type(2)));
typedef __bf16 bf16x4 __attribute__((ext_vector_type(4)));
typedef __bf16 bf16x8 __attribute__((ext_vector_type(8)));
typedef float f32x2 __attribute__((ext_vector_type(2)));
typedef float f32x4 __attribute__((ext_vector_type(4)));
typedef float f32x16 __attribute__((ext_vector_type(16)));
typedef unsigned u32x2 __attribute__((ext_vector_type(2)));
typedef unsigned u32x4 __attribute__((ext_vector_type(4)));
typedef __amdgpu_buffer_rsrc_t rsrc_t;
#define LAS __attribute__((address_space(3)))
#define DI __device__ __forceinline__
#define AI __attribute__((always_inline))
#define SB() __builtin_amdgcn_sched_barrier(0)
#define PIN(x) asm volatile("" : "+v"(x))
#define MFMA(a, b, c) __builtin_amdgcn_mfma_f32_16x16x32_bf16(a, b, c, 0, 0, 0)
#define MFMA32(a, b, c) __builtin_amdgcn_mfma_f32_32x32x16_bf16(a, b, c, 0, 0, 0)
template <int V> using IC = std::integral_constant<int, V>;

constexpr unsigned HID_PANEL = 112 * 8192, H_OFF = 16 * HID_PANEL, H_PANEL = 32 * 16384;
constexpr unsigned PART_OFF = H_OFF + 16 * H_PANEL;
constexpr unsigned GRAN_OFF = PART_OFF + 128 * 65536;
constexpr unsigned RG_OFF = GRAN_OFF + 256 * 1024;
constexpr unsigned CG_OFF = RG_OFF + 16 * 4096;
constexpr unsigned FLAG_OFF = CG_OFF + 256 * 2048;
constexpr unsigned EPC_OFF = FLAG_OFF + 16 * 1024;
constexpr unsigned DUM_OFF = EPC_OFF + 16 * 128;
constexpr unsigned WS_BYTES = DUM_OFF + 256 * 128;
constexpr int FK_M1 = 0, FK_M2 = 1, FK_M3 = 2, FK_H = 3, FK_P = 4, FK_EP = 5;
constexpr unsigned TAB = 147456, T_COL = TAB, T_ROW = TAB + 2048, T_OUT = TAB + 4096, T_CON = TAB + 5120, T_CSP = TAB + 10752, T_AGG = 131072;
constexpr unsigned LDS_BYTES = 163840;
#define N4A 11
static_assert(N4A >= 11 && N4A <= 16, "");
constexpr int LAG = 2, LASTR = 25;
constexpr unsigned SPIN_CAP = 1u << 16;

DI rsrc_t mk(const void* p, unsigned bytes) { return __builtin_amdgcn_make_buffer_rsrc((void*)p, 0, bytes, 0x00020000); }
DI void setm0(unsigned lds) { asm volatile("s_mov_b32 m0, %0\n\ts_nop 0" ::"s"(lds) : "memory"); }
template <int OFF> DI void dma(rsrc_t r, unsigned voff, unsigned soff) {
  asm volatile("buffer_load_dwordx4 %0, %1, %2 offen offset:%3 lds" ::"v"(voff), "s"(r), "s"(soff), "i"(OFF) : "memory");
}
template <int OFF> DI void dma1(rsrc_t r, unsigned voff, unsigned soff) {
  asm volatile("buffer_load_dwordx4 %0, %1, %2 offen offset:%3 sc1 lds" ::"v"(voff), "s"(r), "s"(soff), "i"(OFF) : "memory");
}
#define PINS(x) asm volatile("" : "+s"(x))
typedef const void* const __attribute__((address_space(4)))* kargs_t;
DI const char* karg(int i) { kargs_t ka = (kargs_t)__builtin_amdgcn_kernarg_segment_ptr(); PINS(ka); return (const char*)ka[i]; }
enum { A_X, A_LRE, A_LIM, A_BRE, A_BIM, A_CRE, A_CIM, A_LDT, A_DSK, A_W1, A_B1, A_W2, A_B2, A_GW, A_WS, A_OUT };
template <int N> DI void waitv() { asm volatile("s_waitcnt vmcnt(%0)" ::"i"(N) : "memory"); }
DI void waitl0() { SB(); __builtin_amdgcn_s_waitcnt(0xC07F); asm volatile("" ::: "memory"); }
#define FST(gp, k)
DI void bar() { asm volatile("s_barrier" ::: "memory"); SB(); }
DI bf16x8 rd128(unsigned a) { return *(LAS bf16x8*)(size_t)a; }
DI u32x4 rdu4(unsigned a) { return *(LAS u32x4*)(size_t)a; }
DI f32x4 rdf4(unsigned a) { return *(LAS f32x4*)(size_t)a; }
DI f32x2 rdf2(unsigned a) { return *(LAS f32x2*)(size_t)a; }
DI float rdf(unsigned a) { return *(LAS float*)(size_t)a; }
DI void wrf(unsigned a, float v) { *(LAS float*)(size_t)a = v; }
DI void wrf2(unsigned a, f32x2 v) { *(LAS f32x2*)(size_t)a = v; }
DI bf16x4 rtr(unsigned a) { return __builtin_amdgcn_ds_read_tr16_b64_v4bf16((LAS bf16x4*)(size_t)a); }
DI void sethalf(bf16x8& f, int h, bf16x4 v) {
  if (h == 0) f = __builtin_shufflevector(v, v, 0, 1, 2, 3, -1, -1, -1, -1);
  else { bf16x8 t = __builtin_shufflevector(v, v, -1, -1, -1, -1, 0, 1, 2, 3); f = __builtin_shufflevector(f, t, 0, 1, 2, 3, 12, 13, 14, 15); }
}
DI unsigned f2u(float f) { return __builtin_bit_cast(unsigned, f); }
DI float u2f(unsigned u) { return __builtin_bit_cast(float, u); }
DI float shx(int lane, float v, int m) { return u2f(__builtin_amdgcn_ds_bpermute((lane ^ m) * 4, (int)f2u(v))); }
DI float fexp(float v) { return __builtin_amdgcn_exp2f(v * 1.4426950408889634f); }
DI float bf2f(unsigned v) { return u2f(v << 16); }
DI unsigned pk2(float lo, float hi) { f32x2 v = {lo, hi}; return __builtin_bit_cast(unsigned, __builtin_convertvector(v, bf16x2)); }
DI float rbf(float f) { return u2f(pk2(f, 0.f) << 16); }
DI float dot2(unsigned a, unsigned b, float c) { return __builtin_amdgcn_fdot2_f32_bf16(__builtin_bit_cast(bf16x2, a), __builtin_bit_cast(bf16x2, b), c, false); }
template <int C> DI float dppadd(float v) { return v + u2f(__builtin_amdgcn_update_dpp(0, (int)f2u(v), C, 0xf, 0xf, true)); }
DI float sel4(bool b0, bool b1, float a, float b, float c, float d) { PIN(a); PIN(b); PIN(c); PIN(d); const float lo = b0 ? b : a, hi = b0 ? d : c; return b1 ? hi : lo; }
DI f32x2 cmul(f32x2 a, f32x2 b) { return (f32x2){a.x * b.x - a.y * b.y, a.x * b.y + a.y * b.x}; }
DI f32x2 cfma(f32x2 a, f32x2 b, f32x2 c) { return (f32x2){__builtin_fmaf(a.x, b.x, __builtin_fmaf(-a.y, b.y, c.x)), __builtin_fmaf(a.x, b.y, __builtin_fmaf(a.y, b.x, c.y))}; }
DI void st128(rsrc_t r, u32x4 v, unsigned voff, unsigned soff) { __builtin_amdgcn_raw_buffer_store_b128(v, r, voff, soff, 16); }
DI void st64(rsrc_t r, u32x2 v, unsigned voff, unsigned soff) { __builtin_amdgcn_raw_buffer_store_b64(v, r, voff, soff, 16); }
DI void st32(rsrc_t r, unsigned v, unsigned voff, unsigned soff) { __builtin_amdgcn_raw_buffer_store_b32(v, r, voff, soff, 16); }
DI void stflag(rsrc_t r, unsigned v, int lane, unsigned soff) { if (lane == 0) st32(r, v, 0u, soff); }
DI void ld32a(unsigned& d, rsrc_t r, unsigned voff, unsigned soff) { asm volatile("s_nop 4\n\tbuffer_load_dword %0, %1, %2, %3 offen sc1" : "+v"(d) : "v"(voff), "s"(r), "s"(soff) : "memory"); }
DI void ld16p(unsigned& d, const u16* p, unsigned voff) { asm volatile("s_nop 4\n\tglobal_load_ushort %0, %1, %2" : "+v"(d) : "v"(voff), "s"(p) : "memory"); }
DI void ld64p(u32x2& d, rsrc_t r, unsigned voff, unsigned soff) { asm volatile("s_nop 4\n\tbuffer_load_dwordx2 %0, %1, %2, %3 offen" : "+v"(d) : "v"(voff), "s"(r), "s"(soff) : "memory"); }
DI void ld64a(u32x2& d, rsrc_t r, unsigned voff, unsigned soff) { asm volatile("s_nop 4\n\tbuffer_load_dwordx2 %0, %1, %2, %3 offen sc1" : "+v"(d) : "v"(voff), "s"(r), "s"(soff) : "memory"); }
DI void ld128g(u32x4& d, const u16* p, unsigned voff) { asm volatile("s_nop 4\n\tglobal_load_dwordx4 %0, %1, %2" : "+v"(d) : "v"(voff), "s"(p) : "memory"); }
DI bool anyl(bool b) { return __builtin_amdgcn_ballot_w64(b) != 0; }
DI void ld128p(u32x4& d, rsrc_t r, unsigned voff, unsigned soff) { asm volatile("s_nop 4\n\tbuffer_load_dwordx4 %0, %1, %2, %3 offen" : "+v"(d) : "v"(voff), "s"(r), "s"(soff) : "memory"); }
DI void sincos_f(float t, float& sn, float& cs) {
  const float k = __builtin_rintf(t * 0.63661977236f);
  float r = __builtin_fmaf(k, -1.5707962513f, t); r = __builtin_fmaf(k, -7.5497894159e-08f, r); r = __builtin_fmaf(k, -5.3903029534e-15f, r);
  const float z = r * r;
  const float sp = __builtin_fmaf(r * z, __builtin_fmaf(z, __builtin_fmaf(z, __builtin_fmaf(z, 2.7183114939e-06f, -1.9839334836e-04f), 8.3333293858e-03f), -1.6666666642e-01f), r);
  const float cp = __builtin_fmaf(z, __builtin_fmaf(z, __builtin_fmaf(z, __builtin_fmaf(z, 2.4390448796e-05f, -1.3886763775e-03f), 4.1666623324e-02f), -4.9999999726e-01f), 1.f);
  const int q = (int)k;
  const float a = (q & 1) ? cp : sp, b = (q & 1) ? sp : cp;
  sn = (q & 2) ? -a : a; cs = ((q + 1) & 2) ? -b : b;
}
DI void ld128a(u32x4& d, rsrc_t r, unsigned voff, unsigned soff) { asm volatile("s_nop 4\n\tbuffer_load_dwordx4 %0, %1, %2, %3 offen sc1" : "+v"(d) : "v"(voff), "s"(r), "s"(soff) : "memory"); }

DI unsigned x1off(int a) { return a == 0 ? 131072u : (unsigned)(81920 + a * 16384); }
DI unsigned nq(unsigned s) { return (s + 32768u) & 131071u; }

DI int kb_of(int j, int bl) { return 7 * j + ((j & 1) ? (bl == 6 ? 0 : bl + 1) : bl); }

template <int TA, int TB, bool FIRST, int NR, int LAST, int NB, int SL, class RD, class MM, class UP>
DI void slot(RD& rd, MM& mm, UP& upd) {
  constexpr int NM = TA * TB, q = SL + LAG;
  if constexpr (!(FIRST && q < NM)) { constexpr int ss = (q < NM) ? 1 : ((q < 2 * NM) ? 0 : 1); constexpr int m = q % NM; mm(ss, m / TB, m % TB); }
  SB();
  constexpr int r = (SL * (NR - 1) + LAST - 1) / LAST;
  if constexpr (r < NR && (r * LAST) / (NR - 1) == SL) rd(r);
  if constexpr (SL > LAST && SL - LAST - 1 < NB) upd(SL - LAST - 1);
  SB();
}
template <int TA, int TB, bool FIRST, int NR, int LAST, int NB, class RD, class MM, class UP, int... I>
DI void period_(RD& rd, MM& mm, UP& upd, std::integer_sequence<int, I...>) { (slot<TA, TB, FIRST, NR, LAST, NB, I>(rd, mm, upd), ...); }
template <int TA, int TB, bool FIRST, int NR, int LAST, int NB, class RD, class MM, class UP>
DI void period(RD& rd, MM& mm, UP& upd) {
  static_assert(NR - 1 <= LAST && LAST + NB < 2 * TA * TB, "");
  period_<TA, TB, FIRST, NR, LAST, NB>(rd, mm, upd, std::make_integer_sequence<int, 2 * TA * TB>{});
}
template <int TA, int TB, class MM> DI void drain(MM& mm) {
#pragma unroll
  for (int m = LAG; m < TA * TB; ++m) mm(1, m / TB, m % TB);
}
template <bool FIRST, int SL, class RD, class MM, class UP, class G> DI void slot32(RD& rd, MM& mm, UP& upd, G& g) {
  constexpr int q = SL + 1;
  if constexpr (!(FIRST && q < 8)) mm((q < 8) ? 1 : ((q < 16) ? 0 : 1), q & 7);
  SB();
  if constexpr (SL < 12) { constexpr int r0 = (SL * 4 + 2) / 3, r1 = ((SL + 1) * 4 + 2) / 3; rd(r0); if constexpr (r0 + 1 < r1) rd(r0 + 1); }
  else { upd(2 * (SL - 12)); upd(2 * (SL - 12) + 1); }
  SB();
  if constexpr (SL % 3 != 0 && SL < 12) { g(IC<SL - SL / 3 - 1>{}); SB(); }
}
template <bool FIRST, class RD, class MM, class UP, class G, int... I> DI void period32_(RD& rd, MM& mm, UP& upd, G& g, std::integer_sequence<int, I...>) { (slot32<FIRST, I>(rd, mm, upd, g), ...); }
template <bool FIRST, class RD, class MM, class UP, class G> DI void period32(RD& rd, MM& mm, UP& upd, G& g) { period32_<FIRST>(rd, mm, upd, g, std::make_integer_sequence<int, 16>{}); }
template <class G, class W, int... U> DI void ival_(G& g, W& wk, std::integer_sequence<int, U...>) { ((g(IC<U>{}), SB(), wk(IC<U>{}), SB()), ...); }
template <class G, class W> DI void ival(G&& g, W&& wk) { ival_(g, wk, std::make_integer_sequence<int, 8>{}); }
template <int N, class F, int... I> DI void sfor_(F& f, std::integer_sequence<int, I...>) { (f(IC<I>{}), ...); }
template <int N, class F> DI void sfor(F&& f) { sfor_<N>(f, std::make_integer_sequence<int, N>{}); }

DI void check_flags(unsigned& pv, rsrc_t ws, unsigned voff, unsigned soff, int lane, int lo, int hi, unsigned ep) {
  PIN(pv);
  const bool need = lane >= lo && lane < hi;
  if (__builtin_expect(anyl(need && pv != ep), 0)) {
    for (unsigned sp = 0; sp < SPIN_CAP; ++sp) {
      __builtin_amdgcn_s_sleep(2);
      ld32a(pv, ws, voff, soff); asm volatile("s_waitcnt vmcnt(0)" : "+v"(pv)::"memory");
      if (!anyl(need && pv != ep)) break;
    }
  }
}

#if defined(__HIP_DEVICE_COMPILE__) && __HIP_DEVICE_COMPILE__
__attribute__((target("no-packed-fp32-ops")))
#endif
__global__ __launch_bounds__(512, 2) void fused_kernel(const u16* x, const u16* lre, const u16* lim, const u16* bre, const u16* bim, const u16* cre, const u16* cim,
    const u16* ldt, const u16* dsk, const u16* w1, const u16* b1, const u16* w2, const u16* b2, const u16* gw, char* ws, float* out) {
  extern __shared__ __attribute__((aligned(16))) char shm[];
  const int lane = threadIdx.x & 63, w = __builtin_amdgcn_readfirstlane(threadIdx.x >> 6);
  const int bid = blockIdx.x, xcd = bid & 7, loc = bid >> 3;
  const int ci = (xcd & 1) * 8 + (loc & 7), cj = (xcd >> 1) * 4 + (loc >> 3), cnt = cj & 7, half = cj >> 3, wq = w & 3, rot = (ci + 2 * cj) & 7;
  const int n4 = half ? 32 - N4A : N4A, k4 = half ? N4A : 0;
  const rsrc_t rws = mk(karg(A_WS), WS_BYTES);
  const int wm = (w >> 1) & 1, wn = w & 1, g = lane >> 4, r15 = lane & 15, q2 = (lane >> 2) & 3, p = lane & 3;
  const unsigned flagb = FLAG_OFF + ci * 1024, fvo = (lane & 15) * 4;
  auto nowork = [&](auto) AI {};

  if (w < 4) {
    unsigned epr = 0, epc = 0, pdt = 0, plr = 0, pli = 0;
    const int tx = threadIdx.x;
    unsigned va[4], vb[4], vn[4];
#pragma unroll
    for (int u = 0; u < 4; ++u) vn[u] = (8 * u + (lane >> 3)) * 2048 + (((lane & 7) ^ ((4 * u + g) & 7)) * 16) - 1024 * u;
    rsrc_t ra = mk(karg(A_X), 2048 * 2048), rb;
    unsigned sl, sa, sb; int sx = 0;
    unsigned ta, tb;
    {
      const int s3 = lane - 16, n = 2 * s3 + (wq >> 1), kb = kb_of(half * 8 + (((n & 7) ^ rot) & 7), n >> 3);
      int ks = lane - 44 + rot; if (ks >= n4) ks -= n4; ks += k4;
      const int k2 = lane ^ rot;
      ta = lane < 16 ? (ci * 128 + 32 * wq) * 2048 + k2 * 128 : lane < 44 ? ci * HID_PANEL + kb * 8192 + (wq & 1) * 4096 : H_OFF + ci * H_PANEL + ks * 16384 + wq * 4096;
      tb = lane < 16 ? (cj * 64 + 32 * (wq >> 1)) * 2048 + k2 * 128 : lane < 44 ? (kb * 32 + 16 * (wq & 1)) * 2048 + cnt * 256 : (cnt * 128 + 32 * wq) * 2048 + (ks >> 1) * 128 + (ks & 1);
    }
    auto nexts = [&]() AI { sa = __builtin_amdgcn_readlane(ta, sx); sb = __builtin_amdgcn_readlane(tb, sx); ++sx; PINS(sa); PINS(sb); };
    auto G = [&](auto U) AI { constexpr int u = U.value;
      if constexpr (u == 0) setm0(sl + wq * 4096);
      if constexpr (u == 4) setm0(sl + 16384 + wq * 4096);
      if constexpr (u < 4) dma1<u * 1024>(ra, va[u], sa); else dma<(u - 4) * 1024>(rb, vb[u - 4], sb);
    };
    {
      rb = mk(karg(A_W1) - 4096, 1024 * 7168 + 4096);
      unsigned vw[7];
#pragma unroll
      for (int q = 0; q < 6; ++q) { const int k = 8 * (q & 1) + (lane >> 3), f = ((k >> 1) & 1) | ((q & 1) << 1);
        vw[q] = 4096 + k * 7168 + ((cj & 1) ? 64 : 0) + (q >> 1) * 128 + (((((lane & 7) >> 1) ^ f) * 2) + (lane & 1)) * 16 - (q & 3) * 1024; }
      vw[6] = 4096 + (lane >> 2) * 7168 + ((cj & 1) ? 0 : 384) + (((((lane & 3) >> 1) ^ (lane >> 5)) * 2) + (lane & 1)) * 16 - 2048;
      auto G1 = [&](auto S, auto U) AI { constexpr int s = S.value, u = U.value, a = s % 3, m = s >= 8 ? 7 : (s >= 4 ? 3 : (s >= 2 ? 1 : 0));
        if constexpr (u == 0) { const int k = s ^ (rot & m); sa = (ci * 128 + 32 * wq) * 2048 + k * 128; sb = (k * 64 + 16 * wq) * 7168 + cj * 448; PINS(sa); PINS(sb);
                                setm0((a == 0 ? 131072u : 81920u + a * 16384u) + wq * 4096); }
        if constexpr (u == 4) setm0(a * 32768 + wq * 7168);
        if constexpr (u == 8) setm0(a * 32768 + wq * 7168 + 4096);
        if constexpr (u < 4) dma<u * 1024>(ra, vn[u], sa); else dma<(u & 3) * 1024>(rb, vw[u - 4], sb);
      };
      u32x2 wv[16];
#pragma unroll
      for (int q = 0; q < 16; ++q) wv[q] = (u32x2){0, 0};
      sfor<11>([&](auto U) AI { G1(IC<0>{}, U); });
      ld32a(epr, rws, 0u, flagb + FK_EP * 128); ld32a(epc, rws, 0u, EPC_OFF + cj * 128);
      ld16p(pdt, (const u16*)karg(A_LDT) + 64 * cj, lane * 2u); ld16p(plr, (const u16*)karg(A_LRE) + 64 * cj, lane * 2u); ld16p(pli, (const u16*)karg(A_LIM) + 64 * cj, lane * 2u);
      sfor<11>([&](auto U) AI { G1(IC<1>{}, U); });
      sfor<11>([&](auto U) AI { G1(IC<2>{}, U); });
      const unsigned wvo = 4096 + (lane < 56 ? lane : 55) * 8u;
#pragma unroll
      for (int q = 0; q < 16; ++q) ld64p(wv[q], rb, wvo, (64 * ci + 16 * wq + q) * 7168 + cj * 448);
      waitv<22 + 16>(); PIN(epr); PIN(epc); PIN(pdt); PIN(plr); PIN(pli);
      epr = __builtin_amdgcn_readfirstlane(epr) + 1u; epc = __builtin_amdgcn_readfirstlane(epc) + 1u;
      bar();
      if (w == 0) {
        const float dt = rbf(fexp(bf2f(pdt))), lr = bf2f(plr), li = bf2f(pli);
        const float e = fexp(dt * lr); float sn, cs; sincos_f(dt * li, sn, cs);
        const f32x2 lam = {e * cs, e * sn};
        const float nr = lam.x - 1.f, ni = lam.y, den = 1.f / (lr * lr + li * li);
        wrf2(T_CON + lane * 8, lam); wrf2(T_CON + 512 + lane * 8, (f32x2){(nr * lr + ni * li) * den, (ni * lr - nr * li) * den});
        f32x2 l16 = lam;
#pragma unroll
        for (int q = 0; q < 4; ++q) l16 = cmul(l16, l16);
        f32x2 pw = {1.f, 0.f};
#pragma unroll
        for (int q = 0; q < 9; ++q) { wrf2(T_CON + (2 + q) * 512 + lane * 8, pw); pw = cmul(pw, l16); }
      }
      f32x4 cs4 = {0, 0, 0, 0};
      u32x4 rgv = {0, epr - 1u, 0, epr - 1u};
      unsigned pf = 0;
      const int tc = tx < 224 ? tx : 223;
      const int lcn = (cj & 1) ? (tc < 32 ? 192 + tc : tc - 32) : tc;
      auto gather = [&]() AI {
#pragma unroll
        for (int q = 0; q < 16; ++q) ld64a(wv[q], rws, tc * 8u, CG_OFF + (cj * 16 + q) * 2048);
        ld128a(rgv, rws, tx * 16u, RG_OFF + ci * 4096);
      };
      auto gbad = [&]() AI { PIN(rgv); unsigned d = (rgv[1] ^ epr) | (rgv[3] ^ epr);
#pragma unroll
        for (int q = 0; q < 16; ++q) { PIN(wv[q]); d |= wv[q][1] ^ epc; }
        return d != 0; };
      auto E1 = [](int s) constexpr { return (s == 1 || s == 4 || s == 5) ? 1 : (s == 9 ? 17 : 0); };
      sfor<15>([&](auto S) AI { constexpr int s = S.value;
        if constexpr (s < 2) waitv<11 + 16>(); else if constexpr (s < 14) waitv<11 + E1(s - 1)>(); else waitv<8>();
        waitl0(); bar();
        if constexpr (s == 1) ld16p(pf, (const u16*)karg(A_BRE + wq), (bid * 64 + lane) * 128u);
        if constexpr (s == 5) ld16p(pf, (const u16*)karg(A_W2), (bid * 224 + tc) * 128u);
        if constexpr (s == 9) {
#pragma unroll
          for (int q = 0; q < 16; ++q) wv[q] = (u32x2){0, epc - 1u};
          gather();
        }
        if constexpr (s == 11) {
          if (__builtin_expect(anyl(gbad()), 0))
            for (unsigned sp = 0; sp < SPIN_CAP; ++sp) { __builtin_amdgcn_s_sleep(2); gather(); waitv<0>(); if (!anyl(gbad())) break; }
        }
        if constexpr (s < 13) sfor<11>([&](auto U) AI { constexpr int u = U.value;
          G1(IC<s + 3>{}, U);
          SB();
          if constexpr ((s == 2 || s == 3) && u < 8) {
            constexpr int q = (s - 2) * 8 + u;
            PIN(wv[q]);
            cs4[0] += bf2f(wv[q][0] & 0xffffu); cs4[1] += u2f(wv[q][0] & 0xffff0000u); cs4[2] += bf2f(wv[q][1] & 0xffffu); cs4[3] += u2f(wv[q][1] & 0xffff0000u);
            PIN(cs4);
          }
          if constexpr (s == 3 && u == 8) { if (lane < 56) *(LAS f32x4*)(size_t)(T_CSP + wq * 896 + lane * 16) = cs4; }
          if constexpr (s == 4 && u == 2) {
            const float v = (rdf(T_CSP + tc * 4) + rdf(T_CSP + 896 + tc * 4)) + (rdf(T_CSP + 1792 + tc * 4) + rdf(T_CSP + 2688 + tc * 4));
            st64(rws, (u32x2){f2u(v), epc}, tc * 8u, CG_OFF + (cj * 16 + ci) * 2048);
          }
          if constexpr (s == 11 && u == 1) {
            float v = 0.f;
#pragma unroll
            for (int q = 0; q < 16; ++q) v += u2f(wv[q][0]);
            wrf(T_COL + lcn * 4, v);
            wrf(T_ROW + (tx & 1) * 1024 + (tx >> 1) * 4, u2f(rgv[0])); wrf(T_ROW + (tx & 1) * 1024 + 512 + (tx >> 1) * 4, u2f(rgv[2]));
          }
          SB();
        });
        if constexpr (s == 12) {
          rb = mk(karg(A_BRE + (wq & 1)), 1024 * 2048);
#pragma unroll
          for (int u = 0; u < 4; ++u) { va[u] = (16 * (lane >> 5) + 4 * u + ((lane >> 3) & 3)) * 2048 + (((lane & 7) ^ ((4 * u + g) & 7)) * 16) - 1024 * u; vb[u] = vn[u]; }
          sl = 32768; nexts();
        }
        if constexpr (s >= 13) { ival(G, nowork); sl = nq(sl); nexts(); asm volatile("s_nop 4"); }
        if constexpr (s == 14) { ival(G, nowork); sl = nq(sl); nexts(); }
      });
      asm volatile("" ::"v"(pf));
      waitv<16>(); bar();
    }
    f32x16 acc[2][2];
#pragma unroll
    for (int a = 0; a < 4; ++a)
#pragma unroll
      for (int e = 0; e < 16; ++e) acc[a >> 1][a & 1][e] = 0.f;
    const int hl = lane >> 5, r31 = lane & 31;
    {
      bf16x8 fa[2][4], fb[2][4];
      unsigned cb[8], ba[8];
#pragma unroll
      for (int b = 0; b < 8; ++b) { cb[b] = (b < 4 ? wm * 8192u : 16384 + wn * 8192u) + r31 * 128 + (((2 * (b & 3) + hl) ^ ((r31 >> 1) & 7)) * 16); ba[b] = 32768 + cb[b]; PIN(ba[b]); }
      auto rd = [&](int r) AI { const int k16 = r >> 2, ks = r >> 3, kk = k16 & 1, xx = r & 3;
        if (xx == 0) fa[ks][kk * 2] = rd128(ba[k16]); else if (xx == 3) fa[ks][kk * 2 + 1] = rd128(ba[k16] + 4096); else fb[ks][kk * 2 + xx - 1] = rd128(ba[4 + k16] + (xx - 1) * 4096); };
      auto mm = [&](int ks, int m) AI { const int kk = m >> 2, mt = (m >> 1) & 1, nt = m & 1; acc[mt][nt] = MFMA32(fa[ks][kk * 2 + mt], fb[ks][kk * 2 + nt], acc[mt][nt]); };
      auto up = [&](int b) AI { if (b == 0) sl = nq(sl); if (b == 1) nexts(); ba[b] = nq(sl) + cb[b]; PIN(ba[b]); };
      period32<true>(rd, mm, up, G); waitl0(); waitv<16>(); bar();
      for (int s = 1; s < 16; ++s) {
        if (s == 13) {
          ra = rws; rb = mk(karg(A_W2), 3584 * 2048);
#pragma unroll
          for (int u = 0; u < 4; ++u) { va[u] = lane * 16; vb[u] = (4 * u + g) * 2048 + ((((r15 >> 1) ^ (4 * (u & 1) + g)) * 2) + (lane & 1)) * 16 - 1024 * u; }
        }
        period32<false>(rd, mm, up, G); waitl0(); waitv<16>(); bar();
      }
      ival(G, [&](auto U) AI { if constexpr (U.value < 7) mm(1, U.value + 1); });
      sl = nq(sl); nexts();
    }
    {
      const int seg0 = 4 * wm + hl;
      const unsigned ca = (32 * wn + r31) * 8;
      f32x2 st[2], Pf[2], Gt;
      unsigned pv = epr - 1u;
#define NEXT3() do { sl = nq(sl); nexts(); } while (0)
      const f32x2 lam = rdf2(T_CON + ca);
      st[0] = (f32x2){0, 0}; st[1] = (f32x2){0, 0};
      sfor<2>([&](auto H) AI { constexpr int h2 = H.value;
        waitv<16>(); bar();
        if constexpr (h2 == 1) { stflag(rws, epr, lane, flagb + FK_M3 * 128 + cj * 4); ld32a(pv, rws, fvo, flagb + FK_M2 * 128); }
        ival(G, [&](auto U) AI { constexpr int t = 8 * h2 + U.value;
#pragma unroll
          for (int mt = 0; mt < 2; ++mt) { st[mt] = cfma(lam, st[mt], (f32x2){acc[mt][0][t], acc[mt][1][t]}); PIN(st[mt]); }
        });
        NEXT3();
      });
      wrf2(T_AGG + seg0 * 512 + ca, st[0]); wrf2(T_AGG + (seg0 + 2) * 512 + ca, st[1]);
      waitl0(); waitv<16 + 2>(); bar();
      {
        const f32x2 l16 = rdf2(T_CON + 3 * 512 + ca);
        f32x2 ag[8];
#pragma unroll
        for (int sg = 0; sg < 8; ++sg) ag[sg] = rdf2(T_AGG + sg * 512 + ca);
        Gt = (f32x2){0, 0}; Pf[0] = (f32x2){0, 0}; Pf[1] = (f32x2){0, 0};
        SB();
        ival(G, [&](auto U) AI { constexpr int u = U.value;
          if constexpr (u >= 1 && u < 5) {
#pragma unroll
            for (int sg = 2 * u - 2; sg < 2 * u; ++sg) {
              const int mt = (sg >> 1) & 1; const bool me = sg == seg0 + 2 * mt;
              Pf[mt].x = me ? Gt.x : Pf[mt].x; Pf[mt].y = me ? Gt.y : Pf[mt].y; Gt = cfma(l16, Gt, ag[sg]);
            }
            PIN(Gt); PIN(Pf[0]); PIN(Pf[1]);
          } else if constexpr (u == 5) {
            st64(rws, (u32x2){f2u(hl ? Gt.y : Gt.x), epc}, ca * 2 + hl * 8, GRAN_OFF + (cj * 16 + ci) * 1024);
          }
        });
      }
      NEXT3(); waitv<16 + 2 + 1>(); bar();
      ival(G, nowork); NEXT3(); waitv<16 + 1>(); bar();
      check_flags(pv, rws, fvo, flagb + FK_M2 * 128, lane, 8 * half, 8 * half + 8, epr);
      ival(G, nowork); NEXT3(); waitv<16>(); bar();
      ival(G, nowork); NEXT3(); waitv<16>(); bar();
      u32x4 lb[15];
#pragma unroll
      for (int q = 0; q < 15; ++q) lb[q] = (u32x4){0, epc - 1u, 0, epc - 1u};
      ival(G, [&](auto U) AI { constexpr int u = U.value;
        if constexpr (u < 5) {
#pragma unroll
          for (int q = 3 * u; q < 3 * u + 3; ++q) ld128a(lb[q], rws, ca * 2, GRAN_OFF + (cj * 16 + q) * 1024);
        } });
      NEXT3(); waitv<16 + 15>(); bar();
      ival(G, nowork); NEXT3(); waitv<16 + 15>(); bar();
      ival(G, nowork); NEXT3(); waitv<16>(); bar();
      pv = epr - 1u; ld32a(pv, rws, fvo, flagb + FK_M3 * 128);
      auto bad = [&]() AI { unsigned d = 0;
#pragma unroll
        for (int q = 0; q < 15; ++q) { PIN(lb[q]); const unsigned m = q < ci ? 0xFFFFFFFFu : 0u; d |= ((lb[q][1] ^ epc) | (lb[q][3] ^ epc)) & m; }
        return d != 0; };
      ival(G, [&](auto U) AI { constexpr int u = U.value;
        if constexpr (u == 3) {
          if (__builtin_expect(anyl(bad()), 0)) {
            for (unsigned sp = 0; sp < SPIN_CAP; ++sp) {
              __builtin_amdgcn_s_sleep(2);
#pragma unroll
              for (int q = 0; q < 15; ++q) ld128a(lb[q], rws, ca * 2, GRAN_OFF + (cj * 16 + q) * 1024);
              waitv<0>();
              if (!anyl(bad())) break;
            }
          }
        } else if constexpr (u == 6) {
          stflag(rws, epc, lane, ci == 15 ? EPC_OFF + cj * 128 : DUM_OFF + bid * 128);
        } });
      NEXT3(); waitv<16 + 2>(); bar();
      const f32x2 cof = rdf2(T_CON + 512 + ca);
      {
        const f32x2 l128 = rdf2(T_CON + 10 * 512 + ca);
        const f32x2 pw0 = rdf2(T_CON + (2 + seg0) * 512 + ca), pw1 = rdf2(T_CON + (4 + seg0) * 512 + ca);
        f32x2 cr = {0, 0};
        ival(G, [&](auto U) AI { constexpr int u = U.value;
#pragma unroll
          for (int q = 2 * u; q < 2 * u + 2; ++q)
            if (q < 15) { const float m = q < ci ? 1.f : 0.f;
              if (m != 0.f) cr = cfma(l128, cr, (f32x2){u2f(lb[q][0]), u2f(lb[q][2])}); }
          if constexpr (u == 7) { st[0] = cfma(pw0, cr, Pf[0]); st[1] = cfma(pw1, cr, Pf[1]); PIN(st[0]); PIN(st[1]); }
          PIN(cr);
        });
      }
      NEXT3(); waitv<16 + 2>(); bar();
      {
        unsigned ob[2][2][4];
        sfor<4>([&](auto Q) AI { constexpr int q4 = Q.value, hf = q4 >> 1;
          if constexpr (q4 == 1) check_flags(pv, rws, fvo, flagb + FK_M3 * 128, lane, 8 * half, 8 * half + 8, epr);
          const f32x4 g2[2] = {rdf4(T_ROW + 1536 + (64 * wm + 16 * hl + 4 * q4) * 4), rdf4(T_ROW + 1536 + (64 * wm + 32 + 16 * hl + 4 * q4) * 4)};
          float o[2][2][2];
          ival(G, [&](auto U) AI { constexpr int u = U.value;
            if constexpr (u >= 1 && u < 5) {
              constexpr int e = u - 1, t = 4 * q4 + e;
#pragma unroll
              for (int mt = 0; mt < 2; ++mt) {
                const float gg = 2.f * g2[mt][e];
                st[mt] = cfma(lam, st[mt], (f32x2){acc[mt][0][t], acc[mt][1][t]});
                const f32x2 h = cmul(cof, st[mt]);
                o[mt][0][e & 1] = gg * h.x; o[mt][1][e & 1] = -gg * h.y;
                PIN(st[mt]); PIN(o[mt][0][e & 1]); PIN(o[mt][1][e & 1]);
                if constexpr (e & 1) { ob[mt][0][(t >> 1) & 3] = pk2(o[mt][0][0], o[mt][0][1]); ob[mt][1][(t >> 1) & 3] = pk2(o[mt][1][0], o[mt][1][1]); PIN(ob[mt][0][(t >> 1) & 3]); PIN(ob[mt][1][(t >> 1) & 3]); }
              }
            } else if constexpr ((q4 & 1) && (u == 5 || u == 6)) {
              constexpr int mt = u - 5;
#pragma unroll
              for (int pt = 0; pt < 2; ++pt)
                st128(rws, (u32x4){ob[mt][pt][0], ob[mt][pt][1], ob[mt][pt][2], ob[mt][pt][3]},
                      (2 * wn + (r31 >> 4)) * 4096 + wm * 2048 + hf * 1024 + (2 * mt + hl) * 256 + ((r15 ^ (hf << 2)) * 16), H_OFF + ci * H_PANEL + (2 * cj + pt) * 16384);
            }
          });
          NEXT3();
          if constexpr (q4 == 0) waitv<16>(); else waitv<16 + 4>();
          bar();
        });
      }
      const rsrc_t rx = mk(karg(A_X), 2048 * 2048);
      u32x4 xe[4][2];
#pragma unroll
      for (int q = 0; q < 8; ++q) xe[q >> 1][q & 1] = (u32x4){0, 0, 0, 0};
      ival(G, [&](auto U) AI { constexpr int u = U.value;
        if constexpr (u < 4) {
#pragma unroll
          for (int hh = 0; hh < 2; ++hh) ld128p(xe[u][hh], rx, (64u * wm + r15) * 2048 + wn * 128 + g * 16, (ci * 128 + 16 * u) * 2048 + cnt * 256 + hh * 64);
        } });
      NEXT3(); waitv<16 + 4 + 8>(); bar();
      ival(G, nowork); NEXT3(); waitv<16 + 8>(); bar();
      stflag(rws, epr, lane, flagb + FK_H * 128 + cj * 4);
      ival(G, nowork); NEXT3(); waitv<16 + 1>(); bar();
      f32x4 ev[4][2][2];
      {
        f32x4 bv[2][2], dv[2][2]; float gr[4];
#pragma unroll
        for (int q = 0; q < 4; ++q) { const unsigned oa = T_OUT + (64 * wn + 32 * (q >> 1) + 8 * g + 4 * (q & 1)) * 4; bv[q >> 1][q & 1] = rdf4(oa); dv[q >> 1][q & 1] = rdf4(oa + 512);
                                      gr[q] = rdf(T_ROW + 1536 + (64 * wm + 16 * q + r15) * 4); }
        sfor<6>([&](auto A) AI { constexpr int a6 = A.value;
          if constexpr (a6 == 3) { pv = epr - 1u; ld32a(pv, rws, fvo, flagb + FK_H * 128); }
          ival(G, [&](auto U) AI { constexpr int u = U.value;
            if constexpr ((u & 1) && u < 7 && 3 * a6 + (u >> 1) < 16) {
              constexpr int id = 3 * a6 + (u >> 1), a = id >> 2, hh = (id >> 1) & 1, q = id & 1;
              PIN(xe[a][hh]);
#pragma unroll
              for (int e = 0; e < 4; ++e) {
                const unsigned xw = xe[a][hh][2 * q + (e >> 1)];
                const float xf = (e & 1) ? u2f(xw & 0xffff0000u) : bf2f(xw & 0xffffu);
                ev[a][hh][q][e] = __builtin_fmaf(gr[a], rbf(xf * dv[hh][q][e]) - bv[hh][q][e], bv[hh][q][e]);
              }
              PIN(ev[a][hh][q]);
            } });
          NEXT3();
          if constexpr (a6 == 0 || a6 == 3 || a6 == 4) waitv<16 + 1>(); else waitv<16>();
          bar();
        });
      }
      check_flags(pv, rws, fvo, flagb + FK_H * 128, lane, k4 >> 1, (k4 + n4 + 1) >> 1, epr);
#pragma unroll
      for (int u = 0; u < 4; ++u) vb[u] = vn[u];
      {
        const char *pre = karg(A_CRE), *pim = karg(A_CIM);
        for (int s = 0; s < n4; ++s) {
          if (s == 20) { int ks = 20 + rot; if (ks >= n4) ks -= n4; ks += k4;
                         sa = H_OFF + ci * H_PANEL + ks * 16384 + wq * 4096; sb = (cnt * 128 + 32 * wq) * 2048 + (ks >> 1) * 128 + (ks & 1); }
          rb = mk((sb & 1) ? pim : pre, 1024 * 2048); sb &= ~1u;
          ival(G, nowork); sx &= 63; NEXT3(); waitv<16>(); bar();
        }
      }
      pv = epr - 1u; ld32a(pv, rws, fvo, flagb + FK_P * 128);
      waitv<8 + 1>(); bar();
      waitv<0>();
      const unsigned pb = ((sl + (wq >> 1) * 32768u) & 131071u) + (wq & 1) * 16384;
      if (half) {
        check_flags(pv, rws, fvo, flagb + FK_P * 128, lane, cnt, cnt + 1, epr);
        sfor<16>([&](auto U) AI { constexpr int u = U.value; if constexpr ((u & 3) == 0) setm0(pb + u * 1024);
          dma1<(u & 3) * 1024>(rws, lane * 16u, PART_OFF + (ci * 8 + cnt) * 65536 + wq * 16384 + (u >> 2) * 4096); });
        if (cnt == 0 && w == 0) stflag(rws, epr, lane, flagb + FK_EP * 128);
      } else {
#pragma unroll
        for (int a = 0; a < 4; ++a)
#pragma unroll
          for (int q = 0; q < 4; ++q) *(LAS f32x4*)(size_t)(pb + (a * 4 + 2 * (q >> 1) + (g >> 1)) * 1024 + ((2 * (g & 1) + (q & 1)) * 16 + r15) * 16) = ev[a][q >> 1][q & 1];
      }
      bar();
      waitl0(); waitv<0>(); bar();
      if (!half) { bar(); if (w == 0) stflag(rws, epr, lane, flagb + FK_P * 128 + cnt * 4); }
      waitv<0>();
    }
  } else {
    const int ty = threadIdx.x - 256;
    const unsigned nb0 = r15 * 128 + ((g ^ (r15 >> 1)) * 16), nb1 = r15 * 128 + (((4 + g) ^ (r15 >> 1)) * 16);
    unsigned epr = __builtin_amdgcn_raw_buffer_load_b32(rws, 0, flagb + FK_EP * 128, 16);
    {
      const u32x4* xp = (const u32x4*)(karg(A_X) + (size_t)(ci * 128 + 8 * cj + 2 * wq) * 2048) + lane;
      const u32x4* gp = (const u32x4*)karg(A_GW) + lane;
      const u32x4 xr[2][2] = {{xp[0], xp[64]}, {xp[128], xp[192]}}, gq[2] = {gp[0], gp[64]};
      const int tc = ty < 224 ? ty : 223;
      const unsigned bq = ((const u16*)karg(A_B1))[224 * cj + (tc < 192 ? ((cj & 1) ? 32 : 0) + tc : ((cj & 1) ? -192 : 0) + tc)];
      const unsigned oq = ((const u16*)karg(wq < 2 ? A_B2 : A_DSK))[cnt * 128 + (ty & 127)];
      float sv[6];
#pragma unroll
      for (int r = 0; r < 2; ++r) {
        float a1 = 0.f, a2 = 0.f, a3 = 0.f;
#pragma unroll
        for (int e = 0; e < 8; ++e) { const unsigned v = xr[r][e >> 2][e & 3]; a1 = dot2(v, 0x3F803F80u, a1); a2 = dot2(v, v, a2); a3 = dot2(v, gq[e >> 2][e & 3], a3); }
        sv[r] = a1; sv[2 + r] = a2; sv[4 + r] = a3;
      }
#pragma unroll
      for (int q = 0; q < 6; ++q) { sv[q] = dppadd<0x140>(dppadd<0x141>(dppadd<0x4E>(dppadd<0xB1>(sv[q])))); PIN(sv[q]); }
      const bool r1 = lane & 4;
      float a1 = r1 ? sv[1] : sv[0], a2 = r1 ? sv[3] : sv[2], a3 = r1 ? sv[5] : sv[4];
      a1 += shx(lane, a1, 16); a2 += shx(lane, a2, 16); a3 += shx(lane, a3, 16);
      a1 += shx(lane, a1, 32); a2 += shx(lane, a2, 32); a3 += shx(lane, a3, 32);
      const float mean = a1 * (1.f / 1024.f), var = __builtin_fmaxf(a2 * (1.f / 1024.f) - mean * mean, 0.f), rstd = __builtin_amdgcn_rsqf(var + 1e-5f);
      const float qv = 1.f + fexp(__builtin_fminf(a3, 80.f)), gg = __builtin_amdgcn_rcpf(1.f + fexp(__builtin_fminf(-a3, 80.f)));
      const float val = sel4(lane & 1, lane & 2, rstd, mean * rstd, qv, gg);
      epr = __builtin_amdgcn_readfirstlane(epr) + 1u;
      if (lane < 8) st64(rws, (u32x2){f2u(val), epr}, lane * 8u, RG_OFF + ci * 4096 + (8 * cj + 2 * wq) * 32);
      wrf(T_COL + 1024 + ty * 4, bf2f(bq));
      wrf(T_OUT + ty * 4, bf2f(oq));
    }
    unsigned sl = 32768;
    f32x4 acc[4][4];
    {
      f32x4 ac1[4][7];
#pragma unroll
      for (int a = 0; a < 4; ++a)
#pragma unroll
        for (int b = 0; b < 7; ++b) ac1[a][b] = (f32x4){0, 0, 0, 0};
      {
        bf16x8 fa[2][4], fb[2][7];
        const int f = ((q2 >> 1) & 1) | ((g & 1) << 1);
        const unsigned cb[5] = {wm * 8192u + nb0, wm * 8192u + nb1, (g >> 1) * 7168u + (8 * (g & 1) + q2) * 128 + (((2 * wn) ^ f) * 32) + p * 8,
                                (g >> 1) * 7168u + (8 * (g & 1) + q2) * 128 + (((2 * wn + 1) ^ f) * 32) + p * 8, (g >> 1) * 7168u + 6144 + (8 * (g & 1) + q2) * 64 + ((wn ^ (g & 1)) * 32) + p * 8};
        unsigned ba[5]; unsigned xn = x1off(1), wnx = 32768;
#pragma unroll
        for (int b = 0; b < 5; ++b) { ba[b] = cb[b] + (b < 2 ? x1off(0) : 0u); PIN(ba[b]); }
        auto rd = [&](int r) AI { const int ks = r / 18, xx = r % 18;
          if (xx == 0) fa[ks][0] = rd128(ba[ks]);
          else if (xx <= 12) { const int b = (xx - 1) >> 1, h = (xx - 1) & 1; sethalf(fb[ks][b], h, rtr(ba[2 + (b & 1)] + (b >> 1) * 2048 + ks * 14336 + h * 512)); }
          else if (xx <= 14) { const int h = xx - 13; sethalf(fb[ks][6], h, rtr(ba[4] + ks * 14336 + h * 256)); }
          else fa[ks][xx - 14] = rd128(ba[ks] + (xx - 14) * 2048); };
        auto mm = [&](int ks, int a, int b) AI { ac1[a][b] = MFMA(fb[ks][b], fa[ks][a], ac1[a][b]); };
        auto up = [&](int b) AI { if (b < 5) { ba[b] = cb[b] + (b < 2 ? xn : wnx); PIN(ba[b]); }
                                  else { wnx = (wnx == 65536u) ? 0u : wnx + 32768u; xn = (xn == 131072u) ? 98304u : xn + 16384u; } };
        waitl0(); bar();
        period<4, 7, true, 36, LASTR + 24, 6>(rd, mm, up); waitl0(); bar();
        for (int s = 1; s < 16; ++s) { period<4, 7, false, 36, LASTR + 24, 6>(rd, mm, up); waitl0(); bar(); }
        drain<4, 7>(mm);
      }
      float rs[4], mr[4], qq[4];
#pragma unroll
      for (int a = 0; a < 4; ++a) { const unsigned ra = T_ROW + (64 * wm + 16 * a + r15) * 4; rs[a] = rdf(ra); mr[a] = rdf(ra + 512); qq[a] = rdf(ra + 1024); }
      f32x4 csv[2], b1v[2];
      unsigned hw[4];
      unsigned pv = epr - 1u;
      auto gelu = [&](auto I) AI {
        constexpr int idx = I.value;
        if constexpr (idx < 28) {
          constexpr int lc = idx >> 3, a = idx < 24 ? (idx >> 1) & 3 : idx - 24, bb = idx < 24 ? idx & 1 : 0, b = 2 * lc + bb;
          float y[4];
#pragma unroll
          for (int e = 0; e < 4; ++e) {
            const float u = __builtin_fmaf(ac1[a][b][e], rs[a], __builtin_fmaf(-mr[a], csv[bb][e], b1v[bb][e]));
            const float z = u * __builtin_fmaf(u * u, -0.1029432397f, -2.302208198f);
            const float d = __builtin_fmaf(__builtin_amdgcn_exp2f(z), qq[a], qq[a]);
            y[e] = u * __builtin_amdgcn_rcpf(d);
          }
          hw[bb * 2] = pk2(y[0], y[1]); hw[bb * 2 + 1] = pk2(y[2], y[3]); PIN(hw[bb * 2]); PIN(hw[bb * 2 + 1]);
          if constexpr (lc < 3 && bb == 1)
            st128(rws, (u32x4){hw[0], hw[1], hw[2], hw[3]}, lane * 16u + (4 * wm + a) * 1024, ci * HID_PANEL + kb_of(cj, 2 * lc + wn) * 8192);
          if constexpr (lc == 3)
            st64(rws, (u32x2){hw[0], hw[1]}, lane * 16u + (4 * wm + a) * 1024 + wn * 8, ci * HID_PANEL + kb_of(cj, 6) * 8192);
          if constexpr ((idx & 7) == 7) {
            SB();
            const unsigned co = T_COL + (lc < 2 ? (lc + 1) * 64 + wn * 32 + 4 * g : 192 + wn * 16 + 4 * g) * 4;
            csv[0] = rdf4(co); b1v[0] = rdf4(co + 1024); csv[1] = rdf4(co + 64); b1v[1] = rdf4(co + 64 + 1024);
          }
        }
      };
      { const unsigned co = T_COL + (wn * 32 + 4 * g) * 4; csv[0] = rdf4(co); b1v[0] = rdf4(co + 1024); csv[1] = rdf4(co + 64); b1v[1] = rdf4(co + 64 + 1024); }
      sfor<16>([&](auto S) AI { constexpr int s = S.value;
        if constexpr (s == 6 || s == 10) waitv<2>();
        if constexpr (s == 12) { waitv<2>(); check_flags(pv, rws, fvo, flagb + FK_M1 * 128, lane, 8 * half, 8 * half + 8, epr); }
        SB(); bar();
        if constexpr (s == 6) { if (w == 4) stflag(rws, epr, lane, flagb + FK_M1 * 128 + cj * 4); }
        if constexpr (s == 10) { if (w == 4) stflag(rws, epr, lane, flagb + FK_M2 * 128 + cj * 4); }
        if constexpr (s == 10) { ld32a(pv, rws, fvo, flagb + FK_M1 * 128); SB(); }
        gelu(IC<2 * s>{}); SB(); gelu(IC<2 * s + 1>{});
      });
    }
#pragma unroll
    for (int a = 0; a < 4; ++a)
#pragma unroll
      for (int b = 0; b < 4; ++b) acc[a][b] = (f32x4){0, 0, 0, 0};
    bf16x8 fa[2][4], fb[2][4];
    const int kap = 4 * (g & 1) + q2, hf = p >> 1;
    const unsigned wb = 16384 + (4 * g + q2) * 256 + p * 8, hb = (g >> 1) * 4096 + wm * 2048 + hf * 1024 + (8 * (g & 1) + q2) * 16 + (p & 1) * 8;
    const unsigned cb3[5] = {wm * 4096u + lane * 16, wb + (((4 * wn + 0) ^ kap) * 32), wb + (((4 * wn + 1) ^ kap) * 32), wb + (((4 * wn + 2) ^ kap) * 32), wb + (((4 * wn + 3) ^ kap) * 32)};
    const unsigned cb4[4] = {hb + 64u * hf, hb + 64u * (1 ^ hf), 16384 + wn * 8192u + nb0, 16384 + wn * 8192u + nb1};
    unsigned ba[5];
#pragma unroll
    for (int b = 0; b < 5; ++b) { ba[b] = sl + cb3[b]; PIN(ba[b]); }
    auto rd3 = [&](int r) AI { const int ks = r / 12, xx = r % 12;
      if (xx == 0) fa[ks][0] = rd128(ba[0] + ks * 8192);
      else if (xx <= 8) { const int b = (xx - 1) >> 1, h = (xx - 1) & 1; sethalf(fb[ks][b], h, rtr(ba[1 + b] + ks * 8192 + h * 4096)); }
      else fa[ks][xx - 8] = rd128(ba[0] + ks * 8192 + (xx - 8) * 1024); };
    auto rd4 = [&](int r) AI { const int ks = r / 12, xx = r % 12;
      if (xx < 2) sethalf(fa[ks][0], xx, rtr(ba[xx] + ks * 8192));
      else if (xx < 6) fb[ks][xx - 2] = rd128(ba[2 + ks] + (xx - 2) * 2048);
      else { const int a = (xx - 4) >> 1, h = xx & 1; sethalf(fa[ks][a], h, rtr(ba[h] + ks * 8192 + a * 256)); } };
    auto mm = [&](int ks, int a, int b) AI { acc[a][b] = MFMA(fb[ks][b], fa[ks][a], acc[a][b]); };
    auto up3 = [&](int b) AI { ba[b] = nq(sl) + cb3[b]; PIN(ba[b]); };
    auto up4 = [&](int b) AI { ba[b] = nq(sl) + cb4[b]; PIN(ba[b]); };
    period<4, 4, true, 24, LASTR, 5>(rd3, mm, up3); waitl0(); bar(); sl = nq(sl);
    for (int s = 1; s < 27; ++s) { period<4, 4, false, 24, LASTR, 5>(rd3, mm, up3); waitl0(); waitv<0>(); bar(); sl = nq(sl); }
    period<4, 4, false, 24, LASTR, 4>(rd3, mm, up4); waitl0(); bar(); sl = nq(sl);
    for (int s = 0; s < n4 - 1; ++s) { period<4, 4, false, 24, LASTR, 4>(rd4, mm, up4); waitl0(); bar(); sl = nq(sl); }
    period<4, 4, false, 24, LASTR, 4>(rd4, mm, up4);
    drain<4, 4>(mm);
    SB(); bar();
    const unsigned pb = ((sl + 32768u + (wq >> 1) * 32768u) & 131071u) + (wq & 1) * 16384 + lane * 16;
    if (half) {
      float* op = (float*)karg(A_OUT) + (size_t)(ci * 128 + 64 * wm + r15) * 1024 + cnt * 128 + 64 * wn + 4 * g;
#pragma unroll
      for (int a = 0; a < 4; ++a)
#pragma unroll
        for (int b = 0; b < 4; ++b) *(f32x4*)(op + a * 16384 + b * 16) = acc[a][b] + rdf4(pb + (a * 4 + b) * 1024);
    } else {
#pragma unroll
      for (int a = 0; a < 4; ++a)
#pragma unroll
        for (int b = 0; b < 4; ++b) st128(rws, __builtin_bit_cast(u32x4, acc[a][b] + rdf4(pb + (a * 4 + b) * 1024)), lane * 16u + (a * 4 + b) * 1024, PART_OFF + (ci * 8 + cnt) * 65536 + wq * 16384);
      waitv<0>(); bar();
    }
  }
}

extern "C" void kernel_launch(void* const* d_in, const int* in_sizes, int n_in, void* d_out, int out_size, void* d_ws, size_t ws_size, hipStream_t stream) {
  (void)in_sizes; (void)n_in; (void)out_size; (void)ws_size;
  (void)hipFuncSetAttribute((const void*)fused_kernel, hipFuncAttributeMaxDynamicSharedMemorySize, LDS_BYTES);
  fused_kernel<<<256, 512, LDS_BYTES, stream>>>((const u16*)d_in[0], (const u16*)d_in[1], (const u16*)d_in[2], (const u16*)d_in[3], (const u16*)d_in[4],
      (const u16*)d_in[5], (const u16*)d_in[6], (const u16*)d_in[7], (const u16*)d_in[8], (const u16*)d_in[10], (const u16*)d_in[11], (const u16*)d_in[12],
      (const u16*)d_in[13], (const u16*)d_in[14], (char*)d_ws, (float*)d_out);
}
```

```cpp
#include <hip/hip_runtime.h>
#include <utility>

typedef unsigned short u16;
typedef __bf16 bf16x2 __attribute__((ext_vector_type(2)));
typedef __bf16 bf16x4 __attribute__((ext_vector_type(4)));
typedef __bf16 bf16x8 __attribute__((ext_vector_type(8)));
typedef float f32x2 __attribute__((ext_vector_type(2)));
typedef float f32x4 __attribute__((ext_vector_type(4)));
typedef float f32x16 __attribute__((ext_vector_type(16)));
typedef unsigned u32x2 __attribute__((ext_vector_type(2)));
typedef unsigned u32x4 __attribute__((ext_vector_type(4)));
typedef __amdgpu_buffer_rsrc_t rsrc_t;
#define LAS __attribute__((address_space(3)))
#define DI __device__ __forceinline__
#define AI __attribute__((always_inline))
#define SB() __builtin_amdgcn_sched_barrier(0)
#define PIN(x) asm volatile("" : "+v"(x))
#define MFMA(a, b, c) __builtin_amdgcn_mfma_f32_16x16x32_bf16(a, b, c, 0, 0, 0)
#define MFMA32(a, b, c) __builtin_amdgcn_mfma_f32_32x32x16_bf16(a, b, c, 0, 0, 0)
template <int V> using IC = std::integral_constant<int, V>;

constexpr unsigned HID_PANEL = 112 * 8192, H_OFF = 16 * HID_PANEL, H_PANEL = 32 * 16384;
constexpr unsigned PART_OFF = H_OFF + 16 * H_PANEL;
constexpr unsigned GRAN_OFF = PART_OFF + 128 * 65536;
constexpr unsigned RG_OFF = GRAN_OFF + 256 * 1024;
constexpr unsigned CG_OFF = RG_OFF + 16 * 8192;
constexpr unsigned FLAG_OFF = CG_OFF + 256 * 2048;
constexpr unsigned EPC_OFF = FLAG_OFF + 16 * 1024;
constexpr unsigned DUM_OFF = EPC_OFF + 16 * 128;
constexpr unsigned WS_BYTES = DUM_OFF + 256 * 128;
constexpr int FK_M1 = 0, FK_M2 = 1, FK_M3 = 2, FK_H = 3, FK_P = 4, FK_EP = 5;
constexpr unsigned TAB = 147456, T_ROW = TAB, T_XA = TAB + 2048, T_OUT = TAB + 4096, T_B1 = TAB + 5120, T_CS2 = TAB + 6144, T_WA = TAB + 8192, T_ZERO = TAB + 12288, T_AGG = 131072;
constexpr unsigned LDS_BYTES = 163840;
#define N4A 11
static_assert(N4A >= 11 && N4A <= 16, "");
constexpr int LAG = 2, LASTR = 25;
constexpr unsigned SPIN_CAP = 1u << 16;

DI rsrc_t mk(const void* p, unsigned bytes) { return __builtin_amdgcn_make_buffer_rsrc((void*)p, 0, bytes, 0x00020000); }
DI void setm0(unsigned lds) { asm volatile("s_mov_b32 m0, %0\n\ts_nop 0" ::"s"(lds) : "memory"); }
template <int OFF> DI void dma(rsrc_t r, unsigned voff, unsigned soff) {
  asm volatile("buffer_load_dwordx4 %0, %1, %2 offen offset:%3 lds" ::"v"(voff), "s"(r), "s"(soff), "i"(OFF) : "memory");
}
template <int OFF> DI void dma1(rsrc_t r, unsigned voff, unsigned soff) {
  asm volatile("buffer_load_dwordx4 %0, %1, %2 offen offset:%3 sc1 lds" ::"v"(voff), "s"(r), "s"(soff), "i"(OFF) : "memory");
}
#define PINS(x) asm volatile("" : "+s"(x))
typedef const void* const __attribute__((address_space(4)))* kargs_t;
DI const char* karg(int i) { kargs_t ka = (kargs_t)__builtin_amdgcn_kernarg_segment_ptr(); PINS(ka); return (const char*)ka[i]; }
enum { A_X, A_LRE, A_LIM, A_BRE, A_BIM, A_CRE, A_CIM, A_LDT, A_DSK, A_W1, A_B1, A_W2, A_B2, A_GW, A_WS, A_OUT };
template <int N> DI void waitv() { asm volatile("s_waitcnt vmcnt(%0)" ::"i"(N) : "memory"); }
DI void waitl0() { SB(); __builtin_amdgcn_s_waitcnt(0xC07F); asm volatile("" ::: "memory"); }
#define PH(k)
DI void bar() { asm volatile("s_barrier" ::: "memory"); SB(); }
DI bf16x8 rd128(unsigned a) { return *(LAS bf16x8*)(size_t)a; }
DI u32x4 rdu4(unsigned a) { return *(LAS u32x4*)(size_t)a; }
DI f32x4 rdf4(unsigned a) { return *(LAS f32x4*)(size_t)a; }
DI f32x2 rdf2(unsigned a) { return *(LAS f32x2*)(size_t)a; }
DI float rdf(unsigned a) { return *(LAS float*)(size_t)a; }
DI void wrf(unsigned a, float v) { *(LAS float*)(size_t)a = v; }
DI void wrf2(unsigned a, f32x2 v) { *(LAS f32x2*)(size_t)a = v; }
DI bf16x4 rtr(unsigned a) { return __builtin_amdgcn_ds_read_tr16_b64_v4bf16((LAS bf16x4*)(size_t)a); }
DI void sethalf(bf16x8& f, int h, bf16x4 v) {
  if (h == 0) f = __builtin_shufflevector(v, v, 0, 1, 2, 3, -1, -1, -1, -1);
  else { bf16x8 t = __builtin_shufflevector(v, v, -1, -1, -1, -1, 0, 1, 2, 3); f = __builtin_shufflevector(f, t, 0, 1, 2, 3, 12, 13, 14, 15); }
}
DI unsigned f2u(float f) { return __builtin_bit_cast(unsigned, f); }
DI float u2f(unsigned u) { return __builtin_bit_cast(float, u); }
DI float shx(int lane, float v, int m) { return u2f(__builtin_amdgcn_ds_bpermute((lane ^ m) * 4, (int)f2u(v))); }
DI float sum16(float v) { const auto r = __builtin_amdgcn_permlane16_swap(f2u(v), f2u(v), false, false); return u2f(r[0]) + u2f(r[1]); }
DI float sum32(float v) { const auto r = __builtin_amdgcn_permlane32_swap(f2u(v), f2u(v), false, false); return u2f(r[0]) + u2f(r[1]); }
DI float fexp(float v) { return __builtin_amdgcn_exp2f(v * 1.4426950408889634f); }
DI float bf2f(unsigned v) { return u2f(v << 16); }
DI unsigned pk2(float lo, float hi) { f32x2 v = {lo, hi}; return __builtin_bit_cast(unsigned, __builtin_convertvector(v, bf16x2)); }
DI float rbf(float f) { return u2f(pk2(f, 0.f) << 16); }
DI float dot2(unsigned a, unsigned b, float c) { return __builtin_amdgcn_fdot2_f32_bf16(__builtin_bit_cast(bf16x2, a), __builtin_bit_cast(bf16x2, b), c, false); }
template <int C> DI float dppadd(float v) { return v + u2f(__builtin_amdgcn_update_dpp(0, (int)f2u(v), C, 0xf, 0xf, true)); }
DI float sel4(bool b0, bool b1, float a, float b, float c, float d) { PIN(a); PIN(b); PIN(c); PIN(d); const float lo = b0 ? b : a, hi = b0 ? d : c; return b1 ? hi : lo; }
DI f32x2 cmul(f32x2 a, f32x2 b) { return (f32x2){a.x * b.x - a.y * b.y, a.x * b.y + a.y * b.x}; }
DI f32x2 cfma(f32x2 a, f32x2 b, f32x2 c) { return (f32x2){__builtin_fmaf(a.x, b.x, __builtin_fmaf(-a.y, b.y, c.x)), __builtin_fmaf(a.x, b.y, __builtin_fmaf(a.y, b.x, c.y))}; }
DI void st128(rsrc_t r, u32x4 v, unsigned voff, unsigned soff) { __builtin_amdgcn_raw_buffer_store_b128(v, r, voff, soff, 16); }
DI void st64(rsrc_t r, u32x2 v, unsigned voff, unsigned soff) { __builtin_amdgcn_raw_buffer_store_b64(v, r, voff, soff, 16); }
DI void st32(rsrc_t r, unsigned v, unsigned voff, unsigned soff) { __builtin_amdgcn_raw_buffer_store_b32(v, r, voff, soff, 16); }
DI void stflag(rsrc_t r, unsigned v, int lane, unsigned soff) { if (lane == 0) st32(r, v, 0u, soff); }
DI void ld32a(unsigned& d, rsrc_t r, unsigned voff, unsigned soff) { asm volatile("buffer_load_dword %0, %1, %2, %3 offen sc1" : "=v"(d) : "v"(voff), "s"(r), "s"(soff) : "memory"); }
DI void ld16p(unsigned& d, const u16* p, unsigned voff) { asm volatile("global_load_ushort %0, %1, %2" : "=v"(d) : "v"(voff), "s"(p) : "memory"); }
DI void ld64p(u32x2& d, rsrc_t r, unsigned voff, unsigned soff) { asm volatile("buffer_load_dwordx2 %0, %1, %2, %3 offen" : "=v"(d) : "v"(voff), "s"(r), "s"(soff) : "memory"); }
DI void ld64a(u32x2& d, rsrc_t r, unsigned voff, unsigned soff) { asm volatile("buffer_load_dwordx2 %0, %1, %2, %3 offen sc1" : "=v"(d) : "v"(voff), "s"(r), "s"(soff) : "memory"); }
DI void ld128g(u32x4& d, const u16* p, unsigned voff) { asm volatile("global_load_dwordx4 %0, %1, %2" : "=v"(d) : "v"(voff), "s"(p) : "memory"); }
DI void pfl(unsigned& d, rsrc_t r, unsigned voff, unsigned soff) { asm volatile("buffer_load_dword %0, %1, %2, %3 offen" : "+v"(d) : "v"(voff), "s"(r), "s"(soff) : "memory"); }
DI bool anyl(bool b) { return __builtin_amdgcn_ballot_w64(b) != 0; }
DI void ld128p(u32x4& d, rsrc_t r, unsigned voff, unsigned soff) { asm volatile("buffer_load_dwordx4 %0, %1, %2, %3 offen" : "=v"(d) : "v"(voff), "s"(r), "s"(soff) : "memory"); }
DI void sincos_f(float t, float& sn, float& cs) {
  const float k = __builtin_rintf(t * 0.63661977236f);
  float r = __builtin_fmaf(k, -1.5707962513f, t); r = __builtin_fmaf(k, -7.5497894159e-08f, r); r = __builtin_fmaf(k, -5.3903029534e-15f, r);
  const float z = r * r;
  const float sp = __builtin_fmaf(r * z, __builtin_fmaf(z, __builtin_fmaf(z, __builtin_fmaf(z, 2.7183114939e-06f, -1.9839334836e-04f), 8.3333293858e-03f), -1.6666666642e-01f), r);
  const float cp = __builtin_fmaf(z, __builtin_fmaf(z, __builtin_fmaf(z, __builtin_fmaf(z, 2.4390448796e-05f, -1.3886763775e-03f), 4.1666623324e-02f), -4.9999999726e-01f), 1.f);
  const int q = (int)k;
  const float a = (q & 1) ? cp : sp, b = (q & 1) ? sp : cp;
  sn = (q & 2) ? -a : a; cs = ((q + 1) & 2) ? -b : b;
}
DI void ld128a(u32x4& d, rsrc_t r, unsigned voff, unsigned soff) { asm volatile("buffer_load_dwordx4 %0, %1, %2, %3 offen sc1" : "=v"(d) : "v"(voff), "s"(r), "s"(soff) : "memory"); }

DI unsigned x1off(int a) { return a == 0 ? 131072u : (unsigned)(81920 + a * 16384); }
DI unsigned nq(unsigned s) { return (s + 32768u) & 131071u; }

DI int kb_of(int j, int bl) { return 7 * j + ((j & 1) ? (bl == 6 ? 0 : bl + 1) : bl); }

template <int NR, int LAST> constexpr int nread(int sl) { int n = 0; for (int r = 0; r < NR; ++r) n += (r * LAST) / (NR - 1) <= sl; return n; }
template <int TA, int TB, bool FIRST, int NR, int LAST, int NB, int SL, class RD, class MM, class UP>
DI void slot(RD& rd, MM& mm, UP& upd) {
  constexpr int NM = TA * TB, q = SL + LAG;
  if constexpr (!FIRST && (q == NM || q == NM + TB || q == 2 * NM)) {
    constexpr int cnt = nread<NR, LAST>(SL - 1) - nread<NR, LAST>(SL - 6);
    __builtin_amdgcn_s_waitcnt(0xC07F | (cnt << 8));
  }
  if constexpr (!(FIRST && q < NM)) { constexpr int ss = (q < NM) ? 1 : ((q < 2 * NM) ? 0 : 1); constexpr int m = q % NM; mm(ss, m / TB, m % TB); }
  SB();
  constexpr int r = (SL * (NR - 1) + LAST - 1) / LAST;
  if constexpr (r < NR && (r * LAST) / (NR - 1) == SL) rd(r);
  if constexpr (SL > LAST && SL - LAST - 1 < NB) upd(SL - LAST - 1);
  SB();
}
template <int TA, int TB, bool FIRST, int NR, int LAST, int NB, class RD, class MM, class UP, int... I>
DI void period_(RD& rd, MM& mm, UP& upd, std::integer_sequence<int, I...>) { (slot<TA, TB, FIRST, NR, LAST, NB, I>(rd, mm, upd), ...); }
template <int TA, int TB, bool FIRST, int NR, int LAST, int NB, class RD, class MM, class UP>
DI void period(RD& rd, MM& mm, UP& upd) {
  static_assert(NR - 1 <= LAST && LAST + NB < 2 * TA * TB, "");
  period_<TA, TB, FIRST, NR, LAST, NB>(rd, mm, upd, std::make_integer_sequence<int, 2 * TA * TB>{});
}
template <int TA, int TB, class MM> DI void drain(MM& mm) {
#pragma unroll
  for (int m = LAG; m < TA * TB; ++m) mm(1, m / TB, m % TB);
}
template <bool FIRST, int SL, class RD, class MM, class UP, class G> DI void slot32(RD& rd, MM& mm, UP& upd, G& g) {
  constexpr int q = SL + 1;
  if constexpr (!(FIRST && q < 8)) mm((q < 8) ? 1 : ((q < 16) ? 0 : 1), q & 7);
  SB();
  if constexpr (SL < 11) { constexpr int r0 = (SL * 3 + 1) / 2; rd(r0); if constexpr (!(SL & 1) && r0 + 1 < 16) rd(r0 + 1); }
  else if constexpr (SL == 11) {}
  else { upd(2 * (SL - 12)); upd(2 * (SL - 12) + 1); }
  SB();
  if constexpr ((SL & 3) == 1) { g(IC<SL / 4>{}); SB(); }
}
template <bool FIRST, class RD, class MM, class UP, class G, int... I> DI void period32_(RD& rd, MM& mm, UP& upd, G& g, std::integer_sequence<int, I...>) { (slot32<FIRST, I>(rd, mm, upd, g), ...); }
template <bool FIRST, class RD, class MM, class UP, class G> DI void period32(RD& rd, MM& mm, UP& upd, G& g) { period32_<FIRST>(rd, mm, upd, g, std::make_integer_sequence<int, 16>{}); }
template <class G, class W, int... U> DI void ival_(G& g, W& wk, std::integer_sequence<int, U...>) { ((g(IC<U>{}), SB(), wk(IC<U>{}), SB()), ...); }
template <class G, class W> DI void ival(G&& g, W&& wk) { ival_(g, wk, std::make_integer_sequence<int, 8>{}); }
template <int N, class F, int... I> DI void sfor_(F& f, std::integer_sequence<int, I...>) { (f(IC<I>{}), ...); }
template <int N, class F> DI void sfor(F&& f) { sfor_<N>(f, std::make_integer_sequence<int, N>{}); }

DI void check_flags(unsigned& pv, rsrc_t ws, unsigned voff, unsigned soff, int lane, int lo, int hi, unsigned ep) {
  PIN(pv);
  const bool need = lane >= lo && lane < hi;
  if (__builtin_expect(anyl(need && pv != ep), 0)) {
    for (unsigned sp = 0; sp < SPIN_CAP; ++sp) {
      __builtin_amdgcn_s_sleep(2);
      ld32a(pv, ws, voff, soff); asm volatile("s_waitcnt vmcnt(0)" : "+v"(pv)::"memory");
      if (!anyl(need && pv != ep)) break;
    }
  }
}

#if defined(__HIP_DEVICE_COMPILE__) && __HIP_DEVICE_COMPILE__
__attribute__((target("no-packed-fp32-ops")))
#endif
__global__ __launch_bounds__(512, 2) void fused_kernel(const u16* x, const u16* lre, const u16* lim, const u16* bre, const u16* bim, const u16* cre, const u16* cim,
    const u16* ldt, const u16* dsk, const u16* w1, const u16* b1, const u16* w2, const u16* b2, const u16* gw, char* ws, float* out) {
  extern __shared__ __attribute__((aligned(16))) char shm[];
  const int lane = threadIdx.x & 63, w = __builtin_amdgcn_readfirstlane(threadIdx.x >> 6);
  const int bid = blockIdx.x, xcd = bid & 7, loc = bid >> 3;
  const int ci = (xcd & 1) * 8 + (loc & 7), cj = (xcd >> 1) * 4 + (loc >> 3), cnt = cj & 7, half = cj >> 3, wq = w & 3, rot = (ci + 2 * cj) & 7;
  const int n4 = half ? 32 - N4A : N4A, k4 = half ? N4A : 0;
  const rsrc_t rws = mk(karg(A_WS), WS_BYTES);
  const int wm = (w >> 1) & 1, wn = w & 1, g = lane >> 4, r15 = lane & 15, q2 = (lane >> 2) & 3, p = lane & 3;
  const unsigned flagb = FLAG_OFF + ci * 1024, fvo = (lane & 15) * 4;
  auto nowork = [&](auto) AI {};

  unsigned fsl;
  if (w < 4) {
    constexpr bool isx = true; (void)isx;
    unsigned epr, epc, pdt, plr, pli;
    const int tx = threadIdx.x, hl = lane >> 5, r31 = lane & 31;
    unsigned va[4], vb[4], vn[4];
#pragma unroll
    for (int u = 0; u < 4; ++u) vn[u] = (8 * u + (lane >> 3)) * 2048 + (((lane & 7) ^ ((4 * u + g) & 7)) * 16) - 1024 * u;
    rsrc_t ra = mk(x, 2048 * 2048), rb = mk((const char*)w1 - 4096, 1024 * 7168 + 4096);
    unsigned sl, sa, sb; int sx;
    unsigned ta, tb;
    auto nexts = [&]() AI { sa = __builtin_amdgcn_readlane(ta, sx); sb = __builtin_amdgcn_readlane(tb, sx); ++sx; PINS(sa); PINS(sb); };
    auto G = [&](auto U) AI { constexpr int u = U.value;
      if constexpr (u == 0) setm0(sl + wq * 4096);
      if constexpr (u == 4) setm0(sl + 16384 + wq * 4096);
      if constexpr (u < 4) dma1<u * 1024>(ra, va[u], sa); else dma<(u - 4) * 1024>(rb, vb[u - 4], sb);
    };
    const int seg0 = 4 * wm + hl;
    f32x2 lam, cof, l16, l128, l1k, pw0, pw1;
    unsigned dumv = 0;
    {
      unsigned vw[7];
#pragma unroll
      for (int q = 0; q < 6; ++q) { const int k = 8 * (q & 1) + (lane >> 3), f = ((k >> 1) & 1) | ((q & 1) << 1);
        vw[q] = 4096 + k * 7168 + ((cj & 1) ? 64 : 0) + (q >> 1) * 128 + (((((lane & 7) >> 1) ^ f) * 2) + (lane & 1)) * 16 - (q & 3) * 1024; }
      vw[6] = 4096 + (lane >> 2) * 7168 + ((cj & 1) ? 0 : 384) + (((((lane & 3) >> 1) ^ (lane >> 5)) * 2) + (lane & 1)) * 16 - 2048;
      auto G1 = [&](auto S, auto U) AI { constexpr int s = S.value, u = U.value, a = s % 3;
        if constexpr (u == 0) setm0((a == 0 ? 131072u : 81920u + a * 16384u) + wq * 4096);
        if constexpr (u == 4) setm0(a * 32768 + wq * 7168);
        if constexpr (u == 8) setm0(a * 32768 + wq * 7168 + 4096);
        if constexpr (u < 4) dma<u * 1024>(ra, vn[u], sa); else dma<(u & 3) * 1024>(rb, vw[u - 4], sb);
      };
      sa = (ci * 128 + 32 * wq) * 2048 + (ci & 8) * 128; sb = ((ci & 8) * 64 + 16 * wq) * 7168 + cj * 448;
      sfor<11>([&](auto U) AI { G1(IC<0>{}, U); });
      ld32a(epr, rws, 0u, flagb + FK_EP * 128); ld32a(epc, rws, 0u, EPC_OFF + cj * 128);
      sa += 128; sb += 64 * 7168;
      sfor<11>([&](auto U) AI { G1(IC<1>{}, U); });
      ld16p(pdt, ldt + 64 * cj + 32 * wn, r31 * 2u); ld16p(plr, lre + 64 * cj + 32 * wn, r31 * 2u); ld16p(pli, lim + 64 * cj + 32 * wn, r31 * 2u);
      const int c7 = ci & 7, sstar = c7 < 2 ? c7 : 2;
      {
        int k = lane < 8 ? lane - 2 + (c7 < 2 ? 3 * c7 : c7 - 2) : (lane ^ rot); if (lane < 8) k = 2 + (k >= 6 ? k - 6 : k);
        k = (k ^ (ci & 8)) & 15;
        ta = (ci * 128 + 32 * wq) * 2048 + k * 128; tb = (k * 64 + 16 * wq) * 7168 + cj * 448; sx = 2; nexts();
      }
      waitv<11 + 3>(); PIN(epr); PIN(epc);
      epr = __builtin_amdgcn_readfirstlane(epr) + 1u; epc = __builtin_amdgcn_readfirstlane(epc) + 1u;
      u32x2 wv[4][4]; u32x4 cg[8], rgv[2];
      float cs[4] = {0, 0, 0, 0}, c0s, c1s; unsigned t2a, t2b;
      const int fsw = ((q2 >> 1) & 1) | ((g & 1) << 1);
      const unsigned cf = (g >> 1) * 7168 + (8 * (g & 1) + q2) * 128 + ((wq ^ fsw) * 32) + p * 8, ch = (g >> 1) * 7168 + 6144 + (8 * (g & 1) + q2) * 64 + (((wq & 1) ^ (g & 1)) * 32) + p * 8;
      const int tc = tx < 224 ? tx : 223;
      const unsigned cgo = (tc >= 112 ? 16384 - 1792 : 0) + tc * 16;
      auto gload = [&](int q) AI { if (q < 8) ld128a(cg[q], rws, cgo, CG_OFF + (cj * 16 + q) * 2048); else ld128a(rgv[q - 8], rws, tx * 16u, RG_OFF + ci * 8192 + (q - 8) * 4096); };
      auto gbad = [&]() AI { PIN(rgv[0]); PIN(rgv[1]); unsigned d = (rgv[0][1] ^ epr) | (rgv[0][3] ^ epr) | (rgv[1][1] ^ epr) | (rgv[1][3] ^ epr);
#pragma unroll
        for (int q = 0; q < 8; ++q) { PIN(cg[q]); d |= (cg[q][1] ^ epc) | (cg[q][3] ^ epc); }
        return d != 0; };
      auto E1 = [](int s) constexpr { return s == 4 ? 1 : (s >= 7 && s < 10) ? 3 : s == 10 ? 1 : 0; };
      sfor<16>([&](auto S1) AI { constexpr int s = S1.value - 1;
        if constexpr (s >= 0) { if constexpr (s < 14) waitv<11 + E1(s - 1) + (s >= 2) + (s >= 3)>(); else waitv<8 + 1>(); }
        if constexpr ((s >= 0 && s < 3) || s >= 13) waitl0();
        bar();
        if constexpr (s < 2) {
          if (sstar == s + 1) {
#pragma unroll
            for (int e = 0; e < 4; ++e)
#pragma unroll
              for (int t = 0; t < 4; ++t) wv[t][e] = __builtin_bit_cast(u32x2, rtr((s + 1) * 32768 + (e >> 1) * 14336 + (t < 3 ? cf + t * 2048 + (e & 1) * 512 : ch + (e & 1) * 256)));
          }
        }
        auto work = [&](auto U) AI { constexpr int u = U.value;
          if constexpr ((s == 2 || s == 3) && u < 8) {
            constexpr int t = (s - 2) * 2 + (u >> 2), e = u & 3;
            cs[t] += (bf2f(wv[t][e][0] & 0xffffu) + u2f(wv[t][e][0] & 0xffff0000u)) + (bf2f(wv[t][e][1] & 0xffffu) + u2f(wv[t][e][1] & 0xffff0000u));
            PIN(cs[t]);
          }
          if constexpr (s == 4 && (u == 0 || u == 2)) {
#pragma unroll
            for (int t = 0; t < 4; ++t) { cs[t] = u ? sum32(cs[t]) : sum16(cs[t]); PIN(cs[t]); }
          }
          if constexpr (s == 4 && u == 4)
            st64(rws, (u32x2){f2u(sel4(g & 1, g & 2, cs[0], cs[1], cs[2], cs[3])), epc}, (g < 3 ? g * 64 + wq * 16 + r15 : 192 + (wq & 1) * 16 + r15) * 8u, CG_OFF + (cj * 16 + ci) * 2048);
          if constexpr (s >= 1 && s < 13 && u == 10) {
            if constexpr (s < 5) pfl(dumv, mk(s < 3 ? bre : bim, 1024 * 2048), lane < 8 ? lane * 128u : 0x80000000u, (bid * 64 + ((s - 1) & 1) * 32 + wq * 8) * 128);
            else pfl(dumv, mk(w2, 3584 * 2048), lane < 7 ? lane * 128u : 0x80000000u, (bid * 224 + (s - 5) * 28 + wq * 7) * 128);
          }
          if constexpr (s == 5 && u == 1) {
            PIN(pdt); PIN(plr); PIN(pli);
            const float dt = rbf(fexp(bf2f(pdt))), e = fexp(dt * bf2f(plr)); float sn, cs_; sincos_f(dt * bf2f(pli), sn, cs_);
            lam = (f32x2){e * cs_, e * sn}; PIN(lam);
          }
          if constexpr (s == 5 && u == 6) {
            const float lr = bf2f(plr), li = bf2f(pli), nr = lam.x - 1.f, ni = lam.y, den = 2.f / (lr * lr + li * li);
            cof = (f32x2){(nr * lr + ni * li) * den, (ni * lr - nr * li) * den}; PIN(cof);
          }
          if constexpr (s == 6 && u == 1) { l16 = cmul(lam, lam); l16 = cmul(l16, l16); l16 = cmul(l16, l16); l16 = cmul(l16, l16); PIN(l16); }
          if constexpr (s == 6 && u == 4) {
            const f32x2 l32 = cmul(l16, l16), l64 = cmul(l32, l32); l128 = cmul(l64, l64);
            pw0 = hl ? l16 : (f32x2){1.f, 0.f}; if (wm) pw0 = cmul(pw0, l64);
            pw1 = cmul(pw0, l32); PIN(l128); PIN(pw0); PIN(pw1);
          }
          if constexpr (s == 6 && u == 7) { l1k = cmul(l128, l128); l1k = cmul(l1k, l1k); l1k = cmul(l1k, l1k); PIN(l1k); }
          if constexpr (s >= 7 && s < 11 && (u == 2 || u == 5 || u == 8) && (s - 7) * 3 + u / 3 < 10) gload((s - 7) * 3 + u / 3);
          if constexpr (s == 11 && u == 1) {
            const int s3 = lane - 16, n = 2 * s3 + (wq >> 1), kb = kb_of(half * 8 + (((n & 7) ^ rot) & 7), n >> 3);
            int ks = lane - 44 + rot; if (ks >= n4) ks -= n4; ks += k4;
            const int k2 = lane ^ rot;
            t2a = lane < 16 ? (ci * 128 + 32 * wq) * 2048 + k2 * 128 : lane < 44 ? ci * HID_PANEL + kb * 8192 + (wq & 1) * 4096 : H_OFF + ci * H_PANEL + ks * 16384 + wq * 4096;
            t2b = lane < 16 ? (cj * 64 + 32 * (wq >> 1)) * 2048 + k2 * 128 : lane < 44 ? (kb * 32 + 16 * (wq & 1)) * 2048 + cnt * 256 : (cnt * 128 + 32 * wq) * 2048 + (ks >> 1) * 128 + (ks & 1);
            PIN(t2a); PIN(t2b);
          }
          if constexpr (s == 11 && u == 6) {
#pragma unroll
            for (int q = 0; q < 4; ++q) { va[q] = (16 * hl + 4 * q + ((lane >> 3) & 3)) * 2048 + (((lane & 7) ^ ((4 * q + g) & 7)) * 16) - 1024 * q; PIN(va[q]); }
          }
          if constexpr (s == 12 && u == 1) {
            if (__builtin_expect(anyl(gbad()), 0))
              for (unsigned sp = 0; sp < SPIN_CAP; ++sp) { __builtin_amdgcn_s_sleep(2);
#pragma unroll
                for (int q = 0; q < 10; ++q) gload(q);
                waitv<0>(); if (!anyl(gbad())) break; }
          }
          if constexpr (s == 12 && u == 4) {
            c0s = 0.f; c1s = 0.f;
#pragma unroll
            for (int q = 0; q < 8; ++q) { c0s += u2f(cg[q][0]); c1s += u2f(cg[q][2]); }
            wrf2(T_CS2 + tc * 8, (f32x2){c0s, c1s});
            wrf(T_ROW + (tx & 1) * 1024 + (tx >> 1) * 4, u2f(rgv[0][0])); wrf(T_ROW + (tx & 1) * 1024 + 512 + (tx >> 1) * 4, u2f(rgv[0][2]));
            wrf2(T_XA + tx * 8, (f32x2){u2f(rgv[1][0]), u2f(rgv[1][2])});
          }
          if constexpr (s == 13 && u == 1) {
            const float v = rdf(T_CS2 + tc * 4) + rdf(T_CS2 + 896 + tc * 4), hi = rbf(v), bb = rdf(T_B1 + tc * 4);
            *(LAS u32x4*)(size_t)(T_WA + tc * 16) = (u32x4){pk2(hi, v - hi), pk2(hi, bb), pk2(bb, 0.f), 0u};
          }
        };
        if constexpr (s < 13) { sfor<11>([&](auto U) AI { G1(IC<s + 3>{}, U); SB(); work(U); SB(); }); if constexpr (s < 12) nexts(); }
        if constexpr (s == 12) {
          rb = mk((wq & 1) ? bim : bre, 1024 * 2048);
#pragma unroll
          for (int u = 0; u < 4; ++u) vb[u] = vn[u];
          ta = t2a; tb = t2b;
          sl = 32768; sx = 0; nexts();
        }
        if constexpr (s >= 13) { ival(G, work); sl = nq(sl); nexts(); asm volatile("s_nop 4"); }
        if constexpr (s == 14) { ival(G, nowork); sl = nq(sl); nexts(); }
      });
      waitv<16>(); bar();
    }
    f32x16 acc[2][2];
#pragma unroll
    for (int a = 0; a < 4; ++a)
#pragma unroll
      for (int e = 0; e < 16; ++e) acc[a >> 1][a & 1][e] = 0.f;
    {
      bf16x8 fa[2][4], fb[2][4];
      unsigned cb[8], ba[8];
#pragma unroll
      for (int b = 0; b < 8; ++b) { cb[b] = (b < 4 ? wm * 8192u : 16384 + wn * 8192u) + r31 * 128 + (((2 * (b & 3) + hl) ^ ((r31 >> 1) & 7)) * 16); ba[b] = 32768 + cb[b]; PIN(ba[b]); }
      auto rd = [&](int r) AI { const int k16 = r >> 2, ks = r >> 3, kk = k16 & 1, xx = r & 3;
        if (xx == 0) fa[ks][kk * 2] = rd128(ba[k16]); else if (xx == 3) fa[ks][kk * 2 + 1] = rd128(ba[k16] + 4096); else fb[ks][kk * 2 + xx - 1] = rd128(ba[4 + k16] + (xx - 1) * 4096); };
      auto mm = [&](int ks, int m) AI { const int kk = m >> 2, mt = (m >> 1) & 1, nt = m & 1; acc[mt][nt] = MFMA32(fa[ks][kk * 2 + mt], fb[ks][kk * 2 + nt], acc[mt][nt]); };
      auto up = [&](int b) AI { ba[b] = (sl ^ 65536u) + cb[b]; PIN(ba[b]); if (b == 5) nexts(); };
      period32<true>(rd, mm, up, G); sl = nq(sl); waitl0(); waitv<12>(); bar();
      for (int s = 1; s < 16; ++s) {
        if (s == 13) {
          ra = rws;
#pragma unroll
          for (int u = 0; u < 4; ++u) va[u] = lane * 16;
        }
        period32<false>(rd, mm, up, G); sl = nq(sl); waitl0(); waitv<8>(); bar();
      }
      rb = mk(w2, 3584 * 2048);
#pragma unroll
      for (int u = 0; u < 4; ++u) vb[u] = (4 * u + g) * 2048 + ((((r15 >> 1) ^ (4 * (u & 1) + g)) * 2) + (lane & 1)) * 16 - 1024 * u;
      ival(G, [&](auto U) AI { if constexpr (U.value < 7) mm(1, U.value + 1); });
      sl = nq(sl); nexts();
    }
    {
      const unsigned ca = (32 * wn + r31) * 8;
      f32x2 st[2], Pf[2], Gt;
      unsigned pv;
#define NEXT3() do { sl = nq(sl); nexts(); } while (0)
      st[0] = (f32x2){0, 0}; st[1] = (f32x2){0, 0};
      u32x4 lb[8], xe[8]; unsigned lbo[8], ob[2][2][4];
      f32x4 ev[8][2], bv[2], dv[2], g2[2]; float gr[8], o[2][2][2];
      f32x2 ag[8], cr;
      const char *pre = karg(A_CRE), *pim = karg(A_CIM);
      const rsrc_t rgr = mk(karg(A_WS) + GRAN_OFF + cj * 16384, 16384), rx = mk(karg(A_X), 2048 * 2048);
      auto bad = [&]() AI { unsigned d = 0;
#pragma unroll
        for (int q = 0; q < 8; ++q) { PIN(lb[q]); d |= (int)lbo[q] < 0 ? 0u : ((lb[q][1] ^ epc) | (lb[q][3] ^ epc)); }
        return d != 0; };
      auto E3 = [](int s) constexpr { return s == 3 ? 1 : s == 7 ? 8 : (s == 13 || s == 15) ? 4 : 0; };
      sfor<24>([&](auto S) AI { constexpr int s = S.value;
        if constexpr (s == 3) waitl0();
        if constexpr (s == 0) waitv<12>(); else waitv<16 + E3(s - 2) + E3(s - 1)>();
        if constexpr (s == 4) { if (w == 0) check_flags(pv, rws, fvo, flagb + FK_M2 * 128, lane, 8 * half, 8 * half + 8, epr); }
        if constexpr (s == 12) { if (w == 0) check_flags(pv, rws, fvo, flagb + FK_M3 * 128, lane, 8 * half, 8 * half + 8, epr); }
        bar();
        if constexpr (s == 1) { if (w == 0) { stflag(rws, epr, lane, flagb + FK_M3 * 128 + cj * 4);
                                              ld32a(pv, rws, fvo, flagb + FK_M2 * 128); } }
        if constexpr (s == 9) { if (w == 0) ld32a(pv, rws, fvo, flagb + FK_M3 * 128); }
        if constexpr (s == 18) { if (w == 0) stflag(rws, epr, lane, flagb + FK_H * 128 + cj * 4); }
        if constexpr (s == 21) { if (w == 0) ld32a(pv, rws, fvo, flagb + FK_H * 128); }
        if constexpr (s == 3) {
#pragma unroll
          for (int sg = 0; sg < 8; ++sg) ag[sg] = rdf2(T_AGG + sg * 512 + ca);
          Gt = (f32x2){0, 0}; Pf[0] = (f32x2){0, 0}; Pf[1] = (f32x2){0, 0};
        }
        if constexpr (s == 10) cr = (f32x2){0, 0};
        if constexpr (s >= 12 && s < 16) { g2[0] = rdf4(T_ROW + 1536 + (64 * wm + 16 * hl + 4 * (s - 12)) * 4); g2[1] = rdf4(T_ROW + 1536 + (64 * wm + 32 + 16 * hl + 4 * (s - 12)) * 4); }
        if constexpr (s == 18) {
#pragma unroll
          for (int q = 0; q < 2; ++q) { bv[q] = rdf4(T_OUT + (8 * r15 + 4 * q) * 4); dv[q] = rdf4(T_OUT + 512 + (8 * r15 + 4 * q) * 4); }
#pragma unroll
          for (int q = 0; q < 8; ++q) gr[q] = rdf(T_ROW + 1536 + (32 * wq + 4 * q + g) * 4);
        }
        SB();
        ival(G, [&](auto U) AI { constexpr int u = U.value;
          if constexpr (s < 3 && u < 6 && 6 * s + u < 16) {
            constexpr int t = 6 * s + u;
#pragma unroll
            for (int mt = 0; mt < 2; ++mt) { st[mt] = cfma(lam, st[mt], (f32x2){acc[mt][0][t], acc[mt][1][t]}); PIN(st[mt]); }
          }
          if constexpr (s == 2 && u == 4) { wrf2(T_AGG + seg0 * 512 + ca, st[0]); wrf2(T_AGG + (seg0 + 2) * 512 + ca, st[1]); }
          if constexpr (s == 3 && u >= 1 && u < 5) {
#pragma unroll
            for (int sg = 2 * u - 2; sg < 2 * u; ++sg) {
              const int mt = (sg >> 1) & 1; const bool me = sg == seg0 + 2 * mt;
              Pf[mt].x = me ? Gt.x : Pf[mt].x; Pf[mt].y = me ? Gt.y : Pf[mt].y; Gt = cfma(l16, Gt, ag[sg]);
            }
            PIN(Gt); PIN(Pf[0]); PIN(Pf[1]);
          }
          if constexpr (s == 3 && u == 5) st64(rgr, (u32x2){f2u(hl ? Gt.y : Gt.x), epc}, ca * 2 + hl * 8, ci * 1024);
          if constexpr (s >= 4 && s < 7 && u < 6 && 6 * (s - 4) + u < 16) {
            constexpr int t = 6 * (s - 4) + u;
#pragma unroll
            for (int mt = 0; mt < 2; ++mt) { f32x2 v = cmul(cof, (f32x2){acc[mt][0][t], acc[mt][1][t]}); PIN(v); acc[mt][0][t] = v.x; acc[mt][1][t] = v.y; }
          }
          if constexpr (s == 7 && u < 4) {
#pragma unroll
            for (int q = 2 * u; q < 2 * u + 2; ++q) { lbo[q] = (ci - 8 - 8 * hl + q) * 1024 + ca * 2; ld128a(lb[q], rgr, lbo[q], 0u); }
          }
          if constexpr ((s == 8 || s == 9 || s == 11 || s == 14) && (u == 5 || u == 6)) {
            constexpr int q = (s == 8 ? 0 : s == 9 ? 2 : s == 11 ? 4 : 6) + u - 5;
            if (!half) ld128p(xe[q], rx, g * 2048u + r15 * 16, (ci * 128 + 32 * wq + 4 * q) * 2048 + cnt * 256);
          }
          if constexpr ((s == 0 || s == 5 || s == 6 || s == 8) && u == 7) {
            constexpr int q = s == 0 ? 0 : s == 8 ? 3 : s - 4;
            pfl(dumv, mk(q < 2 ? pre : pim, 1024 * 2048), lane < 8 ? lane * 128u : 0x80000000u, (bid * 64 + (q & 1) * 32 + wq * 8) * 128);
          }
          if constexpr (s == 10 && u == 1) {
            if (__builtin_expect(anyl(bad()), 0)) {
              for (unsigned sp = 0; sp < SPIN_CAP; ++sp) {
                __builtin_amdgcn_s_sleep(2);
#pragma unroll
                for (int q = 0; q < 8; ++q) ld128a(lb[q], rgr, lbo[q], 0u);
                waitv<0>();
                if (!anyl(bad())) break;
              }
            }
          }
          if constexpr (s == 11 && u == 0) { if (ci == 15 && w == 0) stflag(rws, epc, lane, EPC_OFF + cj * 128); }
          if constexpr (s == 10 && u >= 3 && u < 7) {
#pragma unroll
            for (int q = 2 * u - 6; q < 2 * u - 4; ++q) cr = cfma(l128, cr, (f32x2){u2f(lb[q][0]), u2f(lb[q][2])});
            PIN(cr);
          }
          if constexpr (s == 11 && u == 1) {
            const auto sx_ = __builtin_amdgcn_permlane32_swap(f2u(cr.x), f2u(cr.x), false, false), sy_ = __builtin_amdgcn_permlane32_swap(f2u(cr.y), f2u(cr.y), false, false);
            cr = cfma(l1k, (f32x2){u2f(sx_[1]), u2f(sy_[1])}, (f32x2){u2f(sx_[0]), u2f(sy_[0])});
            PIN(cr);
          }
          if constexpr (s == 11 && (u == 2 || u == 3)) {
            constexpr int mt = u - 2;
            st[mt] = cmul(cof, cfma(mt ? pw1 : pw0, cr, Pf[mt])); PIN(st[mt]);
          }
          if constexpr (s >= 12 && s < 16 && u >= 1 && u < 5) {
            constexpr int e = u - 1, t = 4 * (s - 12) + e;
#pragma unroll
            for (int mt = 0; mt < 2; ++mt) {
              st[mt] = cfma(lam, st[mt], (f32x2){acc[mt][0][t], acc[mt][1][t]});
              o[mt][0][e & 1] = g2[mt][e] * st[mt].x; o[mt][1][e & 1] = -g2[mt][e] * st[mt].y;
              PIN(st[mt]); PIN(o[mt][0][e & 1]); PIN(o[mt][1][e & 1]);
              if constexpr (e & 1) { ob[mt][0][(t >> 1) & 3] = pk2(o[mt][0][0], o[mt][0][1]); ob[mt][1][(t >> 1) & 3] = pk2(o[mt][1][0], o[mt][1][1]); PIN(ob[mt][0][(t >> 1) & 3]); PIN(ob[mt][1][(t >> 1) & 3]); }
            }
          }
          if constexpr ((s == 13 || s == 15) && (u == 5 || u == 6)) {
            constexpr int mt = u - 5, hf = (s - 12) >> 1;
#pragma unroll
            for (int pt = 0; pt < 2; ++pt)
              st128(rws, (u32x4){ob[mt][pt][0], ob[mt][pt][1], ob[mt][pt][2], ob[mt][pt][3]},
                    (2 * wn + (r31 >> 4)) * 4096 + wm * 2048 + hf * 1024 + (2 * mt + hl) * 256 + ((r15 ^ (hf << 2)) * 16), H_OFF + ci * H_PANEL + (2 * cj + pt) * 16384);
          }
          if constexpr (s >= 18 && s < 22 && (u & 1)) {
            constexpr int q = 2 * (s - 18) + (u >> 2), hh = (u >> 1) & 1;
            if (!half) {
              PIN(xe[q]);
#pragma unroll
              for (int e = 0; e < 4; ++e) {
                const unsigned xw = xe[q][2 * hh + (e >> 1)];
                const float xf = (e & 1) ? u2f(xw & 0xffff0000u) : bf2f(xw & 0xffffu);
                ev[q][hh][e] = __builtin_fmaf(gr[q], __builtin_fmaf(xf, dv[hh][e], -bv[hh][e]), bv[hh][e]);
              }
            }
            PIN(ev[q][hh]);
          }
        });
        NEXT3();
      });
      waitv<16>();
      if (w == 0) check_flags(pv, rws, fvo, flagb + FK_H * 128, lane, k4 >> 1, (k4 + n4 + 1) >> 1, epr);
      bar();
#pragma unroll
      for (int u = 0; u < 4; ++u) vb[u] = vn[u];
      {
        for (int s = 0; s < n4; ++s) {
          if (s == 20) { int ks = 20 + rot; if (ks >= n4) ks -= n4; ks += k4;
                         sa = H_OFF + ci * H_PANEL + ks * 16384 + wq * 4096; sb = (cnt * 128 + 32 * wq) * 2048 + (ks >> 1) * 128 + (ks & 1); }
          rb = mk((sb & 1) ? pim : pre, 1024 * 2048); sb &= ~1u;
          ival(G, nowork); sx &= 63; NEXT3(); waitv<16>(); bar();
        }
      }
      ld32a(pv, rws, fvo, flagb + FK_P * 128);
      waitv<8 + 1>(); bar();
      waitv<0>();
      const unsigned pb = ((sl + (wq >> 1) * 32768u) & 131071u) + (wq & 1) * 16384;
      if (half) {
        check_flags(pv, rws, fvo, flagb + FK_P * 128, lane, cnt, cnt + 1, epr);
        sfor<16>([&](auto U) AI { constexpr int u = U.value; if constexpr ((u & 3) == 0) setm0(pb + u * 1024);
          dma1<(u & 3) * 1024>(rws, lane * 16u, PART_OFF + (ci * 8 + cnt) * 65536 + wq * 16384 + (u >> 2) * 4096); });
        if (cnt == 0 && w == 0) stflag(rws, epr, lane, flagb + FK_EP * 128);
      } else {
        const unsigned eb = ((sl + (wq >> 1) * 32768u) & 131071u) + (r15 >> 3) * 16384 + ((wq & 1) * 8 + ((r15 >> 1) & 3)) * 1024;
#pragma unroll
        for (int q = 0; q < 8; ++q)
#pragma unroll
          for (int hh = 0; hh < 2; ++hh) { const int bg = 2 * (r15 & 7) + hh;
            *(LAS f32x4*)(size_t)(eb + (q >> 2) * 4096 + (bg & 3) * 256 + (((4 * (q & 3) + g) ^ bg) * 16)) = ev[q][hh]; }
      }
      bar();
      waitl0(); bar();
      waitv<0>(); bar();
      if (!half) { if (w == 0) stflag(rws, epr, lane, flagb + FK_P * 128 + cnt * 4); }
      asm volatile("" ::"v"(dumv));
      fsl = sl;
    }
  } else {
    constexpr bool isx = false; (void)isx;
    const int ty = threadIdx.x - 256;
    const unsigned nb0 = r15 * 128 + ((g ^ (r15 >> 1)) * 16), nb1 = r15 * 128 + (((4 + g) ^ (r15 >> 1)) * 16);
    unsigned epr = __builtin_amdgcn_raw_buffer_load_b32(rws, 0, flagb + FK_EP * 128, 16);
    {
      const u32x4* xp = (const u32x4*)(x + (size_t)(ci * 128 + 8 * cj + 2 * wq) * 1024) + lane;
      const u32x4* gp = (const u32x4*)gw + lane;
      const u32x4 xr[2][2] = {{xp[0], xp[64]}, {xp[128], xp[192]}}, gq[2] = {gp[0], gp[64]};
      const int tc = ty < 224 ? ty : 223;
      const unsigned bq = b1[224 * cj + (tc < 192 ? ((cj & 1) ? 32 : 0) + tc : ((cj & 1) ? -192 : 0) + tc)];
      const unsigned oq = (wq < 2 ? b2 : dsk)[cnt * 128 + (ty & 127)];
      asm volatile("" ::: "memory"); SB();
      float sv[6];
#pragma unroll
      for (int r = 0; r < 2; ++r) {
        float a1 = 0.f, a2 = 0.f, a3 = 0.f;
#pragma unroll
        for (int e = 0; e < 8; ++e) { const unsigned v = xr[r][e >> 2][e & 3]; a1 = dot2(v, 0x3F803F80u, a1); a2 = dot2(v, v, a2); a3 = dot2(v, gq[e >> 2][e & 3], a3); }
        sv[r] = a1; sv[2 + r] = a2; sv[4 + r] = a3;
      }
#pragma unroll
      for (int q = 0; q < 6; ++q) { sv[q] = dppadd<0x140>(dppadd<0x141>(dppadd<0x4E>(dppadd<0xB1>(sv[q])))); PIN(sv[q]); }
      const bool r1 = lane & 4;
      float a1 = r1 ? sv[1] : sv[0], a2 = r1 ? sv[3] : sv[2], a3 = r1 ? sv[5] : sv[4];
      a1 = sum32(sum16(a1)); a2 = sum32(sum16(a2)); a3 = sum32(sum16(a3));
      const float mean = a1 * (1.f / 1024.f), var = __builtin_fmaxf(a2 * (1.f / 1024.f) - mean * mean, 0.f) + 1e-5f, rstd = __builtin_amdgcn_rsqf(var), sd = var * rstd;
      const float qv = 1.f + fexp(__builtin_fminf(a3, 60.f)), gg = __builtin_amdgcn_rcpf(1.f + fexp(__builtin_fminf(-a3, 80.f)));
      const float mh = rbf(mean), sh = rbf(sd);
      const float v0 = sel4(lane & 1, lane & 2, -2.302208198f * rstd, -0.1029432397f * rstd * rstd * rstd, qv * sd, gg);
      const float v1 = sel4(lane & 1, lane & 2, u2f(pk2(-mh, -mh)), u2f(pk2(mh - mean, sh)), u2f(pk2(sd - sh, 0.f)), 0.f);
      epr = __builtin_amdgcn_readfirstlane(epr) + 1u;
      wrf(T_B1 + ty * 4, bf2f(bq));
      wrf(T_OUT + ty * 4, bf2f(oq));
      wrf(T_ZERO + (lane & 3) * 4, 0.f);
      waitl0();
      if (lane < 16) st64(rws, (u32x2){f2u((lane & 8) ? v1 : v0), epr}, (lane >> 3) * 4096u + (lane & 7) * 8, RG_OFF + ci * 8192 + (8 * cj + 2 * wq) * 32);
    }
    unsigned sl = 32768;
    f32x4 acc[4][4];
    {
      rsrc_t rb; unsigned vb[4], sb;
      auto GY = [&](auto S, auto U) AI { constexpr int s = S.value, u = U.value;
        if constexpr (u == 0) {
          if constexpr (s == -1 || s == 12) {
            rb = s < 0 ? mk(karg(A_BRE + (wq & 1)), 1024 * 2048) : mk(karg(A_W2), 3584 * 2048);
#pragma unroll
            for (int q = 0; q < 4; ++q) vb[q] = s < 0 ? (8 * q + (lane >> 3)) * 2048 + (((lane & 7) ^ ((4 * q + g) & 7)) * 16) - 1024 * q
                                                      : (4 * q + g) * 2048 + ((((r15 >> 1) ^ (4 * (q & 1) + g)) * 2) + (lane & 1)) * 16 - 1024 * q;
          }
          if constexpr (s < 12) sb = (cj * 64 + 32 * (wq >> 1)) * 2048 + ((s + 4) ^ rot) * 128;
          else sb = (kb_of(half * 8 + (((2 * (s - 12) + (wq >> 1)) ^ rot) & 7), 0) * 32 + 16 * (wq & 1)) * 2048 + cnt * 256;
          setm0(((s + 1) & 3) * 32768 + 16384 + wq * 4096);
        }
        dma<u * 1024>(rb, vb[u], sb);
      };
      f32x4 ac1[4][7];
#pragma unroll
      for (int a = 0; a < 4; ++a)
#pragma unroll
        for (int b = 0; b < 7; ++b) ac1[a][b] = (f32x4){0, 0, 0, 0};
      {
        bf16x8 fa[2][4], fb[2][7];
        const int f = ((q2 >> 1) & 1) | ((g & 1) << 1);
        const unsigned cb[5] = {wm * 8192u + nb0, wm * 8192u + nb1, (g >> 1) * 7168u + (8 * (g & 1) + q2) * 128 + (((2 * wn) ^ f) * 32) + p * 8,
                                (g >> 1) * 7168u + (8 * (g & 1) + q2) * 128 + (((2 * wn + 1) ^ f) * 32) + p * 8, (g >> 1) * 7168u + 6144 + (8 * (g & 1) + q2) * 64 + ((wn ^ (g & 1)) * 32) + p * 8};
        unsigned ba[5]; unsigned xn = x1off(1), wnx = 32768;
#pragma unroll
        for (int b = 0; b < 5; ++b) { ba[b] = cb[b] + (b < 2 ? x1off(0) : 0u); PIN(ba[b]); }
        auto rd = [&](int r) AI { const int ks = r / 18, xx = r % 18;
          if (xx == 0) fa[ks][0] = rd128(ba[ks]);
          else if (xx <= 12) { const int b = (xx - 1) >> 1, h = (xx - 1) & 1; sethalf(fb[ks][b], h, rtr(ba[2 + (b & 1)] + (b >> 1) * 2048 + ks * 14336 + h * 512)); }
          else if (xx <= 14) { const int h = xx - 13; sethalf(fb[ks][6], h, rtr(ba[4] + ks * 14336 + h * 256)); }
          else fa[ks][xx - 14] = rd128(ba[ks] + (xx - 14) * 2048); };
        auto mm = [&](int ks, int a, int b) AI { ac1[a][b] = MFMA(fb[ks][b], fa[ks][a], ac1[a][b]); };
        auto up = [&](int b) AI { if (b < 5) { ba[b] = cb[b] + (b < 2 ? xn : wnx); PIN(ba[b]); }
                                  else { wnx = (wnx == 65536u) ? 0u : wnx + 32768u; xn = (xn == 131072u) ? 98304u : xn + 16384u; } };
        waitl0(); bar();
        PH(1);
        period<4, 7, true, 36, LASTR + 24, 6>(rd, mm, up); waitl0(); bar();
        for (int s = 1; s < 16; ++s) { period<4, 7, false, 36, LASTR + 24, 6>(rd, mm, up); waitl0(); bar(); }
        PH(2);
#pragma unroll
        for (int a = 0; a < 4; ++a) fa[0][a] = rd128(g ? T_ZERO : T_XA + (64 * wm + 16 * a + r15) * 16);
#pragma unroll
        for (int b = 0; b < 7; ++b) fb[0][b] = rd128(T_WA + ((b < 6 ? (b >> 1) * 64 + wn * 32 + (b & 1) * 16 : 192 + wn * 16) + r15) * 16);
        GY(IC<-1>{}, IC<0>{}); GY(IC<-1>{}, IC<1>{});
        drain<4, 7>(mm);
        SB(); GY(IC<-1>{}, IC<2>{}); GY(IC<-1>{}, IC<3>{}); SB();
        bar();
        sfor<4>([&](auto A) AI { GY(IC<0>{}, A); SB();
#pragma unroll
          for (int b = 0; b < 7; ++b) mm(0, A.value, b);
          SB(); });
      }
      float k0[4], k1[4], qq[4];
#pragma unroll
      for (int a = 0; a < 4; ++a) { const unsigned ra = T_ROW + (64 * wm + 16 * a + r15) * 4; k0[a] = rdf(ra); k1[a] = rdf(ra + 512); qq[a] = rdf(ra + 1024); }
      unsigned hw[4];
      unsigned pv;
      auto gelu = [&](auto I) AI {
        constexpr int idx = I.value;
        if constexpr (idx < 28) {
          constexpr int lc = idx >> 3, a = idx < 24 ? (idx >> 1) & 3 : idx - 24, bb = idx & 1, b = idx < 24 ? 2 * lc + bb : 6;
          float y[4];
#pragma unroll
          for (int e = 0; e < 4; ++e) {
            const float u = ac1[a][b][e];
            const float z = u * __builtin_fmaf(u * u, k1[a], k0[a]);
            const float d = __builtin_fmaf(__builtin_amdgcn_exp2f(z), qq[a], qq[a]);
            y[e] = u * __builtin_amdgcn_rcpf(d);
          }
          hw[bb * 2] = pk2(y[0], y[1]); hw[bb * 2 + 1] = pk2(y[2], y[3]); PIN(hw[bb * 2]); PIN(hw[bb * 2 + 1]);
        }
      };
      auto st2 = [](int s) constexpr { return s < 1 ? 0 : s < 13 ? 1 : 2; };
      sfor<15>([&](auto S1) AI { constexpr int s = S1.value + 1, v = s - 1; constexpr IC<s> S{};
        if constexpr (s == 7) waitv<8 + 2>();
        else if constexpr (s >= 2) waitv<8 + st2(s - 3) + st2(s - 2) + st2(s - 1)>();
        if constexpr (s == 12) { if (w == 4) { waitv<5>(); check_flags(pv, rws, fvo, flagb + FK_M1 * 128, lane, 8 * half, 8 * half + 8, epr); } }
        SB(); bar();
        if constexpr (s == 7) { if (w == 5) stflag(rws, epr, lane, flagb + FK_M1 * 128 + cj * 4); }
        if constexpr (s == 11) { if (w == 4) ld32a(pv, rws, fvo, flagb + FK_M1 * 128); }
        if constexpr (s == 12) { if (w == 5) stflag(rws, epr, lane, flagb + FK_M2 * 128 + cj * 4); }
        if constexpr (s < 15) {
          GY(S, IC<0>{}); GY(S, IC<1>{}); SB(); gelu(IC<2 * v>{}); SB();
          GY(S, IC<2>{}); GY(S, IC<3>{}); SB(); gelu(IC<2 * v + 1>{}); SB();
          if constexpr (v < 12) st128(rws, (u32x4){hw[0], hw[1], hw[2], hw[3]}, lane * 16u + (4 * wm + (v & 3)) * 1024, ci * HID_PANEL + kb_of(cj, 2 * (v >> 2) + wn) * 8192);
          else {
#pragma unroll
            for (int q = 0; q < 2; ++q) st64(rws, (u32x2){hw[2 * q], hw[2 * q + 1]}, lane * 16u + (4 * wm + 2 * (v - 12) + q) * 1024 + wn * 8, ci * HID_PANEL + kb_of(cj, 6) * 8192);
          }
        }
      });
    }
#pragma unroll
    for (int a = 0; a < 4; ++a)
#pragma unroll
      for (int b = 0; b < 4; ++b) acc[a][b] = (f32x4){0, 0, 0, 0};
    PH(3);
    bf16x8 fa[2][4], fb[2][4];
    const int kap = 4 * (g & 1) + q2, hf = p >> 1;
    const unsigned wb = 16384 + (4 * g + q2) * 256 + p * 8, hb = (g >> 1) * 4096 + wm * 2048 + hf * 1024 + (8 * (g & 1) + q2) * 16 + (p & 1) * 8;
    const unsigned cb3[5] = {wm * 4096u + lane * 16, wb + (((4 * wn + 0) ^ kap) * 32), wb + (((4 * wn + 1) ^ kap) * 32), wb + (((4 * wn + 2) ^ kap) * 32), wb + (((4 * wn + 3) ^ kap) * 32)};
    const unsigned cb4[4] = {hb + 64u * hf, hb + 64u * (1 ^ hf), 16384 + wn * 8192u + nb0, 16384 + wn * 8192u + nb1};
    unsigned ba[5];
#pragma unroll
    for (int b = 0; b < 5; ++b) { ba[b] = sl + cb3[b]; PIN(ba[b]); }
    auto rd3 = [&](int r) AI { const int ks = r / 12, xx = r % 12;
      if (xx == 0) fa[ks][0] = rd128(ba[0] + ks * 8192);
      else if (xx <= 8) { const int b = (xx - 1) >> 1, h = (xx - 1) & 1; sethalf(fb[ks][b], h, rtr(ba[1 + b] + ks * 8192 + h * 4096)); }
      else fa[ks][xx - 8] = rd128(ba[0] + ks * 8192 + (xx - 8) * 1024); };
    auto rd4 = [&](int r) AI { const int ks = r / 12, xx = r % 12;
      if (xx < 2) sethalf(fa[ks][0], xx, rtr(ba[xx] + ks * 8192));
      else if (xx < 6) fb[ks][xx - 2] = rd128(ba[2 + ks] + (xx - 2) * 2048);
      else { const int a = (xx - 4) >> 1, h = xx & 1; sethalf(fa[ks][a], h, rtr(ba[h] + ks * 8192 + a * 256)); } };
    auto mm = [&](int ks, int a, int b) AI { acc[a][b] = MFMA(fb[ks][b], fa[ks][a], acc[a][b]); };
    auto up3 = [&](int b) AI { ba[b] = nq(sl) + cb3[b]; PIN(ba[b]); };
    auto up4 = [&](int b) AI { ba[b] = nq(sl) + cb4[b]; PIN(ba[b]); };
    period<4, 4, true, 24, LASTR, 5>(rd3, mm, up3); waitl0(); waitv<8>(); bar(); sl = nq(sl);
    for (int s = 1; s < 27; ++s) { period<4, 4, false, 24, LASTR, 5>(rd3, mm, up3); waitl0(); waitv<0>(); bar(); sl = nq(sl); }
    period<4, 4, false, 24, LASTR, 4>(rd3, mm, up4); waitl0(); bar(); sl = nq(sl);
    PH(4);
    for (int s = 0; s < n4; ++s) { period<4, 4, false, 24, LASTR, 4>(rd4, mm, up4); waitl0(); bar(); sl = nq(sl); }
    drain<4, 4>(mm);
    PH(5);
    const unsigned po = (wq & 1) * 16384 + g * 256;
    if (half) {
      const unsigned pb = ((sl + 65536u + (wq >> 1) * 32768u) & 131071u) + po;
#pragma unroll
      for (int a = 0; a < 4; ++a)
#pragma unroll
        for (int b = 0; b < 4; ++b) *(LAS f32x4*)(size_t)(pb + (a * 4 + b) * 1024 + ((r15 ^ (4 * b + g)) * 16)) = acc[a][b];
      waitl0();
    } else {
      const unsigned pb = ((sl + (wq >> 1) * 32768u) & 131071u) + po;
#pragma unroll
      for (int a = 0; a < 4; ++a)
#pragma unroll
        for (int b = 0; b < 4; ++b) { const unsigned o = (a * 4 + b) * 1024 + ((r15 ^ (4 * b + g)) * 16);
          st128(rws, __builtin_bit_cast(u32x4, acc[a][b] + rdf4(pb + o)), g * 256 + o, PART_OFF + (ci * 8 + cnt) * 65536 + wq * 16384); }
      waitv<0>();
    }
    bar();
    fsl = sl;
  }
  if (half) {
    const int c32 = lane & 31, bg = c32 & 15;
    const unsigned o0 = (w >> 2) * 32768u, o1 = (c32 >> 4) * 16384 + ((w & 3) * 4 + (bg >> 2)) * 1024 + (bg & 3) * 256;
    const unsigned pa = ((fsl + o0) & 131071u) + o1, pc_ = ((fsl + 65536u + o0) & 131071u) + o1;
    float* op = (float*)karg(A_OUT) + (size_t)(ci * 128 + 16 * w + (lane >> 5)) * 1024 + cnt * 128 + c32 * 4;
#pragma unroll
    for (int u = 0; u < 8; ++u) { const unsigned ro = ((2 * u + (lane >> 5)) ^ bg) * 16; *(f32x4*)(op + u * 2048) = rdf4(pa + ro) + rdf4(pc_ + ro); }
  }
}

extern "C" void kernel_launch(void* const* d_in, const int* in_sizes, int n_in, void* d_out, int out_size, void* d_ws, size_t ws_size, hipStream_t stream) {
  (void)in_sizes; (void)n_in; (void)out_size; (void)ws_size;
  (void)hipFuncSetAttribute((const void*)fused_kernel, hipFuncAttributeMaxDynamicSharedMemorySize, LDS_BYTES);
  fused_kernel<<<256, 512, LDS_BYTES, stream>>>((const u16*)d_in[0], (const u16*)d_in[1], (const u16*)d_in[2], (const u16*)d_in[3], (const u16*)d_in[4],
      (const u16*)d_in[5], (const u16*)d_in[6], (const u16*)d_in[7], (const u16*)d_in[8], (const u16*)d_in[10], (const u16*)d_in[11], (const u16*)d_in[12],
      (const u16*)d_in[13], (const u16*)d_in[14], (char*)d_ws, (float*)d_out);
}
```

```cpp
#include <hip/hip_runtime.h>
#include <utility>

typedef unsigned short u16;
typedef __bf16 bf16x2 __attribute__((ext_vector_type(2)));
typedef __bf16 bf16x4 __attribute__((ext_vector_type(4)));
typedef __bf16 bf16x8 __attribute__((ext_vector_type(8)));
typedef float f32x2 __attribute__((ext_vector_type(2)));
typedef float f32x4 __attribute__((ext_vector_type(4)));
typedef float f32x16 __attribute__((ext_vector_type(16)));
typedef unsigned u32x2 __attribute__((ext_vector_type(2)));
typedef unsigned u32x4 __attribute__((ext_vector_type(4)));
typedef __amdgpu_buffer_rsrc_t rsrc_t;
#define LAS __attribute__((address_space(3)))
#define DI __device__ __forceinline__
#define AI __attribute__((always_inline))
#define SB() __builtin_amdgcn_sched_barrier(0)
#define PIN(x) asm volatile("" : "+v"(x))
#define MFMA(a, b, c) __builtin_amdgcn_mfma_f32_16x16x32_bf16(a, b, c, 0, 0, 0)
#define MFMA32(a, b, c) __builtin_amdgcn_mfma_f32_32x32x16_bf16(a, b, c, 0, 0, 0)
template <int V> using IC = std::integral_constant<int, V>;

constexpr unsigned HID_PANEL = 112 * 8192, H_OFF = 16 * HID_PANEL, H_PANEL = 32 * 16384;
constexpr unsigned PART_OFF = H_OFF + 16 * H_PANEL;
constexpr unsigned GRAN_OFF = PART_OFF + 128 * 32768;
constexpr unsigned RG_OFF = GRAN_OFF + 256 * 1024;
constexpr unsigned CG_OFF = RG_OFF + 16 * 8192;
constexpr unsigned FLAG_OFF = CG_OFF + 256 * 2048;
constexpr unsigned EPC_OFF = FLAG_OFF + 16 * 1024;
constexpr unsigned WS_BYTES = EPC_OFF + 16 * 128;
constexpr int FK_M1 = 0, FK_M2 = 1, FK_M3 = 2, FK_H = 3, FK_P = 4, FK_EP = 5;
constexpr unsigned TAB = 147456, T_ROW = TAB, T_XA = TAB + 2048, T_OUT = TAB + 4096, T_B1 = TAB + 5120, T_CS2 = TAB + 6144, T_WA = TAB + 8192, T_ZERO = TAB + 12288, T_AGG = T_WA;
constexpr unsigned LDS_BYTES = 163840;
#define XMAP 0
#define ROTM 3
#define MOVE 3
#define PROFX false
constexpr int LAG = 2, LASTR = 25;
constexpr unsigned SPIN_CAP = 1u << 16;

DI rsrc_t mk(const void* p, unsigned bytes) { return __builtin_amdgcn_make_buffer_rsrc((void*)p, 0, bytes, 0x00020000); }
DI void setm0(unsigned lds) { asm volatile("s_mov_b32 m0, %0\n\ts_nop 0" ::"s"(lds) : "memory"); }
template <int OFF> DI void dma(rsrc_t r, unsigned voff, unsigned soff) {
  asm volatile("buffer_load_dwordx4 %0, %1, %2 offen offset:%3 lds" ::"v"(voff), "s"(r), "s"(soff), "i"(OFF) : "memory");
}
template <int OFF> DI void dma1(rsrc_t r, unsigned voff, unsigned soff) {
  asm volatile("buffer_load_dwordx4 %0, %1, %2 offen offset:%3 sc1 lds" ::"v"(voff), "s"(r), "s"(soff), "i"(OFF) : "memory");
}
#define PINS(x) asm volatile("" : "+s"(x))
typedef const void* const __attribute__((address_space(4)))* kargs_t;
DI const char* karg(int i) { kargs_t ka = (kargs_t)__builtin_amdgcn_kernarg_segment_ptr(); PINS(ka); return (const char*)ka[i]; }
enum { A_X, A_LRE, A_LIM, A_BRE, A_BIM, A_CRE, A_CIM, A_LDT, A_DSK, A_W1, A_B1, A_W2, A_B2, A_GW, A_WS, A_OUT };
template <int N> DI void waitv() { asm volatile("s_waitcnt vmcnt(%0)" ::"i"(N) : "memory"); }
DI void waitl0() { SB(); __builtin_amdgcn_s_waitcnt(0xC07F); asm volatile("" ::: "memory"); }
#define PH(k)
DI void bar() { asm volatile("s_barrier" ::: "memory"); SB(); }
DI bf16x8 rd128(unsigned a) { return *(LAS bf16x8*)(size_t)a; }
DI u32x4 rdu4(unsigned a) { return *(LAS u32x4*)(size_t)a; }
DI f32x4 rdf4(unsigned a) { return *(LAS f32x4*)(size_t)a; }
DI f32x2 rdf2(unsigned a) { return *(LAS f32x2*)(size_t)a; }
DI float rdf(unsigned a) { return *(LAS float*)(size_t)a; }
DI void wrf(unsigned a, float v) { *(LAS float*)(size_t)a = v; }
DI void wrf2(unsigned a, f32x2 v) { *(LAS f32x2*)(size_t)a = v; }
DI bf16x4 rtr(unsigned a) { return __builtin_amdgcn_ds_read_tr16_b64_v4bf16((LAS bf16x4*)(size_t)a); }
DI void sethalf(bf16x8& f, int h, bf16x4 v) {
  if (h == 0) f = __builtin_shufflevector(v, v, 0, 1, 2, 3, -1, -1, -1, -1);
  else { bf16x8 t = __builtin_shufflevector(v, v, -1, -1, -1, -1, 0, 1, 2, 3); f = __builtin_shufflevector(f, t, 0, 1, 2, 3, 12, 13, 14, 15); }
}
DI unsigned f2u(float f) { return __builtin_bit_cast(unsigned, f); }
DI float u2f(unsigned u) { return __builtin_bit_cast(float, u); }
DI float shx(int lane, float v, int m) { return u2f(__builtin_amdgcn_ds_bpermute((lane ^ m) * 4, (int)f2u(v))); }
DI float sum16(float v) { const auto r = __builtin_amdgcn_permlane16_swap(f2u(v), f2u(v), false, false); return u2f(r[0]) + u2f(r[1]); }
DI float sum32(float v) { const auto r = __builtin_amdgcn_permlane32_swap(f2u(v), f2u(v), false, false); return u2f(r[0]) + u2f(r[1]); }
DI float fexp(float v) { return __builtin_amdgcn_exp2f(v * 1.4426950408889634f); }
DI float bf2f(unsigned v) { return u2f(v << 16); }
DI unsigned pk2(float lo, float hi) { f32x2 v = {lo, hi}; return __builtin_bit_cast(unsigned, __builtin_convertvector(v, bf16x2)); }
DI float rbf(float f) { return u2f(pk2(f, 0.f) << 16); }
DI float dot2(unsigned a, unsigned b, float c) { return __builtin_amdgcn_fdot2_f32_bf16(__builtin_bit_cast(bf16x2, a), __builtin_bit_cast(bf16x2, b), c, false); }
template <int C> DI float dppadd(float v) { return v + u2f(__builtin_amdgcn_update_dpp(0, (int)f2u(v), C, 0xf, 0xf, true)); }
DI float sel4(bool b0, bool b1, float a, float b, float c, float d) { PIN(a); PIN(b); PIN(c); PIN(d); const float lo = b0 ? b : a, hi = b0 ? d : c; return b1 ? hi : lo; }
DI f32x2 cmul(f32x2 a, f32x2 b) { return (f32x2){a.x * b.x - a.y * b.y, a.x * b.y + a.y * b.x}; }
DI f32x2 cfma(f32x2 a, f32x2 b, f32x2 c) { return (f32x2){__builtin_fmaf(a.x, b.x, __builtin_fmaf(-a.y, b.y, c.x)), __builtin_fmaf(a.x, b.y, __builtin_fmaf(a.y, b.x, c.y))}; }
DI void st128(rsrc_t r, u32x4 v, unsigned voff, unsigned soff) { __builtin_amdgcn_raw_buffer_store_b128(v, r, voff, soff, 16); }
DI void st64(rsrc_t r, u32x2 v, unsigned voff, unsigned soff) { __builtin_amdgcn_raw_buffer_store_b64(v, r, voff, soff, 16); }
DI void st32(rsrc_t r, unsigned v, unsigned voff, unsigned soff) { __builtin_amdgcn_raw_buffer_store_b32(v, r, voff, soff, 16); }
DI void stflag(rsrc_t r, unsigned v, int lane, unsigned soff) { if (lane == 0) st32(r, v, 0u, soff); }
DI void ld32a(unsigned& d, rsrc_t r, unsigned voff, unsigned soff) { asm volatile("buffer_load_dword %0, %1, %2, %3 offen sc1" : "=v"(d) : "v"(voff), "s"(r), "s"(soff) : "memory"); }
DI void ld16p(unsigned& d, const u16* p, unsigned voff) { asm volatile("global_load_ushort %0, %1, %2" : "=v"(d) : "v"(voff), "s"(p) : "memory"); }
DI void ld64p(u32x2& d, rsrc_t r, unsigned voff, unsigned soff) { asm volatile("buffer_load_dwordx2 %0, %1, %2, %3 offen" : "=v"(d) : "v"(voff), "s"(r), "s"(soff) : "memory"); }
DI void ld64a(u32x2& d, rsrc_t r, unsigned voff, unsigned soff) { asm volatile("buffer_load_dwordx2 %0, %1, %2, %3 offen sc1" : "=v"(d) : "v"(voff), "s"(r), "s"(soff) : "memory"); }
DI void ld128g(u32x4& d, const u16* p, unsigned voff) { asm volatile("global_load_dwordx4 %0, %1, %2" : "=v"(d) : "v"(voff), "s"(p) : "memory"); }
DI void pfl(unsigned& d, rsrc_t r, unsigned voff, unsigned soff) { asm volatile("buffer_load_dword %0, %1, %2, %3 offen" : "+v"(d) : "v"(voff), "s"(r), "s"(soff) : "memory"); }
template <class T> DI T sel(bool c, T a, T b) { return c ? a : b; }
DI bool anyl(bool b) { return __builtin_amdgcn_ballot_w64(b) != 0; }
DI void ld128p(u32x4& d, rsrc_t r, unsigned voff, unsigned soff) { asm volatile("buffer_load_dwordx4 %0, %1, %2, %3 offen" : "=v"(d) : "v"(voff), "s"(r), "s"(soff) : "memory"); }
DI void sincos_f(float t, float& sn, float& cs) {
  const float k = __builtin_rintf(t * 0.63661977236f);
  float r = __builtin_fmaf(k, -1.5707962513f, t); r = __builtin_fmaf(k, -7.5497894159e-08f, r); r = __builtin_fmaf(k, -5.3903029534e-15f, r);
  const float z = r * r;
  const float sp = __builtin_fmaf(r * z, __builtin_fmaf(z, __builtin_fmaf(z, __builtin_fmaf(z, 2.7183114939e-06f, -1.9839334836e-04f), 8.3333293858e-03f), -1.6666666642e-01f), r);
  const float cp = __builtin_fmaf(z, __builtin_fmaf(z, __builtin_fmaf(z, __builtin_fmaf(z, 2.4390448796e-05f, -1.3886763775e-03f), 4.1666623324e-02f), -4.9999999726e-01f), 1.f);
  const int q = (int)k;
  const float a = (q & 1) ? cp : sp, b = (q & 1) ? sp : cp;
  sn = (q & 2) ? -a : a; cs = ((q + 1) & 2) ? -b : b;
}
DI void ld128a(u32x4& d, rsrc_t r, unsigned voff, unsigned soff) { asm volatile("buffer_load_dwordx4 %0, %1, %2, %3 offen sc1" : "=v"(d) : "v"(voff), "s"(r), "s"(soff) : "memory"); }

DI unsigned x1off(int a) { return a == 0 ? 131072u : (unsigned)(81920 + a * 16384); }
DI unsigned nq(unsigned s) { return (s + 32768u) & 131071u; }

DI int kb_of(int j, int bl) { return 7 * j + ((j & 1) ? (bl == 6 ? 0 : bl + 1) : bl); }

template <int NR, int LAST> constexpr int nread(int sl) { int n = 0; for (int r = 0; r < NR; ++r) n += (r * LAST) / (NR - 1) <= sl; return n; }
template <int NM, int R0, bool FIRST, int NR, int LAST, int NB, int SL, class RD, class MM, class UP>
DI void slot(RD& rd, MM& mm, UP& upd) {
  constexpr int q = SL + LAG;
  if constexpr (!FIRST && (q == NM || q == NM + R0 || q == 2 * NM)) {
    constexpr int cnt = nread<NR, LAST>(SL - 1) - nread<NR, LAST>(SL - 6);
    __builtin_amdgcn_s_waitcnt(0xC07F | (cnt << 8)); SB();
  }
  if constexpr (!(FIRST && q < NM)) mm((q < NM) ? 1 : ((q < 2 * NM) ? 0 : 1), q % NM);
  SB();
  constexpr int r = (SL * (NR - 1) + LAST - 1) / LAST;
  if constexpr (r < NR && (r * LAST) / (NR - 1) == SL) rd(r);
  constexpr int per = NB > 2 * NM - 1 - LAST ? 2 : 1, u0 = (SL - LAST - 1) * per;
  if constexpr (SL > LAST && u0 < NB) { upd(u0); if constexpr (per == 2 && u0 + 1 < NB) upd(u0 + 1); }
  SB();
}
template <int NM, int R0, bool FIRST, int NR, int LAST, int NB, class RD, class MM, class UP, int... I>
DI void period_(RD& rd, MM& mm, UP& upd, std::integer_sequence<int, I...>) { (slot<NM, R0, FIRST, NR, LAST, NB, I>(rd, mm, upd), ...); }
template <int NM, int R0, bool FIRST, int NR, int LAST, int NB, class RD, class MM, class UP>
DI void period(RD& rd, MM& mm, UP& upd) {
  static_assert(NR - 1 <= LAST && NB <= 2 * (2 * NM - 1 - LAST), "");
  period_<NM, R0, FIRST, NR, LAST, NB>(rd, mm, upd, std::make_integer_sequence<int, 2 * NM>{});
}
template <bool FIRST, int SL, class RD, class MM, class UP> DI void slot32(RD& rd, MM& mm, UP& upd) {
  if constexpr (SL == 12) { __builtin_amdgcn_s_waitcnt(0xC07F | (8 << 8)); SB(); }
  if constexpr (!(FIRST && SL < 12)) mm((SL + 4) & 15);
  SB();
  if constexpr (SL < 2) { rd(2 * SL); rd(2 * SL + 1); } else if constexpr (SL < 14) rd(SL + 2);
  if constexpr ((SL & 3) == 2) { upd(SL >> 2); upd(4 + (SL >> 2)); }
  SB();
}
template <bool FIRST, class RD, class MM, class UP, int... I> DI void period32_(RD& rd, MM& mm, UP& upd, std::integer_sequence<int, I...>) { (slot32<FIRST, I>(rd, mm, upd), ...); }
template <bool FIRST, class RD, class MM, class UP> DI void period32(RD& rd, MM& mm, UP& upd) { period32_<FIRST>(rd, mm, upd, std::make_integer_sequence<int, 16>{}); }
template <class G, class W, int... U> DI void ival_(G& g, W& wk, std::integer_sequence<int, U...>) { ((g(IC<U>{}), SB(), wk(IC<U>{}), SB()), ...); }
template <class G, class W> DI void ival(G&& g, W&& wk) { ival_(g, wk, std::make_integer_sequence<int, 8>{}); }
template <int N, class F, int... I> DI void sfor_(F& f, std::integer_sequence<int, I...>) { (f(IC<I>{}), ...); }
template <int N, class F> DI void sfor(F&& f) { sfor_<N>(f, std::make_integer_sequence<int, N>{}); }

DI void check_flags(unsigned& pv, rsrc_t ws, unsigned voff, unsigned soff, int lane, int lo, int hi, unsigned ep) {
  PIN(pv);
  const bool need = lane >= lo && lane < hi;
  if (__builtin_expect(anyl(need && pv != ep), 0)) {
    for (unsigned sp = 0; sp < SPIN_CAP; ++sp) {
      __builtin_amdgcn_s_sleep(2);
      ld32a(pv, ws, voff, soff); asm volatile("s_waitcnt vmcnt(0)" : "+v"(pv)::"memory");
      if (!anyl(need && pv != ep)) break;
    }
  }
}

#if defined(__HIP_DEVICE_COMPILE__) && __HIP_DEVICE_COMPILE__
__attribute__((target("no-packed-fp32-ops")))
#endif
__global__ __launch_bounds__(512, 2) void fused_kernel(const u16* x, const u16* lre, const u16* lim, const u16* bre, const u16* bim, const u16* cre, const u16* cim,
    const u16* ldt, const u16* dsk, const u16* w1, const u16* b1, const u16* w2, const u16* b2, const u16* gw, char* ws, float* out) {
  extern __shared__ __attribute__((aligned(16))) char shm[];
  const int lane = threadIdx.x & 63, w = __builtin_amdgcn_readfirstlane(threadIdx.x >> 6);
  const int bid = blockIdx.x, xcd = bid & 7, loc = bid >> 3;
  const int ci = (xcd & 1) * 8 + (loc & 7), cj = (xcd >> 1) * 4 + (loc >> 3);
  const int rot = (ci + cj) & ROTM, cnt = cj & 7, half = cj >> 3, wq = w & 3, tq = threadIdx.x & 255;
  const int n4 = half ? 28 + MOVE : 28 - MOVE;
  const rsrc_t rws = mk(ws, WS_BYTES), rx = mk(x, 2048 * 2048);
  const int wm = (w >> 1) & 1, wn = w & 1, g = lane >> 4, r15 = lane & 15, q2 = (lane >> 2) & 3, p = lane & 3, hl = lane >> 5, r31 = lane & 31;
  const unsigned flagb = FLAG_OFF + ci * 1024, fvo = (lane & 15) * 4;
  unsigned rgb = RG_OFF + ci * 8192; PINS(rgb);
  auto nowork = [&](auto) AI {};
  unsigned va[4], vb[4], vw[7], sl, sa, sb, ta, tb, dumv = 0; int sx; rsrc_t ra, rb;
  auto vnat = [&](int u) AI { return (8u * u + (lane >> 3)) * 2048 + (((lane & 7) ^ ((4 * u + g) & 7)) * 16) - 1024 * u; };
  auto nexts = [&]() AI { sa = __builtin_amdgcn_readlane(ta, sx); sb = __builtin_amdgcn_readlane(tb, sx); ++sx; PINS(sa); PINS(sb); };
  auto G = [&](auto U) AI { constexpr int u = U.value;
    if constexpr (u == 0) setm0(sl + wq * 4096);
    if constexpr (u == 4) setm0(sl + 16384 + wq * 4096);
    if constexpr (u < 4) dma1<u * 1024>(ra, va[u], sa); else dma<(u - 4) * 1024>(rb, vb[u - 4], sb);
  };
  auto G2 = [&](auto S, auto U) AI { constexpr int a = S.value % 3, u = U.value;
    if constexpr (u == 0) setm0((a == 0 ? 131072u : 81920u + a * 16384u) + wq * 4096);
    if constexpr (u == 4) setm0(a * 32768 + wq * 7168);
    if constexpr (u == 8) setm0(a * 32768 + wq * 7168 + 4096);
    if constexpr (u < 4) dma<u * 1024>(rx, va[u], sa); else dma<(u & 3) * 1024>(rb, vw[u - 4], sb);
  };
  const int c7 = ci & 7;
  auto setup2 = [&]() AI {
    rb = mk((const char*)w1 - 4096, 1024 * 7168 + 4096);
#pragma unroll
    for (int q = 0; q < 4; ++q) va[q] = vnat(q);
#pragma unroll
    for (int q = 0; q < 6; ++q) { const int k = 8 * (q & 1) + (lane >> 3), f = ((k >> 1) & 1) | ((q & 1) << 1);
      vw[q] = 4096 + k * 7168 + ((cj & 1) ? 64 : 0) + (q >> 1) * 128 + (((((lane & 7) >> 1) ^ f) * 2) + (lane & 1)) * 16 - (q & 3) * 1024; }
    vw[6] = 4096 + (lane >> 2) * 7168 + ((cj & 1) ? 0 : 384) + (((((lane & 3) >> 1) ^ hl) * 2) + (lane & 1)) * 16 - 2048;
    int k = lane - 2 + (c7 < 2 ? 3 * c7 : c7 - 2); k = 2 + (k >= 6 ? k - 6 : k);
    k = lane < 2 ? lane : lane < 8 ? k : (lane ^ rot);
    k = (k ^ (ci & 8)) & 15;
    ta = (ci * 128 + 32 * wq) * 2048 + k * 128; tb = (k * 64 + 16 * wq) * 7168 + cj * 448;
  };
  auto setup3 = [&]() AI {
    ra = rws;
#pragma unroll
    for (int q = 0; q < 4; ++q) { va[q] = lane * 16; vb[q] = vnat(q); }
  };
  auto table3 = [&](unsigned& ta, unsigned& tb) AI {
    const int ks = 16 * half + (lane ^ rot);
    int n = 2 * (lane - 16) + (wq >> 1), hp = half; if (n >= 56) { n -= 2 * MOVE; hp = 0; }
    const int kb = kb_of(hp * 8 + ((n ^ rot) & 7), n >> 3);
    ta = lane < 16 ? H_OFF + ci * H_PANEL + ks * 16384 + wq * 4096 : ci * HID_PANEL + kb * 8192 + (wq & 1) * 4096;
    tb = lane < 16 ? (cnt * 128 + 32 * wq) * 2048 + (ks >> 1) * 128 + (ks & 1) : (kb * 32 + 16 * (wq & 1)) * 2048 + cnt * 256;
    PIN(ta); PIN(tb);
  };
  auto next3 = [&]() AI { nexts(); rb = mk(sel<const u16*>(sb & 1, cim, cre), 1024 * 2048); sb &= ~1u; };

  if (w < 4) {
    constexpr bool isx = PROFX; (void)isx;
    unsigned epr = __builtin_amdgcn_raw_buffer_load_b32(rws, 0, flagb + FK_EP * 128, 16), epc = __builtin_amdgcn_raw_buffer_load_b32(rws, 0, EPC_OFF + cj * 128, 16);
    const int seg0 = 4 * wm + hl;
    f32x2 lam, cof, l16, l128, l1k, pw0, pw1; unsigned t3a, t3b;
    {
      const int pch = 64 * cj + 32 * wn + r31, tc = tq < 224 ? tq : 223;
      const unsigned pdt = ldt[pch], plr = lre[pch], pli = lim[pch];
      const unsigned bq = b1[224 * cj + (tc < 192 ? ((cj & 1) ? 32 : 0) + tc : ((cj & 1) ? -192 : 0) + tc)];
      const unsigned oq = (wq < 2 ? b2 : dsk)[cnt * 128 + (tq & 127)];
      asm volatile("" ::: "memory"); SB();
      setup2(); sx = 2; nexts(); table3(t3a, t3b);
#pragma unroll
      for (int q = 0; q < 7; ++q) { PIN(vw[q]); PIN(va[q & 3]); }
      const float lr = bf2f(plr), li = bf2f(pli), dt = rbf(fexp(bf2f(pdt))), e = fexp(dt * lr); float sn, cs_; sincos_f(dt * li, sn, cs_);
      lam = (f32x2){e * cs_, e * sn};
      const float nr = lam.x - 1.f, ni = lam.y, den = 2.f / (lr * lr + li * li);
      cof = (f32x2){(nr * lr + ni * li) * den, (ni * lr - nr * li) * den};
      l16 = cmul(lam, lam); l16 = cmul(l16, l16); l16 = cmul(l16, l16); l16 = cmul(l16, l16);
      const f32x2 l32 = cmul(l16, l16), l64 = cmul(l32, l32); l128 = cmul(l64, l64);
      pw0 = hl ? l16 : (f32x2){1.f, 0.f}; if (wm) pw0 = cmul(pw0, l64);
      pw1 = cmul(pw0, l32);
      l1k = cmul(l128, l128); l1k = cmul(l1k, l1k); l1k = cmul(l1k, l1k);
      PIN(lam); PIN(cof); PIN(l16); PIN(l128); PIN(pw0); PIN(pw1); PIN(l1k);
      wrf(T_B1 + tq * 4, bf2f(bq)); wrf(T_OUT + tq * 4, bf2f(oq)); wrf(T_ZERO + (lane & 3) * 4, 0.f);
      epr = __builtin_amdgcn_readfirstlane(epr) + 1u; epc = __builtin_amdgcn_readfirstlane(epc) + 1u;
    }
    {
      f32x16 acc[2][2];
#pragma unroll
      for (int a = 0; a < 4; ++a)
#pragma unroll
        for (int e = 0; e < 16; ++e) acc[a >> 1][a & 1][e] = 0.f;
      u32x2 wv[4][4];
      const int sstar = c7 < 2 ? c7 : 2;
      const int fsw = ((q2 >> 1) & 1) | ((g & 1) << 1);
      const unsigned cf = (g >> 1) * 7168 + (8 * (g & 1) + q2) * 128 + ((wq ^ fsw) * 32) + p * 8, ch = (g >> 1) * 7168 + 6144 + (8 * (g & 1) + q2) * 64 + (((wq & 1) ^ (g & 1)) * 32) + p * 8;
      auto wread = [&](int st) AI {
        if (sstar == st) {
#pragma unroll
          for (int e = 0; e < 4; ++e)
#pragma unroll
            for (int t = 0; t < 4; ++t) wv[t][e] = __builtin_bit_cast(u32x2, rtr(st * 32768 + (e >> 1) * 14336 + (t < 3 ? cf + t * 2048 + (e & 1) * 512 : ch + (e & 1) * 256)));
        } };
      {
        bf16x8 fa[4][2], fb[4][2];
        unsigned cb[8], ba[8], rs = 98304;
#pragma unroll
        for (int b = 0; b < 8; ++b) { cb[b] = (b < 4 ? wm * 8192u : 16384 + wn * 8192u) + r31 * 128 + (((2 * (b & 3) + hl) ^ ((r31 >> 1) & 7)) * 16); ba[b] = rs + cb[b]; PIN(ba[b]); }
        auto rd = [&](int r) AI { const int k16 = r >> 2, xx = r & 3;
          if (xx == 0) fa[k16][0] = rd128(ba[k16]); else if (xx == 3) fa[k16][1] = rd128(ba[k16] + 4096); else fb[k16][xx - 1] = rd128(ba[4 + k16] + (xx - 1) * 4096); };
        auto mm = [&](int m) AI { const int k16 = m >> 2, mt = (m >> 1) & 1, nt = m & 1; acc[mt][nt] = MFMA32(fa[k16][mt], fb[k16][nt], acc[mt][nt]); };
        auto up = [&](int b) AI { if (b == 0) rs = nq(rs); ba[b] = rs + cb[b]; PIN(ba[b]); };
        waitl0(); bar();
        period32<true>(rd, mm, up); waitl0(); bar();
        for (int s = 1; s < 16; ++s) { period32<false>(rd, mm, up); waitl0(); bar(); }
        wread(0);
        sfor<7>([&](auto U) AI { G2(IC<2>{}, IC<U.value + 4>{}); SB(); if constexpr (U.value < 6) { mm(4 + 2 * U.value); mm(5 + 2 * U.value); } SB(); });
        nexts();
      }
      const unsigned ca = (32 * wn + r31) * 8;
      f32x2 st[2] = {{0, 0}, {0, 0}}, Pf[2] = {{0, 0}, {0, 0}}, Gt = {0, 0}, cr = {0, 0}, ag[8];
      u32x4 lb[8], cg[8]; unsigned lbo[8], ob[2][2][4], pv, gd = 0;
      f32x4 g2[2][2]; float o[2][2][2], cs[4] = {0, 0, 0, 0}, c0s = 0.f, c1s = 0.f;
      const rsrc_t rgr = mk(ws + GRAN_OFF + cj * 16384, 16384);
      const int tc = tq < 224 ? tq : 223;
      const unsigned cgo = (tc >= 112 ? 16384 - 1792 : 0) + tc * 16;
      auto bad = [&]() AI { unsigned d = 0;
#pragma unroll
        for (int q = 0; q < 8; ++q) { PIN(lb[q]); d |= (int)lbo[q] < 0 ? 0u : ((lb[q][1] ^ epc) | (lb[q][3] ^ epc)); }
        return d != 0; };
      auto gbad = [&]() AI { unsigned d = 0;
#pragma unroll
        for (int q = 0; q < 8; ++q) { PIN(cg[q]); d |= (cg[q][1] ^ epc) | (cg[q][3] ^ epc); }
        return d != 0; };
      auto E2 = [](int s) constexpr { return (s == 2 || s == 6) ? 1 : s == 4 ? 8 : (s == 10 || s == 11) ? 4 : 0; };
      auto T2 = [](int s) constexpr { return (s == 8 || s == 9) ? 4 : (s >= 0 && s < 13 && s != 4 && s != 10 && s != 11) ? 1 : 0; };
      sfor<15>([&](auto S) AI { constexpr int s = S.value;
        if constexpr (s < 3 || s == 14) waitl0();
        if constexpr (s == 11) waitv<11 + E2(10)>();
        else if constexpr (s >= 1) waitv<T2(s - 2) + (s < 14 ? 11 : 8) + E2(s - 1) + T2(s - 1)>();
        if constexpr (s == 13) { if (w == 0) { waitv<6>(); check_flags(pv, rws, fvo, flagb + FK_H * 128, lane, 8 * half, 8 * half + 8, epr); } }
        bar();
        if constexpr (s < 2) wread(s + 1);
        if constexpr (s == 2) {
#pragma unroll
          for (int sg = 0; sg < 8; ++sg) ag[sg] = rdf2(T_AGG + sg * 512 + ca);
        }
        if constexpr (s == 7 || s == 8) {
#pragma unroll
          for (int q = 0; q < 2; ++q) { if (s == 7) g2[q][0] = rdf4(T_ROW + 1536 + (64 * wm + 32 * q + 16 * hl) * 4); g2[q][s == 7 ? 1 : 0] = rdf4(T_ROW + 1536 + (64 * wm + 32 * q + 16 * hl + 4 * (s - 6)) * 4); }
        }
        if constexpr (s == 9) {
#pragma unroll
          for (int q = 0; q < 2; ++q) g2[q][1] = rdf4(T_ROW + 1536 + (64 * wm + 32 * q + 16 * hl + 12) * 4);
        }
        if constexpr (s == 11) { if (w == 0) stflag(rws, epr, lane, flagb + FK_H * 128 + cj * 4); }
        SB();
        auto work = [&](auto U) AI { constexpr int u = U.value;
          if constexpr (s < 2 && u >= 1 && u < 9) {
            constexpr int t = 8 * s + u - 1;
#pragma unroll
            for (int mt = 0; mt < 2; ++mt) { st[mt] = cfma(lam, st[mt], (f32x2){acc[mt][0][t], acc[mt][1][t]}); PIN(st[mt]); }
          }
          if constexpr (s == 1 && u == 9) { wrf2(T_AGG + seg0 * 512 + ca, st[0]); wrf2(T_AGG + (seg0 + 2) * 512 + ca, st[1]); }
          if constexpr (s == 2 && u >= 1 && u < 9) {
            constexpr int sg = u - 1, mt = (sg >> 1) & 1; const bool me = sg == seg0 + 2 * mt;
            Pf[mt].x = sel<float>(me, Gt.x, Pf[mt].x); Pf[mt].y = sel<float>(me, Gt.y, Pf[mt].y); Gt = cfma(l16, Gt, ag[sg]);
            PIN(Gt); PIN(Pf[mt]);
          }
          if constexpr (s == 2 && u == 9) st64(rgr, (u32x2){f2u(sel<float>(hl, Gt.y, Gt.x)), epc}, ca * 2 + hl * 8, ci * 1024);
          if constexpr ((s == 3 && u < 10) || (s == 4 && u < 6)) {
            constexpr int t = 10 * (s - 3) + u;
#pragma unroll
            for (int mt = 0; mt < 2; ++mt) { f32x2 v = cmul(cof, (f32x2){acc[mt][0][t], acc[mt][1][t]}); PIN(v); acc[mt][0][t] = v.x; acc[mt][1][t] = v.y; }
          }
          if constexpr (s == 4 && u >= 6 && u < 10) {
#pragma unroll
            for (int q = 2 * u - 12; q < 2 * u - 10; ++q) { lbo[q] = (ci - 8 - 8 * hl + q) * 1024 + ca * 2; ld128a(lb[q], rgr, lbo[q], 0u); }
          }
          if constexpr (s == 5 || (s == 6 && u < 5)) {
            constexpr int id = 11 * (s - 5) + u, t = id >> 2, e = id & 3;
            cs[t] += (bf2f(wv[t][e][0] & 0xffffu) + u2f(wv[t][e][0] & 0xffff0000u)) + (bf2f(wv[t][e][1] & 0xffffu) + u2f(wv[t][e][1] & 0xffff0000u));
            PIN(cs[t]);
          }
          if constexpr (s == 6 && (u == 5 || u == 6)) {
#pragma unroll
            for (int t = 0; t < 4; ++t) { cs[t] = u == 6 ? sum32(cs[t]) : sum16(cs[t]); PIN(cs[t]); }
          }
          if constexpr (s == 6 && u == 7)
            st64(rws, (u32x2){f2u(sel4(g & 1, g & 2, cs[0], cs[1], cs[2], cs[3])), epc}, (g < 3 ? g * 64 + wq * 16 + r15 : 192 + (wq & 1) * 16 + r15) * 8u, CG_OFF + (cj * 16 + ci) * 2048);
          if constexpr (s == 6 && u == 9) {
            if (__builtin_expect(anyl(bad()), 0)) {
              for (unsigned sp = 0; sp < SPIN_CAP; ++sp) {
                __builtin_amdgcn_s_sleep(2);
#pragma unroll
                for (int q = 0; q < 8; ++q) ld128a(lb[q], rgr, lbo[q], 0u);
                waitv<0>();
                if (!anyl(bad())) break;
              }
            }
          }
          if constexpr (s == 7 && u < 4) {
#pragma unroll
            for (int q = 2 * u; q < 2 * u + 2; ++q) cr = cfma(l128, cr, (f32x2){u2f(lb[q][0]), u2f(lb[q][2])});
            PIN(cr);
          }
          if constexpr (s == 7 && u == 4) {
            const auto sx_ = __builtin_amdgcn_permlane32_swap(f2u(cr.x), f2u(cr.x), false, false), sy_ = __builtin_amdgcn_permlane32_swap(f2u(cr.y), f2u(cr.y), false, false);
            cr = cfma(l1k, (f32x2){u2f(sx_[1]), u2f(sy_[1])}, (f32x2){u2f(sx_[0]), u2f(sy_[0])});
            PIN(cr);
          }
          if constexpr (s == 7 && (u == 5 || u == 6)) { constexpr int mt = u - 5; st[mt] = cmul(cof, cfma(sel<f32x2>(mt, pw1, pw0), cr, Pf[mt])); PIN(st[mt]); }
          if constexpr ((s == 7 && u >= 7) || (s == 8 && u >= 1 && u < 9 && (u & 1)) || (s == 9 && u >= 1 && u < 9)) {
            constexpr int t = s == 7 ? u - 7 : s == 8 ? 4 + (u >> 1) : 7 + u, e = t & 3;
#pragma unroll
            for (int mt = 0; mt < 2; ++mt) {
              st[mt] = cfma(lam, st[mt], (f32x2){acc[mt][0][t], acc[mt][1][t]});
              const float gg = g2[mt][(t >> 2) & 1][e];
              o[mt][0][e & 1] = gg * st[mt].x; o[mt][1][e & 1] = -gg * st[mt].y;
              PIN(st[mt]); PIN(o[mt][0][e & 1]); PIN(o[mt][1][e & 1]);
              if constexpr (e & 1) { ob[mt][0][(t >> 1) & 3] = pk2(o[mt][0][0], o[mt][0][1]); ob[mt][1][(t >> 1) & 3] = pk2(o[mt][1][0], o[mt][1][1]); PIN(ob[mt][0][(t >> 1) & 3]); PIN(ob[mt][1][(t >> 1) & 3]); }
            }
          }
          if constexpr ((s == 8 || s == 9) && u == 10) {
            constexpr int hf = s - 8;
#pragma unroll
            for (int q = 0; q < 4; ++q) { const int mt = q >> 1, pt = q & 1;
              st128(rws, (u32x4){ob[mt][pt][0], ob[mt][pt][1], ob[mt][pt][2], ob[mt][pt][3]},
                    (2 * wn + (r31 >> 4)) * 4096 + wm * 2048 + hf * 1024 + (2 * mt + hl) * 256 + ((r15 ^ (hf << 2)) * 16), H_OFF + ci * H_PANEL + (2 * cj + pt) * 16384); }
          }
          if constexpr (s == 8 && u == 0) { if (ci == 15 && w == 0) stflag(rws, epc, lane, EPC_OFF + cj * 128); }
          if constexpr ((s == 10 || s == 11) && (u & 1) && u < 8) { constexpr int q = 4 * (s - 10) + (u >> 1); ld128a(cg[q], rws, cgo, CG_OFF + (cj * 16 + q) * 2048); }
          if constexpr (s == 12 && u == 5) { if (w == 0) ld32a(pv, rws, fvo, flagb + FK_H * 128); }
          if constexpr (s < 13 && u == 10 && T2(s) == 1) {
            constexpr int q = s < 4 ? s : s < 8 ? s - 1 : 7;
            if constexpr (q < 2) pfl(dumv, mk(sel<const u16*>(q, cim, cre), 1024 * 2048), lane < 16 ? lane * 128u : 0x80000000u, (bid * 64 + wq * 16) * 128);
            else pfl(dumv, mk(w2, 3584 * 2048), lane < (q < 7 ? 10 : 6) ? lane * 128u : 0x80000000u, (bid * 224 + wq * 56 + (q - 2) * 10) * 128);
          }
          if constexpr (s == 13 && u < 4) {
#pragma unroll
            for (int q = 2 * u; q < 2 * u + 2; ++q) { PIN(cg[q]); gd |= (cg[q][1] ^ epc) | (cg[q][3] ^ epc); c0s += u2f(cg[q][0]); c1s += u2f(cg[q][2]); }
            PIN(gd); PIN(c0s); PIN(c1s);
          }
          if constexpr (s == 13 && u == 4) {
            if (__builtin_expect(anyl(gd != 0), 0)) {
              for (unsigned sp = 0; sp < SPIN_CAP; ++sp) { __builtin_amdgcn_s_sleep(2);
#pragma unroll
                for (int q = 0; q < 8; ++q) ld128a(cg[q], rws, cgo, CG_OFF + (cj * 16 + q) * 2048);
                waitv<0>(); if (!anyl(gbad())) break; }
              c0s = 0.f; c1s = 0.f;
#pragma unroll
              for (int q = 0; q < 8; ++q) { c0s += u2f(cg[q][0]); c1s += u2f(cg[q][2]); }
            }
            wrf2(T_CS2 + tc * 8, (f32x2){c0s, c1s});
          }
          if constexpr (s == 14 && u == 1) {
            const float v = rdf(T_CS2 + tc * 4) + rdf(T_CS2 + 896 + tc * 4), hi = rbf(v), bb = rdf(T_B1 + tc * 4);
            *(LAS u32x4*)(size_t)(T_WA + tc * 16) = (u32x4){pk2(hi, v - hi), pk2(hi, bb), pk2(bb, 0.f), 0u};
          }
        };
        if constexpr (s < 13) { sfor<11>([&](auto U) AI { G2(IC<s + 3>{}, U); SB(); work(U); SB(); }); if constexpr (s < 12) nexts(); }
        if constexpr (s == 12) { ra = rws;
#pragma unroll
          for (int q = 0; q < 4; ++q) { vb[q] = va[q]; va[q] = lane * 16; }
          ta = t3a; tb = t3b; sl = 32768; sx = 0; next3(); }
        if constexpr (s >= 13) { ival(G, work); sl = nq(sl); next3(); asm volatile("s_nop 4"); }
        if constexpr (s == 14) ival(G, nowork);
      });
      asm volatile("" ::"v"(dumv));
      waitl0(); waitv<16>(); bar();
    }
    f32x4 acc[4][4];
#pragma unroll
    for (int a = 0; a < 4; ++a)
#pragma unroll
      for (int b = 0; b < 4; ++b) acc[a][b] = (f32x4){0, 0, 0, 0};
    bf16x8 fa[2][4], fb[2][4];
    const int kap = 4 * (g & 1) + q2, hf = p >> 1;
    const unsigned wb = 16384 + (4 * g + q2) * 256 + p * 8, hb = (g >> 1) * 4096 + wm * 2048 + hf * 1024 + (8 * (g & 1) + q2) * 16 + (p & 1) * 8;
    const unsigned cb3[4] = {hb + 64u * hf, hb + 64u * (1 ^ hf), 16384 + wn * 8192u + r15 * 128 + ((g ^ (r15 >> 1)) * 16), 16384 + wn * 8192u + r15 * 128 + (((4 + g) ^ (r15 >> 1)) * 16)};
    const unsigned cb4[5] = {wm * 4096u + lane * 16, wb + (((4 * wn + 0) ^ kap) * 32), wb + (((4 * wn + 1) ^ kap) * 32), wb + (((4 * wn + 2) ^ kap) * 32), wb + (((4 * wn + 3) ^ kap) * 32)};
    unsigned ba[5];
    sl = 32768;
#pragma unroll
    for (int b = 0; b < 4; ++b) { ba[b] = sl + cb3[b]; PIN(ba[b]); }
    auto rd3 = [&](int r) AI { const int ks = r / 12, xx = r % 12;
      if (xx < 2) sethalf(fa[ks][0], xx, rtr(ba[xx] + ks * 8192));
      else if (xx < 6) fb[ks][xx - 2] = rd128(ba[2 + ks] + (xx - 2) * 2048);
      else { const int a = (xx - 4) >> 1, h = xx & 1; sethalf(fa[ks][a], h, rtr(ba[h] + ks * 8192 + a * 256)); } };
    auto rd4 = [&](int r) AI { const int ks = r / 12, xx = r % 12;
      if (xx == 0) fa[ks][0] = rd128(ba[0] + ks * 8192);
      else if (xx <= 8) { const int b = (xx - 1) >> 1, h = (xx - 1) & 1; sethalf(fb[ks][b], h, rtr(ba[1 + b] + ks * 8192 + h * 4096)); }
      else fa[ks][xx - 8] = rd128(ba[0] + ks * 8192 + (xx - 8) * 1024); };
    auto mm = [&](int ks, int m) AI { acc[m >> 2][m & 3] = MFMA(fb[ks][m & 3], fa[ks][m >> 2], acc[m >> 2][m & 3]); };
    auto up3 = [&](int b) AI { if (b == 0) sl = nq(sl); ba[b] = sl + cb3[b]; PIN(ba[b]); };
    auto up4 = [&](int b) AI { if (b == 0) sl = nq(sl); ba[b] = sl + cb4[b]; PIN(ba[b]); };
    period<16, 4, true, 24, LASTR, 4>(rd3, mm, up3); waitl0(); waitv<8>(); bar();
    for (int s = 1; s < 15; ++s) { period<16, 4, false, 24, LASTR, 4>(rd3, mm, up3); waitl0(); waitv<0>(); bar(); }
    period<16, 4, false, 24, LASTR, 5>(rd3, mm, up4); waitl0(); bar();
    for (int s = 0; s < n4; ++s) { period<16, 4, false, 24, LASTR, 5>(rd4, mm, up4); waitl0(); bar(); }
#pragma unroll
    for (int m = LAG; m < 16; ++m) mm(1, m);
    if (half) {
      const unsigned pb = ((sl + 65536u + wm * 32768u) & 131071u) + wn * 16384 + g * 256;
#pragma unroll
      for (int a = 0; a < 4; ++a)
#pragma unroll
        for (int b = 0; b < 4; ++b) *(LAS f32x4*)(size_t)(pb + (a * 4 + b) * 1024 + ((r15 ^ (4 * b + g)) * 16)) = acc[a][b];
      waitl0();
    } else {
#pragma unroll
      for (int a = 0; a < 4; ++a)
#pragma unroll
        for (int bp = 0; bp < 2; ++bp)
          st128(rws, (u32x4){pk2(acc[a][2 * bp][0], acc[a][2 * bp][1]), pk2(acc[a][2 * bp][2], acc[a][2 * bp][3]), pk2(acc[a][2 * bp + 1][0], acc[a][2 * bp + 1][1]), pk2(acc[a][2 * bp + 1][2], acc[a][2 * bp + 1][3])},
                (a * 2 + bp) * 1024 + g * 256 + ((r15 ^ (8 * wn + 4 * bp + g)) * 16), PART_OFF + (ci * 8 + cnt) * 32768 + wq * 8192);
      waitv<0>();
    }
    bar();
    if (!half && w == 0) stflag(rws, epr, lane, flagb + FK_P * 128 + cnt * 4);
  } else {
    constexpr bool isx = !PROFX; (void)isx;
    unsigned epr, pv;
    {
      u32x4 xr[2][2], gq[2], rgv[2];
      ld32a(epr, rws, 0u, flagb + FK_EP * 128);
#pragma unroll
      for (int q = 0; q < 4; ++q) ld128p(xr[q >> 1][q & 1], rx, lane * 16u, (ci * 128 + 8 * cj + 2 * wq + (q >> 1)) * 2048 + (q & 1) * 1024);
      ld128g(gq[0], gw, lane * 16u); ld128g(gq[1], gw + 512, lane * 16u);
      ra = rx; rb = mk(sel<const u16*>(wq & 1, bim, bre), 1024 * 2048);
#pragma unroll
      for (int q = 0; q < 4; ++q) { va[q] = (16 * hl + 4 * q + ((lane >> 3) & 3)) * 2048 + (((lane & 7) ^ ((4 * q + g) & 7)) * 16) - 1024 * q; vb[q] = vnat(q); }
      auto src1 = [&](int s) AI { const int k = s ^ (s < 2 ? 0 : s < 4 ? (rot & 1) : rot); sa = (ci * 128 + 32 * wq) * 2048 + k * 128; sb = (cj * 64 + 32 * (wq >> 1)) * 2048 + k * 128; sl = ((s + 3) & 3) * 32768; };
      src1(0); ival(G, nowork); src1(1); ival(G, nowork);
      waitv<16>(); PIN(epr); PIN(gq[0]); PIN(gq[1]);
      epr = __builtin_amdgcn_readfirstlane(epr) + 1u;
      {
        float sv[6];
#pragma unroll
        for (int r = 0; r < 2; ++r) {
          PIN(xr[r][0]); PIN(xr[r][1]);
          float a1 = 0.f, a2 = 0.f, a3 = 0.f;
#pragma unroll
          for (int e = 0; e < 8; ++e) { const unsigned v = xr[r][e >> 2][e & 3]; a1 = dot2(v, 0x3F803F80u, a1); a2 = dot2(v, v, a2); a3 = dot2(v, gq[e >> 2][e & 3], a3); }
          sv[r] = a1; sv[2 + r] = a2; sv[4 + r] = a3;
        }
#pragma unroll
        for (int q = 0; q < 6; ++q) PIN(sv[q]);
        waitv<8>(); bar();
        PH(1);
        src1(2); ival(G, [&](auto U) AI { constexpr int u = U.value;
          if constexpr (u < 6) { sv[u] = dppadd<0x140>(dppadd<0x141>(dppadd<0x4E>(dppadd<0xB1>(sv[u])))); PIN(sv[u]); } });
        src1(3); ival(G, nowork);
        const bool r1 = lane & 4;
        float a1 = sel<float>(r1, sv[1], sv[0]), a2 = sel<float>(r1, sv[3], sv[2]), a3 = sel<float>(r1, sv[5], sv[4]);
        waitv<16>(); bar();
        float mean, sd, rstd, qv, gg;
        src1(4); ival(G, [&](auto U) AI { constexpr int u = U.value;
          if constexpr (u == 0) { a1 = sum32(sum16(a1)); PIN(a1); }
          if constexpr (u == 1) { a2 = sum32(sum16(a2)); PIN(a2); }
          if constexpr (u == 2) { a3 = sum32(sum16(a3)); PIN(a3); }
          if constexpr (u == 3) { mean = a1 * (1.f / 1024.f); const float var = __builtin_fmaxf(a2 * (1.f / 1024.f) - mean * mean, 0.f) + 1e-5f; rstd = __builtin_amdgcn_rsqf(var); sd = var * rstd;
                                  PIN(mean); PIN(rstd); PIN(sd); }
          if constexpr (u == 4) { qv = 1.f + fexp(__builtin_fminf(a3, 60.f)); gg = __builtin_amdgcn_rcpf(1.f + fexp(__builtin_fminf(-a3, 80.f))); PIN(qv); PIN(gg); }
          if constexpr (u == 5) { a1 = sel4(lane & 1, lane & 2, -2.302208198f * rstd, -0.1029432397f * rstd * rstd * rstd, qv * sd, gg); PIN(a1); }
          if constexpr (u == 6) { const float mh = rbf(mean), sh = rbf(sd);
                                  a2 = sel4(lane & 1, lane & 2, u2f(pk2(-mh, -mh)), u2f(pk2(mh - mean, sh)), u2f(pk2(sd - sh, 0.f)), 0.f); PIN(a2); }
          if constexpr (u == 7) { if (lane < 16) st64(rws, (u32x2){f2u(sel<float>(lane & 8, a2, a1)), epr}, (lane >> 3) * 4096u + (lane & 7) * 8, rgb + (8 * cj + 2 * wq) * 32); }
        });
      }
      auto T1 = [](int s) constexpr { return (s >= 0 && s < 8) ? 1 : s == 8 ? 2 : 0; };
      const rsrc_t rw1 = mk(w1, 1024 * 7168);
      sfor<11>([&](auto S1) AI { constexpr int s = S1.value + 1;
        waitv<16 + T1(s - 3) + T1(s - 2) + T1(s - 1)>(); bar();
        src1(s + 4); ival(G, nowork);
        if constexpr (s < 8) pfl(dumv, rw1, lane < 8 ? lane * 128u : 0x80000000u, (bid * 224 + wq * 56 + (s - 1) * 8) * 128);
        if constexpr (s == 8) { ld128a(rgv[0], rws, tq * 16u, rgb); ld128a(rgv[1], rws, tq * 16u + 4096, rgb); }
      });
      setup2(); sx = 0; nexts();
      const unsigned sb0 = sb;
      waitv<16>(); bar();
      sfor<4>([&](auto U) AI { G2(IC<0>{}, U); }); nexts();
      const unsigned sb1 = sb;
      {
        auto rbad = [&]() AI { PIN(rgv[0]); PIN(rgv[1]); return ((rgv[0][1] ^ epr) | (rgv[0][3] ^ epr) | (rgv[1][1] ^ epr) | (rgv[1][3] ^ epr)) != 0; };
        if (__builtin_expect(anyl(rbad()), 0))
          for (unsigned sp = 0; sp < SPIN_CAP; ++sp) { __builtin_amdgcn_s_sleep(2);
            ld128a(rgv[0], rws, tq * 16u, rgb); ld128a(rgv[1], rws, tq * 16u + 4096, rgb);
            waitv<0>(); if (!anyl(rbad())) break; }
      }
      sfor<4>([&](auto U) AI { G2(IC<1>{}, U); }); nexts();
      wrf(T_ROW + (tq & 1) * 1024 + (tq >> 1) * 4, u2f(rgv[0][0])); wrf(T_ROW + (tq & 1) * 1024 + 512 + (tq >> 1) * 4, u2f(rgv[0][2]));
      wrf2(T_XA + tq * 8, (f32x2){u2f(rgv[1][0]), u2f(rgv[1][2])});
      waitv<16>(); bar();
      sb = sb0; sfor<7>([&](auto U) AI { G2(IC<0>{}, IC<U.value + 4>{}); });
      sfor<4>([&](auto U) AI { G2(IC<2>{}, U); });
      waitv<19>(); bar();
      sb = sb1; sfor<7>([&](auto U) AI { G2(IC<1>{}, IC<U.value + 4>{}); });
      table3(ta, tb); sx = 3; next3();
    }
    {
      f32x4 ac1[28];
#pragma unroll
      for (int m = 0; m < 28; ++m) ac1[m] = (f32x4){0, 0, 0, 0};
      {
        bf16x8 fa[2][4], fb[2][8];
        const int f = ((q2 >> 1) & 1) | ((g & 1) << 1);
        const unsigned nb = wm * 8192u + r15 * 128, n0 = (g ^ (r15 >> 1)) * 16, n1 = ((4 + g) ^ (r15 >> 1)) * 16, wk = (g >> 1) * 7168u + p * 8;
        const unsigned cb[8] = {nb + wn * 4096 + n0, nb + wn * 4096 + n1, nb + (1 - wn) * 4096 + n0, nb + (1 - wn) * 4096 + n1,
                                wk + (8 * (g & 1) + q2) * 128 + (((2 * wn) ^ f) * 32), wk + (8 * (g & 1) + q2) * 128 + (((2 * wn + 1) ^ f) * 32),
                                wk + 6144 + (8 * (g & 1) + q2) * 64 + (g & 1) * 32, wk + 6144 + (8 * (g & 1) + q2) * 64 + ((g & 1) ^ 1) * 32};
        unsigned ba[8]; unsigned xn = 98304, wnx = 32768;
#pragma unroll
        for (int b = 0; b < 8; ++b) { ba[b] = cb[b] + (b < 4 ? 131072u : 0u); PIN(ba[b]); }
        auto rd = [&](int r) AI { const int ks = r / 20, xx = r % 20;
          if (xx == 0) fa[ks][0] = rd128(ba[ks]);
          else if (xx <= 12) { const int b = (xx - 1) >> 1, h = (xx - 1) & 1; sethalf(fb[ks][b], h, rtr(ba[4 + (b & 1)] + (b >> 1) * 2048 + ks * 14336 + h * 512)); }
          else if (xx <= 16) { const int b = (xx - 1) >> 1, h = (xx - 1) & 1; sethalf(fb[ks][b], h, rtr(ba[b] + ks * 14336 + h * 256)); }
          else { const int a = xx - 16; fa[ks][a] = rd128(ba[(a >> 1) * 2 + ks] + (a & 1) * 2048); } };
        auto mm = [&](int ks, int m) AI { const int a = m < 16 ? m >> 3 : (m - 4) / 6, b = m < 16 ? m & 7 : (m - 4) % 6; ac1[m] = MFMA(fb[ks][b], fa[ks][a], ac1[m]); };
        auto up = [&](int b) AI { if (b < 8) { ba[b] = cb[b] + sel<unsigned>(b < 4, xn, wnx); PIN(ba[b]); }
                                  else { wnx = (wnx == 65536u) ? 0u : wnx + 32768u; xn = (xn == 131072u) ? 98304u : xn + 16384u; } };
        waitl0(); waitv<11>(); bar();
        PH(2);
        period<28, 8, true, 40, LASTR + 24, 9>(rd, mm, up); waitl0(); waitv<0>(); bar();
        for (int s = 1; s < 16; ++s) { period<28, 8, false, 40, LASTR + 24, 9>(rd, mm, up); waitl0(); bar(); }
        PH(3);
        setup3(); sl = 0;
        ival(G, [&](auto U) AI { constexpr int u = U.value;
#pragma unroll
          for (int m = LAG + 4 * u; m < LAG + 4 * u + 4; ++m) if (m < 28) mm(1, m); });
#pragma unroll
        for (int a = 0; a < 4; ++a) fa[0][a] = rd128(g ? T_ZERO : T_XA + (64 * wm + 16 * (a ^ (2 * wn)) + r15) * 16);
#pragma unroll
        for (int b = 0; b < 8; ++b) fb[0][b] = rd128(T_WA + ((b < 6 ? (b >> 1) * 64 + wn * 32 + (b & 1) * 16 : 96 + b * 16) + r15) * 16);
        SB();
        sl = nq(sl); next3();
        bar();
        ival(G, [&](auto U) AI { constexpr int u = U.value;
#pragma unroll
          for (int m = 4 * u; m < 4 * u + 4; ++m) if (m < 28) mm(0, m); });
        sl = nq(sl); next3();
      }
      float k0[4], k1[4], qq[4];
#pragma unroll
      for (int a = 0; a < 4; ++a) { const unsigned ro = T_ROW + (64 * wm + 16 * (a ^ (2 * wn)) + r15) * 4; k0[a] = rdf(ro); k1[a] = rdf(ro + 512); qq[a] = rdf(ro + 1024); }
      u32x4 hw[4];
      auto st3 = [](int s) constexpr { return (s == 4 || s == 8 || s == 12) ? 4 : s == 14 ? 2 : 0; };
      sfor<15>([&](auto S1) AI { constexpr int s = S1.value + 1, v = s - 1, a = v & 3, m0 = (a < 2 ? 8 * a : 4 + 6 * a) + 2 * (v >> 2);
        if constexpr (s == 7 || s == 11) waitv<16>();
        else if constexpr (s >= 2) waitv<16 + st3(s - 3) + st3(s - 2) + st3(s - 1)>();
        if constexpr (s == 12) { if (w == 4) { waitv<8>(); check_flags(pv, rws, fvo, flagb + FK_M1 * 128, lane, 8 * half, 8 * half + 8, epr); } }
        SB(); bar();
        if constexpr (s == 7) { if (w == 5) stflag(rws, epr, lane, flagb + FK_M1 * 128 + cj * 4); }
        if constexpr (s == 11) { if (w == 4) ld32a(pv, rws, fvo, flagb + FK_M1 * 128); if (w == 5) stflag(rws, epr, lane, flagb + FK_M2 * 128 + cj * 4); }
        float t[8];
#define UU(e) ac1[m0 + ((e) >> 2)][(e) & 3]
#define LVL(expr) do { _Pragma("unroll") for (int e = 0; e < 8; ++e) { t[e] = (expr); PIN(t[e]); } } while (0)
        ival(G, [&](auto U) AI { constexpr int u = U.value;
          if constexpr (s < 15) {
            if constexpr (u == 0) LVL(UU(e) * UU(e));
            if constexpr (u == 1) LVL(__builtin_fmaf(t[e], k1[a], k0[a]));
            if constexpr (u == 2) LVL(UU(e) * t[e]);
            if constexpr (u == 3) LVL(__builtin_amdgcn_exp2f(t[e]));
            if constexpr (u == 4) LVL(__builtin_fmaf(t[e], qq[a], qq[a]));
            if constexpr (u == 5) LVL(__builtin_amdgcn_rcpf(t[e]));
            if constexpr (u == 6) LVL(UU(e) * t[e]);
            if constexpr (u == 7) { hw[a] = (u32x4){pk2(t[0], t[1]), pk2(t[2], t[3]), pk2(t[4], t[5]), pk2(t[6], t[7])}; PIN(hw[a]); }
          } });
#pragma unroll
        for (int q = 0; q < st3(s); ++q) st128(rws, hw[q], lane * 16u + (4 * wm + (q ^ (2 * wn))) * 1024, ci * HID_PANEL + kb_of(cj, v < 12 ? 2 * (v >> 2) + wn : 6) * 8192);
        sl = nq(sl);
        if constexpr (s < 11) next3(); else nexts();
        if constexpr (s == 11) {
          rb = mk(w2, 3584 * 2048);
#pragma unroll
          for (int q = 0; q < 4; ++q) vb[q] = (4 * q + g) * 2048 + ((((r15 >> 1) ^ (4 * (q & 1) + g)) * 2) + (lane & 1)) * 16 - 1024 * q;
        }
      });
    }
    PH(4);
    waitv<16 + 2>(); bar(); ival(G, nowork); sl = nq(sl); nexts();
    waitv<16>(); bar();
    if (w == 5) stflag(rws, epr, lane, flagb + FK_M3 * 128 + cj * 4);
    for (int o = 0; o < 2; ++o) {
      if (w == 4) ld32a(pv, rws, fvo, flagb + (FK_M2 + o) * 128);
      for (int s = 0; s < 2; ++s) { ival(G, nowork); sl = nq(sl); nexts(); waitv<16>(); bar(); }
      ival(G, nowork); sl = nq(sl); nexts(); waitv<16>();
      if (w == 4) check_flags(pv, rws, fvo, flagb + (FK_M2 + o) * 128, lane, o ? 0 : 8 * half, 8 * half + 8, epr);
      bar();
      for (int s = 0; s < (o ? n4 - 16 : 5); ++s) { ival(G, nowork); sl = nq(sl); nexts(); waitv<16>(); bar(); }
    }
    if (half) {
      sfor<8>([&](auto U) AI { constexpr int u = U.value; if constexpr ((u & 3) == 0) setm0(sl + wq * 8192 + u * 1024);
        dma<(u & 3) * 1024>(rx, (4u * (u & 3) + g) * 2048 + r15 * 16 - (u & 3) * 1024, (ci * 128 + 32 * wq + 16 * (u >> 2)) * 2048 + cnt * 256); });
      ld32a(pv, rws, fvo, flagb + FK_P * 128);
      waitv<8 + 9>();
    } else waitv<8>();
    bar();
    waitv<0>();
    if (half) {
      check_flags(pv, rws, fvo, flagb + FK_P * 128, lane, cnt, cnt + 1, epr);
      sfor<8>([&](auto U) AI { constexpr int u = U.value; if constexpr ((u & 3) == 0) setm0(nq(sl) + wq * 8192 + u * 1024);
        dma1<(u & 3) * 1024>(rws, lane * 16u, PART_OFF + (ci * 8 + cnt) * 32768 + wq * 8192 + (u >> 2) * 4096); });
      if (cnt == 0 && w == 4) stflag(rws, epr, lane, flagb + FK_EP * 128);
    }
    bar();
    bar();
    PH(5);
    asm volatile("" ::"v"(dumv));
    waitv<0>(); bar();
  }
  if (half) {
    const int bg = r31 & 15, sw = (r31 >> 4) * 8 + (bg >> 3) * 4 + (bg & 3);
    const unsigned pa = ((sl + 65536u + (w >> 2) * 32768u) & 131071u) + (r31 >> 4) * 16384 + (wq * 4 + (bg >> 2)) * 1024 + (bg & 3) * 256;
    const unsigned pp = nq(sl) + ((w >> 2) * 2 + (r31 >> 4)) * 8192 + (wq * 2 + (bg >> 3)) * 1024 + (bg & 3) * 256 + ((bg >> 2) & 1) * 8;
    const f32x4 bv = rdf4(T_OUT + r31 * 16), dv = rdf4(T_OUT + 512 + r31 * 16);
    float* op = out + (size_t)(ci * 128 + 16 * w + hl) * 1024 + cnt * 128 + r31 * 4;
#pragma unroll
    for (int u = 0; u < 8; ++u) {
      const int row = 2 * u + hl;
      const f32x4 av = rdf4(pa + ((row ^ bg) * 16));
      const u32x2 pw = *(LAS u32x2*)(size_t)(pp + ((row ^ sw) * 16)), xw = *(LAS u32x2*)(size_t)(sl + (16 * w + row) * 256 + r31 * 8);
      const float gg = rdf(T_ROW + 1536 + (16 * w + row) * 4);
      const f32x4 pf = {bf2f(pw[0] & 0xffffu), u2f(pw[0] & 0xffff0000u), bf2f(pw[1] & 0xffffu), u2f(pw[1] & 0xffff0000u)};
      const f32x4 xf = {bf2f(xw[0] & 0xffffu), u2f(xw[0] & 0xffff0000u), bf2f(xw[1] & 0xffffu), u2f(xw[1] & 0xffff0000u)};
      f32x4 r;
#pragma unroll
      for (int e = 0; e < 4; ++e) r[e] = (av[e] + pf[e]) + __builtin_fmaf(gg, __builtin_fmaf(xf[e], dv[e], -bv[e]), bv[e]);
      __builtin_nontemporal_store(r, (f32x4*)(op + u * 2048));
    }
  }
}

extern "C" void kernel_launch(void* const* d_in, const int* in_sizes, int n_in, void* d_out, int out_size, void* d_ws, size_t ws_size, hipStream_t stream) {
  (void)in_sizes; (void)n_in; (void)out_size; (void)ws_size;
  (void)hipFuncSetAttribute((const void*)fused_kernel, hipFuncAttributeMaxDynamicSharedMemorySize, LDS_BYTES);
  fused_kernel<<<256, 512, LDS_BYTES, stream>>>((const u16*)d_in[0], (const u16*)d_in[1], (const u16*)d_in[2], (const u16*)d_in[3], (const u16*)d_in[4],
      (const u16*)d_in[5], (const u16*)d_in[6], (const u16*)d_in[7], (const u16*)d_in[8], (const u16*)d_in[10], (const u16*)d_in[11], (const u16*)d_in[12],
      (const u16*)d_in[13], (const u16*)d_in[14], (char*)d_ws, (float*)d_out);
}
```

```cpp
#include <hip/hip_runtime.h>
#include <utility>

typedef unsigned short u16;
typedef __bf16 bf16x2 __attribute__((ext_vector_type(2)));
typedef __bf16 bf16x4 __attribute__((ext_vector_type(4)));
typedef __bf16 bf16x8 __attribute__((ext_vector_type(8)));
typedef float f32x2 __attribute__((ext_vector_type(2)));
typedef float f32x4 __attribute__((ext_vector_type(4)));
typedef float f32x16 __attribute__((ext_vector_type(16)));
typedef unsigned u32x2 __attribute__((ext_vector_type(2)));
typedef unsigned u32x4 __attribute__((ext_vector_type(4)));
typedef __amdgpu_buffer_rsrc_t rsrc_t;
#define LAS __attribute__((address_space(3)))
#define DI __device__ __forceinline__
#define AI __attribute__((always_inline))
#define SB() __builtin_amdgcn_sched_barrier(0)
#define PIN(x) asm volatile("" : "+v"(x))
#define MFMA(a, b, c) __builtin_amdgcn_mfma_f32_16x16x32_bf16(a, b, c, 0, 0, 0)
#define MFMA32(a, b, c) __builtin_amdgcn_mfma_f32_32x32x16_bf16(a, b, c, 0, 0, 0)
template <int V> using IC = std::integral_constant<int, V>;

constexpr unsigned HID_PANEL = 112 * 8192, H_OFF = 16 * HID_PANEL, H_PANEL = 32 * 16384;
constexpr unsigned PART_OFF = H_OFF + 16 * H_PANEL;
constexpr unsigned GRAN_OFF = PART_OFF + 128 * 32768;
constexpr unsigned RG_OFF = GRAN_OFF + 256 * 1024;
constexpr unsigned CG_OFF = RG_OFF + 16 * 8192;
constexpr unsigned FLAG_OFF = CG_OFF + 256 * 2048;
constexpr unsigned EPC_OFF = FLAG_OFF + 16 * 1024;
constexpr unsigned WS_BYTES = EPC_OFF + 16 * 128;
constexpr int FK_M1 = 0, FK_M2 = 1, FK_M3 = 2, FK_H = 3, FK_P = 4, FK_EP = 5;
constexpr unsigned TAB = 147456, T_ROW = TAB, T_XA = TAB + 2048, T_OUT = TAB + 4096, T_B1 = TAB + 5120, T_CS2 = TAB + 6144, T_WA = TAB + 8192, T_ZERO = TAB + 12288, T_AGG = T_WA;
constexpr unsigned LDS_BYTES = 163840;
#define XMAP 1
#define ROTM 3
#define OAUX 2
#define MOVE 3
#define PROFX false
constexpr int LAG = 2, LASTR = 25;
constexpr unsigned SPIN_CAP = 1u << 16;

DI rsrc_t mk(const void* p, unsigned bytes) { return __builtin_amdgcn_make_buffer_rsrc((void*)p, 0, bytes, 0x00020000); }
DI void setm0(unsigned lds) { asm volatile("s_mov_b32 m0, %0\n\ts_nop 0" ::"s"(lds) : "memory"); }
template <int OFF> DI void dma(rsrc_t r, unsigned voff, unsigned soff) {
  asm volatile("buffer_load_dwordx4 %0, %1, %2 offen offset:%3 sc1 lds" ::"v"(voff), "s"(r), "s"(soff), "i"(OFF) : "memory");
}
template <int OFF> DI void dma1(rsrc_t r, unsigned voff, unsigned soff) {
  asm volatile("buffer_load_dwordx4 %0, %1, %2 offen offset:%3 sc1 lds" ::"v"(voff), "s"(r), "s"(soff), "i"(OFF) : "memory");
}
#define PINS(x) asm volatile("" : "+s"(x))
typedef const void* const __attribute__((address_space(4)))* kargs_t;
DI const char* karg(int i) { kargs_t ka = (kargs_t)__builtin_amdgcn_kernarg_segment_ptr(); PINS(ka); return (const char*)ka[i]; }
enum { A_X, A_LRE, A_LIM, A_BRE, A_BIM, A_CRE, A_CIM, A_LDT, A_DSK, A_W1, A_B1, A_W2, A_B2, A_GW, A_WS, A_OUT };
template <int N> DI void waitv() { asm volatile("s_waitcnt vmcnt(%0)" ::"i"(N) : "memory"); }
DI void waitl0() { SB(); __builtin_amdgcn_s_waitcnt(0xC07F); asm volatile("" ::: "memory"); }
#define PH(k)
DI void bar() { asm volatile("s_barrier" ::: "memory"); SB(); }
DI bf16x8 rd128(unsigned a) { return *(LAS bf16x8*)(size_t)a; }
DI u32x4 rdu4(unsigned a) { return *(LAS u32x4*)(size_t)a; }
DI f32x4 rdf4(unsigned a) { return *(LAS f32x4*)(size_t)a; }
DI f32x2 rdf2(unsigned a) { return *(LAS f32x2*)(size_t)a; }
DI float rdf(unsigned a) { return *(LAS float*)(size_t)a; }
DI void wrf(unsigned a, float v) { *(LAS float*)(size_t)a = v; }
DI void wrf2(unsigned a, f32x2 v) { *(LAS f32x2*)(size_t)a = v; }
DI bf16x4 rtr(unsigned a) { return __builtin_amdgcn_ds_read_tr16_b64_v4bf16((LAS bf16x4*)(size_t)a); }
DI void sethalf(bf16x8& f, int h, bf16x4 v) {
  if (h == 0) f = __builtin_shufflevector(v, v, 0, 1, 2, 3, -1, -1, -1, -1);
  else { bf16x8 t = __builtin_shufflevector(v, v, -1, -1, -1, -1, 0, 1, 2, 3); f = __builtin_shufflevector(f, t, 0, 1, 2, 3, 12, 13, 14, 15); }
}
DI unsigned f2u(float f) { return __builtin_bit_cast(unsigned, f); }
DI float u2f(unsigned u) { return __builtin_bit_cast(float, u); }
DI float shx(int lane, float v, int m) { return u2f(__builtin_amdgcn_ds_bpermute((lane ^ m) * 4, (int)f2u(v))); }
DI float sum16(float v) { const auto r = __builtin_amdgcn_permlane16_swap(f2u(v), f2u(v), false, false); return u2f(r[0]) + u2f(r[1]); }
DI float sum32(float v) { const auto r = __builtin_amdgcn_permlane32_swap(f2u(v), f2u(v), false, false); return u2f(r[0]) + u2f(r[1]); }
DI float fexp(float v) { return __builtin_amdgcn_exp2f(v * 1.4426950408889634f); }
DI float bf2f(unsigned v) { return u2f(v << 16); }
DI unsigned pk2(float lo, float hi) { f32x2 v = {lo, hi}; return __builtin_bit_cast(unsigned, __builtin_convertvector(v, bf16x2)); }
DI float rbf(float f) { return u2f(pk2(f, 0.f) << 16); }
DI float dot2(unsigned a, unsigned b, float c) { return __builtin_amdgcn_fdot2_f32_bf16(__builtin_bit_cast(bf16x2, a), __builtin_bit_cast(bf16x2, b), c, false); }
template <int C> DI float dppadd(float v) { return v + u2f(__builtin_amdgcn_update_dpp(0, (int)f2u(v), C, 0xf, 0xf, true)); }
DI float sel4(bool b0, bool b1, float a, float b, float c, float d) { PIN(a); PIN(b); PIN(c); PIN(d); const float lo = b0 ? b : a, hi = b0 ? d : c; return b1 ? hi : lo; }
DI f32x2 cmul(f32x2 a, f32x2 b) { return (f32x2){a.x * b.x - a.y * b.y, a.x * b.y + a.y * b.x}; }
DI f32x2 cfma(f32x2 a, f32x2 b, f32x2 c) { return (f32x2){__builtin_fmaf(a.x, b.x, __builtin_fmaf(-a.y, b.y, c.x)), __builtin_fmaf(a.x, b.y, __builtin_fmaf(a.y, b.x, c.y))}; }
template <int AUX = 16> DI void st128(rsrc_t r, u32x4 v, unsigned voff, unsigned soff) {
  __builtin_amdgcn_raw_buffer_store_b128(v, r, voff, soff, AUX);
  asm volatile("s_nop 1" ::"v"(v));
}
DI void st64(rsrc_t r, u32x2 v, unsigned voff, unsigned soff) { __builtin_amdgcn_raw_buffer_store_b64(v, r, voff, soff, 16); }
DI void st32(rsrc_t r, unsigned v, unsigned voff, unsigned soff) { __builtin_amdgcn_raw_buffer_store_b32(v, r, voff, soff, 16); }
DI void stflag(rsrc_t r, unsigned v, int lane, unsigned soff) { if (lane == 0) st32(r, v, 0u, soff); }
DI void ld32a(unsigned& d, rsrc_t r, unsigned voff, unsigned soff) { asm volatile("buffer_load_dword %0, %1, %2, %3 offen sc1" : "=v"(d) : "v"(voff), "s"(r), "s"(soff) : "memory"); }
DI void ld16p(unsigned& d, const u16* p, unsigned voff) { asm volatile("global_load_ushort %0, %1, %2" : "=v"(d) : "v"(voff), "s"(p) : "memory"); }
DI void ld64p(u32x2& d, rsrc_t r, unsigned voff, unsigned soff) { asm volatile("buffer_load_dwordx2 %0, %1, %2, %3 offen" : "=v"(d) : "v"(voff), "s"(r), "s"(soff) : "memory"); }
DI void ld64a(u32x2& d, rsrc_t r, unsigned voff, unsigned soff) { asm volatile("buffer_load_dwordx2 %0, %1, %2, %3 offen sc1" : "=v"(d) : "v"(voff), "s"(r), "s"(soff) : "memory"); }
DI void ld128g(u32x4& d, const u16* p, unsigned voff) { asm volatile("global_load_dwordx4 %0, %1, %2" : "=v"(d) : "v"(voff), "s"(p) : "memory"); }
DI void pfl(unsigned& d, rsrc_t r, unsigned voff, unsigned soff) { asm volatile("buffer_load_dword %0, %1, %2, %3 offen" : "+v"(d) : "v"(voff), "s"(r), "s"(soff) : "memory"); }
template <class T> DI T sel(bool c, T a, T b) { return c ? a : b; }
DI bool anyl(bool b) { return __builtin_amdgcn_ballot_w64(b) != 0; }
DI void ld128p(u32x4& d, rsrc_t r, unsigned voff, unsigned soff) { asm volatile("buffer_load_dwordx4 %0, %1, %2, %3 offen" : "=v"(d) : "v"(voff), "s"(r), "s"(soff) : "memory"); }
DI void sincos_f(float t, float& sn, float& cs) {
  const float k = __builtin_rintf(t * 0.63661977236f);
  float r = __builtin_fmaf(k, -1.5707962513f, t); r = __builtin_fmaf(k, -7.5497894159e-08f, r); r = __builtin_fmaf(k, -5.3903029534e-15f, r);
  const float z = r * r;
  const float sp = __builtin_fmaf(r * z, __builtin_fmaf(z, __builtin_fmaf(z, __builtin_fmaf(z, 2.7183114939e-06f, -1.9839334836e-04f), 8.3333293858e-03f), -1.6666666642e-01f), r);
  const float cp = __builtin_fmaf(z, __builtin_fmaf(z, __builtin_fmaf(z, __builtin_fmaf(z, 2.4390448796e-05f, -1.3886763775e-03f), 4.1666623324e-02f), -4.9999999726e-01f), 1.f);
  const int q = (int)k;
  const float a = (q & 1) ? cp : sp, b = (q & 1) ? sp : cp;
  sn = (q & 2) ? -a : a; cs = ((q + 1) & 2) ? -b : b;
}
DI void ld128a(u32x4& d, rsrc_t r, unsigned voff, unsigned soff) { asm volatile("buffer_load_dwordx4 %0, %1, %2, %3 offen sc1" : "=v"(d) : "v"(voff), "s"(r), "s"(soff) : "memory"); }

DI unsigned x1off(int a) { return a == 0 ? 131072u : (unsigned)(81920 + a * 16384); }
DI unsigned nq(unsigned s) { return (s + 32768u) & 131071u; }

DI int kb_of(int j, int bl) { return 7 * j + ((j & 1) ? (bl == 6 ? 0 : bl + 1) : bl); }

template <int NR, int LAST> constexpr int nread(int sl) { int n = 0; for (int r = 0; r < NR; ++r) n += (r * LAST) / (NR - 1) <= sl; return n; }
template <int NM, int R0, bool FIRST, int NR, int LAST, int NB, int SL, class RD, class MM, class UP>
DI void slot(RD& rd, MM& mm, UP& upd) {
  constexpr int q = SL + LAG;
  if constexpr (!FIRST && (q == NM || q == NM + R0 || q == 2 * NM)) {
    constexpr int cnt = nread<NR, LAST>(SL - 1) - nread<NR, LAST>(SL - 6);
    __builtin_amdgcn_s_waitcnt(0xC07F | (cnt << 8)); SB();
  }
  if constexpr (!(FIRST && q < NM)) mm((q < NM) ? 1 : ((q < 2 * NM) ? 0 : 1), q % NM);
  SB();
  constexpr int r = (SL * (NR - 1) + LAST - 1) / LAST;
  if constexpr (r < NR && (r * LAST) / (NR - 1) == SL) rd(r);
  constexpr int per = NB > 2 * NM - 1 - LAST ? 2 : 1, u0 = (SL - LAST - 1) * per;
  if constexpr (SL > LAST && u0 < NB) { upd(u0); if constexpr (per == 2 && u0 + 1 < NB) upd(u0 + 1); }
  SB();
}
template <int NM, int R0, bool FIRST, int NR, int LAST, int NB, class RD, class MM, class UP, int... I>
DI void period_(RD& rd, MM& mm, UP& upd, std::integer_sequence<int, I...>) { (slot<NM, R0, FIRST, NR, LAST, NB, I>(rd, mm, upd), ...); }
template <int NM, int R0, bool FIRST, int NR, int LAST, int NB, class RD, class MM, class UP>
DI void period(RD& rd, MM& mm, UP& upd) {
  static_assert(NR - 1 <= LAST && NB <= 2 * (2 * NM - 1 - LAST), "");
  period_<NM, R0, FIRST, NR, LAST, NB>(rd, mm, upd, std::make_integer_sequence<int, 2 * NM>{});
}
template <bool FIRST, int SL, class RD, class MM, class UP> DI void slot32(RD& rd, MM& mm, UP& upd) {
  if constexpr (SL == 12) { __builtin_amdgcn_s_waitcnt(0xC07F | (8 << 8)); SB(); }
  if constexpr (!(FIRST && SL < 12)) mm((SL + 4) & 15);
  SB();
  if constexpr (SL < 2) { rd(2 * SL); rd(2 * SL + 1); } else if constexpr (SL < 14) rd(SL + 2);
  if constexpr ((SL & 3) == 2) { upd(SL >> 2); upd(4 + (SL >> 2)); }
  SB();
}
template <bool FIRST, class RD, class MM, class UP, int... I> DI void period32_(RD& rd, MM& mm, UP& upd, std::integer_sequence<int, I...>) { (slot32<FIRST, I>(rd, mm, upd), ...); }
template <bool FIRST, class RD, class MM, class UP> DI void period32(RD& rd, MM& mm, UP& upd) { period32_<FIRST>(rd, mm, upd, std::make_integer_sequence<int, 16>{}); }
template <class G, class W, int... U> DI void ival_(G& g, W& wk, std::integer_sequence<int, U...>) { ((g(IC<U>{}), SB(), wk(IC<U>{}), SB()), ...); }
template <class G, class W> DI void ival(G&& g, W&& wk) { ival_(g, wk, std::make_integer_sequence<int, 8>{}); }
template <int N, class F, int... I> DI void sfor_(F& f, std::integer_sequence<int, I...>) { (f(IC<I>{}), ...); }
template <int N, class F> DI void sfor(F&& f) { sfor_<N>(f, std::make_integer_sequence<int, N>{}); }

DI void check_flags(unsigned& pv, rsrc_t ws, unsigned voff, unsigned soff, int lane, int lo, int hi, unsigned ep) {
  PIN(pv);
  const bool need = lane >= lo && lane < hi;
  if (__builtin_expect(anyl(need && pv != ep), 0)) {
    for (unsigned sp = 0; sp < SPIN_CAP; ++sp) {
      __builtin_amdgcn_s_sleep(2);
      ld32a(pv, ws, voff, soff); asm volatile("s_waitcnt vmcnt(0)" : "+v"(pv)::"memory");
      if (!anyl(need && pv != ep)) break;
    }
  }
}

#if defined(__HIP_DEVICE_COMPILE__) && __HIP_DEVICE_COMPILE__
__attribute__((target("no-packed-fp32-ops")))
#endif
__global__ __launch_bounds__(512, 2) void fused_kernel(const u16* x, const u16* lre, const u16* lim, const u16* bre, const u16* bim, const u16* cre, const u16* cim,
    const u16* ldt, const u16* dsk, const u16* w1, const u16* b1, const u16* w2, const u16* b2, const u16* gw, char* ws, float* out) {
  extern __shared__ __attribute__((aligned(16))) char shm[];
  const int lane = threadIdx.x & 63, w = __builtin_amdgcn_readfirstlane(threadIdx.x >> 6);
  const int bid = blockIdx.x, xcd = bid & 7, loc = bid >> 3;
  const int ci = (xcd & 3) * 4 + (loc & 3), cj = (loc >> 4) * 8 + (xcd >> 2) * 4 + ((loc >> 2) & 3);
  const int rot = (ci + cj) & ROTM, cnt = cj & 7, half = cj >> 3, wq = w & 3, tq = threadIdx.x & 255;
  const int n4 = half ? 28 + MOVE : 28 - MOVE;
  const rsrc_t rws = mk(ws, WS_BYTES), rx = mk(x, 2048 * 2048);
  const int wm = (w >> 1) & 1, wn = w & 1, g = lane >> 4, r15 = lane & 15, q2 = (lane >> 2) & 3, p = lane & 3, hl = lane >> 5, r31 = lane & 31;
  const unsigned flagb = FLAG_OFF + ci * 1024, fvo = (lane & 15) * 4;
  unsigned rgb = RG_OFF + ci * 8192; PINS(rgb);
  auto nowork = [&](auto) AI {};
  unsigned va[4], vb[4], vw[7], sl, sa, sb, ta, tb, dumv = 0; int sx; rsrc_t ra, rb;
  auto vnat = [&](int u) AI { return (8u * u + (lane >> 3)) * 2048 + (((lane & 7) ^ ((4 * u + g) & 7)) * 16) - 1024 * u; };
  auto nexts = [&]() AI { sa = __builtin_amdgcn_readlane(ta, sx); sb = __builtin_amdgcn_readlane(tb, sx); ++sx; PINS(sa); PINS(sb); };
  auto G = [&](auto U) AI { constexpr int u = U.value;
    if constexpr (u == 0) setm0(sl + wq * 4096);
    if constexpr (u == 4) setm0(sl + 16384 + wq * 4096);
    if constexpr (u < 4) dma1<u * 1024>(ra, va[u], sa); else dma<(u - 4) * 1024>(rb, vb[u - 4], sb);
  };
  auto G2 = [&](auto S, auto U) AI { constexpr int a = S.value % 3, u = U.value;
    if constexpr (u == 0) setm0((a == 0 ? 131072u : 81920u + a * 16384u) + wq * 4096);
    if constexpr (u == 4) setm0(a * 32768 + wq * 7168);
    if constexpr (u == 8) setm0(a * 32768 + wq * 7168 + 4096);
    if constexpr (u < 4) dma<u * 1024>(rx, va[u], sa); else dma<(u & 3) * 1024>(rb, vw[u - 4], sb);
  };
  const int c7 = ci & 7;
  auto setup2 = [&]() AI {
    rb = mk((const char*)w1 - 4096, 1024 * 7168 + 4096);
#pragma unroll
    for (int q = 0; q < 4; ++q) va[q] = vnat(q);
#pragma unroll
    for (int q = 0; q < 6; ++q) { const int k = 8 * (q & 1) + (lane >> 3), f = ((k >> 1) & 1) | ((q & 1) << 1);
      vw[q] = 4096 + k * 7168 + ((cj & 1) ? 64 : 0) + (q >> 1) * 128 + (((((lane & 7) >> 1) ^ f) * 2) + (lane & 1)) * 16 - (q & 3) * 1024; }
    vw[6] = 4096 + (lane >> 2) * 7168 + ((cj & 1) ? 0 : 384) + (((((lane & 3) >> 1) ^ hl) * 2) + (lane & 1)) * 16 - 2048;
    int k = lane - 2 + (c7 < 2 ? 3 * c7 : c7 - 2); k = 2 + (k >= 6 ? k - 6 : k);
    k = lane < 2 ? lane : lane < 8 ? k : (lane ^ rot);
    k = (k ^ (ci & 8)) & 15;
    ta = (ci * 128 + 32 * wq) * 2048 + k * 128; tb = (k * 64 + 16 * wq) * 7168 + cj * 448;
  };
  auto setup3 = [&]() AI {
    ra = rws;
#pragma unroll
    for (int q = 0; q < 4; ++q) { va[q] = lane * 16; vb[q] = vnat(q); }
  };
  auto table3 = [&](unsigned& ta, unsigned& tb) AI {
    const int ks = 16 * half + (lane ^ rot);
    int n = 2 * (lane - 16) + (wq >> 1), hp = half; if (n >= 56) { n -= 2 * MOVE; hp = 0; }
    const int kb = kb_of(hp * 8 + ((n ^ rot) & 7), n >> 3);
    ta = lane < 16 ? H_OFF + ci * H_PANEL + ks * 16384 + wq * 4096 : ci * HID_PANEL + kb * 8192 + (wq & 1) * 4096;
    tb = lane < 16 ? (cnt * 128 + 32 * wq) * 2048 + (ks >> 1) * 128 + (ks & 1) : (kb * 32 + 16 * (wq & 1)) * 2048 + cnt * 256;
    PIN(ta); PIN(tb);
  };
  auto next3 = [&]() AI { nexts(); rb = mk(sel<const u16*>(sb & 1, cim, cre), 1024 * 2048); sb &= ~1u; };

  if (w < 4) {
    constexpr bool isx = PROFX; (void)isx;
    unsigned epr = __builtin_amdgcn_raw_buffer_load_b32(rws, 0, flagb + FK_EP * 128, 16), epc = __builtin_amdgcn_raw_buffer_load_b32(rws, 0, EPC_OFF + cj * 128, 16);
    const int seg0 = 4 * wm + hl;
    f32x2 lam, cof, l16, l128, l1k, pw0, pw1; unsigned t3a, t3b;
    {
      const int pch = 64 * cj + 32 * wn + r31, tc = tq < 224 ? tq : 223;
      const unsigned pdt = ldt[pch], plr = lre[pch], pli = lim[pch];
      const unsigned bq = b1[224 * cj + (tc < 192 ? ((cj & 1) ? 32 : 0) + tc : ((cj & 1) ? -192 : 0) + tc)];
      const unsigned oq = (wq < 2 ? b2 : dsk)[cnt * 128 + (tq & 127)];
      asm volatile("" ::: "memory"); SB();
      setup2(); sx = 2; nexts(); table3(t3a, t3b);
#pragma unroll
      for (int q = 0; q < 7; ++q) { PIN(vw[q]); PIN(va[q & 3]); }
      const float lr = bf2f(plr), li = bf2f(pli), dt = rbf(fexp(bf2f(pdt))), e = fexp(dt * lr); float sn, cs_; sincos_f(dt * li, sn, cs_);
      lam = (f32x2){e * cs_, e * sn};
      const float nr = lam.x - 1.f, ni = lam.y, den = 2.f / (lr * lr + li * li);
      cof = (f32x2){(nr * lr + ni * li) * den, (ni * lr - nr * li) * den};
      l16 = cmul(lam, lam); l16 = cmul(l16, l16); l16 = cmul(l16, l16); l16 = cmul(l16, l16);
      const f32x2 l32 = cmul(l16, l16), l64 = cmul(l32, l32); l128 = cmul(l64, l64);
      pw0 = hl ? l16 : (f32x2){1.f, 0.f}; if (wm) pw0 = cmul(pw0, l64);
      pw1 = cmul(pw0, l32);
      l1k = cmul(l128, l128); l1k = cmul(l1k, l1k); l1k = cmul(l1k, l1k);
      PIN(lam); PIN(cof); PIN(l16); PIN(l128); PIN(pw0); PIN(pw1); PIN(l1k);
      wrf(T_B1 + tq * 4, bf2f(bq)); wrf(T_OUT + tq * 4, bf2f(oq)); wrf(T_ZERO + (lane & 3) * 4, 0.f);
      epr = __builtin_amdgcn_readfirstlane(epr) + 1u; epc = __builtin_amdgcn_readfirstlane(epc) + 1u;
    }
    {
      f32x16 acc[2][2];
#pragma unroll
      for (int a = 0; a < 4; ++a)
#pragma unroll
        for (int e = 0; e < 16; ++e) acc[a >> 1][a & 1][e] = 0.f;
      u32x2 wv[4][4];
      const int sstar = c7 < 2 ? c7 : 2;
      const int fsw = ((q2 >> 1) & 1) | ((g & 1) << 1);
      const unsigned cf = (g >> 1) * 7168 + (8 * (g & 1) + q2) * 128 + ((wq ^ fsw) * 32) + p * 8, ch = (g >> 1) * 7168 + 6144 + (8 * (g & 1) + q2) * 64 + (((wq & 1) ^ (g & 1)) * 32) + p * 8;
      auto wread = [&](int st) AI {
        if (sstar == st) {
#pragma unroll
          for (int e = 0; e < 4; ++e)
#pragma unroll
            for (int t = 0; t < 4; ++t) wv[t][e] = __builtin_bit_cast(u32x2, rtr(st * 32768 + (e >> 1) * 14336 + (t < 3 ? cf + t * 2048 + (e & 1) * 512 : ch + (e & 1) * 256)));
        } };
      {
        bf16x8 fa[4][2], fb[4][2];
        unsigned cb[8], ba[8], rs = 98304;
#pragma unroll
        for (int b = 0; b < 8; ++b) { cb[b] = (b < 4 ? wm * 8192u : 16384 + wn * 8192u) + r31 * 128 + (((2 * (b & 3) + hl) ^ ((r31 >> 1) & 7)) * 16); ba[b] = rs + cb[b]; PIN(ba[b]); }
        auto rd = [&](int r) AI { const int k16 = r >> 2, xx = r & 3;
          if (xx == 0) fa[k16][0] = rd128(ba[k16]); else if (xx == 3) fa[k16][1] = rd128(ba[k16] + 4096); else fb[k16][xx - 1] = rd128(ba[4 + k16] + (xx - 1) * 4096); };
        auto mm = [&](int m) AI { const int k16 = m >> 2, mt = (m >> 1) & 1, nt = m & 1; acc[mt][nt] = MFMA32(fa[k16][mt], fb[k16][nt], acc[mt][nt]); };
        auto up = [&](int b) AI { if (b == 0) rs = nq(rs); ba[b] = rs + cb[b]; PIN(ba[b]); };
        waitl0(); bar();
        period32<true>(rd, mm, up); waitl0(); bar();
        for (int s = 1; s < 16; ++s) { period32<false>(rd, mm, up); waitl0(); bar(); }
        wread(0);
        sfor<7>([&](auto U) AI { G2(IC<2>{}, IC<U.value + 4>{}); SB(); if constexpr (U.value < 6) { mm(4 + 2 * U.value); mm(5 + 2 * U.value); } SB(); });
        nexts();
      }
      const unsigned ca = (32 * wn + r31) * 8;
      f32x2 st[2] = {{0, 0}, {0, 0}}, Pf[2] = {{0, 0}, {0, 0}}, Gt = {0, 0}, cr = {0, 0}, ag[8];
      u32x4 lb[8], cg[8]; unsigned lbo[8], ob[2][2][4], pv, gd = 0;
      f32x4 g2[2][2]; float o[2][2][2], cs[4] = {0, 0, 0, 0}, c0s = 0.f, c1s = 0.f;
      const rsrc_t rgr = mk(ws + GRAN_OFF + cj * 16384, 16384);
      const int tc = tq < 224 ? tq : 223;
      const unsigned cgo = (tc >= 112 ? 16384 - 1792 : 0) + tc * 16;
      auto bad = [&]() AI { unsigned d = 0;
#pragma unroll
        for (int q = 0; q < 8; ++q) { PIN(lb[q]); d |= (int)lbo[q] < 0 ? 0u : ((lb[q][1] ^ epc) | (lb[q][3] ^ epc)); }
        return d != 0; };
      auto gbad = [&]() AI { unsigned d = 0;
#pragma unroll
        for (int q = 0; q < 8; ++q) { PIN(cg[q]); d |= (cg[q][1] ^ epc) | (cg[q][3] ^ epc); }
        return d != 0; };
      auto E2 = [](int s) constexpr { return (s == 2 || s == 6) ? 1 : s == 4 ? 8 : (s == 10 || s == 11) ? 4 : 0; };
      auto T2 = [](int s) constexpr { return (s == 8 || s == 9) ? 4 : (s >= 0 && s < 13 && s != 4 && s != 10 && s != 11) ? 1 : 0; };
      sfor<15>([&](auto S) AI { constexpr int s = S.value;
        if constexpr (s < 3 || s == 14) waitl0();
        if constexpr (s == 11) waitv<11 + E2(10)>();
        else if constexpr (s >= 1) waitv<T2(s - 2) + (s < 14 ? 11 : 8) + E2(s - 1) + T2(s - 1)>();
        if constexpr (s == 13) { if (w == 0) { waitv<6>(); check_flags(pv, rws, fvo, flagb + FK_H * 128, lane, 8 * half, 8 * half + 8, epr); } }
        bar();
        if constexpr (s < 2) wread(s + 1);
        if constexpr (s == 2) {
#pragma unroll
          for (int sg = 0; sg < 8; ++sg) ag[sg] = rdf2(T_AGG + sg * 512 + ca);
        }
        if constexpr (s == 7 || s == 8) {
#pragma unroll
          for (int q = 0; q < 2; ++q) { if (s == 7) g2[q][0] = rdf4(T_ROW + 1536 + (64 * wm + 32 * q + 16 * hl) * 4); g2[q][s == 7 ? 1 : 0] = rdf4(T_ROW + 1536 + (64 * wm + 32 * q + 16 * hl + 4 * (s - 6)) * 4); }
        }
        if constexpr (s == 9) {
#pragma unroll
          for (int q = 0; q < 2; ++q) g2[q][1] = rdf4(T_ROW + 1536 + (64 * wm + 32 * q + 16 * hl + 12) * 4);
        }
        if constexpr (s == 11) { if (w == 0) stflag(rws, epr, lane, flagb + FK_H * 128 + cj * 4); }
        SB();
        auto work = [&](auto U) AI { constexpr int u = U.value;
          if constexpr (s < 2 && u >= 1 && u < 9) {
            constexpr int t = 8 * s + u - 1;
#pragma unroll
            for (int mt = 0; mt < 2; ++mt) { st[mt] = cfma(lam, st[mt], (f32x2){acc[mt][0][t], acc[mt][1][t]}); PIN(st[mt]); }
          }
          if constexpr (s == 1 && u == 9) { wrf2(T_AGG + seg0 * 512 + ca, st[0]); wrf2(T_AGG + (seg0 + 2) * 512 + ca, st[1]); }
          if constexpr (s == 2 && u >= 1 && u < 9) {
            constexpr int sg = u - 1, mt = (sg >> 1) & 1; const bool me = sg == seg0 + 2 * mt;
            Pf[mt].x = sel<float>(me, Gt.x, Pf[mt].x); Pf[mt].y = sel<float>(me, Gt.y, Pf[mt].y); Gt = cfma(l16, Gt, ag[sg]);
            PIN(Gt); PIN(Pf[mt]);
          }
          if constexpr (s == 2 && u == 9) st64(rgr, (u32x2){f2u(sel<float>(hl, Gt.y, Gt.x)), epc}, ca * 2 + hl * 8, ci * 1024);
          if constexpr ((s == 3 && u < 10) || (s == 4 && u < 6)) {
            constexpr int t = 10 * (s - 3) + u;
#pragma unroll
            for (int mt = 0; mt < 2; ++mt) { f32x2 v = cmul(cof, (f32x2){acc[mt][0][t], acc[mt][1][t]}); PIN(v); acc[mt][0][t] = v.x; acc[mt][1][t] = v.y; }
          }
          if constexpr (s == 4 && u >= 6 && u < 10) {
#pragma unroll
            for (int q = 2 * u - 12; q < 2 * u - 10; ++q) { lbo[q] = (ci - 8 - 8 * hl + q) * 1024 + ca * 2; ld128a(lb[q], rgr, lbo[q], 0u); }
          }
          if constexpr (s == 5 || (s == 6 && u < 5)) {
            constexpr int id = 11 * (s - 5) + u, t = id >> 2, e = id & 3;
            cs[t] += (bf2f(wv[t][e][0] & 0xffffu) + u2f(wv[t][e][0] & 0xffff0000u)) + (bf2f(wv[t][e][1] & 0xffffu) + u2f(wv[t][e][1] & 0xffff0000u));
            PIN(cs[t]);
          }
          if constexpr (s == 6 && (u == 5 || u == 6)) {
#pragma unroll
            for (int t = 0; t < 4; ++t) { cs[t] = u == 6 ? sum32(cs[t]) : sum16(cs[t]); PIN(cs[t]); }
          }
          if constexpr (s == 6 && u == 7)
            st64(rws, (u32x2){f2u(sel4(g & 1, g & 2, cs[0], cs[1], cs[2], cs[3])), epc}, (g < 3 ? g * 64 + wq * 16 + r15 : 192 + (wq & 1) * 16 + r15) * 8u, CG_OFF + (cj * 16 + ci) * 2048);
          if constexpr (s == 6 && u == 9) {
            if (__builtin_expect(anyl(bad()), 0)) {
              for (unsigned sp = 0; sp < SPIN_CAP; ++sp) {
                __builtin_amdgcn_s_sleep(2);
#pragma unroll
                for (int q = 0; q < 8; ++q) ld128a(lb[q], rgr, lbo[q], 0u);
                waitv<0>();
                if (!anyl(bad())) break;
              }
            }
          }
          if constexpr (s == 7 && u < 4) {
#pragma unroll
            for (int q = 2 * u; q < 2 * u + 2; ++q) cr = cfma(l128, cr, (f32x2){u2f(lb[q][0]), u2f(lb[q][2])});
            PIN(cr);
          }
          if constexpr (s == 7 && u == 4) {
            const auto sx_ = __builtin_amdgcn_permlane32_swap(f2u(cr.x), f2u(cr.x), false, false), sy_ = __builtin_amdgcn_permlane32_swap(f2u(cr.y), f2u(cr.y), false, false);
            cr = cfma(l1k, (f32x2){u2f(sx_[1]), u2f(sy_[1])}, (f32x2){u2f(sx_[0]), u2f(sy_[0])});
            PIN(cr);
          }
          if constexpr (s == 7 && (u == 5 || u == 6)) { constexpr int mt = u - 5; st[mt] = cmul(cof, cfma(sel<f32x2>(mt, pw1, pw0), cr, Pf[mt])); PIN(st[mt]); }
          if constexpr ((s == 7 && u >= 7) || (s == 8 && u >= 1 && u < 9 && (u & 1)) || (s == 9 && u >= 1 && u < 9)) {
            constexpr int t = s == 7 ? u - 7 : s == 8 ? 4 + (u >> 1) : 7 + u, e = t & 3;
#pragma unroll
            for (int mt = 0; mt < 2; ++mt) {
              st[mt] = cfma(lam, st[mt], (f32x2){acc[mt][0][t], acc[mt][1][t]});
              const float gg = g2[mt][(t >> 2) & 1][e];
              o[mt][0][e & 1] = gg * st[mt].x; o[mt][1][e & 1] = -gg * st[mt].y;
              PIN(st[mt]); PIN(o[mt][0][e & 1]); PIN(o[mt][1][e & 1]);
              if constexpr (e & 1) { ob[mt][0][(t >> 1) & 3] = pk2(o[mt][0][0], o[mt][0][1]); ob[mt][1][(t >> 1) & 3] = pk2(o[mt][1][0], o[mt][1][1]); PIN(ob[mt][0][(t >> 1) & 3]); PIN(ob[mt][1][(t >> 1) & 3]); }
            }
          }
          if constexpr ((s == 8 || s == 9) && u == 10) {
            constexpr int hf = s - 8;
#pragma unroll
            for (int q = 0; q < 4; ++q) { const int mt = q >> 1, pt = q & 1;
              st128(rws, (u32x4){ob[mt][pt][0], ob[mt][pt][1], ob[mt][pt][2], ob[mt][pt][3]},
                    (2 * wn + (r31 >> 4)) * 4096 + wm * 2048 + hf * 1024 + (2 * mt + hl) * 256 + ((r15 ^ (hf << 2)) * 16), H_OFF + ci * H_PANEL + (2 * cj + pt) * 16384); }
          }
          if constexpr (s == 8 && u == 0) { if (ci == 15 && w == 0) stflag(rws, epc, lane, EPC_OFF + cj * 128); }
          if constexpr ((s == 10 || s == 11) && (u & 1) && u < 8) { constexpr int q = 4 * (s - 10) + (u >> 1); ld128a(cg[q], rws, cgo, CG_OFF + (cj * 16 + q) * 2048); }
          if constexpr (s == 12 && u == 5) { if (w == 0) ld32a(pv, rws, fvo, flagb + FK_H * 128); }
          if constexpr (s < 13 && u == 10 && T2(s) == 1) {
            constexpr int q = s < 4 ? s : s < 8 ? s - 1 : 7;
            if constexpr (q < 2) pfl(dumv, mk(sel<const u16*>(q, cim, cre), 1024 * 2048), lane < 16 ? lane * 128u : 0x80000000u, (bid * 64 + wq * 16) * 128);
            else pfl(dumv, mk(w2, 3584 * 2048), lane < (q < 7 ? 10 : 6) ? lane * 128u : 0x80000000u, (bid * 224 + wq * 56 + (q - 2) * 10) * 128);
          }
          if constexpr (s == 13 && u < 4) {
#pragma unroll
            for (int q = 2 * u; q < 2 * u + 2; ++q) { PIN(cg[q]); gd |= (cg[q][1] ^ epc) | (cg[q][3] ^ epc); c0s += u2f(cg[q][0]); c1s += u2f(cg[q][2]); }
            PIN(gd); PIN(c0s); PIN(c1s);
          }
          if constexpr (s == 13 && u == 4) {
            if (__builtin_expect(anyl(gd != 0), 0)) {
              for (unsigned sp = 0; sp < SPIN_CAP; ++sp) { __builtin_amdgcn_s_sleep(2);
#pragma unroll
                for (int q = 0; q < 8; ++q) ld128a(cg[q], rws, cgo, CG_OFF + (cj * 16 + q) * 2048);
                waitv<0>(); if (!anyl(gbad())) break; }
              c0s = 0.f; c1s = 0.f;
#pragma unroll
              for (int q = 0; q < 8; ++q) { c0s += u2f(cg[q][0]); c1s += u2f(cg[q][2]); }
            }
            wrf2(T_CS2 + tc * 8, (f32x2){c0s, c1s});
          }
          if constexpr (s == 14 && u == 1) {
            const float v = rdf(T_CS2 + tc * 4) + rdf(T_CS2 + 896 + tc * 4), hi = rbf(v), bb = rdf(T_B1 + tc * 4);
            *(LAS u32x4*)(size_t)(T_WA + tc * 16) = (u32x4){pk2(hi, v - hi), pk2(hi, bb), pk2(bb, 0.f), 0u};
          }
        };
        if constexpr (s < 13) { sfor<11>([&](auto U) AI { G2(IC<s + 3>{}, U); SB(); work(U); SB(); }); if constexpr (s < 12) nexts(); }
        if constexpr (s == 12) { ra = rws;
#pragma unroll
          for (int q = 0; q < 4; ++q) { vb[q] = va[q]; va[q] = lane * 16; }
          ta = t3a; tb = t3b; sl = 32768; sx = 0; next3(); }
        if constexpr (s >= 13) { ival(G, work); sl = nq(sl); next3(); asm volatile("s_nop 4"); }
        if constexpr (s == 14) ival(G, nowork);
      });
      asm volatile("" ::"v"(dumv));
      waitl0(); waitv<16>(); bar();
    }
    f32x4 acc[4][4];
#pragma unroll
    for (int a = 0; a < 4; ++a)
#pragma unroll
      for (int b = 0; b < 4; ++b) acc[a][b] = (f32x4){0, 0, 0, 0};
    bf16x8 fa[2][4], fb[2][4];
    const int kap = 4 * (g & 1) + q2, hf = p >> 1;
    const unsigned wb = 16384 + (4 * g + q2) * 256 + p * 8, hb = (g >> 1) * 4096 + wm * 2048 + hf * 1024 + (8 * (g & 1) + q2) * 16 + (p & 1) * 8;
    const unsigned cb3[4] = {hb + 64u * hf, hb + 64u * (1 ^ hf), 16384 + wn * 8192u + r15 * 128 + ((g ^ (r15 >> 1)) * 16), 16384 + wn * 8192u + r15 * 128 + (((4 + g) ^ (r15 >> 1)) * 16)};
    const unsigned cb4[5] = {wm * 4096u + lane * 16, wb + (((4 * wn + 0) ^ kap) * 32), wb + (((4 * wn + 1) ^ kap) * 32), wb + (((4 * wn + 2) ^ kap) * 32), wb + (((4 * wn + 3) ^ kap) * 32)};
    unsigned ba[5];
    sl = 32768;
#pragma unroll
    for (int b = 0; b < 4; ++b) { ba[b] = sl + cb3[b]; PIN(ba[b]); }
    auto rd3 = [&](int r) AI { const int ks = r / 12, xx = r % 12;
      if (xx < 2) sethalf(fa[ks][0], xx, rtr(ba[xx] + ks * 8192));
      else if (xx < 6) fb[ks][xx - 2] = rd128(ba[2 + ks] + (xx - 2) * 2048);
      else { const int a = (xx - 4) >> 1, h = xx & 1; sethalf(fa[ks][a], h, rtr(ba[h] + ks * 8192 + a * 256)); } };
    auto rd4 = [&](int r) AI { const int ks = r / 12, xx = r % 12;
      if (xx == 0) fa[ks][0] = rd128(ba[0] + ks * 8192);
      else if (xx <= 8) { const int b = (xx - 1) >> 1, h = (xx - 1) & 1; sethalf(fb[ks][b], h, rtr(ba[1 + b] + ks * 8192 + h * 4096)); }
      else fa[ks][xx - 8] = rd128(ba[0] + ks * 8192 + (xx - 8) * 1024); };
    auto mm = [&](int ks, int m) AI { acc[m >> 2][m & 3] = MFMA(fb[ks][m & 3], fa[ks][m >> 2], acc[m >> 2][m & 3]); };
    auto up3 = [&](int b) AI { if (b == 0) sl = nq(sl); ba[b] = sl + cb3[b]; PIN(ba[b]); };
    auto up4 = [&](int b) AI { if (b == 0) sl = nq(sl); ba[b] = sl + cb4[b]; PIN(ba[b]); };
    period<16, 4, true, 24, LASTR, 4>(rd3, mm, up3); waitl0(); waitv<8>(); bar();
    for (int s = 1; s < 15; ++s) { period<16, 4, false, 24, LASTR, 4>(rd3, mm, up3); waitl0(); waitv<0>(); bar(); }
    period<16, 4, false, 24, LASTR, 5>(rd3, mm, up4); waitl0(); bar();
    for (int s = 0; s < n4; ++s) { period<16, 4, false, 24, LASTR, 5>(rd4, mm, up4); waitl0(); bar(); }
#pragma unroll
    for (int m = LAG; m < 16; ++m) mm(1, m);
    if (half) {
      const unsigned pb = ((sl + 65536u + wm * 32768u) & 131071u) + wn * 16384 + g * 256;
#pragma unroll
      for (int a = 0; a < 4; ++a)
#pragma unroll
        for (int b = 0; b < 4; ++b) *(LAS f32x4*)(size_t)(pb + (a * 4 + b) * 1024 + ((r15 ^ (4 * b + g)) * 16)) = acc[a][b];
      waitl0();
    } else {
#pragma unroll
      for (int a = 0; a < 4; ++a)
#pragma unroll
        for (int bp = 0; bp < 2; ++bp)
          st128(rws, (u32x4){pk2(acc[a][2 * bp][0], acc[a][2 * bp][1]), pk2(acc[a][2 * bp][2], acc[a][2 * bp][3]), pk2(acc[a][2 * bp + 1][0], acc[a][2 * bp + 1][1]), pk2(acc[a][2 * bp + 1][2], acc[a][2 * bp + 1][3])},
                (a * 2 + bp) * 1024 + g * 256 + ((r15 ^ (8 * wn + 4 * bp + g)) * 16), PART_OFF + (ci * 8 + cnt) * 32768 + wq * 8192);
      waitv<0>();
    }
    bar();
    if (!half && w == 0) stflag(rws, epr, lane, flagb + FK_P * 128 + cnt * 4);
  } else {
    constexpr bool isx = !PROFX; (void)isx;
    unsigned epr, pv;
    {
      u32x4 xr[2][2], gq[2], rgv[2];
      ld32a(epr, rws, 0u, flagb + FK_EP * 128);
#pragma unroll
      for (int q = 0; q < 4; ++q) ld128p(xr[q >> 1][q & 1], rx, lane * 16u, (ci * 128 + 8 * cj + 2 * wq + (q >> 1)) * 2048 + (q & 1) * 1024);
      ld128g(gq[0], gw, lane * 16u); ld128g(gq[1], gw + 512, lane * 16u);
      ra = rx; rb = mk(sel<const u16*>(wq & 1, bim, bre), 1024 * 2048);
#pragma unroll
      for (int q = 0; q < 4; ++q) { va[q] = (16 * hl + 4 * q + ((lane >> 3) & 3)) * 2048 + (((lane & 7) ^ ((4 * q + g) & 7)) * 16) - 1024 * q; vb[q] = vnat(q); }
      auto src1 = [&](int s) AI { const int k = s ^ (s < 2 ? 0 : s < 4 ? (rot & 1) : rot); sa = (ci * 128 + 32 * wq) * 2048 + k * 128; sb = (cj * 64 + 32 * (wq >> 1)) * 2048 + k * 128; sl = ((s + 3) & 3) * 32768; };
      src1(0); ival(G, nowork); src1(1); ival(G, nowork);
      waitv<16>(); PIN(epr); PIN(gq[0]); PIN(gq[1]);
      epr = __builtin_amdgcn_readfirstlane(epr) + 1u;
      {
        float sv[6];
#pragma unroll
        for (int r = 0; r < 2; ++r) {
          PIN(xr[r][0]); PIN(xr[r][1]);
          float a1 = 0.f, a2 = 0.f, a3 = 0.f;
#pragma unroll
          for (int e = 0; e < 8; ++e) { const unsigned v = xr[r][e >> 2][e & 3]; a1 = dot2(v, 0x3F803F80u, a1); a2 = dot2(v, v, a2); a3 = dot2(v, gq[e >> 2][e & 3], a3); }
          sv[r] = a1; sv[2 + r] = a2; sv[4 + r] = a3;
        }
#pragma unroll
        for (int q = 0; q < 6; ++q) PIN(sv[q]);
        waitv<8>(); bar();
        PH(1);
        src1(2); ival(G, [&](auto U) AI { constexpr int u = U.value;
          if constexpr (u < 6) { sv[u] = dppadd<0x140>(dppadd<0x141>(dppadd<0x4E>(dppadd<0xB1>(sv[u])))); PIN(sv[u]); } });
        src1(3); ival(G, nowork);
        const bool r1 = lane & 4;
        float a1 = sel<float>(r1, sv[1], sv[0]), a2 = sel<float>(r1, sv[3], sv[2]), a3 = sel<float>(r1, sv[5], sv[4]);
        waitv<16>(); bar();
        float mean, sd, rstd, qv, gg;
        src1(4); ival(G, [&](auto U) AI { constexpr int u = U.value;
          if constexpr (u == 0) { a1 = sum32(sum16(a1)); PIN(a1); }
          if constexpr (u == 1) { a2 = sum32(sum16(a2)); PIN(a2); }
          if constexpr (u == 2) { a3 = sum32(sum16(a3)); PIN(a3); }
          if constexpr (u == 3) { mean = a1 * (1.f / 1024.f); const float var = __builtin_fmaxf(a2 * (1.f / 1024.f) - mean * mean, 0.f) + 1e-5f; rstd = __builtin_amdgcn_rsqf(var); sd = var * rstd;
                                  PIN(mean); PIN(rstd); PIN(sd); }
          if constexpr (u == 4) { qv = 1.f + fexp(__builtin_fminf(a3, 60.f)); gg = __builtin_amdgcn_rcpf(1.f + fexp(__builtin_fminf(-a3, 80.f))); PIN(qv); PIN(gg); }
          if constexpr (u == 5) { a1 = sel4(lane & 1, lane & 2, -2.302208198f * rstd, -0.1029432397f * rstd * rstd * rstd, qv * sd, gg); PIN(a1); }
          if constexpr (u == 6) { const float mh = rbf(mean), sh = rbf(sd);
                                  a2 = sel4(lane & 1, lane & 2, u2f(pk2(-mh, -mh)), u2f(pk2(mh - mean, sh)), u2f(pk2(sd - sh, 0.f)), 0.f); PIN(a2); }
          if constexpr (u == 7) { if (lane < 16) st64(rws, (u32x2){f2u(sel<float>(lane & 8, a2, a1)), epr}, (lane >> 3) * 4096u + (lane & 7) * 8, rgb + (8 * cj + 2 * wq) * 32); }
        });
      }
      auto T1 = [](int s) constexpr { return (s >= 0 && s < 8) ? 1 : s == 8 ? 2 : 0; };
      const rsrc_t rw1 = mk(w1, 1024 * 7168);
      sfor<11>([&](auto S1) AI { constexpr int s = S1.value + 1;
        waitv<16 + T1(s - 3) + T1(s - 2) + T1(s - 1)>(); bar();
        src1(s + 4); ival(G, nowork);
        if constexpr (s < 8) pfl(dumv, rw1, lane < 8 ? lane * 128u : 0x80000000u, (bid * 224 + wq * 56 + (s - 1) * 8) * 128);
        if constexpr (s == 8) { ld128a(rgv[0], rws, tq * 16u, rgb); ld128a(rgv[1], rws, tq * 16u + 4096, rgb); }
      });
      setup2(); sx = 0; nexts();
      const unsigned sb0 = sb;
      waitv<16>(); bar();
      sfor<4>([&](auto U) AI { G2(IC<0>{}, U); }); nexts();
      const unsigned sb1 = sb;
      {
        auto rbad = [&]() AI { PIN(rgv[0]); PIN(rgv[1]); return ((rgv[0][1] ^ epr) | (rgv[0][3] ^ epr) | (rgv[1][1] ^ epr) | (rgv[1][3] ^ epr)) != 0; };
        if (__builtin_expect(anyl(rbad()), 0))
          for (unsigned sp = 0; sp < SPIN_CAP; ++sp) { __builtin_amdgcn_s_sleep(2);
            ld128a(rgv[0], rws, tq * 16u, rgb); ld128a(rgv[1], rws, tq * 16u + 4096, rgb);
            waitv<0>(); if (!anyl(rbad())) break; }
      }
      sfor<4>([&](auto U) AI { G2(IC<1>{}, U); }); nexts();
      wrf(T_ROW + (tq & 1) * 1024 + (tq >> 1) * 4, u2f(rgv[0][0])); wrf(T_ROW + (tq & 1) * 1024 + 512 + (tq >> 1) * 4, u2f(rgv[0][2]));
      wrf2(T_XA + tq * 8, (f32x2){u2f(rgv[1][0]), u2f(rgv[1][2])});
      waitv<16>(); bar();
      sb = sb0; sfor<7>([&](auto U) AI { G2(IC<0>{}, IC<U.value + 4>{}); });
      sfor<4>([&](auto U) AI { G2(IC<2>{}, U); });
      waitv<19>(); bar();
      sb = sb1; sfor<7>([&](auto U) AI { G2(IC<1>{}, IC<U.value + 4>{}); });
      table3(ta, tb); sx = 3; next3();
    }
    {
      f32x4 ac1[28];
#pragma unroll
      for (int m = 0; m < 28; ++m) ac1[m] = (f32x4){0, 0, 0, 0};
      {
        bf16x8 fa[2][4], fb[2][8];
        const int f = ((q2 >> 1) & 1) | ((g & 1) << 1);
        const unsigned nb = wm * 8192u + r15 * 128, n0 = (g ^ (r15 >> 1)) * 16, n1 = ((4 + g) ^ (r15 >> 1)) * 16, wk = (g >> 1) * 7168u + p * 8;
        const unsigned cb[8] = {nb + wn * 4096 + n0, nb + wn * 4096 + n1, nb + (1 - wn) * 4096 + n0, nb + (1 - wn) * 4096 + n1,
                                wk + (8 * (g & 1) + q2) * 128 + (((2 * wn) ^ f) * 32), wk + (8 * (g & 1) + q2) * 128 + (((2 * wn + 1) ^ f) * 32),
                                wk + 6144 + (8 * (g & 1) + q2) * 64 + (g & 1) * 32, wk + 6144 + (8 * (g & 1) + q2) * 64 + ((g & 1) ^ 1) * 32};
        unsigned ba[8]; unsigned xn = 98304, wnx = 32768;
#pragma unroll
        for (int b = 0; b < 8; ++b) { ba[b] = cb[b] + (b < 4 ? 131072u : 0u); PIN(ba[b]); }
        auto rd = [&](int r) AI { const int ks = r / 20, xx = r % 20;
          if (xx == 0) fa[ks][0] = rd128(ba[ks]);
          else if (xx <= 12) { const int b = (xx - 1) >> 1, h = (xx - 1) & 1; sethalf(fb[ks][b], h, rtr(ba[4 + (b & 1)] + (b >> 1) * 2048 + ks * 14336 + h * 512)); }
          else if (xx <= 16) { const int b = (xx - 1) >> 1, h = (xx - 1) & 1; sethalf(fb[ks][b], h, rtr(ba[b] + ks * 14336 + h * 256)); }
          else { const int a = xx - 16; fa[ks][a] = rd128(ba[(a >> 1) * 2 + ks] + (a & 1) * 2048); } };
        auto mm = [&](int ks, int m) AI { const int a = m < 16 ? m >> 3 : (m - 4) / 6, b = m < 16 ? m & 7 : (m - 4) % 6; ac1[m] = MFMA(fb[ks][b], fa[ks][a], ac1[m]); };
        auto up = [&](int b) AI { if (b < 8) { ba[b] = cb[b] + sel<unsigned>(b < 4, xn, wnx); PIN(ba[b]); }
                                  else { wnx = (wnx == 65536u) ? 0u : wnx + 32768u; xn = (xn == 131072u) ? 98304u : xn + 16384u; } };
        waitl0(); waitv<11>(); bar();
        PH(2);
        period<28, 8, true, 40, LASTR + 24, 9>(rd, mm, up); waitl0(); waitv<0>(); bar();
        for (int s = 1; s < 16; ++s) { period<28, 8, false, 40, LASTR + 24, 9>(rd, mm, up); waitl0(); bar(); }
        PH(3);
        setup3(); sl = 0;
        ival(G, [&](auto U) AI { constexpr int u = U.value;
#pragma unroll
          for (int m = LAG + 4 * u; m < LAG + 4 * u + 4; ++m) if (m < 28) mm(1, m); });
#pragma unroll
        for (int a = 0; a < 4; ++a) fa[0][a] = rd128(g ? T_ZERO : T_XA + (64 * wm + 16 * (a ^ (2 * wn)) + r15) * 16);
#pragma unroll
        for (int b = 0; b < 8; ++b) fb[0][b] = rd128(T_WA + ((b < 6 ? (b >> 1) * 64 + wn * 32 + (b & 1) * 16 : 96 + b * 16) + r15) * 16);
        SB();
        sl = nq(sl); next3();
        bar();
        ival(G, [&](auto U) AI { constexpr int u = U.value;
#pragma unroll
          for (int m = 4 * u; m < 4 * u + 4; ++m) if (m < 28) mm(0, m); });
        sl = nq(sl); next3();
      }
      float k0[4], k1[4], qq[4];
#pragma unroll
      for (int a = 0; a < 4; ++a) { const unsigned ro = T_ROW + (64 * wm + 16 * (a ^ (2 * wn)) + r15) * 4; k0[a] = rdf(ro); k1[a] = rdf(ro + 512); qq[a] = rdf(ro + 1024); }
      u32x4 hw[4];
      auto st3 = [](int s) constexpr { return (s == 4 || s == 8 || s == 12) ? 4 : s == 14 ? 2 : 0; };
      sfor<15>([&](auto S1) AI { constexpr int s = S1.value + 1, v = s - 1, a = v & 3, m0 = (a < 2 ? 8 * a : 4 + 6 * a) + 2 * (v >> 2);
        if constexpr (s == 7 || s == 11) waitv<16>();
        else if constexpr (s >= 2) waitv<16 + st3(s - 3) + st3(s - 2) + st3(s - 1)>();
        if constexpr (s == 12) { if (w == 4) { waitv<8>(); check_flags(pv, rws, fvo, flagb + FK_M1 * 128, lane, 8 * half, 8 * half + 8, epr); } }
        SB(); bar();
        if constexpr (s == 7) { if (w == 5) stflag(rws, epr, lane, flagb + FK_M1 * 128 + cj * 4); }
        if constexpr (s == 11) { if (w == 4) ld32a(pv, rws, fvo, flagb + FK_M1 * 128); if (w == 5) stflag(rws, epr, lane, flagb + FK_M2 * 128 + cj * 4); }
        float t[8];
#define UU(e) ac1[m0 + ((e) >> 2)][(e) & 3]
#define LVL(expr) do { _Pragma("unroll") for (int e = 0; e < 8; ++e) { t[e] = (expr); PIN(t[e]); } } while (0)
        ival(G, [&](auto U) AI { constexpr int u = U.value;
          if constexpr (s < 15) {
            if constexpr (u == 0) LVL(UU(e) * UU(e));
            if constexpr (u == 1) LVL(__builtin_fmaf(t[e], k1[a], k0[a]));
            if constexpr (u == 2) LVL(UU(e) * t[e]);
            if constexpr (u == 3) LVL(__builtin_amdgcn_exp2f(t[e]));
            if constexpr (u == 4) LVL(__builtin_fmaf(t[e], qq[a], qq[a]));
            if constexpr (u == 5) LVL(__builtin_amdgcn_rcpf(t[e]));
            if constexpr (u == 6) LVL(UU(e) * t[e]);
            if constexpr (u == 7) { hw[a] = (u32x4){pk2(t[0], t[1]), pk2(t[2], t[3]), pk2(t[4], t[5]), pk2(t[6], t[7])}; PIN(hw[a]); }
          } });
#pragma unroll
        for (int q = 0; q < st3(s); ++q) st128(rws, hw[q], lane * 16u + (4 * wm + (q ^ (2 * wn))) * 1024, ci * HID_PANEL + kb_of(cj, v < 12 ? 2 * (v >> 2) + wn : 6) * 8192);
        sl = nq(sl);
        if constexpr (s < 11) next3(); else nexts();
        if constexpr (s == 11) {
          rb = mk(w2, 3584 * 2048);
#pragma unroll
          for (int q = 0; q < 4; ++q) vb[q] = (4 * q + g) * 2048 + ((((r15 >> 1) ^ (4 * (q & 1) + g)) * 2) + (lane & 1)) * 16 - 1024 * q;
        }
      });
    }
    PH(4);
    waitv<16 + 2>(); bar(); ival(G, nowork); sl = nq(sl); nexts();
    waitv<16>(); bar();
    if (w == 5) stflag(rws, epr, lane, flagb + FK_M3 * 128 + cj * 4);
    for (int o = 0; o < 2; ++o) {
      if (w == 4) ld32a(pv, rws, fvo, flagb + (FK_M2 + o) * 128);
      for (int s = 0; s < 2; ++s) { ival(G, nowork); sl = nq(sl); nexts(); waitv<16>(); bar(); }
      ival(G, nowork); sl = nq(sl); nexts(); waitv<16>();
      if (w == 4) check_flags(pv, rws, fvo, flagb + (FK_M2 + o) * 128, lane, o ? 0 : 8 * half, 8 * half + 8, epr);
      bar();
      for (int s = 0; s < (o ? n4 - 16 : 5); ++s) { ival(G, nowork); sl = nq(sl); nexts(); waitv<16>(); bar(); }
    }
    if (half) {
      sfor<8>([&](auto U) AI { constexpr int u = U.value; if constexpr ((u & 3) == 0) setm0(sl + wq * 8192 + u * 1024);
        dma<(u & 3) * 1024>(rx, (4u * (u & 3) + g) * 2048 + r15 * 16 - (u & 3) * 1024, (ci * 128 + 32 * wq + 16 * (u >> 2)) * 2048 + cnt * 256); });
      ld32a(pv, rws, fvo, flagb + FK_P * 128);
      waitv<8 + 9>();
    } else waitv<8>();
    bar();
    waitv<0>();
    if (half) {
      check_flags(pv, rws, fvo, flagb + FK_P * 128, lane, cnt, cnt + 1, epr);
      sfor<8>([&](auto U) AI { constexpr int u = U.value; if constexpr ((u & 3) == 0) setm0(nq(sl) + wq * 8192 + u * 1024);
        dma1<(u & 3) * 1024>(rws, lane * 16u, PART_OFF + (ci * 8 + cnt) * 32768 + wq * 8192 + (u >> 2) * 4096); });
      if (cnt == 0 && w == 4) stflag(rws, epr, lane, flagb + FK_EP * 128);
    }
    bar();
    bar();
    PH(5);
    asm volatile("" ::"v"(dumv));
    waitv<0>(); bar();
  }
  if (half) {
    const int bg = r31 & 15, sw = (r31 >> 4) * 8 + (bg >> 3) * 4 + (bg & 3);
    const unsigned pa = ((sl + 65536u + (w >> 2) * 32768u) & 131071u) + (r31 >> 4) * 16384 + (wq * 4 + (bg >> 2)) * 1024 + (bg & 3) * 256;
    const unsigned pp = nq(sl) + ((w >> 2) * 2 + (r31 >> 4)) * 8192 + (wq * 2 + (bg >> 3)) * 1024 + (bg & 3) * 256 + ((bg >> 2) & 1) * 8;
    const f32x4 bv = rdf4(T_OUT + r31 * 16), dv = rdf4(T_OUT + 512 + r31 * 16);
#pragma unroll
    for (int u = 0; u < 8; ++u) {
      const int row = 2 * u + hl;
      const f32x4 av = rdf4(pa + ((row ^ bg) * 16));
      const u32x2 pw = *(LAS u32x2*)(size_t)(pp + ((row ^ sw) * 16)), xw = *(LAS u32x2*)(size_t)(sl + (16 * w + row) * 256 + r31 * 8);
      const float gg = rdf(T_ROW + 1536 + (16 * w + row) * 4);
      const f32x4 pf = {bf2f(pw[0] & 0xffffu), u2f(pw[0] & 0xffff0000u), bf2f(pw[1] & 0xffffu), u2f(pw[1] & 0xffff0000u)};
      const f32x4 xf = {bf2f(xw[0] & 0xffffu), u2f(xw[0] & 0xffff0000u), bf2f(xw[1] & 0xffffu), u2f(xw[1] & 0xffff0000u)};
      f32x4 r;
#pragma unroll
      for (int e = 0; e < 4; ++e) r[e] = (av[e] + pf[e]) + __builtin_fmaf(gg, __builtin_fmaf(xf[e], dv[e], -bv[e]), bv[e]);
      st128<OAUX>(mk(out, 8u << 20), __builtin_bit_cast(u32x4, r), (unsigned)((16 * w + hl + 2 * u) * 4096 + r31 * 16), ci * 128 * 4096 + cnt * 512);
    }
  }
}

extern "C" void kernel_launch(void* const* d_in, const int* in_sizes, int n_in, void* d_out, int out_size, void* d_ws, size_t ws_size, hipStream_t stream) {
  (void)in_sizes; (void)n_in; (void)out_size; (void)ws_size;
  (void)hipFuncSetAttribute((const void*)fused_kernel, hipFuncAttributeMaxDynamicSharedMemorySize, LDS_BYTES);
  fused_kernel<<<256, 512, LDS_BYTES, stream>>>((const u16*)d_in[0], (const u16*)d_in[1], (const u16*)d_in[2], (const u16*)d_in[3], (const u16*)d_in[4],
      (const u16*)d_in[5], (const u16*)d_in[6], (const u16*)d_in[7], (const u16*)d_in[8], (const u16*)d_in[10], (const u16*)d_in[11], (const u16*)d_in[12],
      (const u16*)d_in[13], (const u16*)d_in[14], (char*)d_ws, (float*)d_out);
}
```

```cpp
#include <hip/hip_runtime.h>
#include <utility>

typedef unsigned short u16;
typedef __bf16 bf16x2 __attribute__((ext_vector_type(2)));
typedef __bf16 bf16x4 __attribute__((ext_vector_type(4)));
typedef __bf16 bf16x8 __attribute__((ext_vector_type(8)));
typedef float f32x2 __attribute__((ext_vector_type(2)));
typedef float f32x4 __attribute__((ext_vector_type(4)));
typedef float f32x16 __attribute__((ext_vector_type(16)));
typedef unsigned u32x2 __attribute__((ext_vector_type(2)));
typedef unsigned u32x4 __attribute__((ext_vector_type(4)));
typedef __amdgpu_buffer_rsrc_t rsrc_t;
#define LAS __attribute__((address_space(3)))
#define DI __device__ __forceinline__
#define AI __attribute__((always_inline))
#define SB() __builtin_amdgcn_sched_barrier(0)
#define PIN(x) asm volatile("" : "+v"(x))
#define MFMA(a, b, c) __builtin_amdgcn_mfma_f32_16x16x32_bf16(a, b, c, 0, 0, 0)
#define MFMA32(a, b, c) __builtin_amdgcn_mfma_f32_32x32x16_bf16(a, b, c, 0, 0, 0)
template <int V> using IC = std::integral_constant<int, V>;

constexpr unsigned HID_PANEL = 112 * 8192, H_OFF = 16 * HID_PANEL, H_PANEL = 32 * 16384;
constexpr unsigned PART_OFF = H_OFF + 16 * H_PANEL;
constexpr unsigned GRAN_OFF = PART_OFF + 128 * 32768;
constexpr unsigned RG_OFF = GRAN_OFF + 256 * 1024;
constexpr unsigned CG_OFF = RG_OFF + 16 * 8192;
constexpr unsigned FLAG_OFF = CG_OFF + 256 * 2048;
constexpr unsigned EPC_OFF = FLAG_OFF + 16 * 1024;
constexpr unsigned WS_BYTES = EPC_OFF + 16 * 128;
constexpr int FK_M1 = 0, FK_M2 = 1, FK_M3 = 2, FK_M4 = 3, FK_H = 4, FK_P = 5, FK_EP = 6;
constexpr unsigned TAB = 147456, T_ROW = TAB, T_XA = TAB + 2048, T_OUT = TAB + 4096, T_B1 = TAB + 5120, T_CS2 = TAB + 6144, T_WA = TAB + 8192, T_ZERO = TAB + 12288, T_AGG = T_WA, T_DUM = TAB + 12544, T_CON = TAB + 12800;
constexpr unsigned LDS_BYTES = 163840;
#define XMAP 1
#define ROTM 3
#define OAUX 2
#define MOVE 3
#define PROFX false
constexpr int LAG = 2, LASTR = 25;
constexpr unsigned SPIN_CAP = 1u << 16;

DI rsrc_t mk(const void* p, unsigned bytes) { return __builtin_amdgcn_make_buffer_rsrc((void*)p, 0, bytes, 0x00020000); }
DI void setm0(unsigned lds) { asm volatile("s_mov_b32 m0, %0\n\ts_nop 0" ::"s"(lds) : "memory"); }
template <int OFF> DI void dma(rsrc_t r, unsigned voff, unsigned soff) {
  asm volatile("buffer_load_dwordx4 %0, %1, %2 offen offset:%3 lds" ::"v"(voff), "s"(r), "s"(soff), "i"(OFF) : "memory");
}
template <int OFF> DI void dma1(rsrc_t r, unsigned voff, unsigned soff) {
  asm volatile("buffer_load_dwordx4 %0, %1, %2 offen offset:%3 sc1 lds" ::"v"(voff), "s"(r), "s"(soff), "i"(OFF) : "memory");
}
#define PIN2(x, y) asm volatile("" : "+v"(x), "+v"(y))
#define PINS(x) asm volatile("" : "+s"(x))
typedef const void* const __attribute__((address_space(4)))* kargs_t;
DI const char* karg(int i) { kargs_t ka = (kargs_t)__builtin_amdgcn_kernarg_segment_ptr(); PINS(ka); return (const char*)ka[i]; }
enum { A_X, A_LRE, A_LIM, A_BRE, A_BIM, A_CRE, A_CIM, A_LDT, A_DSK, A_W1, A_B1, A_W2, A_B2, A_GW, A_WS, A_OUT };
template <int N> DI void waitv() { asm volatile("s_waitcnt vmcnt(%0)" ::"i"(N) : "memory"); }
DI void waitl0() { SB(); __builtin_amdgcn_s_waitcnt(0xC07F); asm volatile("" ::: "memory"); }
#define PH(k)
DI void bar() { asm volatile("s_barrier" ::: "memory"); SB(); }
DI bf16x8 rd128(unsigned a) { return *(LAS bf16x8*)(size_t)a; }
DI u32x4 rdu4(unsigned a) { return *(LAS u32x4*)(size_t)a; }
DI f32x4 rdf4(unsigned a) { return *(LAS f32x4*)(size_t)a; }
DI f32x2 rdf2(unsigned a) { return *(LAS f32x2*)(size_t)a; }
DI float rdf(unsigned a) { return *(LAS float*)(size_t)a; }
DI void wrf(unsigned a, float v) { *(LAS float*)(size_t)a = v; }
DI void wrf2(unsigned a, f32x2 v) { *(LAS f32x2*)(size_t)a = v; }
DI bf16x4 rtr(unsigned a) { return __builtin_amdgcn_ds_read_tr16_b64_v4bf16((LAS bf16x4*)(size_t)a); }
DI void sethalf(bf16x8& f, int h, bf16x4 v) {
  if (h == 0) f = __builtin_shufflevector(v, v, 0, 1, 2, 3, -1, -1, -1, -1);
  else { bf16x8 t = __builtin_shufflevector(v, v, -1, -1, -1, -1, 0, 1, 2, 3); f = __builtin_shufflevector(f, t, 0, 1, 2, 3, 12, 13, 14, 15); }
}
DI unsigned f2u(float f) { return __builtin_bit_cast(unsigned, f); }
DI float u2f(unsigned u) { return __builtin_bit_cast(float, u); }
DI float shx(int lane, float v, int m) { return u2f(__builtin_amdgcn_ds_bpermute((lane ^ m) * 4, (int)f2u(v))); }
DI float sum16(float v) { const auto r = __builtin_amdgcn_permlane16_swap(f2u(v), f2u(v), false, false); return u2f(r[0]) + u2f(r[1]); }
DI float sum32(float v) { const auto r = __builtin_amdgcn_permlane32_swap(f2u(v), f2u(v), false, false); return u2f(r[0]) + u2f(r[1]); }
DI float fexp(float v) { return __builtin_amdgcn_exp2f(v * 1.4426950408889634f); }
DI float bf2f(unsigned v) { return u2f(v << 16); }
DI unsigned pk2(float lo, float hi) { f32x2 v = {lo, hi}; return __builtin_bit_cast(unsigned, __builtin_convertvector(v, bf16x2)); }
DI float rbf(float f) { return u2f(pk2(f, 0.f) << 16); }
DI float dot2(unsigned a, unsigned b, float c) { return __builtin_amdgcn_fdot2_f32_bf16(__builtin_bit_cast(bf16x2, a), __builtin_bit_cast(bf16x2, b), c, false); }
template <int C> DI float dppadd(float v) { return v + u2f(__builtin_amdgcn_update_dpp(0, (int)f2u(v), C, 0xf, 0xf, true)); }
DI float sel4(bool b0, bool b1, float a, float b, float c, float d) { PIN(a); PIN(b); PIN(c); PIN(d); const float lo = b0 ? b : a, hi = b0 ? d : c; return b1 ? hi : lo; }
DI f32x2 cmul(f32x2 a, f32x2 b) { return (f32x2){a.x * b.x - a.y * b.y, a.x * b.y + a.y * b.x}; }
DI f32x2 cmulc(f32x2 a, f32x2 b) { return (f32x2){__builtin_fmaf(a.x, b.x, -(a.y * b.y)), __builtin_fmaf(-a.x, b.y, -(a.y * b.x))}; }
DI f32x2 cfma(f32x2 a, f32x2 b, f32x2 c) { return (f32x2){__builtin_fmaf(a.x, b.x, __builtin_fmaf(-a.y, b.y, c.x)), __builtin_fmaf(a.x, b.y, __builtin_fmaf(a.y, b.x, c.y))}; }
template <int AUX = 16> DI void st128(rsrc_t r, u32x4 v, unsigned voff, unsigned soff) {
  __builtin_amdgcn_raw_buffer_store_b128(v, r, voff, soff, AUX);
  asm volatile("s_nop 1" ::"v"(v));
}
DI void st64(rsrc_t r, u32x2 v, unsigned voff, unsigned soff) { __builtin_amdgcn_raw_buffer_store_b64(v, r, voff, soff, 16); }
DI void st32(rsrc_t r, unsigned v, unsigned voff, unsigned soff) { __builtin_amdgcn_raw_buffer_store_b32(v, r, voff, soff, 16); }
DI void stflag(rsrc_t r, unsigned v, int lane, unsigned soff) { if (lane == 0) st32(r, v, 0u, soff); }
DI void ld32a(unsigned& d, rsrc_t r, unsigned voff, unsigned soff) { asm volatile("buffer_load_dword %0, %1, %2, %3 offen sc1" : "=v"(d) : "v"(voff), "s"(r), "s"(soff) : "memory"); }
DI void ld16p(unsigned& d, const u16* p, unsigned voff) { asm volatile("global_load_ushort %0, %1, %2" : "=v"(d) : "v"(voff), "s"(p) : "memory"); }
DI void ld64p(u32x2& d, rsrc_t r, unsigned voff, unsigned soff) { asm volatile("buffer_load_dwordx2 %0, %1, %2, %3 offen" : "=v"(d) : "v"(voff), "s"(r), "s"(soff) : "memory"); }
DI void ld64a(u32x2& d, rsrc_t r, unsigned voff, unsigned soff) { asm volatile("buffer_load_dwordx2 %0, %1, %2, %3 offen sc1" : "=v"(d) : "v"(voff), "s"(r), "s"(soff) : "memory"); }
DI void ld128g(u32x4& d, const u16* p, unsigned voff) { asm volatile("global_load_dwordx4 %0, %1, %2" : "=v"(d) : "v"(voff), "s"(p) : "memory"); }
DI void pfl(unsigned& d, rsrc_t r, unsigned voff, unsigned soff) { asm volatile("buffer_load_dword %0, %1, %2, %3 offen" : "+v"(d) : "v"(voff), "s"(r), "s"(soff) : "memory"); }
template <class T> DI T sel(bool c, T a, T b) { return c ? a : b; }
DI void pfg(const char* p) { asm volatile("global_load_lds_dword %0, off" ::"v"(p) : "memory"); }
DI bool anyl(bool b) { return __builtin_amdgcn_ballot_w64(b) != 0; }
DI void ld128p(u32x4& d, rsrc_t r, unsigned voff, unsigned soff) { asm volatile("buffer_load_dwordx4 %0, %1, %2, %3 offen" : "=v"(d) : "v"(voff), "s"(r), "s"(soff) : "memory"); }
DI void sincos_f(float t, float& sn, float& cs) {
  const float k = __builtin_rintf(t * 0.63661977236f);
  float r = __builtin_fmaf(k, -1.5707962513f, t); r = __builtin_fmaf(k, -7.5497894159e-08f, r); r = __builtin_fmaf(k, -5.3903029534e-15f, r);
  const float z = r * r;
  const float sp = __builtin_fmaf(r * z, __builtin_fmaf(z, __builtin_fmaf(z, __builtin_fmaf(z, 2.7183114939e-06f, -1.9839334836e-04f), 8.3333293858e-03f), -1.6666666642e-01f), r);
  const float cp = __builtin_fmaf(z, __builtin_fmaf(z, __builtin_fmaf(z, __builtin_fmaf(z, 2.4390448796e-05f, -1.3886763775e-03f), 4.1666623324e-02f), -4.9999999726e-01f), 1.f);
  const int q = (int)k;
  const float a = (q & 1) ? cp : sp, b = (q & 1) ? sp : cp;
  sn = (q & 2) ? -a : a; cs = ((q + 1) & 2) ? -b : b;
}
DI void ld128a(u32x4& d, rsrc_t r, unsigned voff, unsigned soff) { asm volatile("buffer_load_dwordx4 %0, %1, %2, %3 offen sc1" : "=v"(d) : "v"(voff), "s"(r), "s"(soff) : "memory"); }

DI unsigned x1off(int a) { return a == 0 ? 131072u : (unsigned)(81920 + a * 16384); }
DI unsigned nq(unsigned s) { return (s + 32768u) & 131071u; }

DI int kb_of(int j, int bl) { return 7 * j + ((j & 1) ? (bl == 6 ? 0 : bl + 1) : bl); }

template <int NR, int LAST> constexpr int nread(int sl) { int n = 0; for (int r = 0; r < NR; ++r) n += (r * LAST) / (NR - 1) <= sl; return n; }
template <int NM, int R0, bool FIRST, int NR, int LAST, int NB, int SL, class RD, class MM, class UP>
DI void slot(RD& rd, MM& mm, UP& upd) {
  constexpr int q = SL + LAG;
  if constexpr (!FIRST && (q == NM || q == NM + R0 || q == 2 * NM)) {
    constexpr int cnt = nread<NR, LAST>(SL - 1) - nread<NR, LAST>(SL - 6);
    __builtin_amdgcn_s_waitcnt(0xC07F | (cnt << 8)); SB();
  }
  if constexpr (!(FIRST && q < NM)) mm((q < NM) ? 1 : ((q < 2 * NM) ? 0 : 1), q % NM);
  SB();
  constexpr int r = (SL * (NR - 1) + LAST - 1) / LAST;
  if constexpr (r < NR && (r * LAST) / (NR - 1) == SL) rd(r);
  constexpr int per = NB > 2 * NM - 1 - LAST ? 2 : 1, u0 = (SL - LAST - 1) * per;
  if constexpr (SL > LAST && u0 < NB) { upd(u0); if constexpr (per == 2 && u0 + 1 < NB) upd(u0 + 1); }
  SB();
}
template <int NM, int R0, bool FIRST, int NR, int LAST, int NB, class RD, class MM, class UP, int... I>
DI void period_(RD& rd, MM& mm, UP& upd, std::integer_sequence<int, I...>) { (slot<NM, R0, FIRST, NR, LAST, NB, I>(rd, mm, upd), ...); }
template <int NM, int R0, bool FIRST, int NR, int LAST, int NB, class RD, class MM, class UP>
DI void period(RD& rd, MM& mm, UP& upd) {
  static_assert(NR - 1 <= LAST && NB <= 2 * (2 * NM - 1 - LAST), "");
  period_<NM, R0, FIRST, NR, LAST, NB>(rd, mm, upd, std::make_integer_sequence<int, 2 * NM>{});
}
template <bool FIRST, int SL, class RD, class MM, class UP> DI void slot32(RD& rd, MM& mm, UP& upd) {
  if constexpr (SL == 12) { __builtin_amdgcn_s_waitcnt(0xC07F | (8 << 8)); SB(); }
  if constexpr (!(FIRST && SL < 12)) mm((SL + 4) & 15);
  SB();
  if constexpr (SL < 2) { rd(2 * SL); rd(2 * SL + 1); } else if constexpr (SL < 14) rd(SL + 2);
  if constexpr ((SL & 3) == 2) { upd(SL >> 2); upd(4 + (SL >> 2)); }
  SB();
}
template <bool FIRST, class RD, class MM, class UP, int... I> DI void period32_(RD& rd, MM& mm, UP& upd, std::integer_sequence<int, I...>) { (slot32<FIRST, I>(rd, mm, upd), ...); }
template <bool FIRST, class RD, class MM, class UP> DI void period32(RD& rd, MM& mm, UP& upd) { period32_<FIRST>(rd, mm, upd, std::make_integer_sequence<int, 16>{}); }
template <class G, class W, int... U> DI void ival_(G& g, W& wk, std::integer_sequence<int, U...>) { ((g(IC<U>{}), SB(), wk(IC<U>{}), SB()), ...); }
template <class G, class W> DI void ival(G&& g, W&& wk) { ival_(g, wk, std::make_integer_sequence<int, 8>{}); }
template <int N, class F, int... I> DI void sfor_(F& f, std::integer_sequence<int, I...>) { (f(IC<I>{}), ...); }
template <int N, class F> DI void sfor(F&& f) { sfor_<N>(f, std::make_integer_sequence<int, N>{}); }

DI void check_flags(unsigned& pv, rsrc_t ws, unsigned voff, unsigned soff, int lane, int lo, int hi, unsigned ep) {
  PIN(pv);
  const bool need = lane >= lo && lane < hi;
  if (__builtin_expect(anyl(need && pv != ep), 0)) {
    for (unsigned sp = 0; sp < SPIN_CAP; ++sp) {
      __builtin_amdgcn_s_sleep(2);
      ld32a(pv, ws, voff, soff); asm volatile("s_waitcnt vmcnt(0)" : "+v"(pv)::"memory");
      if (!anyl(need && pv != ep)) break;
    }
  }
}

#if defined(__HIP_DEVICE_COMPILE__) && __HIP_DEVICE_COMPILE__
__attribute__((target("no-packed-fp32-ops")))
#endif
__global__ __launch_bounds__(512, 2) void fused_kernel(const u16* x, const u16* lre, const u16* lim, const u16* bre, const u16* bim, const u16* cre, const u16* cim,
    const u16* ldt, const u16* dsk, const u16* w1, const u16* b1, const u16* w2, const u16* b2, const u16* gw, char* ws, float* out) {
  extern __shared__ __attribute__((aligned(16))) char shm[];
  unsigned long long pc0; asm volatile("s_getpc_b64 %0" : "=s"(pc0));
  const int lane = threadIdx.x & 63, w = __builtin_amdgcn_readfirstlane(threadIdx.x >> 6);
  const int bid = blockIdx.x, xcd = bid & 7, loc = bid >> 3;
  const int ci = (xcd & 3) * 4 + (loc & 3), cj = (loc >> 4) * 8 + (xcd >> 2) * 4 + ((loc >> 2) & 3);
  const int rot = (ci + cj) & ROTM, cnt = cj & 7, half = cj >> 3, wq = w & 3, tq = threadIdx.x & 255;
  const int n4 = half ? 28 + MOVE : 28 - MOVE;
  const rsrc_t rws = mk(ws, WS_BYTES), rx = mk(x, 2048 * 2048);
  const int wm = (w >> 1) & 1, wn = w & 1, g = lane >> 4, r15 = lane & 15, q2 = (lane >> 2) & 3, p = lane & 3, hl = lane >> 5, r31 = lane & 31;
  const unsigned flagb = FLAG_OFF + ci * 1024, fvo = (lane & 15) * 4;
  unsigned rgb = RG_OFF + ci * 8192; PINS(rgb);
  auto nowork = [&](auto) AI {};
  unsigned va[4], vb[4], vw[7], sl, sa, sb, ta, tb; int sx; rsrc_t ra, rb;
  auto vnat = [&](int u) AI { return (8u * u + (lane >> 3)) * 2048 + (((lane & 7) ^ ((4 * u + g) & 7)) * 16) - 1024 * u; };
  auto nexts = [&]() AI { sa = __builtin_amdgcn_readlane(ta, sx); sb = __builtin_amdgcn_readlane(tb, sx); ++sx; PINS(sa); PINS(sb); };
  auto G = [&](auto U) AI { constexpr int u = U.value;
    if constexpr (u == 0) setm0(sl + wq * 4096);
    if constexpr (u == 4) setm0(sl + 16384 + wq * 4096);
    if constexpr (u < 4) dma1<u * 1024>(ra, va[u], sa); else dma<(u - 4) * 1024>(rb, vb[u - 4], sb);
  };
  auto G2 = [&](auto S, auto U) AI { constexpr int a = S.value % 3, u = U.value;
    if constexpr (u == 0) setm0((a == 0 ? 131072u : 81920u + a * 16384u) + wq * 4096);
    if constexpr (u == 4) setm0(a * 32768 + wq * 7168);
    if constexpr (u == 8) setm0(a * 32768 + wq * 7168 + 4096);
    if constexpr (u < 4) dma<u * 1024>(rx, va[u], sa); else dma<(u & 3) * 1024>(rb, vw[u - 4], sb);
  };
  const int c7 = ci & 7;
  auto setup2 = [&]() AI {
    rb = mk((const char*)w1 - 4096, 1024 * 7168 + 4096);
#pragma unroll
    for (int q = 0; q < 4; ++q) va[q] = vnat(q);
#pragma unroll
    for (int q = 0; q < 6; ++q) { const int k = 8 * (q & 1) + (lane >> 3), f = ((k >> 1) & 1) | ((q & 1) << 1);
      vw[q] = 4096 + k * 7168 + ((cj & 1) ? 64 : 0) + (q >> 1) * 128 + (((((lane & 7) >> 1) ^ f) * 2) + (lane & 1)) * 16 - (q & 3) * 1024; }
    vw[6] = 4096 + (lane >> 2) * 7168 + ((cj & 1) ? 0 : 384) + (((((lane & 3) >> 1) ^ hl) * 2) + (lane & 1)) * 16 - 2048;
    int k = lane - 2 + (c7 < 2 ? 3 * c7 : c7 - 2); k = 2 + (k >= 6 ? k - 6 : k);
    k = lane < 2 ? lane : lane < 8 ? k : (lane ^ rot);
    k = (k ^ (ci & 8)) & 15;
    ta = (ci * 128 + 32 * wq) * 2048 + k * 128; tb = (k * 64 + 16 * wq) * 7168 + cj * 448;
  };
  auto setup3 = [&]() AI {
    ra = rws;
#pragma unroll
    for (int q = 0; q < 4; ++q) { va[q] = lane * 16; vb[q] = vnat(q); }
  };
  auto table3 = [&](unsigned& ta, unsigned& tb) AI {
    const int ks = 16 * half + (lane ^ rot);
    int n = 2 * (lane - 16) + (wq >> 1), hp = half; if (n >= 56) { n -= 2 * MOVE; hp = 0; }
    const int kb = kb_of(hp * 8 + ((n ^ rot) & 7), n >> 3);
    ta = lane < 16 ? H_OFF + ci * H_PANEL + ks * 16384 + wq * 4096 : ci * HID_PANEL + kb * 8192 + (wq & 1) * 4096;
    tb = lane < 16 ? (cnt * 128 + 32 * wq) * 2048 + (ks >> 1) * 128 + (ks & 1) : (kb * 32 + 16 * (wq & 1)) * 2048 + cnt * 256;
    PIN(ta); PIN(tb);
  };
  auto next3 = [&]() AI { nexts(); rb = mk(sel<const u16*>(sb & 1, cim, cre), 1024 * 2048); sb &= ~1u; };

  if (w < 4) {
    constexpr bool isx = PROFX; (void)isx;
    unsigned epr, epc;
    const unsigned ca = (32 * wn + r31) * 8;
    const int seg0 = 4 * wm + hl;
    f32x2 lam, cof, l16, l128, l1k, pw0, pw1; unsigned t3a, t3b;
    {
      setm0(T_DUM);
      const int row = bid * 8 + (lane & 7);
      const char* pp = (const char*)(hl ? (row < 1024 ? bre : bim - 1024 * 1024) : x) + (size_t)row * 2048 + (4 * wq + ((lane >> 3) & 3)) * 128;
      for (int q = 0; q <= wq; ++q) __builtin_amdgcn_s_sleep(6);
      pfg(pp);
      pfg((const char*)pc0 + (((loc * 4 + wq) % 24) * 16 + (lane & 15)) * 128);
      ld32a(epr, rws, 0u, flagb + FK_EP * 128); ld32a(epc, rws, 0u, EPC_OFF + cj * 128);
      setup2(); sx = 2; nexts(); table3(t3a, t3b);
#pragma unroll
      for (int q = 0; q < 7; ++q) { PIN(vw[q]); PIN(va[q & 3]); }
      wrf(T_ZERO + (lane & 3) * 4, 0.f);
    }
    {
      f32x16 acc[2][2];
#pragma unroll
      for (int a = 0; a < 4; ++a)
#pragma unroll
        for (int e = 0; e < 16; ++e) acc[a >> 1][a & 1][e] = 0.f;
      bf16x8 wv[4][2];
      const int sstar = c7 < 2 ? c7 : 2;
      const int fsw = ((q2 >> 1) & 1) | ((g & 1) << 1);
      unsigned cf = sstar * 32768 + (g >> 1) * 7168 + (8 * (g & 1) + q2) * 128 + ((wq ^ fsw) * 32) + p * 8;
      unsigned ch = sstar * 32768 + (g >> 1) * 7168 + 6144 + (8 * (g & 1) + q2) * 64 + (((wq & 1) ^ (g & 1)) * 32) + p * 8;
      PIN(cf); PIN(ch);
      auto wread = [&](int st) AI {
        if (sstar == st) {
#pragma unroll
          for (int e = 0; e < 4; ++e)
#pragma unroll
            for (int t = 0; t < 4; ++t) sethalf(wv[t][e >> 1], e & 1, rtr((e >> 1) * 14336 + (t < 3 ? cf + t * 2048 + (e & 1) * 512 : ch + (e & 1) * 256)));
        } };
      {
        bf16x8 fa[4][2], fb[4][2];
        unsigned cb[8], ba[8], rs = 98304;
#pragma unroll
        for (int b = 0; b < 8; ++b) { cb[b] = (b < 4 ? wm * 8192u : 16384 + wn * 8192u) + r31 * 128 + (((2 * (b & 3) + hl) ^ ((r31 >> 1) & 7)) * 16); ba[b] = rs + cb[b]; PIN(ba[b]); }
        auto rd = [&](int r) AI { const int k16 = r >> 2, xx = r & 3;
          if (xx == 0) fa[k16][0] = rd128(ba[k16]); else if (xx == 3) fa[k16][1] = rd128(ba[k16] + 4096); else fb[k16][xx - 1] = rd128(ba[4 + k16] + (xx - 1) * 4096); };
        auto mm = [&](int m) AI { const int k16 = m >> 2, mt = (m >> 1) & 1, nt = m & 1; acc[mt][nt] = MFMA32(fa[k16][mt], fb[k16][nt], acc[mt][nt]); };
        auto up = [&](int b) AI { if (b == 0) rs = nq(rs); ba[b] = rs + cb[b]; PIN(ba[b]); };
        const char* pp = lane < 56 ? (const char*)w1 + ((bid * 4 + wq) * 56 + lane) * 128
                                    : lane < 60 ? ws + ci * HID_PANEL + (lane & 3) * (HID_PANEL / 4) + wq * 128 : lane < 62 ? ws + H_OFF + ci * H_PANEL + (lane & 1) * (H_PANEL / 2) + wq * 128
                                    : lane == 62 ? ws + PART_OFF + (ci * 8 + cnt) * 32768 : (const char*)out + (size_t)(ci * 128 + wq * 32) * 4096;
        PIN(pp);
        waitl0(); bar();
        period32<true>(rd, mm, up); waitl0(); bar();
        const int sp = 4 + bid % 6;
        for (int o = 0; o < 2; ++o) {
          for (int s = o ? sp : 1; s < (o ? 16 : sp); ++s) { period32<false>(rd, mm, up); waitl0(); bar(); }
          if (o == 0) pfg(pp);
        }
        waitv<0>(); PIN(epr); PIN(epc);
        epr = __builtin_amdgcn_readfirstlane(epr) + 1u; epc = __builtin_amdgcn_readfirstlane(epc) + 1u;
        lam = rdf2(T_CON + ca);
        wread(0);
        sfor<7>([&](auto U) AI { G2(IC<2>{}, IC<U.value + 4>{}); SB(); if constexpr (U.value < 6) { mm(4 + 2 * U.value); mm(5 + 2 * U.value); } SB(); });
        nexts();
      }
      f32x2 st[2] = {{0, 0}, {0, 0}}, Pf[2] = {{0, 0}, {0, 0}}, Gt = {0, 0}, cr = {0, 0}, ag[8];
      u32x4 lb[8], cg[8]; unsigned lbo[8], lbe[8], ob[2][2][4], pv, gd = 0;
      f32x4 cs[4]; f32x2 so[2]; float c0s = 0.f, c1s = 0.f;
      const rsrc_t rgr = mk(ws + GRAN_OFF + cj * 16384, 16384);
      const int cp = 28 * wq + (r31 < 28 ? r31 : 27);
      const unsigned cgo = hl * 16384 + cp * 16;
      auto bad = [&]() AI { unsigned d = 0;
#pragma unroll
        for (int q = 0; q < 8; ++q) { PIN(lb[q]); d |= (int)lbo[q] < 0 ? 0u : ((lb[q][1] ^ epc) | (lb[q][3] ^ epc)); }
        return d != 0; };
      auto gbad = [&]() AI { unsigned d = 0;
#pragma unroll
        for (int q = 0; q < 8; ++q) { PIN(cg[q]); d |= (cg[q][1] ^ epc) | (cg[q][3] ^ epc); }
        return d != 0; };
      auto E2 = [](int s) constexpr { return (s == 2 || s == 3) ? 1 : s == 4 ? 8 : (s == 10 || s == 11) ? 4 : 0; };
      auto T2 = [](int s) constexpr { return (s == 7 || s == 8) ? 4 : 0; };
      sfor<15>([&](auto S) AI { constexpr int s = S.value;
        if constexpr (s < 3) waitl0();
        if constexpr (s == 10) waitv<11 + E2(9)>();
        else if constexpr (s >= 1) waitv<T2(s - 2) + (s < 14 ? 11 : 8) + E2(s - 1) + T2(s - 1)>();
        if constexpr (s == 13) { if (w == 0) { waitv<8>(); check_flags(pv, rws, fvo, flagb + FK_H * 128, lane, 8 * half, 8 * half + 8, epr); } }
        bar();
        if constexpr (s < 2) wread(s + 1);
        if constexpr (s == 2) {
#pragma unroll
          for (int sg = 0; sg < 8; ++sg) ag[sg] = rdf2(T_AGG + sg * 512 + ca);
          l16 = rdf2(T_CON + 1024 + ca); cof = rdf2(T_CON + 512 + ca);
        }
        if constexpr (s == 5) { pw1 = rdf2(T_CON + 1536 + ca); pw0 = rdf2(T_CON + 2048 + ca); l128 = rdf2(T_CON + 2560 + ca); l1k = rdf2(T_CON + 3072 + ca); }
        if constexpr (s == 10) { if (w == 0) stflag(rws, epr, lane, flagb + FK_H * 128 + cj * 4); }
        SB();
        auto work = [&](auto U) AI { constexpr int u = U.value;
          if constexpr (s < 2 && u >= 1 && u < 9) {
            constexpr int t = 8 * s + u - 1;
#pragma unroll
            for (int mt = 0; mt < 2; ++mt) st[mt] = cfma(lam, st[mt], (f32x2){acc[mt][0][t], acc[mt][1][t]});
            PIN2(st[0], st[1]);
          }
          if constexpr (s == 1 && u == 9) { wrf2(T_AGG + seg0 * 512 + ca, st[0]); wrf2(T_AGG + (seg0 + 2) * 512 + ca, st[1]); }
          if constexpr (s == 2 && u >= 1 && u < 9) {
            constexpr int sg = u - 1, mt = (sg >> 1) & 1; const bool me = sg == seg0 + 2 * mt;
            Pf[mt].x = sel<float>(me, Gt.x, Pf[mt].x); Pf[mt].y = sel<float>(me, Gt.y, Pf[mt].y); Gt = cfma(l16, Gt, ag[sg]);
            PIN2(Gt, Pf[mt]);
          }
          if constexpr (s == 2 && u == 9) st64(rgr, (u32x2){f2u(sel<float>(hl, Gt.y, Gt.x)), epc}, ca * 2 + hl * 8, ci * 1024);
          if constexpr (s == 3 && u < 4) {
            const bf16x8 one = __builtin_bit_cast(bf16x8, (u32x4){0x3F803F80u, 0x3F803F80u, 0x3F803F80u, 0x3F803F80u});
            cs[u] = MFMA(one, wv[u][1], MFMA(one, wv[u][0], ((f32x4){0, 0, 0, 0}))); PIN(cs[u]);
          }
          if constexpr (s == 3 && u == 9)
            st64(rws, (u32x2){f2u(sel4(g & 1, g & 2, cs[0][0], cs[1][0], cs[2][0], cs[3][0])), epc}, (g < 3 ? g * 64 + wq * 16 + r15 : 192 + (wq & 1) * 16 + r15) * 8u, CG_OFF + (cj * 16 + ci) * 2048);
          if constexpr ((s == 3 && u >= 4 && u < 9) || (s == 4 && u < 5) || (s == 5 && u >= 1 && u < 7)) {
            constexpr int t = s == 3 ? u - 4 : s == 4 ? 5 + u : 9 + u;
            f32x2 v0 = cmulc(cof, (f32x2){acc[0][0][t], acc[0][1][t]}), v1 = cmulc(cof, (f32x2){acc[1][0][t], acc[1][1][t]}); PIN2(v0, v1);
            acc[0][0][t] = v0.x; acc[0][1][t] = v0.y; acc[1][0][t] = v1.x; acc[1][1][t] = v1.y;
          }
          if constexpr (s == 4 && u >= 6 && u < 10) {
#pragma unroll
            for (int q = 2 * u - 12; q < 2 * u - 10; ++q) { lbo[q] = (ci - 8 - 8 * hl + q) * 1024 + ca * 2; ld128a(lb[q], rgr, lbo[q], 0u); }
          }
          if constexpr (s == 5 && u == 8) {
            if (!wm) pw0 = (f32x2){1.f, 0.f};
            pw0 = cmul(pw0, (f32x2){sel<float>(hl, l16.x, 1.f), sel<float>(hl, l16.y, 0.f)}); pw1 = cmul(pw0, pw1); PIN2(pw0, pw1);
          }
          if constexpr (s == 5 && (u == 7 || u == 9)) {
#pragma unroll
            for (int q = 2 * u - 14; q < 2 * u - 10; ++q) { lbe[q] = (int)lbo[q] < 0 ? 0u : epc; PIN(lbe[q]); }
          }
          if constexpr (s == 6 && u < 8) {
            PIN(lb[u]); gd |= (lb[u][1] ^ lbe[u]) | (lb[u][3] ^ lbe[u]); cr = cfma(l128, cr, (f32x2){u2f(lb[u][0]), u2f(lb[u][2])});
            PIN2(cr, gd);
          }
          if constexpr (s == 6 && u == 8) {
            if (__builtin_expect(anyl(gd != 0), 0)) {
              for (unsigned sp = 0; sp < SPIN_CAP; ++sp) {
                __builtin_amdgcn_s_sleep(2);
#pragma unroll
                for (int q = 0; q < 8; ++q) ld128a(lb[q], rgr, lbo[q], 0u);
                waitv<0>();
                if (!anyl(bad())) break;
              }
              cr = (f32x2){0, 0};
#pragma unroll
              for (int q = 0; q < 8; ++q) cr = cfma(l128, cr, (f32x2){u2f(lb[q][0]), u2f(lb[q][2])});
            }
            gd = 0;
            const auto sx_ = __builtin_amdgcn_permlane32_swap(f2u(cr.x), f2u(cr.x), false, false), sy_ = __builtin_amdgcn_permlane32_swap(f2u(cr.y), f2u(cr.y), false, false);
            cr = cfma(l1k, (f32x2){u2f(sx_[1]), u2f(sy_[1])}, (f32x2){u2f(sx_[0]), u2f(sy_[0])});
            PIN(cr);
          }
          if constexpr (s == 6 && u == 9) { st[0] = cmulc(cof, cfma(pw0, cr, Pf[0])); PIN(st[0]); }
          if constexpr (s == 7 && u == 0) { st[1] = cmulc(cof, cfma(pw1, cr, Pf[1])); PIN(st[1]); }
          if constexpr ((s == 7 && u >= 2 && u < 10) || (s == 8 && u >= 1 && u < 9)) {
            constexpr int t = s == 7 ? u - 2 : 7 + u;
            if constexpr (t & 1) { so[0] = st[0]; so[1] = st[1]; }
#pragma unroll
            for (int mt = 0; mt < 2; ++mt) st[mt] = cfma((f32x2){lam.x, -lam.y}, st[mt], (f32x2){acc[mt][0][t], acc[mt][1][t]});
            if constexpr (t & 1) {
#pragma unroll
              for (int mt = 0; mt < 2; ++mt) { ob[mt][0][(t >> 1) & 3] = pk2(so[mt].x, st[mt].x); ob[mt][1][(t >> 1) & 3] = pk2(so[mt].y, st[mt].y); PIN2(ob[mt][0][(t >> 1) & 3], ob[mt][1][(t >> 1) & 3]); }
            }
            PIN2(st[0], st[1]);
          }
          if constexpr ((s == 7 || s == 8) && u == 10) {
            constexpr int hf = s - 7;
#pragma unroll
            for (int q = 0; q < 4; ++q) { const int mt = q >> 1, pt = q & 1;
              st128(rws, (u32x4){ob[mt][pt][0], ob[mt][pt][1], ob[mt][pt][2], ob[mt][pt][3]},
                    (2 * wn + (r31 >> 4)) * 4096 + wm * 2048 + hf * 1024 + (2 * mt + hl) * 256 + ((r15 ^ (hf << 2)) * 16), H_OFF + ci * H_PANEL + (2 * cj + pt) * 16384); }
          }
          if constexpr (s == 9 && u == 0) { if (ci == 15 && w == 1) stflag(rws, epc, lane, EPC_OFF + cj * 128); }
          if constexpr (s == 12 && u == 2) { if (w == 0) ld32a(pv, rws, fvo, flagb + FK_H * 128); }
          if constexpr ((s == 10 || s == 11) && (u & 1) && u < 8) { constexpr int q = 4 * (s - 10) + (u >> 1); ld128a(cg[q], rws, cgo, CG_OFF + (cj * 16 + q) * 2048); }
          if constexpr (s == 13 && u < 4) {
#pragma unroll
            for (int q = 2 * u; q < 2 * u + 2; ++q) { PIN(cg[q]); gd |= (cg[q][1] ^ epc) | (cg[q][3] ^ epc); c0s += u2f(cg[q][0]); c1s += u2f(cg[q][2]); }
            PIN(gd); PIN(c0s); PIN(c1s);
          }
          if constexpr (s == 13 && u == 4) {
            if (__builtin_expect(anyl(gd != 0), 0)) {
              for (unsigned sp = 0; sp < SPIN_CAP; ++sp) { __builtin_amdgcn_s_sleep(2);
#pragma unroll
                for (int q = 0; q < 8; ++q) ld128a(cg[q], rws, cgo, CG_OFF + (cj * 16 + q) * 2048);
                waitv<0>(); if (!anyl(gbad())) break; }
              c0s = 0.f; c1s = 0.f;
#pragma unroll
              for (int q = 0; q < 8; ++q) { c0s += u2f(cg[q][0]); c1s += u2f(cg[q][2]); }
            }
            c0s = sum32(c0s); c1s = sum32(c1s); PIN(c0s); PIN(c1s);
          }
          if constexpr (s == 13 && u == 6) {
            const float v = sel<float>(hl, c1s, c0s), hi = rbf(v), bb = rdf(T_B1 + (2 * cp + hl) * 4);
            *(LAS u32x4*)(size_t)(T_WA + (2 * cp + hl) * 16) = (u32x4){pk2(hi, v - hi), pk2(hi, bb), pk2(bb, 0.f), 0u};
          }
        };
        if constexpr (s < 13) { sfor<11>([&](auto U) AI { G2(IC<s + 3>{}, U); SB(); work(U); SB(); }); if constexpr (s < 12) nexts(); }
        if constexpr (s == 12) { ra = rws;
#pragma unroll
          for (int q = 0; q < 4; ++q) { vb[q] = va[q]; va[q] = lane * 16; }
          ta = t3a; tb = t3b; sl = 32768; sx = 0; next3(); }
        if constexpr (s >= 13) { ival(G, work); sl = nq(sl); next3(); asm volatile("s_nop 4"); }
        if constexpr (s == 14) ival(G, nowork);
      });
      waitl0(); waitv<16>(); bar();
    }
    f32x4 acc[4][4];
#pragma unroll
    for (int a = 0; a < 4; ++a)
#pragma unroll
      for (int b = 0; b < 4; ++b) acc[a][b] = (f32x4){0, 0, 0, 0};
    bf16x8 fa[2][4], fb[2][4];
    const int kap = 4 * (g & 1) + q2, hf = p >> 1;
    const unsigned wb = 16384 + (4 * g + q2) * 256 + p * 8, hb = (g >> 1) * 4096 + wm * 2048 + hf * 1024 + (8 * (g & 1) + q2) * 16 + (p & 1) * 8;
    const unsigned cb3[4] = {hb + 64u * hf, hb + 64u * (1 ^ hf), 16384 + wn * 8192u + r15 * 128 + ((g ^ (r15 >> 1)) * 16), 16384 + wn * 8192u + r15 * 128 + (((4 + g) ^ (r15 >> 1)) * 16)};
    const unsigned cb4[5] = {wm * 4096u + lane * 16, wb + (((4 * wn + 0) ^ kap) * 32), wb + (((4 * wn + 1) ^ kap) * 32), wb + (((4 * wn + 2) ^ kap) * 32), wb + (((4 * wn + 3) ^ kap) * 32)};
    unsigned ba[5];
    sl = 32768;
#pragma unroll
    for (int b = 0; b < 4; ++b) { ba[b] = sl + cb3[b]; PIN(ba[b]); }
    auto rd3 = [&](int r) AI { const int ks = r / 12, xx = r % 12;
      if (xx < 2) sethalf(fa[ks][0], xx, rtr(ba[xx] + ks * 8192));
      else if (xx < 6) fb[ks][xx - 2] = rd128(ba[2 + ks] + (xx - 2) * 2048);
      else { const int a = (xx - 4) >> 1, h = xx & 1; sethalf(fa[ks][a], h, rtr(ba[h] + ks * 8192 + a * 256)); } };
    auto rd4 = [&](int r) AI { const int ks = r / 12, xx = r % 12;
      if (xx == 0) fa[ks][0] = rd128(ba[0] + ks * 8192);
      else if (xx <= 8) { const int b = (xx - 1) >> 1, h = (xx - 1) & 1; sethalf(fb[ks][b], h, rtr(ba[1 + b] + ks * 8192 + h * 4096)); }
      else fa[ks][xx - 8] = rd128(ba[0] + ks * 8192 + (xx - 8) * 1024); };
    auto mm = [&](int ks, int m) AI { acc[m >> 2][m & 3] = MFMA(fb[ks][m & 3], fa[ks][m >> 2], acc[m >> 2][m & 3]); };
    auto up3 = [&](int b) AI { if (b == 0) sl = nq(sl); ba[b] = sl + cb3[b]; PIN(ba[b]); };
    auto up4 = [&](int b) AI { if (b == 0) sl = nq(sl); ba[b] = sl + cb4[b]; PIN(ba[b]); };
    period<16, 4, true, 24, LASTR, 4>(rd3, mm, up3); waitl0(); waitv<8>(); bar();
    for (int s = 1; s < 15; ++s) { period<16, 4, false, 24, LASTR, 4>(rd3, mm, up3); waitl0(); waitv<0>(); bar(); }
    period<16, 4, false, 24, LASTR, 5>(rd3, mm, up4); waitl0(); bar();
    {
      float gr[4]; int seen = 0;
#pragma unroll
      for (int a = 0; a < 4; ++a) gr[a] = rdf(T_ROW + 1536 + (64 * wm + 16 * a + r15) * 4);
      auto mmg = [&](int ks, int m) AI { mm(ks, m); ++seen; const int t = seen - 7;
        if (t >= 0 && t < 16) { acc[t >> 2][t & 3] *= gr[t >> 2]; PIN(acc[t >> 2][t & 3]); } };
      period<16, 4, false, 24, LASTR, 5>(rd4, mmg, up4); waitl0(); bar();
    }
    for (int s = 1; s < n4; ++s) { period<16, 4, false, 24, LASTR, 5>(rd4, mm, up4); waitl0(); bar(); }
#pragma unroll
    for (int m = LAG; m < 16; ++m) mm(1, m);
    if (half) {
      const unsigned pb = ((sl + 65536u + wm * 32768u) & 131071u) + wn * 16384 + g * 256;
#pragma unroll
      for (int a = 0; a < 4; ++a)
#pragma unroll
        for (int b = 0; b < 4; ++b) *(LAS f32x4*)(size_t)(pb + (a * 4 + b) * 1024 + ((r15 ^ (4 * b + g)) * 16)) = acc[a][b];
      waitl0();
    } else {
#pragma unroll
      for (int a = 0; a < 4; ++a)
#pragma unroll
        for (int bp = 0; bp < 2; ++bp)
          st128(rws, (u32x4){pk2(acc[a][2 * bp][0], acc[a][2 * bp][1]), pk2(acc[a][2 * bp][2], acc[a][2 * bp][3]), pk2(acc[a][2 * bp + 1][0], acc[a][2 * bp + 1][1]), pk2(acc[a][2 * bp + 1][2], acc[a][2 * bp + 1][3])},
                (a * 2 + bp) * 1024 + g * 256 + ((r15 ^ (8 * wn + 4 * bp + g)) * 16), PART_OFF + (ci * 8 + cnt) * 32768 + wq * 8192);
      waitv<0>();
    }
    bar();
    if (!half && w == 0) stflag(rws, epr, lane, flagb + FK_P * 128 + cnt * 4);
  } else {
    constexpr bool isx = !PROFX; (void)isx;
    unsigned epr, pv, t3a, t3b;
    {
      u32x4 xr[2][2], gq[2], rgv[2];
      ra = rx; rb = mk(sel<const u16*>(wq & 1, bim, bre), 1024 * 2048);
#pragma unroll
      for (int q = 0; q < 4; ++q) { va[q] = (16 * hl + 4 * q + ((lane >> 3) & 3)) * 2048 + (((lane & 7) ^ ((4 * q + g) & 7)) * 16) - 1024 * q; vb[q] = vnat(q); }
      auto src1 = [&](int s) AI { const int k = s ^ (s < 2 ? 0 : s < 4 ? (rot & 1) : rot); sa = (ci * 128 + 32 * wq) * 2048 + k * 128; sb = (cj * 64 + 32 * (wq >> 1)) * 2048 + k * 128; sl = ((s + 3) & 3) * 32768; };
      src1(0); ival(G, nowork); src1(1); ival(G, nowork);
      waitv<8>(); bar();
      PH(1);
      src1(2); ival(G, nowork);
      unsigned pdt, plr, pli, bq, oq; f32x2 lp; float lr, li;
      const int tc = tq < 224 ? tq : 223;
      ld32a(epr, rws, 0u, flagb + FK_EP * 128); ld128g(gq[0], gw, lane * 16u); ld128g(gq[1], gw + 512, lane * 16u);
      ld16p(pdt, ldt + 64 * cj, lane * 2u); ld16p(plr, lre + 64 * cj, lane * 2u); ld16p(pli, lim + 64 * cj, lane * 2u);
      ld16p(bq, b1, (224 * cj + (tc < 192 ? ((cj & 1) ? 32 : 0) + tc : ((cj & 1) ? -192 : 0) + tc)) * 2u);
      ld16p(oq, sel<const u16*>(wq < 2, b2, dsk), (cnt * 128 + (tq & 127)) * 2u);
      src1(3); ival(G, nowork);
      float sv[6] = {0, 0, 0, 0, 0, 0}, a1, a2, a3, mean, sd, rstd, qv, gg;
      auto E1 = [](int s) constexpr { return (s == 1 || s == 2) ? 2 : s == 8 ? 1 : s == 11 ? 2 : 0; };
      sfor<12>([&](auto S) AI { constexpr int s = S.value;
        if constexpr (s < 2) waitv<16 + 8>(); else waitv<16 + E1(s - 2) + E1(s - 1)>();
        bar();
        src1(s + 4); ival(G, [&](auto U) AI { constexpr int u = U.value;
          if constexpr ((s == 1 || s == 2) && (u == 2 || u == 5)) ld128p(xr[s - 1][u / 3], rx, lane * 16u, (ci * 128 + 8 * cj + 2 * wq + s - 1) * 2048 + (u / 3) * 1024);
          if constexpr (s >= 4 && s < 7 && u < 6 && 6 * (s - 4) + u < 16) {
            constexpr int id = 6 * (s - 4) + u, r = id >> 3, e = id & 7;
            PIN(xr[r][e >> 2]); PIN(gq[e >> 2]);
            const unsigned v = xr[r][e >> 2][e & 3], gv = gq[e >> 2][e & 3];
            const float x0 = bf2f(v & 0xffffu), x1 = u2f(v & 0xffff0000u);
            sv[r] += x0 + x1; sv[2 + r] = __builtin_fmaf(x1, x1, __builtin_fmaf(x0, x0, sv[2 + r]));
            sv[4 + r] = __builtin_fmaf(x1, u2f(gv & 0xffff0000u), __builtin_fmaf(x0, bf2f(gv & 0xffffu), sv[4 + r]));
            PIN(sv[r]); PIN(sv[2 + r]); PIN(sv[4 + r]);
          }
          if constexpr (s == 7 && u < 6) { sv[u] = dppadd<0x140>(dppadd<0x141>(dppadd<0x4E>(dppadd<0xB1>(sv[u])))); PIN(sv[u]); }
          if constexpr (s == 7 && u == 6) { const bool r1 = lane & 4; a1 = sel<float>(r1, sv[1], sv[0]); a2 = sel<float>(r1, sv[3], sv[2]); a3 = sel<float>(r1, sv[5], sv[4]);
                                            a1 = sum32(sum16(a1)); PIN(a1); PIN(a2); PIN(a3); PIN(epr); epr = __builtin_amdgcn_readfirstlane(epr) + 1u; }
          if constexpr (s == 8 && u == 0) { a2 = sum32(sum16(a2)); PIN(a2); }
          if constexpr (s == 8 && u == 1) { a3 = sum32(sum16(a3)); PIN(a3); }
          if constexpr (s == 8 && u == 2) { mean = a1 * (1.f / 1024.f); const float var = __builtin_fmaxf(a2 * (1.f / 1024.f) - mean * mean, 0.f) + 1e-5f; rstd = __builtin_amdgcn_rsqf(var); sd = var * rstd;
                                            PIN(mean); PIN(rstd); PIN(sd); }
          if constexpr (s == 8 && u == 3) { qv = 1.f + fexp(__builtin_fminf(a3, 60.f)); gg = __builtin_amdgcn_rcpf(1.f + fexp(__builtin_fminf(-a3, 80.f))); PIN(qv); PIN(gg); }
          if constexpr (s == 8 && u == 4) { a1 = sel4(lane & 1, lane & 2, -2.302208198f * rstd, -0.1029432397f * rstd * rstd * rstd, qv * sd, gg); PIN(a1); }
          if constexpr (s == 8 && u == 5) { const float mh = rbf(mean), sh = rbf(sd);
                                            a2 = sel4(lane & 1, lane & 2, u2f(pk2(-mh, -mh)), u2f(pk2(mh - mean, sh)), u2f(pk2(sd - sh, 0.f)), 0.f); PIN(a2); }
          if constexpr (s == 8 && u == 6) { if (lane < 16) st64(rws, (u32x2){f2u(sel<float>(lane & 8, a2, a1)), epr}, (lane >> 3) * 4096u + (lane & 7) * 8, rgb + (8 * cj + 2 * wq) * 32); }
          if constexpr (s == 2 && u == 0) { PIN(pdt); PIN(plr); PIN(pli); lr = bf2f(plr); li = bf2f(pli); const float dt = rbf(fexp(bf2f(pdt))); lp = (f32x2){fexp(dt * lr), dt * li}; PIN(lp);
                                            PIN(bq); PIN(oq); wrf(T_B1 + tq * 4, bf2f(bq)); wrf(T_OUT + tq * 4, bf2f(oq)); }
          if constexpr (s == 3 && u == 1) { float sn, cs_; sincos_f(lp.y, sn, cs_); lp = (f32x2){lp.x * cs_, lp.x * sn}; PIN(lp); wrf2(T_CON + lane * 8, lp); }
          if constexpr (s == 3 && u == 5) { const float nr = lp.x - 1.f, ni = lp.y, den = 2.f * __builtin_amdgcn_rcpf(lr * lr + li * li);
                                            wrf2(T_CON + 512 + lane * 8, (f32x2){(nr * lr + ni * li) * den, (ni * lr - nr * li) * den}); }
          if constexpr ((s == 9 || (s == 10 && u < 2))) { constexpr int q = 8 * (s - 9) + u; lp = cmul(lp, lp); PIN(lp);
            if constexpr (q >= 3 && q < 7) wrf2(T_CON + (q - 1) * 512 + lane * 8, lp);
            if constexpr (q == 9) wrf2(T_CON + 3072 + lane * 8, lp); }
          if constexpr (s == 10 && u == 3) table3(t3a, t3b);
          if constexpr (s == 11 && u == 6) { ld128a(rgv[0], rws, tq * 16u, rgb); ld128a(rgv[1], rws, tq * 16u + 4096, rgb); }
        });
      });
      setup2(); sx = 0; nexts();
      const unsigned sb0 = sb;
      waitv<16 + 2>(); bar();
      sfor<4>([&](auto U) AI { G2(IC<0>{}, U); }); nexts();
      const unsigned sb1 = sb; asm volatile("s_nop 4");
      sfor<4>([&](auto U) AI { G2(IC<1>{}, U); }); nexts();
      waitv<16 + 2>(); bar();
      sb = sb0; sfor<7>([&](auto U) AI { G2(IC<0>{}, IC<U.value + 4>{}); });
      sfor<4>([&](auto U) AI { G2(IC<2>{}, U); });
      waitv<19>(); bar();
      sb = sb1; sfor<7>([&](auto U) AI { G2(IC<1>{}, IC<U.value + 4>{}); });
      {
        auto rbad = [&]() AI { PIN(rgv[0]); PIN(rgv[1]); return ((rgv[0][1] ^ epr) | (rgv[0][3] ^ epr) | (rgv[1][1] ^ epr) | (rgv[1][3] ^ epr)) != 0; };
        if (__builtin_expect(anyl(rbad()), 0))
          for (unsigned sp = 0; sp < SPIN_CAP; ++sp) { __builtin_amdgcn_s_sleep(2);
            ld128a(rgv[0], rws, tq * 16u, rgb); ld128a(rgv[1], rws, tq * 16u + 4096, rgb);
            waitv<0>(); if (!anyl(rbad())) break; }
      }
      wrf(T_ROW + (tq & 1) * 1024 + (tq >> 1) * 4, u2f(rgv[0][0])); wrf(T_ROW + (tq & 1) * 1024 + 512 + (tq >> 1) * 4, u2f(rgv[0][2]));
      wrf2(T_XA + tq * 8, (f32x2){u2f(rgv[1][0]), u2f(rgv[1][2])});
      ta = t3a; tb = t3b; sx = 3; next3();
    }
    {
      f32x4 ac1[28];
#pragma unroll
      for (int m = 0; m < 28; ++m) ac1[m] = (f32x4){0, 0, 0, 0};
      {
        bf16x8 fa[2][4], fb[2][8];
        const int f = ((q2 >> 1) & 1) | ((g & 1) << 1);
        const unsigned nb = wm * 8192u + r15 * 128, n0 = (g ^ (r15 >> 1)) * 16, n1 = ((4 + g) ^ (r15 >> 1)) * 16, wk = (g >> 1) * 7168u + p * 8;
        const unsigned cb[8] = {nb + wn * 4096 + n0, nb + wn * 4096 + n1, nb + (1 - wn) * 4096 + n0, nb + (1 - wn) * 4096 + n1,
                                wk + (8 * (g & 1) + q2) * 128 + (((2 * wn) ^ f) * 32), wk + (8 * (g & 1) + q2) * 128 + (((2 * wn + 1) ^ f) * 32),
                                wk + 6144 + (8 * (g & 1) + q2) * 64 + (g & 1) * 32, wk + 6144 + (8 * (g & 1) + q2) * 64 + ((g & 1) ^ 1) * 32};
        unsigned ba[8]; unsigned xn = 98304, wnx = 32768;
        const char* pp0 = lane < 32 ? (const char*)sel<const u16*>(lane & 16, cim, cre) + ((bid * 4 + wq) * 16 + r15) * 128 : (const char*)w2 + ((bid * 4 + wq) * 56 + r15) * 128;
        const char* pp1 = (const char*)w2 + ((bid * 4 + wq) * 56 + (lane < 40 ? 16 + lane : 55)) * 128;
        PIN2(pp0, pp1);
        setm0(T_DUM);
#pragma unroll
        for (int b = 0; b < 8; ++b) { ba[b] = cb[b] + (b < 4 ? 131072u : 0u); PIN(ba[b]); }
        auto rd = [&](int r) AI { const int ks = r / 20, xx = r % 20;
          if (xx == 0) fa[ks][0] = rd128(ba[ks]);
          else if (xx <= 12) { const int b = (xx - 1) >> 1, h = (xx - 1) & 1; sethalf(fb[ks][b], h, rtr(ba[4 + (b & 1)] + (b >> 1) * 2048 + ks * 14336 + h * 512)); }
          else if (xx <= 16) { const int b = (xx - 1) >> 1, h = (xx - 1) & 1; sethalf(fb[ks][b], h, rtr(ba[b] + ks * 14336 + h * 256)); }
          else { const int a = xx - 16; fa[ks][a] = rd128(ba[(a >> 1) * 2 + ks] + (a & 1) * 2048); } };
        auto mm = [&](int ks, int m) AI { const int a = m < 16 ? m >> 3 : (m - 4) / 6, b = m < 16 ? m & 7 : (m - 4) % 6; ac1[m] = MFMA(fb[ks][b], fa[ks][a], ac1[m]); };
        auto up = [&](int b) AI { if (b < 8) { ba[b] = cb[b] + sel<unsigned>(b < 4, xn, wnx); PIN(ba[b]); }
                                  else { wnx = (wnx == 65536u) ? 0u : wnx + 32768u; xn = (xn == 131072u) ? 98304u : xn + 16384u; } };
        waitl0(); waitv<11>(); bar();
        PH(2);
        period<28, 8, true, 40, LASTR + 24, 9>(rd, mm, up); waitl0(); waitv<0>(); bar();
        const int sp = 2 + bid % 5;
        for (int o = 0; o < 3; ++o) {
          for (int s = o == 0 ? 1 : o == 1 ? sp : sp + 7; s < (o == 0 ? sp : o == 1 ? sp + 7 : 16); ++s) { period<28, 8, false, 40, LASTR + 24, 9>(rd, mm, up); waitl0(); bar(); }
          if (o < 2) pfg(pp0);
          pp0 = pp1;
        }
        PH(3);
        setup3(); sl = 0;
        ival(G, [&](auto U) AI { constexpr int u = U.value;
#pragma unroll
          for (int m = LAG + 4 * u; m < LAG + 4 * u + 4; ++m) if (m < 28) mm(1, m); });
#pragma unroll
        for (int a = 0; a < 4; ++a) fa[0][a] = rd128(g ? T_ZERO : T_XA + (64 * wm + 16 * (a ^ (2 * wn)) + r15) * 16);
#pragma unroll
        for (int b = 0; b < 8; ++b) fb[0][b] = rd128(T_WA + ((b < 6 ? (b >> 1) * 64 + wn * 32 + (b & 1) * 16 : 96 + b * 16) + r15) * 16);
        SB();
        sl = nq(sl); next3();
        bar();
        ival(G, [&](auto U) AI { constexpr int u = U.value;
#pragma unroll
          for (int m = 4 * u; m < 4 * u + 4; ++m) if (m < 28) mm(0, m); });
        sl = nq(sl); next3();
      }
      float k0[4], k1[4], qq[4];
#pragma unroll
      for (int a = 0; a < 4; ++a) { const unsigned ro = T_ROW + (64 * wm + 16 * (a ^ (2 * wn)) + r15) * 4; k0[a] = rdf(ro); k1[a] = rdf(ro + 512); qq[a] = rdf(ro + 1024); }
      u32x4 hw[4]; float t[8];
      auto lv0 = [](int s) constexpr { return s < 1 ? 0 : s < 7 ? (6 * (s - 1) < 32 ? 6 * (s - 1) : 32) : (32 + 4 * (s - 7) < 112 ? 32 + 4 * (s - 7) : 112); };
      auto st3 = [](int s) constexpr { return (s == 6 || s == 14 || s == 22) ? 4 : s == 26 ? 2 : 0; };
      sfor<36>([&](auto S1) AI { constexpr int s = S1.value + 1;
        if constexpr (st3(s - 2) > 0) waitv<8>();
        else if constexpr (s >= 2) waitv<16 + st3(s - 1)>();
        if constexpr (s == 12 || s == 20 || s == 28 || s == 36) {
          if (w == 4) { waitv<8>(); check_flags(pv, rws, fvo, flagb + (FK_M1 + (s - 12) / 8) * 128, lane, s == 36 ? 0 : 8 * half, 8 * half + 8, epr); } }
        SB(); bar();
        if constexpr (s == 16) { PH(4); }
        if constexpr (st3(s - 2) > 0) { if (w == 5) stflag(rws, epr, lane, flagb + (FK_M1 + (s - 4) / 8) * 128 + cj * 4); }
        if constexpr (s == 11 || s == 19 || s == 27 || s == 35) { if (w == 4) ld32a(pv, rws, fvo, flagb + (FK_M1 + (s - 11) / 8) * 128); }
#define UU(e) ac1[m0 + ((e) >> 2)][(e) & 3]
#define LVL(expr) do { _Pragma("unroll") for (int e = 0; e < 8; ++e) { t[e] = (expr); PIN(t[e]); } } while (0)
        ival(G, [&](auto U) AI { constexpr int u = U.value, n = lv0(s + 1) - lv0(s), li = n == 6 ? (u < 3 ? u : u - 1) : u >> 1;
          if constexpr ((n == 6 ? (u != 3 && u != 7) : !(u & 1)) && li < n) {
            constexpr int L = lv0(s) + li, v = L >> 3, l = L & 7, a = v & 3, m0 = (a < 2 ? 8 * a : 4 + 6 * a) + 2 * (v >> 2);
            if constexpr (l == 0) LVL(UU(e) * UU(e));
            if constexpr (l == 1) LVL(__builtin_fmaf(t[e], k1[a], k0[a]));
            if constexpr (l == 2) LVL(UU(e) * t[e]);
            if constexpr (l == 3) LVL(__builtin_amdgcn_exp2f(t[e]));
            if constexpr (l == 4) LVL(__builtin_fmaf(t[e], qq[a], qq[a]));
            if constexpr (l == 5) LVL(__builtin_amdgcn_rcpf(t[e]));
            if constexpr (l == 6) LVL(UU(e) * t[e]);
            if constexpr (l == 7) { hw[a] = (u32x4){pk2(t[0], t[1]), pk2(t[2], t[3]), pk2(t[4], t[5]), pk2(t[6], t[7])}; PIN(hw[a]); }
          } });
#pragma unroll
        for (int q = 0; q < st3(s); ++q) st128(rws, hw[q], lane * 16u + (4 * wm + (q ^ (2 * wn))) * 1024, ci * HID_PANEL + kb_of(cj, s < 26 ? 2 * (s >> 3) + wn : 6) * 8192);
        sl = nq(sl);
        if constexpr (s < 11) next3(); else nexts();
        if constexpr (s == 11) {
          rb = mk(w2, 3584 * 2048);
#pragma unroll
          for (int q = 0; q < 4; ++q) vb[q] = (4 * q + g) * 2048 + ((((r15 >> 1) ^ (4 * (q & 1) + g)) * 2) + (lane & 1)) * 16 - 1024 * q;
        }
      });
    }
    static_assert(MOVE >= 0 && MOVE <= 3, "");
    for (int s = 21; s < n4 - 4; ++s) { waitv<16>(); bar(); ival(G, nowork); sl = nq(sl); nexts(); }
    waitv<16>(); bar();
    if (half) {
      sfor<8>([&](auto U) AI { constexpr int u = U.value; if constexpr ((u & 3) == 0) setm0(sl + wq * 8192 + u * 1024);
        dma<(u & 3) * 1024>(rx, (4u * (u & 3) + g) * 2048 + r15 * 16 - (u & 3) * 1024, (ci * 128 + 32 * wq + 16 * (u >> 2)) * 2048 + cnt * 256); });
      waitv<8 + 8>();
    } else waitv<8>();
    bar();
    if (half) waitv<8>(); else waitv<0>();
    bar();
    ld32a(pv, rws, fvo, flagb + FK_P * 128);
    bar();
    waitv<0>();
    if (half) {
      check_flags(pv, rws, fvo, flagb + FK_P * 128, lane, cnt, cnt + 1, epr);
      sfor<8>([&](auto U) AI { constexpr int u = U.value; if constexpr ((u & 3) == 0) setm0(nq(sl) + wq * 8192 + u * 1024);
        dma1<(u & 3) * 1024>(rws, lane * 16u, PART_OFF + (ci * 8 + cnt) * 32768 + wq * 8192 + (u >> 2) * 4096); });
      if (cnt == 0 && w == 4) stflag(rws, epr, lane, flagb + FK_EP * 128);
    }
    PH(5);
    waitv<0>(); bar();
  }
  if (half) {
    const int bg = r31 & 15, sw = (r31 >> 4) * 8 + (bg >> 3) * 4 + (bg & 3);
    const unsigned pa = ((sl + 65536u + (w >> 2) * 32768u) & 131071u) + (r31 >> 4) * 16384 + (wq * 4 + (bg >> 2)) * 1024 + (bg & 3) * 256;
    const unsigned pp = nq(sl) + ((w >> 2) * 2 + (r31 >> 4)) * 8192 + (wq * 2 + (bg >> 3)) * 1024 + (bg & 3) * 256 + ((bg >> 2) & 1) * 8;
    const f32x4 bv = rdf4(T_OUT + r31 * 16), dv = rdf4(T_OUT + 512 + r31 * 16);
#pragma unroll
    for (int u = 0; u < 8; ++u) {
      const int row = 2 * u + hl;
      const f32x4 av = rdf4(pa + ((row ^ bg) * 16));
      const u32x2 pw = *(LAS u32x2*)(size_t)(pp + ((row ^ sw) * 16)), xw = *(LAS u32x2*)(size_t)(sl + (16 * w + row) * 256 + r31 * 8);
      const float gg = rdf(T_ROW + 1536 + (16 * w + row) * 4);
      const f32x4 pf = {bf2f(pw[0] & 0xffffu), u2f(pw[0] & 0xffff0000u), bf2f(pw[1] & 0xffffu), u2f(pw[1] & 0xffff0000u)};
      const f32x4 xf = {bf2f(xw[0] & 0xffffu), u2f(xw[0] & 0xffff0000u), bf2f(xw[1] & 0xffffu), u2f(xw[1] & 0xffff0000u)};
      f32x4 r;
#pragma unroll
      for (int e = 0; e < 4; ++e) r[e] = (av[e] + pf[e]) + __builtin_fmaf(gg, __builtin_fmaf(xf[e], dv[e], -bv[e]), bv[e]);
      st128<OAUX>(mk(out, 8u << 20), __builtin_bit_cast(u32x4, r), (unsigned)((16 * w + hl + 2 * u) * 4096 + r31 * 16), ci * 128 * 4096 + cnt * 512);
    }
  }
}

extern "C" void kernel_launch(void* const* d_in, const int* in_sizes, int n_in, void* d_out, int out_size, void* d_ws, size_t ws_size, hipStream_t stream) {
  (void)in_sizes; (void)n_in; (void)out_size; (void)ws_size;
  (void)hipFuncSetAttribute((const void*)fused_kernel, hipFuncAttributeMaxDynamicSharedMemorySize, LDS_BYTES);
  fused_kernel<<<256, 512, LDS_BYTES, stream>>>((const u16*)d_in[0], (const u16*)d_in[1], (const u16*)d_in[2], (const u16*)d_in[3], (const u16*)d_in[4],
      (const u16*)d_in[5], (const u16*)d_in[6], (const u16*)d_in[7], (const u16*)d_in[8], (const u16*)d_in[10], (const u16*)d_in[11], (const u16*)d_in[12],
      (const u16*)d_in[13], (const u16*)d_in[14], (char*)d_ws, (float*)d_out);
}
```

```cpp
#include <hip/hip_runtime.h>
#include <utility>

typedef unsigned short u16;
typedef __bf16 bf16x2 __attribute__((ext_vector_type(2)));
typedef __bf16 bf16x4 __attribute__((ext_vector_type(4)));
typedef __bf16 bf16x8 __attribute__((ext_vector_type(8)));
typedef float f32x2 __attribute__((ext_vector_type(2)));
typedef float f32x4 __attribute__((ext_vector_type(4)));
typedef float f32x16 __attribute__((ext_vector_type(16)));
typedef unsigned u32x2 __attribute__((ext_vector_type(2)));
typedef unsigned u32x4 __attribute__((ext_vector_type(4)));
typedef __amdgpu_buffer_rsrc_t rsrc_t;
#define LAS __attribute__((address_space(3)))
#define DI __device__ __forceinline__
#define AI __attribute__((always_inline))
#define SB() __builtin_amdgcn_sched_barrier(0)
#define PIN(x) asm volatile("" : "+v"(x))
#define MFMA(a, b, c) __builtin_amdgcn_mfma_f32_16x16x32_bf16(a, b, c, 0, 0, 0)
#define MFMA32(a, b, c) __builtin_amdgcn_mfma_f32_32x32x16_bf16(a, b, c, 0, 0, 0)
template <int V> using IC = std::integral_constant<int, V>;

constexpr unsigned HID_PANEL = 112 * 8192, H_OFF = 16 * HID_PANEL, H_PANEL = 32 * 16384;
constexpr unsigned PART_OFF = H_OFF + 16 * H_PANEL;
constexpr unsigned GRAN_OFF = PART_OFF + 128 * 32768;
constexpr unsigned RG_OFF = GRAN_OFF + 256 * 1024;
constexpr unsigned CG_OFF = RG_OFF + 16 * 8192;
constexpr unsigned FLAG_OFF = CG_OFF + 256 * 2048;
constexpr unsigned EPC_OFF = FLAG_OFF + 16 * 1024;
constexpr unsigned WS_BYTES = EPC_OFF + 16 * 128;
constexpr int FK_M1 = 0, FK_M2 = 1, FK_M3 = 2, FK_M4 = 3, FK_H = 4, FK_P = 5, FK_EP = 6;
constexpr unsigned TAB = 147456, T_ROW = TAB, T_XA = TAB + 2048, T_OUT = TAB + 4096, T_B1 = TAB + 5120, T_CS2 = TAB + 6144, T_WA = TAB + 8192, T_ZERO = TAB + 12288, T_AGG = T_WA, T_DUM = TAB + 12544, T_CON = TAB + 12800;
constexpr unsigned LDS_BYTES = 163840;
#define XMAP 1
#define ROTM 3
#define OAUX 2
#define SP0 4
#define SPN 6
#define MOVE 3
#define PROFX false
constexpr int LAG = 2, LASTR = 25;
constexpr unsigned SPIN_CAP = 1u << 16;

DI rsrc_t mk(const void* p, unsigned bytes) { return __builtin_amdgcn_make_buffer_rsrc((void*)p, 0, bytes, 0x00020000); }
DI void setm0(unsigned lds) { asm volatile("s_mov_b32 m0, %0\n\ts_nop 0" ::"s"(lds) : "memory"); }
template <int OFF> DI void dma(rsrc_t r, unsigned voff, unsigned soff) {
  asm volatile("buffer_load_dwordx4 %0, %1, %2 offen offset:%3 lds" ::"v"(voff), "s"(r), "s"(soff), "i"(OFF) : "memory");
}
template <int OFF> DI void dma1(rsrc_t r, unsigned voff, unsigned soff) {
  asm volatile("buffer_load_dwordx4 %0, %1, %2 offen offset:%3 sc1 lds" ::"v"(voff), "s"(r), "s"(soff), "i"(OFF) : "memory");
}
#define PIN2(x, y) asm volatile("" : "+v"(x), "+v"(y))
#define PINS(x) asm volatile("" : "+s"(x))
typedef const void* const __attribute__((address_space(4)))* kargs_t;
DI const char* karg(int i) { kargs_t ka = (kargs_t)__builtin_amdgcn_kernarg_segment_ptr(); PINS(ka); return (const char*)ka[i]; }
enum { A_X, A_LRE, A_LIM, A_BRE, A_BIM, A_CRE, A_CIM, A_LDT, A_DSK, A_W1, A_B1, A_W2, A_B2, A_GW, A_WS, A_OUT };
template <int N> DI void waitv() { asm volatile("s_waitcnt vmcnt(%0)" ::"i"(N) : "memory"); }
DI void waitl0() { SB(); __builtin_amdgcn_s_waitcnt(0xC07F); asm volatile("" ::: "memory"); }
#define PH(k)
DI void bar() { asm volatile("s_barrier" ::: "memory"); SB(); }
DI bf16x8 rd128(unsigned a) { return *(LAS bf16x8*)(size_t)a; }
DI u32x4 rdu4(unsigned a) { return *(LAS u32x4*)(size_t)a; }
DI f32x4 rdf4(unsigned a) { return *(LAS f32x4*)(size_t)a; }
DI f32x2 rdf2(unsigned a) { return *(LAS f32x2*)(size_t)a; }
DI float rdf(unsigned a) { return *(LAS float*)(size_t)a; }
DI void wrf(unsigned a, float v) { *(LAS float*)(size_t)a = v; }
DI void wrf2(unsigned a, f32x2 v) { *(LAS f32x2*)(size_t)a = v; }
DI bf16x4 rtr(unsigned a) { return __builtin_amdgcn_ds_read_tr16_b64_v4bf16((LAS bf16x4*)(size_t)a); }
DI void sethalf(bf16x8& f, int h, bf16x4 v) {
  if (h == 0) f = __builtin_shufflevector(v, v, 0, 1, 2, 3, -1, -1, -1, -1);
  else { bf16x8 t = __builtin_shufflevector(v, v, -1, -1, -1, -1, 0, 1, 2, 3); f = __builtin_shufflevector(f, t, 0, 1, 2, 3, 12, 13, 14, 15); }
}
DI unsigned f2u(float f) { return __builtin_bit_cast(unsigned, f); }
DI float u2f(unsigned u) { return __builtin_bit_cast(float, u); }
DI float shx(int lane, float v, int m) { return u2f(__builtin_amdgcn_ds_bpermute((lane ^ m) * 4, (int)f2u(v))); }
DI float sum16(float v) { const auto r = __builtin_amdgcn_permlane16_swap(f2u(v), f2u(v), false, false); return u2f(r[0]) + u2f(r[1]); }
DI float sum32(float v) { const auto r = __builtin_amdgcn_permlane32_swap(f2u(v), f2u(v), false, false); return u2f(r[0]) + u2f(r[1]); }
DI float fexp(float v) { return __builtin_amdgcn_exp2f(v * 1.4426950408889634f); }
DI float bf2f(unsigned v) { return u2f(v << 16); }
DI unsigned pk2(float lo, float hi) { f32x2 v = {lo, hi}; return __builtin_bit_cast(unsigned, __builtin_convertvector(v, bf16x2)); }
DI float rbf(float f) { return u2f(pk2(f, 0.f) << 16); }
DI float dot2(unsigned a, unsigned b, float c) { return __builtin_amdgcn_fdot2_f32_bf16(__builtin_bit_cast(bf16x2, a), __builtin_bit_cast(bf16x2, b), c, false); }
template <int C> DI float dppadd(float v) { return v + u2f(__builtin_amdgcn_update_dpp(0, (int)f2u(v), C, 0xf, 0xf, true)); }
DI float sel4(bool b0, bool b1, float a, float b, float c, float d) { PIN(a); PIN(b); PIN(c); PIN(d); const float lo = b0 ? b : a, hi = b0 ? d : c; return b1 ? hi : lo; }
DI f32x2 cmul(f32x2 a, f32x2 b) { return (f32x2){a.x * b.x - a.y * b.y, a.x * b.y + a.y * b.x}; }
DI f32x2 cmulc(f32x2 a, f32x2 b) { return (f32x2){__builtin_fmaf(a.x, b.x, -(a.y * b.y)), __builtin_fmaf(-a.x, b.y, -(a.y * b.x))}; }
DI f32x2 cfma(f32x2 a, f32x2 b, f32x2 c) { return (f32x2){__builtin_fmaf(a.x, b.x, __builtin_fmaf(-a.y, b.y, c.x)), __builtin_fmaf(a.x, b.y, __builtin_fmaf(a.y, b.x, c.y))}; }
template <int AUX = 16> DI void st128(rsrc_t r, u32x4 v, unsigned voff, unsigned soff) {
  __builtin_amdgcn_raw_buffer_store_b128(v, r, voff, soff, AUX);
  asm volatile("s_nop 1" ::"v"(v));
}
DI void st64(rsrc_t r, u32x2 v, unsigned voff, unsigned soff) { __builtin_amdgcn_raw_buffer_store_b64(v, r, voff, soff, 16); }
DI void st32(rsrc_t r, unsigned v, unsigned voff, unsigned soff) { __builtin_amdgcn_raw_buffer_store_b32(v, r, voff, soff, 16); }
DI void stflag(rsrc_t r, unsigned v, int lane, unsigned soff) { if (lane == 0) st32(r, v, 0u, soff); }
DI void ld32a(unsigned& d, rsrc_t r, unsigned voff, unsigned soff) { asm volatile("buffer_load_dword %0, %1, %2, %3 offen sc1" : "=v"(d) : "v"(voff), "s"(r), "s"(soff) : "memory"); }
DI void ld16p(unsigned& d, const u16* p, unsigned voff) { asm volatile("global_load_ushort %0, %1, %2" : "=v"(d) : "v"(voff), "s"(p) : "memory"); }
DI void ld64p(u32x2& d, rsrc_t r, unsigned voff, unsigned soff) { asm volatile("buffer_load_dwordx2 %0, %1, %2, %3 offen" : "=v"(d) : "v"(voff), "s"(r), "s"(soff) : "memory"); }
DI void ld64a(u32x2& d, rsrc_t r, unsigned voff, unsigned soff) { asm volatile("buffer_load_dwordx2 %0, %1, %2, %3 offen sc1" : "=v"(d) : "v"(voff), "s"(r), "s"(soff) : "memory"); }
DI void ld128g(u32x4& d, const u16* p, unsigned voff) { asm volatile("global_load_dwordx4 %0, %1, %2" : "=v"(d) : "v"(voff), "s"(p) : "memory"); }
DI void pfl(unsigned& d, rsrc_t r, unsigned voff, unsigned soff) { asm volatile("buffer_load_dword %0, %1, %2, %3 offen" : "+v"(d) : "v"(voff), "s"(r), "s"(soff) : "memory"); }
template <class T> DI T sel(bool c, T a, T b) { return c ? a : b; }
DI void pfg(const char* p) { asm volatile("global_load_lds_dword %0, off" ::"v"(p) : "memory"); }
DI bool anyl(bool b) { return __builtin_amdgcn_ballot_w64(b) != 0; }
DI void ld128p(u32x4& d, rsrc_t r, unsigned voff, unsigned soff) { asm volatile("buffer_load_dwordx4 %0, %1, %2, %3 offen" : "=v"(d) : "v"(voff), "s"(r), "s"(soff) : "memory"); }
DI void sincos_f(float t, float& sn, float& cs) {
  const float k = __builtin_rintf(t * 0.63661977236f);
  float r = __builtin_fmaf(k, -1.5707962513f, t); r = __builtin_fmaf(k, -7.5497894159e-08f, r); r = __builtin_fmaf(k, -5.3903029534e-15f, r);
  const float z = r * r;
  const float sp = __builtin_fmaf(r * z, __builtin_fmaf(z, __builtin_fmaf(z, __builtin_fmaf(z, 2.7183114939e-06f, -1.9839334836e-04f), 8.3333293858e-03f), -1.6666666642e-01f), r);
  const float cp = __builtin_fmaf(z, __builtin_fmaf(z, __builtin_fmaf(z, __builtin_fmaf(z, 2.4390448796e-05f, -1.3886763775e-03f), 4.1666623324e-02f), -4.9999999726e-01f), 1.f);
  const int q = (int)k;
  const float a = (q & 1) ? cp : sp, b = (q & 1) ? sp : cp;
  sn = (q & 2) ? -a : a; cs = ((q + 1) & 2) ? -b : b;
}
DI void ld128a(u32x4& d, rsrc_t r, unsigned voff, unsigned soff) { asm volatile("buffer_load_dwordx4 %0, %1, %2, %3 offen sc1" : "=v"(d) : "v"(voff), "s"(r), "s"(soff) : "memory"); }

DI unsigned x1off(int a) { return a == 0 ? 131072u : (unsigned)(81920 + a * 16384); }
DI unsigned nq(unsigned s) { return (s + 32768u) & 131071u; }

DI int kb_of(int j, int bl) { return 7 * j + ((j & 1) ? (bl == 6 ? 0 : bl + 1) : bl); }

template <int NR, int LAST> constexpr int nread(int sl) { int n = 0; for (int r = 0; r < NR; ++r) n += (r * LAST) / (NR - 1) <= sl; return n; }
template <int NM, int R0, bool FIRST, int NR, int LAST, int NB, int SL, class RD, class MM, class UP>
DI void slot(RD& rd, MM& mm, UP& upd) {
  constexpr int q = SL + LAG;
  if constexpr (!FIRST && (q == NM || q == NM + R0 || q == 2 * NM)) {
    constexpr int cnt = nread<NR, LAST>(SL - 1) - nread<NR, LAST>(SL - 6);
    __builtin_amdgcn_s_waitcnt(0xC07F | (cnt << 8)); SB();
  }
  if constexpr (!(FIRST && q < NM)) mm((q < NM) ? 1 : ((q < 2 * NM) ? 0 : 1), q % NM);
  SB();
  constexpr int r = (SL * (NR - 1) + LAST - 1) / LAST;
  if constexpr (r < NR && (r * LAST) / (NR - 1) == SL) rd(r);
  constexpr int per = NB > 2 * NM - 1 - LAST ? 2 : 1, u0 = (SL - LAST - 1) * per;
  if constexpr (SL > LAST && u0 < NB) { upd(u0); if constexpr (per == 2 && u0 + 1 < NB) upd(u0 + 1); }
  SB();
}
template <int NM, int R0, bool FIRST, int NR, int LAST, int NB, class RD, class MM, class UP, int... I>
DI void period_(RD& rd, MM& mm, UP& upd, std::integer_sequence<int, I...>) { (slot<NM, R0, FIRST, NR, LAST, NB, I>(rd, mm, upd), ...); }
template <int NM, int R0, bool FIRST, int NR, int LAST, int NB, class RD, class MM, class UP>
DI void period(RD& rd, MM& mm, UP& upd) {
  static_assert(NR - 1 <= LAST && NB <= 2 * (2 * NM - 1 - LAST), "");
  period_<NM, R0, FIRST, NR, LAST, NB>(rd, mm, upd, std::make_integer_sequence<int, 2 * NM>{});
}
template <bool FIRST, int SL, class RD, class MM, class UP> DI void slot32(RD& rd, MM& mm, UP& upd) {
  if constexpr (SL == 12) { __builtin_amdgcn_s_waitcnt(0xC07F | (8 << 8)); SB(); }
  if constexpr (!(FIRST && SL < 12)) mm((SL + 4) & 15);
  SB();
  if constexpr (SL < 2) { rd(2 * SL); rd(2 * SL + 1); } else if constexpr (SL < 14) rd(SL + 2);
  if constexpr ((SL & 3) == 2) { upd(SL >> 2); upd(4 + (SL >> 2)); }
  SB();
}
template <bool FIRST, class RD, class MM, class UP, int... I> DI void period32_(RD& rd, MM& mm, UP& upd, std::integer_sequence<int, I...>) { (slot32<FIRST, I>(rd, mm, upd), ...); }
template <bool FIRST, class RD, class MM, class UP> DI void period32(RD& rd, MM& mm, UP& upd) { period32_<FIRST>(rd, mm, upd, std::make_integer_sequence<int, 16>{}); }
template <class G, class W, int... U> DI void ival_(G& g, W& wk, std::integer_sequence<int, U...>) { ((g(IC<U>{}), SB(), wk(IC<U>{}), SB()), ...); }
template <class G, class W> DI void ival(G&& g, W&& wk) { ival_(g, wk, std::make_integer_sequence<int, 8>{}); }
template <int N, class F, int... I> DI void sfor_(F& f, std::integer_sequence<int, I...>) { (f(IC<I>{}), ...); }
template <int N, class F> DI void sfor(F&& f) { sfor_<N>(f, std::make_integer_sequence<int, N>{}); }

DI void check_flags(unsigned& pv, rsrc_t ws, unsigned voff, unsigned soff, int lane, int lo, int hi, unsigned ep) {
  PIN(pv);
  const bool need = lane >= lo && lane < hi;
  if (__builtin_expect(anyl(need && pv != ep), 0)) {
    for (unsigned sp = 0; sp < SPIN_CAP; ++sp) {
      __builtin_amdgcn_s_sleep(2);
      ld32a(pv, ws, voff, soff); asm volatile("s_waitcnt vmcnt(0)" : "+v"(pv)::"memory");
      if (!anyl(need && pv != ep)) break;
    }
  }
}

#if defined(__HIP_DEVICE_COMPILE__) && __HIP_DEVICE_COMPILE__
__attribute__((target("no-packed-fp32-ops")))
#endif
__global__ __launch_bounds__(512, 2) void fused_kernel(const u16* x, const u16* lre, const u16* lim, const u16* bre, const u16* bim, const u16* cre, const u16* cim,
    const u16* ldt, const u16* dsk, const u16* w1, const u16* b1, const u16* w2, const u16* b2, const u16* gw, char* ws, float* out) {
  extern __shared__ __attribute__((aligned(16))) char shm[];
  unsigned long long pc0; asm volatile("s_getpc_b64 %0" : "=s"(pc0));
  const int lane = threadIdx.x & 63, w = __builtin_amdgcn_readfirstlane(threadIdx.x >> 6);
  const int bid = blockIdx.x, xcd = bid & 7, loc = bid >> 3;
  const int ci = (xcd & 3) * 4 + (loc & 3), cj = (loc >> 4) * 8 + (xcd >> 2) * 4 + ((loc >> 2) & 3);
  const int rot = (ci + cj) & ROTM, cnt = cj & 7, half = cj >> 3, wq = w & 3, tq = threadIdx.x & 255;
  const int n4 = half ? 28 + MOVE : 28 - MOVE;
  const rsrc_t rws = mk(ws, WS_BYTES), rx = mk(x, 2048 * 2048);
  const int wm = (w >> 1) & 1, wn = w & 1, g = lane >> 4, r15 = lane & 15, q2 = (lane >> 2) & 3, p = lane & 3, hl = lane >> 5, r31 = lane & 31;
  const unsigned flagb = FLAG_OFF + ci * 1024, fvo = (lane & 15) * 4;
  unsigned rgb = RG_OFF + ci * 8192; PINS(rgb);
  auto nowork = [&](auto) AI {};
  unsigned va[4], vb[4], vw[7], sl, sa, sb, ta, tb; int sx; rsrc_t ra, rb;
  auto vnat = [&](int u) AI { return (8u * u + (lane >> 3)) * 2048 + (((lane & 7) ^ ((4 * u + g) & 7)) * 16) - 1024 * u; };
  auto nexts = [&]() AI { sa = __builtin_amdgcn_readlane(ta, sx); sb = __builtin_amdgcn_readlane(tb, sx); ++sx; PINS(sa); PINS(sb); };
  auto G = [&](auto U) AI { constexpr int u = U.value;
    if constexpr (u == 0) setm0(sl + wq * 4096);
    if constexpr (u == 4) setm0(sl + 16384 + wq * 4096);
    if constexpr (u < 4) dma1<u * 1024>(ra, va[u], sa); else dma<(u - 4) * 1024>(rb, vb[u - 4], sb);
  };
  auto G2 = [&](auto S, auto U) AI { constexpr int a = S.value % 3, u = U.value;
    if constexpr (u == 0) setm0((a == 0 ? 131072u : 81920u + a * 16384u) + wq * 4096);
    if constexpr (u == 4) setm0(a * 32768 + wq * 7168);
    if constexpr (u == 8) setm0(a * 32768 + wq * 7168 + 4096);
    if constexpr (u < 4) dma<u * 1024>(rx, va[u], sa); else dma<(u & 3) * 1024>(rb, vw[u - 4], sb);
  };
  const int c7 = ci & 7;
  auto setup2 = [&]() AI {
    rb = mk((const char*)w1 - 4096, 1024 * 7168 + 4096);
#pragma unroll
    for (int q = 0; q < 4; ++q) va[q] = vnat(q);
#pragma unroll
    for (int q = 0; q < 6; ++q) { const int k = 8 * (q & 1) + (lane >> 3), f = ((k >> 1) & 1) | ((q & 1) << 1);
      vw[q] = 4096 + k * 7168 + ((cj & 1) ? 64 : 0) + (q >> 1) * 128 + (((((lane & 7) >> 1) ^ f) * 2) + (lane & 1)) * 16 - (q & 3) * 1024; }
    vw[6] = 4096 + (lane >> 2) * 7168 + ((cj & 1) ? 0 : 384) + (((((lane & 3) >> 1) ^ hl) * 2) + (lane & 1)) * 16 - 2048;
    int k = lane - 2 + (c7 < 2 ? 3 * c7 : c7 - 2); k = 2 + (k >= 6 ? k - 6 : k);
    k = lane < 2 ? lane : lane < 8 ? k : (lane ^ rot);
    k = (k ^ (ci & 8)) & 15;
    ta = (ci * 128 + 32 * wq) * 2048 + k * 128; tb = (k * 64 + 16 * wq) * 7168 + cj * 448;
  };
  auto setup3 = [&]() AI {
    ra = rws;
#pragma unroll
    for (int q = 0; q < 4; ++q) { va[q] = lane * 16; vb[q] = vnat(q); }
  };
  auto table3 = [&](unsigned& ta, unsigned& tb) AI {
    const int ks = 16 * half + (lane ^ rot);
    int n = 2 * (lane - 16) + (wq >> 1), hp = half; if (n >= 56) { n -= 2 * MOVE; hp = 0; }
    const int kb = kb_of(hp * 8 + ((n ^ rot) & 7), n >> 3);
    ta = lane < 16 ? H_OFF + ci * H_PANEL + ks * 16384 + wq * 4096 : ci * HID_PANEL + kb * 8192 + (wq & 1) * 4096;
    tb = lane < 16 ? (cnt * 128 + 32 * wq) * 2048 + (ks >> 1) * 128 + (ks & 1) : (kb * 32 + 16 * (wq & 1)) * 2048 + cnt * 256;
    PIN(ta); PIN(tb);
  };
  auto next3 = [&]() AI { nexts(); rb = mk(sel<const u16*>(sb & 1, cim, cre), 1024 * 2048); sb &= ~1u; };

  if (w < 4) {
    constexpr bool isx = PROFX; (void)isx;
    unsigned epr, epc;
    const unsigned ca = (32 * wn + r31) * 8;
    const int seg0 = 4 * wm + hl;
    f32x2 lam, cof, l16, l128, l1k, pw0, pw1; unsigned t3a, t3b;
    {
      setm0(T_DUM);
      const int row = bid * 8 + (lane & 7);
      const char* pp = (const char*)(hl ? (row < 1024 ? bre : bim - 1024 * 1024) : x) + (size_t)row * 2048 + (4 * wq + ((lane >> 3) & 3)) * 128;
      for (int q = 0; q <= wq; ++q) __builtin_amdgcn_s_sleep(6);
      pfg(pp);
      pfg((const char*)pc0 + (((loc * 4 + wq) % 24) * 16 + (lane & 15)) * 128);
      ld32a(epr, rws, 0u, flagb + FK_EP * 128); ld32a(epc, rws, 0u, EPC_OFF + cj * 128);
      setup2(); sx = 2; nexts(); table3(t3a, t3b);
#pragma unroll
      for (int q = 0; q < 7; ++q) { PIN(vw[q]); PIN(va[q & 3]); }
      wrf(T_ZERO + (lane & 3) * 4, 0.f);
    }
    {
      f32x16 acc[2][2];
#pragma unroll
      for (int a = 0; a < 4; ++a)
#pragma unroll
        for (int e = 0; e < 16; ++e) acc[a >> 1][a & 1][e] = 0.f;
      bf16x8 wv[4][2];
      const int sstar = c7 < 2 ? c7 : 2;
      const int fsw = ((q2 >> 1) & 1) | ((g & 1) << 1);
      unsigned cf = sstar * 32768 + (g >> 1) * 7168 + (8 * (g & 1) + q2) * 128 + ((wq ^ fsw) * 32) + p * 8;
      unsigned ch = sstar * 32768 + (g >> 1) * 7168 + 6144 + (8 * (g & 1) + q2) * 64 + (((wq & 1) ^ (g & 1)) * 32) + p * 8;
      PIN(cf); PIN(ch);
      auto wread = [&](int st) AI {
        if (sstar == st) {
#pragma unroll
          for (int e = 0; e < 4; ++e)
#pragma unroll
            for (int t = 0; t < 4; ++t) sethalf(wv[t][e >> 1], e & 1, rtr((e >> 1) * 14336 + (t < 3 ? cf + t * 2048 + (e & 1) * 512 : ch + (e & 1) * 256)));
        } };
      {
        bf16x8 fa[4][2], fb[4][2];
        unsigned cb[8], ba[8], rs = 98304;
#pragma unroll
        for (int b = 0; b < 8; ++b) { cb[b] = (b < 4 ? wm * 8192u : 16384 + wn * 8192u) + r31 * 128 + (((2 * (b & 3) + hl) ^ ((r31 >> 1) & 7)) * 16); ba[b] = rs + cb[b]; PIN(ba[b]); }
        auto rd = [&](int r) AI { const int k16 = r >> 2, xx = r & 3;
          if (xx == 0) fa[k16][0] = rd128(ba[k16]); else if (xx == 3) fa[k16][1] = rd128(ba[k16] + 4096); else fb[k16][xx - 1] = rd128(ba[4 + k16] + (xx - 1) * 4096); };
        auto mm = [&](int m) AI { const int k16 = m >> 2, mt = (m >> 1) & 1, nt = m & 1; acc[mt][nt] = MFMA32(fa[k16][mt], fb[k16][nt], acc[mt][nt]); };
        auto up = [&](int b) AI { if (b == 0) rs = nq(rs); ba[b] = rs + cb[b]; PIN(ba[b]); };
        const int pg = ((loc * 4 + wq) * 8 + (lane & 7)) * 65536;
        const char* pp = lane < 56 ? (const char*)w1 + ((bid * 4 + wq) * 56 + lane) * 128
                       : pg < (7 << 20) ? (const char*)w1 + pg : pg < (9 << 20) ? (const char*)cre + (pg - (7 << 20)) : pg < (11 << 20) ? (const char*)cim + (pg - (9 << 20))
                       : pg < (18 << 20) ? (const char*)w2 + (pg - (11 << 20)) : pg < (26 << 20) ? (const char*)out + (pg - (18 << 20))
                       : ws + (pg - (26 << 20) < (int)WS_BYTES ? pg - (26 << 20) : 0);
        PIN(pp);
        waitl0(); bar();
        period32<true>(rd, mm, up); waitl0(); bar();
        const int sp = SP0 + bid % SPN;
        for (int o = 0; o < 2; ++o) {
          for (int s = o ? sp : 1; s < (o ? 16 : sp); ++s) { period32<false>(rd, mm, up); waitl0(); bar(); }
          if (o == 0) pfg(pp);
        }
        waitv<0>(); PIN(epr); PIN(epc);
        epr = __builtin_amdgcn_readfirstlane(epr) + 1u; epc = __builtin_amdgcn_readfirstlane(epc) + 1u;
        lam = rdf2(T_CON + ca);
        wread(0);
        sfor<7>([&](auto U) AI { G2(IC<2>{}, IC<U.value + 4>{}); SB(); if constexpr (U.value < 6) { mm(4 + 2 * U.value); mm(5 + 2 * U.value); } SB(); });
        nexts();
      }
      f32x2 st[2] = {{0, 0}, {0, 0}}, Pf[2] = {{0, 0}, {0, 0}}, Gt = {0, 0}, cr = {0, 0}, ag[8];
      u32x4 lb[8], cg[8]; unsigned lbo[8], lbe[8], ob[2][2][4], pv, gd = 0;
      f32x4 cs[4]; f32x2 so[2]; float c0s = 0.f, c1s = 0.f;
      const rsrc_t rgr = mk(ws + GRAN_OFF + cj * 16384, 16384);
      const int cp = 28 * wq + (r31 < 28 ? r31 : 27);
      const unsigned cgo = hl * 16384 + cp * 16;
      auto bad = [&]() AI { unsigned d = 0;
#pragma unroll
        for (int q = 0; q < 8; ++q) { PIN(lb[q]); d |= (int)lbo[q] < 0 ? 0u : ((lb[q][1] ^ epc) | (lb[q][3] ^ epc)); }
        return d != 0; };
      auto gbad = [&]() AI { unsigned d = 0;
#pragma unroll
        for (int q = 0; q < 8; ++q) { PIN(cg[q]); d |= (cg[q][1] ^ epc) | (cg[q][3] ^ epc); }
        return d != 0; };
      auto E2 = [](int s) constexpr { return (s == 2 || s == 3) ? 1 : s == 4 ? 8 : (s == 10 || s == 11) ? 4 : 0; };
      auto T2 = [](int s) constexpr { return (s == 7 || s == 8) ? 4 : 0; };
      sfor<15>([&](auto S) AI { constexpr int s = S.value;
        if constexpr (s < 3) waitl0();
        if constexpr (s == 10) waitv<11 + E2(9)>();
        else if constexpr (s >= 1) waitv<T2(s - 2) + (s < 14 ? 11 : 8) + E2(s - 1) + T2(s - 1)>();
        if constexpr (s == 13) { if (w == 0) { waitv<8>(); check_flags(pv, rws, fvo, flagb + FK_H * 128, lane, 8 * half, 8 * half + 8, epr); } }
        bar();
        if constexpr (s < 2) wread(s + 1);
        if constexpr (s == 2) {
#pragma unroll
          for (int sg = 0; sg < 8; ++sg) ag[sg] = rdf2(T_AGG + sg * 512 + ca);
          l16 = rdf2(T_CON + 1024 + ca); cof = rdf2(T_CON + 512 + ca);
        }
        if constexpr (s == 5) { pw1 = rdf2(T_CON + 1536 + ca); pw0 = rdf2(T_CON + 2048 + ca); l128 = rdf2(T_CON + 2560 + ca); l1k = rdf2(T_CON + 3072 + ca); }
        if constexpr (s == 10) { if (w == 0) stflag(rws, epr, lane, flagb + FK_H * 128 + cj * 4); }
        SB();
        auto work = [&](auto U) AI { constexpr int u = U.value;
          if constexpr (s < 2 && u >= 1 && u < 9) {
            constexpr int t = 8 * s + u - 1;
#pragma unroll
            for (int mt = 0; mt < 2; ++mt) st[mt] = cfma(lam, st[mt], (f32x2){acc[mt][0][t], acc[mt][1][t]});
            PIN2(st[0], st[1]);
          }
          if constexpr (s == 1 && u == 9) { wrf2(T_AGG + seg0 * 512 + ca, st[0]); wrf2(T_AGG + (seg0 + 2) * 512 + ca, st[1]); }
          if constexpr (s == 2 && u >= 1 && u < 9) {
            constexpr int sg = u - 1, mt = (sg >> 1) & 1; const bool me = sg == seg0 + 2 * mt;
            Pf[mt].x = sel<float>(me, Gt.x, Pf[mt].x); Pf[mt].y = sel<float>(me, Gt.y, Pf[mt].y); Gt = cfma(l16, Gt, ag[sg]);
            PIN2(Gt, Pf[mt]);
          }
          if constexpr (s == 2 && u == 9) st64(rgr, (u32x2){f2u(sel<float>(hl, Gt.y, Gt.x)), epc}, ca * 2 + hl * 8, ci * 1024);
          if constexpr (s == 3 && u < 4) {
            const bf16x8 one = __builtin_bit_cast(bf16x8, (u32x4){0x3F803F80u, 0x3F803F80u, 0x3F803F80u, 0x3F803F80u});
            cs[u] = MFMA(one, wv[u][1], MFMA(one, wv[u][0], ((f32x4){0, 0, 0, 0}))); PIN(cs[u]);
          }
          if constexpr (s == 3 && u == 9)
            st64(rws, (u32x2){f2u(sel4(g & 1, g & 2, cs[0][0], cs[1][0], cs[2][0], cs[3][0])), epc}, (g < 3 ? g * 64 + wq * 16 + r15 : 192 + (wq & 1) * 16 + r15) * 8u, CG_OFF + (cj * 16 + ci) * 2048);
          if constexpr ((s == 3 && u >= 4 && u < 9) || (s == 4 && u < 5) || (s == 5 && u >= 1 && u < 7)) {
            constexpr int t = s == 3 ? u - 4 : s == 4 ? 5 + u : 9 + u;
            f32x2 v0 = cmulc(cof, (f32x2){acc[0][0][t], acc[0][1][t]}), v1 = cmulc(cof, (f32x2){acc[1][0][t], acc[1][1][t]}); PIN2(v0, v1);
            acc[0][0][t] = v0.x; acc[0][1][t] = v0.y; acc[1][0][t] = v1.x; acc[1][1][t] = v1.y;
          }
          if constexpr (s == 4 && u >= 6 && u < 10) {
#pragma unroll
            for (int q = 2 * u - 12; q < 2 * u - 10; ++q) { lbo[q] = (ci - 8 - 8 * hl + q) * 1024 + ca * 2; ld128a(lb[q], rgr, lbo[q], 0u); }
          }
          if constexpr (s == 5 && u == 8) {
            if (!wm) pw0 = (f32x2){1.f, 0.f};
            pw0 = cmul(pw0, (f32x2){sel<float>(hl, l16.x, 1.f), sel<float>(hl, l16.y, 0.f)}); pw1 = cmul(pw0, pw1); PIN2(pw0, pw1);
          }
          if constexpr (s == 5 && (u == 7 || u == 9)) {
#pragma unroll
            for (int q = 2 * u - 14; q < 2 * u - 10; ++q) { lbe[q] = (int)lbo[q] < 0 ? 0u : epc; PIN(lbe[q]); }
          }
          if constexpr (s == 6 && u < 8) {
            PIN(lb[u]); gd |= (lb[u][1] ^ lbe[u]) | (lb[u][3] ^ lbe[u]); cr = cfma(l128, cr, (f32x2){u2f(lb[u][0]), u2f(lb[u][2])});
            PIN2(cr, gd);
          }
          if constexpr (s == 6 && u == 8) {
            if (__builtin_expect(anyl(gd != 0), 0)) {
              for (unsigned sp = 0; sp < SPIN_CAP; ++sp) {
                __builtin_amdgcn_s_sleep(2);
#pragma unroll
                for (int q = 0; q < 8; ++q) ld128a(lb[q], rgr, lbo[q], 0u);
                waitv<0>();
                if (!anyl(bad())) break;
              }
              cr = (f32x2){0, 0};
#pragma unroll
              for (int q = 0; q < 8; ++q) cr = cfma(l128, cr, (f32x2){u2f(lb[q][0]), u2f(lb[q][2])});
            }
            gd = 0;
            const auto sx_ = __builtin_amdgcn_permlane32_swap(f2u(cr.x), f2u(cr.x), false, false), sy_ = __builtin_amdgcn_permlane32_swap(f2u(cr.y), f2u(cr.y), false, false);
            cr = cfma(l1k, (f32x2){u2f(sx_[1]), u2f(sy_[1])}, (f32x2){u2f(sx_[0]), u2f(sy_[0])});
            PIN(cr);
          }
          if constexpr (s == 6 && u == 9) { st[0] = cmulc(cof, cfma(pw0, cr, Pf[0])); PIN(st[0]); }
          if constexpr (s == 7 && u == 0) { st[1] = cmulc(cof, cfma(pw1, cr, Pf[1])); PIN(st[1]); }
          if constexpr ((s == 7 && u >= 2 && u < 10) || (s == 8 && u >= 1 && u < 9)) {
            constexpr int t = s == 7 ? u - 2 : 7 + u;
            if constexpr (t & 1) { so[0] = st[0]; so[1] = st[1]; }
#pragma unroll
            for (int mt = 0; mt < 2; ++mt) st[mt] = cfma((f32x2){lam.x, -lam.y}, st[mt], (f32x2){acc[mt][0][t], acc[mt][1][t]});
            if constexpr (t & 1) {
#pragma unroll
              for (int mt = 0; mt < 2; ++mt) { ob[mt][0][(t >> 1) & 3] = pk2(so[mt].x, st[mt].x); ob[mt][1][(t >> 1) & 3] = pk2(so[mt].y, st[mt].y); PIN2(ob[mt][0][(t >> 1) & 3], ob[mt][1][(t >> 1) & 3]); }
            }
            PIN2(st[0], st[1]);
          }
          if constexpr ((s == 7 || s == 8) && u == 10) {
            constexpr int hf = s - 7;
#pragma unroll
            for (int q = 0; q < 4; ++q) { const int mt = q >> 1, pt = q & 1;
              st128(rws, (u32x4){ob[mt][pt][0], ob[mt][pt][1], ob[mt][pt][2], ob[mt][pt][3]},
                    (2 * wn + (r31 >> 4)) * 4096 + wm * 2048 + hf * 1024 + (2 * mt + hl) * 256 + ((r15 ^ (hf << 2)) * 16), H_OFF + ci * H_PANEL + (2 * cj + pt) * 16384); }
          }
          if constexpr (s == 9 && u == 0) { if (ci == 15 && w == 1) stflag(rws, epc, lane, EPC_OFF + cj * 128); }
          if constexpr (s == 12 && u == 2) { if (w == 0) ld32a(pv, rws, fvo, flagb + FK_H * 128); }
          if constexpr ((s == 10 || s == 11) && (u & 1) && u < 8) { constexpr int q = 4 * (s - 10) + (u >> 1); ld128a(cg[q], rws, cgo, CG_OFF + (cj * 16 + q) * 2048); }
          if constexpr (s == 13 && u < 4) {
#pragma unroll
            for (int q = 2 * u; q < 2 * u + 2; ++q) { PIN(cg[q]); gd |= (cg[q][1] ^ epc) | (cg[q][3] ^ epc); c0s += u2f(cg[q][0]); c1s += u2f(cg[q][2]); }
            PIN(gd); PIN(c0s); PIN(c1s);
          }
          if constexpr (s == 13 && u == 4) {
            if (__builtin_expect(anyl(gd != 0), 0)) {
              for (unsigned sp = 0; sp < SPIN_CAP; ++sp) { __builtin_amdgcn_s_sleep(2);
#pragma unroll
                for (int q = 0; q < 8; ++q) ld128a(cg[q], rws, cgo, CG_OFF + (cj * 16 + q) * 2048);
                waitv<0>(); if (!anyl(gbad())) break; }
              c0s = 0.f; c1s = 0.f;
#pragma unroll
              for (int q = 0; q < 8; ++q) { c0s += u2f(cg[q][0]); c1s += u2f(cg[q][2]); }
            }
            c0s = sum32(c0s); c1s = sum32(c1s); PIN(c0s); PIN(c1s);
          }
          if constexpr (s == 13 && u == 6) {
            const float v = sel<float>(hl, c1s, c0s), hi = rbf(v), bb = rdf(T_B1 + (2 * cp + hl) * 4);
            *(LAS u32x4*)(size_t)(T_WA + (2 * cp + hl) * 16) = (u32x4){pk2(hi, v - hi), pk2(hi, bb), pk2(bb, 0.f), 0u};
          }
        };
        if constexpr (s < 13) { sfor<11>([&](auto U) AI { G2(IC<s + 3>{}, U); SB(); work(U); SB(); }); if constexpr (s < 12) nexts(); }
        if constexpr (s == 12) { ra = rws;
#pragma unroll
          for (int q = 0; q < 4; ++q) { vb[q] = va[q]; va[q] = lane * 16; }
          ta = t3a; tb = t3b; sl = 32768; sx = 0; next3(); }
        if constexpr (s >= 13) { ival(G, work); sl = nq(sl); next3(); asm volatile("s_nop 4"); }
        if constexpr (s == 14) ival(G, nowork);
      });
      waitl0(); waitv<16>(); bar();
    }
    f32x4 acc[4][4];
#pragma unroll
    for (int a = 0; a < 4; ++a)
#pragma unroll
      for (int b = 0; b < 4; ++b) acc[a][b] = (f32x4){0, 0, 0, 0};
    bf16x8 fa[2][4], fb[2][4];
    const int kap = 4 * (g & 1) + q2, hf = p >> 1;
    const unsigned wb = 16384 + (4 * g + q2) * 256 + p * 8, hb = (g >> 1) * 4096 + wm * 2048 + hf * 1024 + (8 * (g & 1) + q2) * 16 + (p & 1) * 8;
    const unsigned cb3[4] = {hb + 64u * hf, hb + 64u * (1 ^ hf), 16384 + wn * 8192u + r15 * 128 + ((g ^ (r15 >> 1)) * 16), 16384 + wn * 8192u + r15 * 128 + (((4 + g) ^ (r15 >> 1)) * 16)};
    const unsigned cb4[5] = {wm * 4096u + lane * 16, wb + (((4 * wn + 0) ^ kap) * 32), wb + (((4 * wn + 1) ^ kap) * 32), wb + (((4 * wn + 2) ^ kap) * 32), wb + (((4 * wn + 3) ^ kap) * 32)};
    unsigned ba[5];
    sl = 32768;
#pragma unroll
    for (int b = 0; b < 4; ++b) { ba[b] = sl + cb3[b]; PIN(ba[b]); }
    auto rd3 = [&](int r) AI { const int ks = r / 12, xx = r % 12;
      if (xx < 2) sethalf(fa[ks][0], xx, rtr(ba[xx] + ks * 8192));
      else if (xx < 6) fb[ks][xx - 2] = rd128(ba[2 + ks] + (xx - 2) * 2048);
      else { const int a = (xx - 4) >> 1, h = xx & 1; sethalf(fa[ks][a], h, rtr(ba[h] + ks * 8192 + a * 256)); } };
    auto rd4 = [&](int r) AI { const int ks = r / 12, xx = r % 12;
      if (xx == 0) fa[ks][0] = rd128(ba[0] + ks * 8192);
      else if (xx <= 8) { const int b = (xx - 1) >> 1, h = (xx - 1) & 1; sethalf(fb[ks][b], h, rtr(ba[1 + b] + ks * 8192 + h * 4096)); }
      else fa[ks][xx - 8] = rd128(ba[0] + ks * 8192 + (xx - 8) * 1024); };
    auto mm = [&](int ks, int m) AI { acc[m >> 2][m & 3] = MFMA(fb[ks][m & 3], fa[ks][m >> 2], acc[m >> 2][m & 3]); };
    auto up3 = [&](int b) AI { if (b == 0) sl = nq(sl); ba[b] = sl + cb3[b]; PIN(ba[b]); };
    auto up4 = [&](int b) AI { if (b == 0) sl = nq(sl); ba[b] = sl + cb4[b]; PIN(ba[b]); };
    period<16, 4, true, 24, LASTR, 4>(rd3, mm, up3); waitl0(); waitv<8>(); bar();
    for (int s = 1; s < 15; ++s) { period<16, 4, false, 24, LASTR, 4>(rd3, mm, up3); waitl0(); waitv<0>(); bar(); }
    period<16, 4, false, 24, LASTR, 5>(rd3, mm, up4); waitl0(); bar();
    {
      float gr[4]; int seen = 0;
#pragma unroll
      for (int a = 0; a < 4; ++a) gr[a] = rdf(T_ROW + 1536 + (64 * wm + 16 * a + r15) * 4);
      auto mmg = [&](int ks, int m) AI { mm(ks, m); ++seen; const int t = seen - 7;
        if (t >= 0 && t < 16) { acc[t >> 2][t & 3] *= gr[t >> 2]; PIN(acc[t >> 2][t & 3]); } };
      period<16, 4, false, 24, LASTR, 5>(rd4, mmg, up4); waitl0(); bar();
    }
    for (int s = 1; s < n4; ++s) { period<16, 4, false, 24, LASTR, 5>(rd4, mm, up4); waitl0(); bar(); }
#pragma unroll
    for (int m = LAG; m < 16; ++m) mm(1, m);
    if (half) {
      const unsigned pb = ((sl + 65536u + wm * 32768u) & 131071u) + wn * 16384 + g * 256;
#pragma unroll
      for (int a = 0; a < 4; ++a)
#pragma unroll
        for (int b = 0; b < 4; ++b) *(LAS f32x4*)(size_t)(pb + (a * 4 + b) * 1024 + ((r15 ^ (4 * b + g)) * 16)) = acc[a][b];
      waitl0();
    } else {
#pragma unroll
      for (int a = 0; a < 4; ++a)
#pragma unroll
        for (int bp = 0; bp < 2; ++bp)
          st128(rws, (u32x4){pk2(acc[a][2 * bp][0], acc[a][2 * bp][1]), pk2(acc[a][2 * bp][2], acc[a][2 * bp][3]), pk2(acc[a][2 * bp + 1][0], acc[a][2 * bp + 1][1]), pk2(acc[a][2 * bp + 1][2], acc[a][2 * bp + 1][3])},
                (a * 2 + bp) * 1024 + g * 256 + ((r15 ^ (8 * wn + 4 * bp + g)) * 16), PART_OFF + (ci * 8 + cnt) * 32768 + wq * 8192);
      waitv<0>();
    }
    bar();
    if (!half && w == 0) stflag(rws, epr, lane, flagb + FK_P * 128 + cnt * 4);
  } else {
    constexpr bool isx = !PROFX; (void)isx;
    unsigned epr, pv, t3a, t3b;
    {
      u32x4 xr[2][2], gq[2], rgv[2];
      ra = rx; rb = mk(sel<const u16*>(wq & 1, bim, bre), 1024 * 2048);
#pragma unroll
      for (int q = 0; q < 4; ++q) { va[q] = (16 * hl + 4 * q + ((lane >> 3) & 3)) * 2048 + (((lane & 7) ^ ((4 * q + g) & 7)) * 16) - 1024 * q; vb[q] = vnat(q); }
      auto src1 = [&](int s) AI { const int k = s ^ (s < 2 ? 0 : s < 4 ? (rot & 1) : rot); sa = (ci * 128 + 32 * wq) * 2048 + k * 128; sb = (cj * 64 + 32 * (wq >> 1)) * 2048 + k * 128; sl = ((s + 3) & 3) * 32768; };
      src1(0); ival(G, nowork); src1(1); ival(G, nowork);
      waitv<8>(); bar();
      PH(1);
      src1(2); ival(G, nowork);
      unsigned pdt, plr, pli, bq, oq; f32x2 lp; float lr, li;
      const int tc = tq < 224 ? tq : 223;
      ld32a(epr, rws, 0u, flagb + FK_EP * 128); ld128g(gq[0], gw, lane * 16u); ld128g(gq[1], gw + 512, lane * 16u);
      ld16p(pdt, ldt + 64 * cj, lane * 2u); ld16p(plr, lre + 64 * cj, lane * 2u); ld16p(pli, lim + 64 * cj, lane * 2u);
      ld16p(bq, b1, (224 * cj + (tc < 192 ? ((cj & 1) ? 32 : 0) + tc : ((cj & 1) ? -192 : 0) + tc)) * 2u);
      ld16p(oq, sel<const u16*>(wq < 2, b2, dsk), (cnt * 128 + (tq & 127)) * 2u);
      src1(3); ival(G, nowork);
      float sv[6] = {0, 0, 0, 0, 0, 0}, a1, a2, a3, mean, sd, rstd, qv, gg;
      auto E1 = [](int s) constexpr { return (s == 1 || s == 2) ? 2 : s == 8 ? 1 : s == 11 ? 2 : 0; };
      sfor<12>([&](auto S) AI { constexpr int s = S.value;
        if constexpr (s < 2) waitv<16 + 8>(); else waitv<16 + E1(s - 2) + E1(s - 1)>();
        bar();
        src1(s + 4); ival(G, [&](auto U) AI { constexpr int u = U.value;
          if constexpr ((s == 1 || s == 2) && (u == 2 || u == 5)) ld128p(xr[s - 1][u / 3], rx, lane * 16u, (ci * 128 + 8 * cj + 2 * wq + s - 1) * 2048 + (u / 3) * 1024);
          if constexpr (s >= 4 && s < 7 && u < 6 && 6 * (s - 4) + u < 16) {
            constexpr int id = 6 * (s - 4) + u, r = id >> 3, e = id & 7;
            PIN(xr[r][e >> 2]); PIN(gq[e >> 2]);
            const unsigned v = xr[r][e >> 2][e & 3], gv = gq[e >> 2][e & 3];
            const float x0 = bf2f(v & 0xffffu), x1 = u2f(v & 0xffff0000u);
            sv[r] += x0 + x1; sv[2 + r] = __builtin_fmaf(x1, x1, __builtin_fmaf(x0, x0, sv[2 + r]));
            sv[4 + r] = __builtin_fmaf(x1, u2f(gv & 0xffff0000u), __builtin_fmaf(x0, bf2f(gv & 0xffffu), sv[4 + r]));
            PIN(sv[r]); PIN(sv[2 + r]); PIN(sv[4 + r]);
          }
          if constexpr (s == 7 && u < 6) { sv[u] = dppadd<0x140>(dppadd<0x141>(dppadd<0x4E>(dppadd<0xB1>(sv[u])))); PIN(sv[u]); }
          if constexpr (s == 7 && u == 6) { const bool r1 = lane & 4; a1 = sel<float>(r1, sv[1], sv[0]); a2 = sel<float>(r1, sv[3], sv[2]); a3 = sel<float>(r1, sv[5], sv[4]);
                                            a1 = sum32(sum16(a1)); PIN(a1); PIN(a2); PIN(a3); PIN(epr); epr = __builtin_amdgcn_readfirstlane(epr) + 1u; }
          if constexpr (s == 8 && u == 0) { a2 = sum32(sum16(a2)); PIN(a2); }
          if constexpr (s == 8 && u == 1) { a3 = sum32(sum16(a3)); PIN(a3); }
          if constexpr (s == 8 && u == 2) { mean = a1 * (1.f / 1024.f); const float var = __builtin_fmaxf(a2 * (1.f / 1024.f) - mean * mean, 0.f) + 1e-5f; rstd = __builtin_amdgcn_rsqf(var); sd = var * rstd;
                                            PIN(mean); PIN(rstd); PIN(sd); }
          if constexpr (s == 8 && u == 3) { qv = 1.f + fexp(__builtin_fminf(a3, 60.f)); gg = __builtin_amdgcn_rcpf(1.f + fexp(__builtin_fminf(-a3, 80.f))); PIN(qv); PIN(gg); }
          if constexpr (s == 8 && u == 4) { a1 = sel4(lane & 1, lane & 2, -2.302208198f * rstd, -0.1029432397f * rstd * rstd * rstd, qv * sd, gg); PIN(a1); }
          if constexpr (s == 8 && u == 5) { const float mh = rbf(mean), sh = rbf(sd);
                                            a2 = sel4(lane & 1, lane & 2, u2f(pk2(-mh, -mh)), u2f(pk2(mh - mean, sh)), u2f(pk2(sd - sh, 0.f)), 0.f); PIN(a2); }
          if constexpr (s == 8 && u == 6) { if (lane < 16) st64(rws, (u32x2){f2u(sel<float>(lane & 8, a2, a1)), epr}, (lane >> 3) * 4096u + (lane & 7) * 8, rgb + (8 * cj + 2 * wq) * 32); }
          if constexpr (s == 2 && u == 0) { PIN(pdt); PIN(plr); PIN(pli); lr = bf2f(plr); li = bf2f(pli); const float dt = rbf(fexp(bf2f(pdt))); lp = (f32x2){fexp(dt * lr), dt * li}; PIN(lp);
                                            PIN(bq); PIN(oq); wrf(T_B1 + tq * 4, bf2f(bq)); wrf(T_OUT + tq * 4, bf2f(oq)); }
          if constexpr (s == 3 && u == 1) { float sn, cs_; sincos_f(lp.y, sn, cs_); lp = (f32x2){lp.x * cs_, lp.x * sn}; PIN(lp); wrf2(T_CON + lane * 8, lp); }
          if constexpr (s == 3 && u == 5) { const float nr = lp.x - 1.f, ni = lp.y, den = 2.f * __builtin_amdgcn_rcpf(lr * lr + li * li);
                                            wrf2(T_CON + 512 + lane * 8, (f32x2){(nr * lr + ni * li) * den, (ni * lr - nr * li) * den}); }
          if constexpr ((s == 9 || (s == 10 && u < 2))) { constexpr int q = 8 * (s - 9) + u; lp = cmul(lp, lp); PIN(lp);
            if constexpr (q >= 3 && q < 7) wrf2(T_CON + (q - 1) * 512 + lane * 8, lp);
            if constexpr (q == 9) wrf2(T_CON + 3072 + lane * 8, lp); }
          if constexpr (s == 10 && u == 3) table3(t3a, t3b);
          if constexpr (s == 11 && u == 6) { ld128a(rgv[0], rws, tq * 16u, rgb); ld128a(rgv[1], rws, tq * 16u + 4096, rgb); }
        });
      });
      setup2(); sx = 0; nexts();
      const unsigned sb0 = sb;
      waitv<16 + 2>(); bar();
      sfor<4>([&](auto U) AI { G2(IC<0>{}, U); }); nexts();
      const unsigned sb1 = sb; asm volatile("s_nop 4");
      sfor<4>([&](auto U) AI { G2(IC<1>{}, U); }); nexts();
      waitv<16 + 2>(); bar();
      sb = sb0; sfor<7>([&](auto U) AI { G2(IC<0>{}, IC<U.value + 4>{}); });
      sfor<4>([&](auto U) AI { G2(IC<2>{}, U); });
      waitv<19>(); bar();
      sb = sb1; sfor<7>([&](auto U) AI { G2(IC<1>{}, IC<U.value + 4>{}); });
      {
        auto rbad = [&]() AI { PIN(rgv[0]); PIN(rgv[1]); return ((rgv[0][1] ^ epr) | (rgv[0][3] ^ epr) | (rgv[1][1] ^ epr) | (rgv[1][3] ^ epr)) != 0; };
        if (__builtin_expect(anyl(rbad()), 0))
          for (unsigned sp = 0; sp < SPIN_CAP; ++sp) { __builtin_amdgcn_s_sleep(2);
            ld128a(rgv[0], rws, tq * 16u, rgb); ld128a(rgv[1], rws, tq * 16u + 4096, rgb);
            waitv<0>(); if (!anyl(rbad())) break; }
      }
      wrf(T_ROW + (tq & 1) * 1024 + (tq >> 1) * 4, u2f(rgv[0][0])); wrf(T_ROW + (tq & 1) * 1024 + 512 + (tq >> 1) * 4, u2f(rgv[0][2]));
      wrf2(T_XA + tq * 8, (f32x2){u2f(rgv[1][0]), u2f(rgv[1][2])});
      ta = t3a; tb = t3b; sx = 3; next3();
    }
    {
      f32x4 ac1[28];
#pragma unroll
      for (int m = 0; m < 28; ++m) ac1[m] = (f32x4){0, 0, 0, 0};
      {
        bf16x8 fa[2][4], fb[2][8];
        const int f = ((q2 >> 1) & 1) | ((g & 1) << 1);
        const unsigned nb = wm * 8192u + r15 * 128, n0 = (g ^ (r15 >> 1)) * 16, n1 = ((4 + g) ^ (r15 >> 1)) * 16, wk = (g >> 1) * 7168u + p * 8;
        const unsigned cb[8] = {nb + wn * 4096 + n0, nb + wn * 4096 + n1, nb + (1 - wn) * 4096 + n0, nb + (1 - wn) * 4096 + n1,
                                wk + (8 * (g & 1) + q2) * 128 + (((2 * wn) ^ f) * 32), wk + (8 * (g & 1) + q2) * 128 + (((2 * wn + 1) ^ f) * 32),
                                wk + 6144 + (8 * (g & 1) + q2) * 64 + (g & 1) * 32, wk + 6144 + (8 * (g & 1) + q2) * 64 + ((g & 1) ^ 1) * 32};
        unsigned ba[8]; unsigned xn = 98304, wnx = 32768;
        const char* pp0 = lane < 32 ? (const char*)sel<const u16*>(lane & 16, cim, cre) + ((bid * 4 + wq) * 16 + r15) * 128 : (const char*)w2 + ((bid * 4 + wq) * 56 + r15) * 128;
        const char* pp1 = (const char*)w2 + ((bid * 4 + wq) * 56 + (lane < 40 ? 16 + lane : 55)) * 128;
        PIN2(pp0, pp1);
        setm0(T_DUM);
#pragma unroll
        for (int b = 0; b < 8; ++b) { ba[b] = cb[b] + (b < 4 ? 131072u : 0u); PIN(ba[b]); }
        auto rd = [&](int r) AI { const int ks = r / 20, xx = r % 20;
          if (xx == 0) fa[ks][0] = rd128(ba[ks]);
          else if (xx <= 12) { const int b = (xx - 1) >> 1, h = (xx - 1) & 1; sethalf(fb[ks][b], h, rtr(ba[4 + (b & 1)] + (b >> 1) * 2048 + ks * 14336 + h * 512)); }
          else if (xx <= 16) { const int b = (xx - 1) >> 1, h = (xx - 1) & 1; sethalf(fb[ks][b], h, rtr(ba[b] + ks * 14336 + h * 256)); }
          else { const int a = xx - 16; fa[ks][a] = rd128(ba[(a >> 1) * 2 + ks] + (a & 1) * 2048); } };
        auto mm = [&](int ks, int m) AI { const int a = m < 16 ? m >> 3 : (m - 4) / 6, b = m < 16 ? m & 7 : (m - 4) % 6; ac1[m] = MFMA(fb[ks][b], fa[ks][a], ac1[m]); };
        auto up = [&](int b) AI { if (b < 8) { ba[b] = cb[b] + sel<unsigned>(b < 4, xn, wnx); PIN(ba[b]); }
                                  else { wnx = (wnx == 65536u) ? 0u : wnx + 32768u; xn = (xn == 131072u) ? 98304u : xn + 16384u; } };
        waitl0(); waitv<11>(); bar();
        PH(2);
        period<28, 8, true, 40, LASTR + 24, 9>(rd, mm, up); waitl0(); waitv<0>(); bar();
        const int sp = 2 + bid % 5;
        for (int o = 0; o < 3; ++o) {
          for (int s = o == 0 ? 1 : o == 1 ? sp : sp + 7; s < (o == 0 ? sp : o == 1 ? sp + 7 : 16); ++s) { period<28, 8, false, 40, LASTR + 24, 9>(rd, mm, up); waitl0(); bar(); }
          if (o < 2) pfg(pp0);
          pp0 = pp1;
        }
        PH(3);
        setup3(); sl = 0;
        ival(G, [&](auto U) AI { constexpr int u = U.value;
#pragma unroll
          for (int m = LAG + 4 * u; m < LAG + 4 * u + 4; ++m) if (m < 28) mm(1, m); });
#pragma unroll
        for (int a = 0; a < 4; ++a) fa[0][a] = rd128(g ? T_ZERO : T_XA + (64 * wm + 16 * (a ^ (2 * wn)) + r15) * 16);
#pragma unroll
        for (int b = 0; b < 8; ++b) fb[0][b] = rd128(T_WA + ((b < 6 ? (b >> 1) * 64 + wn * 32 + (b & 1) * 16 : 96 + b * 16) + r15) * 16);
        SB();
        sl = nq(sl); next3();
        bar();
        ival(G, [&](auto U) AI { constexpr int u = U.value;
#pragma unroll
          for (int m = 4 * u; m < 4 * u + 4; ++m) if (m < 28) mm(0, m); });
        sl = nq(sl); next3();
      }
      float k0[4], k1[4], qq[4];
#pragma unroll
      for (int a = 0; a < 4; ++a) { const unsigned ro = T_ROW + (64 * wm + 16 * (a ^ (2 * wn)) + r15) * 4; k0[a] = rdf(ro); k1[a] = rdf(ro + 512); qq[a] = rdf(ro + 1024); }
      u32x4 hw[4]; float t[8];
      auto lv0 = [](int s) constexpr { return s < 1 ? 0 : s < 7 ? (6 * (s - 1) < 32 ? 6 * (s - 1) : 32) : (32 + 4 * (s - 7) < 112 ? 32 + 4 * (s - 7) : 112); };
      auto st3 = [](int s) constexpr { return (s == 6 || s == 14 || s == 22) ? 4 : s == 26 ? 2 : 0; };
      sfor<36>([&](auto S1) AI { constexpr int s = S1.value + 1;
        if constexpr (st3(s - 2) > 0) waitv<8>();
        else if constexpr (s >= 2) waitv<16 + st3(s - 1)>();
        if constexpr (s == 12 || s == 20 || s == 28 || s == 36) {
          if (w == 4) { waitv<8>(); check_flags(pv, rws, fvo, flagb + (FK_M1 + (s - 12) / 8) * 128, lane, s == 36 ? 0 : 8 * half, 8 * half + 8, epr); } }
        SB(); bar();
        if constexpr (s == 16) { PH(4); }
        if constexpr (st3(s - 2) > 0) { if (w == 5) stflag(rws, epr, lane, flagb + (FK_M1 + (s - 4) / 8) * 128 + cj * 4); }
        if constexpr (s == 11 || s == 19 || s == 27 || s == 35) { if (w == 4) ld32a(pv, rws, fvo, flagb + (FK_M1 + (s - 11) / 8) * 128); }
#define UU(e) ac1[m0 + ((e) >> 2)][(e) & 3]
#define LVL(expr) do { _Pragma("unroll") for (int e = 0; e < 8; ++e) { t[e] = (expr); PIN(t[e]); } } while (0)
        ival(G, [&](auto U) AI { constexpr int u = U.value, n = lv0(s + 1) - lv0(s), li = n == 6 ? (u < 3 ? u : u - 1) : u >> 1;
          if constexpr ((n == 6 ? (u != 3 && u != 7) : !(u & 1)) && li < n) {
            constexpr int L = lv0(s) + li, v = L >> 3, l = L & 7, a = v & 3, m0 = (a < 2 ? 8 * a : 4 + 6 * a) + 2 * (v >> 2);
            if constexpr (l == 0) LVL(UU(e) * UU(e));
            if constexpr (l == 1) LVL(__builtin_fmaf(t[e], k1[a], k0[a]));
            if constexpr (l == 2) LVL(UU(e) * t[e]);
            if constexpr (l == 3) LVL(__builtin_amdgcn_exp2f(t[e]));
            if constexpr (l == 4) LVL(__builtin_fmaf(t[e], qq[a], qq[a]));
            if constexpr (l == 5) LVL(__builtin_amdgcn_rcpf(t[e]));
            if constexpr (l == 6) LVL(UU(e) * t[e]);
            if constexpr (l == 7) { hw[a] = (u32x4){pk2(t[0], t[1]), pk2(t[2], t[3]), pk2(t[4], t[5]), pk2(t[6], t[7])}; PIN(hw[a]); }
          } });
#pragma unroll
        for (int q = 0; q < st3(s); ++q) st128(rws, hw[q], lane * 16u + (4 * wm + (q ^ (2 * wn))) * 1024, ci * HID_PANEL + kb_of(cj, s < 26 ? 2 * (s >> 3) + wn : 6) * 8192);
        sl = nq(sl);
        if constexpr (s < 11) next3(); else nexts();
        if constexpr (s == 11) {
          rb = mk(w2, 3584 * 2048);
#pragma unroll
          for (int q = 0; q < 4; ++q) vb[q] = (4 * q + g) * 2048 + ((((r15 >> 1) ^ (4 * (q & 1) + g)) * 2) + (lane & 1)) * 16 - 1024 * q;
        }
      });
    }
    static_assert(MOVE >= 0 && MOVE <= 3, "");
    for (int s = 21; s < n4 - 4; ++s) { waitv<16>(); bar(); ival(G, nowork); sl = nq(sl); nexts(); }
    waitv<16>(); bar();
    if (half) {
      sfor<8>([&](auto U) AI { constexpr int u = U.value; if constexpr ((u & 3) == 0) setm0(sl + wq * 8192 + u * 1024);
        dma<(u & 3) * 1024>(rx, (4u * (u & 3) + g) * 2048 + r15 * 16 - (u & 3) * 1024, (ci * 128 + 32 * wq + 16 * (u >> 2)) * 2048 + cnt * 256); });
      waitv<8 + 8>();
    } else waitv<8>();
    bar();
    if (half) waitv<8>(); else waitv<0>();
    bar();
    ld32a(pv, rws, fvo, flagb + FK_P * 128);
    bar();
    waitv<0>();
    if (half) {
      check_flags(pv, rws, fvo, flagb + FK_P * 128, lane, cnt, cnt + 1, epr);
      sfor<8>([&](auto U) AI { constexpr int u = U.value; if constexpr ((u & 3) == 0) setm0(nq(sl) + wq * 8192 + u * 1024);
        dma1<(u & 3) * 1024>(rws, lane * 16u, PART_OFF + (ci * 8 + cnt) * 32768 + wq * 8192 + (u >> 2) * 4096); });
      if (cnt == 0 && w == 4) stflag(rws, epr, lane, flagb + FK_EP * 128);
    }
    PH(5);
    waitv<0>(); bar();
  }
  if (half) {
    const int bg = r31 & 15, sw = (r31 >> 4) * 8 + (bg >> 3) * 4 + (bg & 3);
    const unsigned pa = ((sl + 65536u + (w >> 2) * 32768u) & 131071u) + (r31 >> 4) * 16384 + (wq * 4 + (bg >> 2)) * 1024 + (bg & 3) * 256;
    const unsigned pp = nq(sl) + ((w >> 2) * 2 + (r31 >> 4)) * 8192 + (wq * 2 + (bg >> 3)) * 1024 + (bg & 3) * 256 + ((bg >> 2) & 1) * 8;
    const f32x4 bv = rdf4(T_OUT + r31 * 16), dv = rdf4(T_OUT + 512 + r31 * 16);
#pragma unroll
    for (int u = 0; u < 8; ++u) {
      const int row = 2 * u + hl;
      const f32x4 av = rdf4(pa + ((row ^ bg) * 16));
      const u32x2 pw = *(LAS u32x2*)(size_t)(pp + ((row ^ sw) * 16)), xw = *(LAS u32x2*)(size_t)(sl + (16 * w + row) * 256 + r31 * 8);
      const float gg = rdf(T_ROW + 1536 + (16 * w + row) * 4);
      const f32x4 pf = {bf2f(pw[0] & 0xffffu), u2f(pw[0] & 0xffff0000u), bf2f(pw[1] & 0xffffu), u2f(pw[1] & 0xffff0000u)};
      const f32x4 xf = {bf2f(xw[0] & 0xffffu), u2f(xw[0] & 0xffff0000u), bf2f(xw[1] & 0xffffu), u2f(xw[1] & 0xffff0000u)};
      f32x4 r;
#pragma unroll
      for (int e = 0; e < 4; ++e) r[e] = (av[e] + pf[e]) + __builtin_fmaf(gg, __builtin_fmaf(xf[e], dv[e], -bv[e]), bv[e]);
      st128<OAUX>(mk(out, 8u << 20), __builtin_bit_cast(u32x4, r), (unsigned)((16 * w + hl + 2 * u) * 4096 + r31 * 16), ci * 128 * 4096 + cnt * 512);
    }
  }
}

extern "C" void kernel_launch(void* const* d_in, const int* in_sizes, int n_in, void* d_out, int out_size, void* d_ws, size_t ws_size, hipStream_t stream) {
  (void)in_sizes; (void)n_in; (void)out_size; (void)ws_size;
  (void)hipFuncSetAttribute((const void*)fused_kernel, hipFuncAttributeMaxDynamicSharedMemorySize, LDS_BYTES);
  fused_kernel<<<256, 512, LDS_BYTES, stream>>>((const u16*)d_in[0], (const u16*)d_in[1], (const u16*)d_in[2], (const u16*)d_in[3], (const u16*)d_in[4],
      (const u16*)d_in[5], (const u16*)d_in[6], (const u16*)d_in[7], (const u16*)d_in[8], (const u16*)d_in[10], (const u16*)d_in[11], (const u16*)d_in[12],
      (const u16*)d_in[13], (const u16*)d_in[14], (char*)d_ws, (float*)d_out);
}
```

```cpp
#include <hip/hip_runtime.h>
#include <utility>

typedef unsigned short u16;
typedef __bf16 bf16x2 __attribute__((ext_vector_type(2)));
typedef __bf16 bf16x4 __attribute__((ext_vector_type(4)));
typedef __bf16 bf16x8 __attribute__((ext_vector_type(8)));
typedef float f32x2 __attribute__((ext_vector_type(2)));
typedef float f32x4 __attribute__((ext_vector_type(4)));
typedef float f32x16 __attribute__((ext_vector_type(16)));
typedef unsigned u32x2 __attribute__((ext_vector_type(2)));
typedef unsigned u32x4 __attribute__((ext_vector_type(4)));
typedef __amdgpu_buffer_rsrc_t rsrc_t;
#define LAS __attribute__((address_space(3)))
#define DI __device__ __forceinline__
#define AI __attribute__((always_inline))
#define SB() __builtin_amdgcn_sched_barrier(0)
#define PIN(x) asm volatile("" : "+v"(x))
#define MFMA(a, b, c) __builtin_amdgcn_mfma_f32_16x16x32_bf16(a, b, c, 0, 0, 0)
#define MFMA32(a, b, c) __builtin_amdgcn_mfma_f32_32x32x16_bf16(a, b, c, 0, 0, 0)
template <int V> using IC = std::integral_constant<int, V>;

constexpr unsigned HID_PANEL = 112 * 8192, H_OFF = 16 * HID_PANEL, H_PANEL = 32 * 16384;
constexpr unsigned PART_OFF = H_OFF + 16 * H_PANEL;
constexpr unsigned GRAN_OFF = PART_OFF + 128 * 32768;
constexpr unsigned RG_OFF = GRAN_OFF + 256 * 1024;
constexpr unsigned CG_OFF = RG_OFF + 16 * 8192;
constexpr unsigned FLAG_OFF = CG_OFF + 256 * 2048;
constexpr unsigned EPC_OFF = FLAG_OFF + 16 * 1024;
constexpr unsigned WS_BYTES = EPC_OFF + 16 * 128;
constexpr int FK_M1 = 0, FK_M2 = 1, FK_M3 = 2, FK_M4 = 3, FK_H = 4, FK_P = 5, FK_EP = 6;
constexpr unsigned TAB = 147456, T_ROW = TAB, T_XA = TAB + 2048, T_OUT = TAB + 4096, T_B1 = TAB + 5120, T_CS2 = TAB + 6144, T_WA = TAB + 8192, T_ZERO = TAB + 12288, T_AGG = T_WA, T_DUM = TAB + 12544, T_CON = TAB + 12800;
constexpr unsigned LDS_BYTES = 163840;
#define XMAP 1
#define ROTM 3
#define OAUX 2
#define SP0 4
#define SPN 6
#define PTE 1
#define MOVE 3
#define PROFX false
constexpr int LAG = 2, LASTR = 25;
constexpr unsigned SPIN_CAP = 1u << 16;

DI rsrc_t mk(const void* p, unsigned bytes) { return __builtin_amdgcn_make_buffer_rsrc((void*)p, 0, bytes, 0x00020000); }
DI void setm0(unsigned lds) { asm volatile("s_mov_b32 m0, %0\n\ts_nop 0" ::"s"(lds) : "memory"); }
template <int OFF> DI void dma(rsrc_t r, unsigned voff, unsigned soff) {
  asm volatile("buffer_load_dwordx4 %0, %1, %2 offen offset:%3 lds" ::"v"(voff), "s"(r), "s"(soff), "i"(OFF) : "memory");
}
template <int OFF> DI void dma1(rsrc_t r, unsigned voff, unsigned soff) {
  asm volatile("buffer_load_dwordx4 %0, %1, %2 offen offset:%3 sc1 lds" ::"v"(voff), "s"(r), "s"(soff), "i"(OFF) : "memory");
}
#define PIN2(x, y) asm volatile("" : "+v"(x), "+v"(y))
#define PINS(x) asm volatile("" : "+s"(x))
typedef const void* const __attribute__((address_space(4)))* kargs_t;
DI const char* karg(int i) { kargs_t ka = (kargs_t)__builtin_amdgcn_kernarg_segment_ptr(); PINS(ka); return (const char*)ka[i]; }
enum { A_X, A_LRE, A_LIM, A_BRE, A_BIM, A_CRE, A_CIM, A_LDT, A_DSK, A_W1, A_B1, A_W2, A_B2, A_GW, A_WS, A_OUT };
template <int N> DI void waitv() { asm volatile("s_waitcnt vmcnt(%0)" ::"i"(N) : "memory"); }
DI void waitl0() { SB(); __builtin_amdgcn_s_waitcnt(0xC07F); asm volatile("" ::: "memory"); }
#define PH(k)
DI void bar() { asm volatile("s_barrier" ::: "memory"); SB(); }
DI bf16x8 rd128(unsigned a) { return *(LAS bf16x8*)(size_t)a; }
DI u32x4 rdu4(unsigned a) { return *(LAS u32x4*)(size_t)a; }
DI f32x4 rdf4(unsigned a) { return *(LAS f32x4*)(size_t)a; }
DI f32x2 rdf2(unsigned a) { return *(LAS f32x2*)(size_t)a; }
DI float rdf(unsigned a) { return *(LAS float*)(size_t)a; }
DI void wrf(unsigned a, float v) { *(LAS float*)(size_t)a = v; }
DI void wrf2(unsigned a, f32x2 v) { *(LAS f32x2*)(size_t)a = v; }
DI bf16x4 rtr(unsigned a) { return __builtin_amdgcn_ds_read_tr16_b64_v4bf16((LAS bf16x4*)(size_t)a); }
DI void sethalf(bf16x8& f, int h, bf16x4 v) {
  if (h == 0) f = __builtin_shufflevector(v, v, 0, 1, 2, 3, -1, -1, -1, -1);
  else { bf16x8 t = __builtin_shufflevector(v, v, -1, -1, -1, -1, 0, 1, 2, 3); f = __builtin_shufflevector(f, t, 0, 1, 2, 3, 12, 13, 14, 15); }
}
DI unsigned f2u(float f) { return __builtin_bit_cast(unsigned, f); }
DI float u2f(unsigned u) { return __builtin_bit_cast(float, u); }
DI float shx(int lane, float v, int m) { return u2f(__builtin_amdgcn_ds_bpermute((lane ^ m) * 4, (int)f2u(v))); }
DI float sum16(float v) { const auto r = __builtin_amdgcn_permlane16_swap(f2u(v), f2u(v), false, false); return u2f(r[0]) + u2f(r[1]); }
DI float sum32(float v) { const auto r = __builtin_amdgcn_permlane32_swap(f2u(v), f2u(v), false, false); return u2f(r[0]) + u2f(r[1]); }
DI float fexp(float v) { return __builtin_amdgcn_exp2f(v * 1.4426950408889634f); }
DI float bf2f(unsigned v) { return u2f(v << 16); }
DI unsigned pk2(float lo, float hi) { f32x2 v = {lo, hi}; return __builtin_bit_cast(unsigned, __builtin_convertvector(v, bf16x2)); }
DI float rbf(float f) { return u2f(pk2(f, 0.f) << 16); }
DI float dot2(unsigned a, unsigned b, float c) { return __builtin_amdgcn_fdot2_f32_bf16(__builtin_bit_cast(bf16x2, a), __builtin_bit_cast(bf16x2, b), c, false); }
template <int C> DI float dppadd(float v) { return v + u2f(__builtin_amdgcn_update_dpp(0, (int)f2u(v), C, 0xf, 0xf, true)); }
DI float sel4(bool b0, bool b1, float a, float b, float c, float d) { PIN(a); PIN(b); PIN(c); PIN(d); const float lo = b0 ? b : a, hi = b0 ? d : c; return b1 ? hi : lo; }
DI f32x2 cmul(f32x2 a, f32x2 b) { return (f32x2){a.x * b.x - a.y * b.y, a.x * b.y + a.y * b.x}; }
DI f32x2 cmulc(f32x2 a, f32x2 b) { return (f32x2){__builtin_fmaf(a.x, b.x, -(a.y * b.y)), __builtin_fmaf(-a.x, b.y, -(a.y * b.x))}; }
DI f32x2 cfma(f32x2 a, f32x2 b, f32x2 c) { return (f32x2){__builtin_fmaf(a.x, b.x, __builtin_fmaf(-a.y, b.y, c.x)), __builtin_fmaf(a.x, b.y, __builtin_fmaf(a.y, b.x, c.y))}; }
template <int AUX = 16> DI void st128(rsrc_t r, u32x4 v, unsigned voff, unsigned soff) {
  __builtin_amdgcn_raw_buffer_store_b128(v, r, voff, soff, AUX);
  asm volatile("s_nop 1" ::"v"(v));
}
DI void st64(rsrc_t r, u32x2 v, unsigned voff, unsigned soff) { __builtin_amdgcn_raw_buffer_store_b64(v, r, voff, soff, 16); }
DI void st32(rsrc_t r, unsigned v, unsigned voff, unsigned soff) { __builtin_amdgcn_raw_buffer_store_b32(v, r, voff, soff, 16); }
DI void stflag(rsrc_t r, unsigned v, int lane, unsigned soff) { if (lane == 0) st32(r, v, 0u, soff); }
DI void ld32a(unsigned& d, rsrc_t r, unsigned voff, unsigned soff) { asm volatile("buffer_load_dword %0, %1, %2, %3 offen sc1" : "=v"(d) : "v"(voff), "s"(r), "s"(soff) : "memory"); }
DI void ld16p(unsigned& d, const u16* p, unsigned voff) { asm volatile("global_load_ushort %0, %1, %2" : "=v"(d) : "v"(voff), "s"(p) : "memory"); }
DI void ld64p(u32x2& d, rsrc_t r, unsigned voff, unsigned soff) { asm volatile("buffer_load_dwordx2 %0, %1, %2, %3 offen" : "=v"(d) : "v"(voff), "s"(r), "s"(soff) : "memory"); }
DI void ld64a(u32x2& d, rsrc_t r, unsigned voff, unsigned soff) { asm volatile("buffer_load_dwordx2 %0, %1, %2, %3 offen sc1" : "=v"(d) : "v"(voff), "s"(r), "s"(soff) : "memory"); }
DI void ld128g(u32x4& d, const u16* p, unsigned voff) { asm volatile("global_load_dwordx4 %0, %1, %2" : "=v"(d) : "v"(voff), "s"(p) : "memory"); }
DI void pfl(unsigned& d, rsrc_t r, unsigned voff, unsigned soff) { asm volatile("buffer_load_dword %0, %1, %2, %3 offen" : "+v"(d) : "v"(voff), "s"(r), "s"(soff) : "memory"); }
template <class T> DI T sel(bool c, T a, T b) { return c ? a : b; }
DI void pfg(const char* p) { asm volatile("global_load_lds_dword %0, off" ::"v"(p) : "memory"); }
DI bool anyl(bool b) { return __builtin_amdgcn_ballot_w64(b) != 0; }
DI void ld128p(u32x4& d, rsrc_t r, unsigned voff, unsigned soff) { asm volatile("buffer_load_dwordx4 %0, %1, %2, %3 offen" : "=v"(d) : "v"(voff), "s"(r), "s"(soff) : "memory"); }
DI void sincos_f(float t, float& sn, float& cs) {
  const float k = __builtin_rintf(t * 0.63661977236f);
  float r = __builtin_fmaf(k, -1.5707962513f, t); r = __builtin_fmaf(k, -7.5497894159e-08f, r); r = __builtin_fmaf(k, -5.3903029534e-15f, r);
  const float z = r * r;
  const float sp = __builtin_fmaf(r * z, __builtin_fmaf(z, __builtin_fmaf(z, __builtin_fmaf(z, 2.7183114939e-06f, -1.9839334836e-04f), 8.3333293858e-03f), -1.6666666642e-01f), r);
  const float cp = __builtin_fmaf(z, __builtin_fmaf(z, __builtin_fmaf(z, __builtin_fmaf(z, 2.4390448796e-05f, -1.3886763775e-03f), 4.1666623324e-02f), -4.9999999726e-01f), 1.f);
  const int q = (int)k;
  const float a = (q & 1) ? cp : sp, b = (q & 1) ? sp : cp;
  sn = (q & 2) ? -a : a; cs = ((q + 1) & 2) ? -b : b;
}
DI void ld128a(u32x4& d, rsrc_t r, unsigned voff, unsigned soff) { asm volatile("buffer_load_dwordx4 %0, %1, %2, %3 offen sc1" : "=v"(d) : "v"(voff), "s"(r), "s"(soff) : "memory"); }

DI unsigned x1off(int a) { return a == 0 ? 131072u : (unsigned)(81920 + a * 16384); }
DI unsigned nq(unsigned s) { return (s + 32768u) & 131071u; }

DI int kb_of(int j, int bl) { return 7 * j + ((j & 1) ? (bl == 6 ? 0 : bl + 1) : bl); }

template <int NR, int LAST> constexpr int nread(int sl) { int n = 0; for (int r = 0; r < NR; ++r) n += (r * LAST) / (NR - 1) <= sl; return n; }
template <int NM, int R0, bool FIRST, int NR, int LAST, int NB, int SL, class RD, class MM, class UP>
DI void slot(RD& rd, MM& mm, UP& upd) {
  constexpr int q = SL + LAG;
  if constexpr (!FIRST && (q == NM || q == NM + R0 || q == 2 * NM)) {
    constexpr int cnt = nread<NR, LAST>(SL - 1) - nread<NR, LAST>(SL - 6);
    __builtin_amdgcn_s_waitcnt(0xC07F | (cnt << 8)); SB();
  }
  if constexpr (!(FIRST && q < NM)) mm((q < NM) ? 1 : ((q < 2 * NM) ? 0 : 1), q % NM);
  SB();
  constexpr int r = (SL * (NR - 1) + LAST - 1) / LAST;
  if constexpr (r < NR && (r * LAST) / (NR - 1) == SL) rd(r);
  constexpr int per = NB > 2 * NM - 1 - LAST ? 2 : 1, u0 = (SL - LAST - 1) * per;
  if constexpr (SL > LAST && u0 < NB) { upd(u0); if constexpr (per == 2 && u0 + 1 < NB) upd(u0 + 1); }
  SB();
}
template <int NM, int R0, bool FIRST, int NR, int LAST, int NB, class RD, class MM, class UP, int... I>
DI void period_(RD& rd, MM& mm, UP& upd, std::integer_sequence<int, I...>) { (slot<NM, R0, FIRST, NR, LAST, NB, I>(rd, mm, upd), ...); }
template <int NM, int R0, bool FIRST, int NR, int LAST, int NB, class RD, class MM, class UP>
DI void period(RD& rd, MM& mm, UP& upd) {
  static_assert(NR - 1 <= LAST && NB <= 2 * (2 * NM - 1 - LAST), "");
  period_<NM, R0, FIRST, NR, LAST, NB>(rd, mm, upd, std::make_integer_sequence<int, 2 * NM>{});
}
template <bool FIRST, int SL, class RD, class MM, class UP> DI void slot32(RD& rd, MM& mm, UP& upd) {
  if constexpr (SL == 12) { __builtin_amdgcn_s_waitcnt(0xC07F | (8 << 8)); SB(); }
  if constexpr (!(FIRST && SL < 12)) mm((SL + 4) & 15);
  SB();
  if constexpr (SL < 2) { rd(2 * SL); rd(2 * SL + 1); } else if constexpr (SL < 14) rd(SL + 2);
  if constexpr ((SL & 3) == 2) { upd(SL >> 2); upd(4 + (SL >> 2)); }
  SB();
}
template <bool FIRST, class RD, class MM, class UP, int... I> DI void period32_(RD& rd, MM& mm, UP& upd, std::integer_sequence<int, I...>) { (slot32<FIRST, I>(rd, mm, upd), ...); }
template <bool FIRST, class RD, class MM, class UP> DI void period32(RD& rd, MM& mm, UP& upd) { period32_<FIRST>(rd, mm, upd, std::make_integer_sequence<int, 16>{}); }
template <class G, class W, int... U> DI void ival_(G& g, W& wk, std::integer_sequence<int, U...>) { ((g(IC<U>{}), SB(), wk(IC<U>{}), SB()), ...); }
template <class G, class W> DI void ival(G&& g, W&& wk) { ival_(g, wk, std::make_integer_sequence<int, 8>{}); }
template <int N, class F, int... I> DI void sfor_(F& f, std::integer_sequence<int, I...>) { (f(IC<I>{}), ...); }
template <int N, class F> DI void sfor(F&& f) { sfor_<N>(f, std::make_integer_sequence<int, N>{}); }

DI void check_flags(unsigned& pv, rsrc_t ws, unsigned voff, unsigned soff, int lane, int lo, int hi, unsigned ep) {
  PIN(pv);
  const bool need = lane >= lo && lane < hi;
  if (__builtin_expect(anyl(need && pv != ep), 0)) {
    for (unsigned sp = 0; sp < SPIN_CAP; ++sp) {
      __builtin_amdgcn_s_sleep(2);
      ld32a(pv, ws, voff, soff); asm volatile("s_waitcnt vmcnt(0)" : "+v"(pv)::"memory");
      if (!anyl(need && pv != ep)) break;
    }
  }
}

#if defined(__HIP_DEVICE_COMPILE__) && __HIP_DEVICE_COMPILE__
__attribute__((target("no-packed-fp32-ops")))
#endif
__global__ __launch_bounds__(512, 2) void fused_kernel(const u16* x, const u16* lre, const u16* lim, const u16* bre, const u16* bim, const u16* cre, const u16* cim,
    const u16* ldt, const u16* dsk, const u16* w1, const u16* b1, const u16* w2, const u16* b2, const u16* gw, char* ws, float* out) {
  extern __shared__ __attribute__((aligned(16))) char shm[];
  unsigned long long pc0; asm volatile("s_getpc_b64 %0" : "=s"(pc0));
  const int lane = threadIdx.x & 63, w = __builtin_amdgcn_readfirstlane(threadIdx.x >> 6);
  const int bid = blockIdx.x, xcd = bid & 7, loc = bid >> 3;
  const int ci = (xcd & 3) * 4 + (loc & 3), cj = (loc >> 4) * 8 + (xcd >> 2) * 4 + ((loc >> 2) & 3);
  const int rot = (ci + cj) & ROTM, cnt = cj & 7, half = cj >> 3, wq = w & 3, tq = threadIdx.x & 255;
  const int n4 = half ? 28 + MOVE : 28 - MOVE;
  const rsrc_t rws = mk(ws, WS_BYTES), rx = mk(x, 2048 * 2048);
  const int wm = (w >> 1) & 1, wn = w & 1, g = lane >> 4, r15 = lane & 15, q2 = (lane >> 2) & 3, p = lane & 3, hl = lane >> 5, r31 = lane & 31;
  const unsigned flagb = FLAG_OFF + ci * 1024, fvo = (lane & 15) * 4;
  unsigned rgb = RG_OFF + ci * 8192; PINS(rgb);
  auto nowork = [&](auto) AI {};
  unsigned va[4], vb[4], vw[7], sl, sa, sb, ta, tb; int sx; rsrc_t ra, rb;
  auto vnat = [&](int u) AI { return (8u * u + (lane >> 3)) * 2048 + (((lane & 7) ^ ((4 * u + g) & 7)) * 16) - 1024 * u; };
  auto nexts = [&]() AI { sa = __builtin_amdgcn_readlane(ta, sx); sb = __builtin_amdgcn_readlane(tb, sx); ++sx; PINS(sa); PINS(sb); };
  auto G = [&](auto U) AI { constexpr int u = U.value;
    if constexpr (u == 0) setm0(sl + wq * 4096);
    if constexpr (u == 4) setm0(sl + 16384 + wq * 4096);
    if constexpr (u < 4) dma1<u * 1024>(ra, va[u], sa); else dma<(u - 4) * 1024>(rb, vb[u - 4], sb);
  };
  auto G2 = [&](auto S, auto U) AI { constexpr int a = S.value % 3, u = U.value;
    if constexpr (u == 0) setm0((a == 0 ? 131072u : 81920u + a * 16384u) + wq * 4096);
    if constexpr (u == 4) setm0(a * 32768 + wq * 7168);
    if constexpr (u == 8) setm0(a * 32768 + wq * 7168 + 4096);
    if constexpr (u < 4) dma<u * 1024>(rx, va[u], sa); else dma<(u & 3) * 1024>(rb, vw[u - 4], sb);
  };
  const int c7 = ci & 7;
  auto setup2 = [&]() AI {
    rb = mk((const char*)w1 - 4096, 1024 * 7168 + 4096);
#pragma unroll
    for (int q = 0; q < 4; ++q) va[q] = vnat(q);
#pragma unroll
    for (int q = 0; q < 6; ++q) { const int k = 8 * (q & 1) + (lane >> 3), f = ((k >> 1) & 1) | ((q & 1) << 1);
      vw[q] = 4096 + k * 7168 + ((cj & 1) ? 64 : 0) + (q >> 1) * 128 + (((((lane & 7) >> 1) ^ f) * 2) + (lane & 1)) * 16 - (q & 3) * 1024; }
    vw[6] = 4096 + (lane >> 2) * 7168 + ((cj & 1) ? 0 : 384) + (((((lane & 3) >> 1) ^ hl) * 2) + (lane & 1)) * 16 - 2048;
    int k = lane - 2 + (c7 < 2 ? 3 * c7 : c7 - 2); k = 2 + (k >= 6 ? k - 6 : k);
    k = lane < 2 ? lane : lane < 8 ? k : (lane ^ rot);
    k = (k ^ (ci & 8)) & 15;
    ta = (ci * 128 + 32 * wq) * 2048 + k * 128; tb = (k * 64 + 16 * wq) * 7168 + cj * 448;
  };
  auto setup3 = [&]() AI {
    ra = rws;
#pragma unroll
    for (int q = 0; q < 4; ++q) { va[q] = lane * 16; vb[q] = vnat(q); }
  };
  auto table3 = [&](unsigned& ta, unsigned& tb) AI {
    const int ks = 16 * half + (lane ^ rot);
    int n = 2 * (lane - 16) + (wq >> 1), hp = half; if (n >= 56) { n -= 2 * MOVE; hp = 0; }
    const int kb = kb_of(hp * 8 + ((n ^ rot) & 7), n >> 3);
    ta = lane < 16 ? H_OFF + ci * H_PANEL + ks * 16384 + wq * 4096 : ci * HID_PANEL + kb * 8192 + (wq & 1) * 4096;
    tb = lane < 16 ? (cnt * 128 + 32 * wq) * 2048 + (ks >> 1) * 128 + (ks & 1) : (kb * 32 + 16 * (wq & 1)) * 2048 + cnt * 256;
    PIN(ta); PIN(tb);
  };
  auto next3 = [&]() AI { nexts(); rb = mk(sel<const u16*>(sb & 1, cim, cre), 1024 * 2048); sb &= ~1u; };

  if (w < 4) {
    constexpr bool isx = PROFX; (void)isx;
    unsigned epr, epc;
    const unsigned ca = (32 * wn + r31) * 8;
    const int seg0 = 4 * wm + hl;
    f32x2 lam, cof, l16, l128, l1k, pw0, pw1; unsigned t3a, t3b;
    {
      setm0(T_DUM);
      const int row = bid * 8 + (lane & 7);
      const char* pp = (const char*)(hl ? (row < 1024 ? bre : bim - 1024 * 1024) : x) + (size_t)row * 2048 + (4 * wq + ((lane >> 3) & 3)) * 128;
      for (int q = 0; q <= wq; ++q) __builtin_amdgcn_s_sleep(6);
      pfg(pp);
      pfg((const char*)pc0 + (((loc * 4 + wq) % 24) * 16 + (lane & 15)) * 128);
      ld32a(epr, rws, 0u, flagb + FK_EP * 128); ld32a(epc, rws, 0u, EPC_OFF + cj * 128);
      setup2(); sx = 2; nexts(); table3(t3a, t3b);
#pragma unroll
      for (int q = 0; q < 7; ++q) { PIN(vw[q]); PIN(va[q & 3]); }
      wrf(T_ZERO + (lane & 3) * 4, 0.f);
    }
    {
      f32x16 acc[2][2];
#pragma unroll
      for (int a = 0; a < 4; ++a)
#pragma unroll
        for (int e = 0; e < 16; ++e) acc[a >> 1][a & 1][e] = 0.f;
      bf16x8 wv[4][2];
      const int sstar = c7 < 2 ? c7 : 2;
      const int fsw = ((q2 >> 1) & 1) | ((g & 1) << 1);
      unsigned cf = sstar * 32768 + (g >> 1) * 7168 + (8 * (g & 1) + q2) * 128 + ((wq ^ fsw) * 32) + p * 8;
      unsigned ch = sstar * 32768 + (g >> 1) * 7168 + 6144 + (8 * (g & 1) + q2) * 64 + (((wq & 1) ^ (g & 1)) * 32) + p * 8;
      PIN(cf); PIN(ch);
      auto wread = [&](int st) AI {
        if (sstar == st) {
#pragma unroll
          for (int e = 0; e < 4; ++e)
#pragma unroll
            for (int t = 0; t < 4; ++t) sethalf(wv[t][e >> 1], e & 1, rtr((e >> 1) * 14336 + (t < 3 ? cf + t * 2048 + (e & 1) * 512 : ch + (e & 1) * 256)));
        } };
      {
        bf16x8 fa[4][2], fb[4][2];
        unsigned cb[8], ba[8], rs = 98304;
#pragma unroll
        for (int b = 0; b < 8; ++b) { cb[b] = (b < 4 ? wm * 8192u : 16384 + wn * 8192u) + r31 * 128 + (((2 * (b & 3) + hl) ^ ((r31 >> 1) & 7)) * 16); ba[b] = rs + cb[b]; PIN(ba[b]); }
        auto rd = [&](int r) AI { const int k16 = r >> 2, xx = r & 3;
          if (xx == 0) fa[k16][0] = rd128(ba[k16]); else if (xx == 3) fa[k16][1] = rd128(ba[k16] + 4096); else fb[k16][xx - 1] = rd128(ba[4 + k16] + (xx - 1) * 4096); };
        auto mm = [&](int m) AI { const int k16 = m >> 2, mt = (m >> 1) & 1, nt = m & 1; acc[mt][nt] = MFMA32(fa[k16][mt], fb[k16][nt], acc[mt][nt]); };
        auto up = [&](int b) AI { if (b == 0) rs = nq(rs); ba[b] = rs + cb[b]; PIN(ba[b]); };
        const char* pp = (const char*)w1 + ((bid * 4 + wq) * 56 + (lane < 56 ? lane : 55)) * 128;
        const int l128k = (lane < 56 ? lane : 55) * 131072;
        const char* pt = wq == 0 ? (const char*)w1 + l128k : wq == 1 ? (const char*)w2 + l128k
                       : wq == 2 ? (lane < 4 ? (const char*)sel<const u16*>(lane & 2, cim, cre) + cnt * 262144 + (lane & 1) * 131072
                                  : lane < 8 ? (const char*)out + ci * 524288 + (lane & 3) * 131072
                                  : lane == 8 ? ws + PART_OFF + (ci * 8 + cnt) * 32768 : lane == 9 ? ws + GRAN_OFF + cj * 16384 : lane == 10 ? ws + CG_OFF + cj * 32768 : ws + rgb)
                       : (lane < 8 ? ws + ci * HID_PANEL + lane * 131008 : ws + H_OFF + ci * H_PANEL + (lane & 3) * 131072);
        PIN(pt);
        PIN(pp);
        if (PTE) pfg(pt);
        waitl0(); bar();
        period32<true>(rd, mm, up); waitl0(); bar();
        const int sp = SP0 + bid % SPN;
        for (int o = 0; o < 2; ++o) {
          for (int s = o ? sp : 1; s < (o ? 16 : sp); ++s) { period32<false>(rd, mm, up); waitl0(); bar(); }
          if (o == 0) { pfg(pp); if (!PTE) pfg(pt); }
        }
        waitv<0>(); PIN(epr); PIN(epc);
        epr = __builtin_amdgcn_readfirstlane(epr) + 1u; epc = __builtin_amdgcn_readfirstlane(epc) + 1u;
        lam = rdf2(T_CON + ca);
        wread(0);
        sfor<7>([&](auto U) AI { G2(IC<2>{}, IC<U.value + 4>{}); SB(); if constexpr (U.value < 6) { mm(4 + 2 * U.value); mm(5 + 2 * U.value); } SB(); });
        nexts();
      }
      f32x2 st[2] = {{0, 0}, {0, 0}}, Pf[2] = {{0, 0}, {0, 0}}, Gt = {0, 0}, cr = {0, 0}, ag[8];
      u32x4 lb[8], cg[8]; unsigned lbo[8], lbe[8], ob[2][2][4], pv, gd = 0;
      f32x4 cs[4]; f32x2 so[2]; float c0s = 0.f, c1s = 0.f;
      const rsrc_t rgr = mk(ws + GRAN_OFF + cj * 16384, 16384);
      const int cp = 28 * wq + (r31 < 28 ? r31 : 27);
      const unsigned cgo = hl * 16384 + cp * 16;
      auto bad = [&]() AI { unsigned d = 0;
#pragma unroll
        for (int q = 0; q < 8; ++q) { PIN(lb[q]); d |= (int)lbo[q] < 0 ? 0u : ((lb[q][1] ^ epc) | (lb[q][3] ^ epc)); }
        return d != 0; };
      auto gbad = [&]() AI { unsigned d = 0;
#pragma unroll
        for (int q = 0; q < 8; ++q) { PIN(cg[q]); d |= (cg[q][1] ^ epc) | (cg[q][3] ^ epc); }
        return d != 0; };
      auto E2 = [](int s) constexpr { return (s == 2 || s == 3) ? 1 : s == 4 ? 8 : (s == 10 || s == 11) ? 4 : 0; };
      auto T2 = [](int s) constexpr { return (s == 7 || s == 8) ? 4 : 0; };
      sfor<15>([&](auto S) AI { constexpr int s = S.value;
        if constexpr (s < 3) waitl0();
        if constexpr (s == 10) waitv<11 + E2(9)>();
        else if constexpr (s >= 1) waitv<T2(s - 2) + (s < 14 ? 11 : 8) + E2(s - 1) + T2(s - 1)>();
        if constexpr (s == 13) { if (w == 0) { waitv<8>(); check_flags(pv, rws, fvo, flagb + FK_H * 128, lane, 8 * half, 8 * half + 8, epr); } }
        bar();
        if constexpr (s < 2) wread(s + 1);
        if constexpr (s == 2) {
#pragma unroll
          for (int sg = 0; sg < 8; ++sg) ag[sg] = rdf2(T_AGG + sg * 512 + ca);
          l16 = rdf2(T_CON + 1024 + ca); cof = rdf2(T_CON + 512 + ca);
        }
        if constexpr (s == 5) { pw1 = rdf2(T_CON + 1536 + ca); pw0 = rdf2(T_CON + 2048 + ca); l128 = rdf2(T_CON + 2560 + ca); l1k = rdf2(T_CON + 3072 + ca); }
        if constexpr (s == 10) { if (w == 0) stflag(rws, epr, lane, flagb + FK_H * 128 + cj * 4); }
        SB();
        auto work = [&](auto U) AI { constexpr int u = U.value;
          if constexpr (s < 2 && u >= 1 && u < 9) {
            constexpr int t = 8 * s + u - 1;
#pragma unroll
            for (int mt = 0; mt < 2; ++mt) st[mt] = cfma(lam, st[mt], (f32x2){acc[mt][0][t], acc[mt][1][t]});
            PIN2(st[0], st[1]);
          }
          if constexpr (s == 1 && u == 9) { wrf2(T_AGG + seg0 * 512 + ca, st[0]); wrf2(T_AGG + (seg0 + 2) * 512 + ca, st[1]); }
          if constexpr (s == 2 && u >= 1 && u < 9) {
            constexpr int sg = u - 1, mt = (sg >> 1) & 1; const bool me = sg == seg0 + 2 * mt;
            Pf[mt].x = sel<float>(me, Gt.x, Pf[mt].x); Pf[mt].y = sel<float>(me, Gt.y, Pf[mt].y); Gt = cfma(l16, Gt, ag[sg]);
            PIN2(Gt, Pf[mt]);
          }
          if constexpr (s == 2 && u == 9) st64(rgr, (u32x2){f2u(sel<float>(hl, Gt.y, Gt.x)), epc}, ca * 2 + hl * 8, ci * 1024);
          if constexpr (s == 3 && u < 4) {
            const bf16x8 one = __builtin_bit_cast(bf16x8, (u32x4){0x3F803F80u, 0x3F803F80u, 0x3F803F80u, 0x3F803F80u});
            cs[u] = MFMA(one, wv[u][1], MFMA(one, wv[u][0], ((f32x4){0, 0, 0, 0}))); PIN(cs[u]);
          }
          if constexpr (s == 3 && u == 9)
            st64(rws, (u32x2){f2u(sel4(g & 1, g & 2, cs[0][0], cs[1][0], cs[2][0], cs[3][0])), epc}, (g < 3 ? g * 64 + wq * 16 + r15 : 192 + (wq & 1) * 16 + r15) * 8u, CG_OFF + (cj * 16 + ci) * 2048);
          if constexpr ((s == 3 && u >= 4 && u < 9) || (s == 4 && u < 5) || (s == 5 && u >= 1 && u < 7)) {
            constexpr int t = s == 3 ? u - 4 : s == 4 ? 5 + u : 9 + u;
            f32x2 v0 = cmulc(cof, (f32x2){acc[0][0][t], acc[0][1][t]}), v1 = cmulc(cof, (f32x2){acc[1][0][t], acc[1][1][t]}); PIN2(v0, v1);
            acc[0][0][t] = v0.x; acc[0][1][t] = v0.y; acc[1][0][t] = v1.x; acc[1][1][t] = v1.y;
          }
          if constexpr (s == 4 && u >= 6 && u < 10) {
#pragma unroll
            for (int q = 2 * u - 12; q < 2 * u - 10; ++q) { lbo[q] = (ci - 8 - 8 * hl + q) * 1024 + ca * 2; ld128a(lb[q], rgr, lbo[q], 0u); }
          }
          if constexpr (s == 5 && u == 8) {
            if (!wm) pw0 = (f32x2){1.f, 0.f};
            pw0 = cmul(pw0, (f32x2){sel<float>(hl, l16.x, 1.f), sel<float>(hl, l16.y, 0.f)}); pw1 = cmul(pw0, pw1); PIN2(pw0, pw1);
          }
          if constexpr (s == 5 && (u == 7 || u == 9)) {
#pragma unroll
            for (int q = 2 * u - 14; q < 2 * u - 10; ++q) { lbe[q] = (int)lbo[q] < 0 ? 0u : epc; PIN(lbe[q]); }
          }
          if constexpr (s == 6 && u < 8) {
            PIN(lb[u]); gd |= (lb[u][1] ^ lbe[u]) | (lb[u][3] ^ lbe[u]); cr = cfma(l128, cr, (f32x2){u2f(lb[u][0]), u2f(lb[u][2])});
            PIN2(cr, gd);
          }
          if constexpr (s == 6 && u == 8) {
            if (__builtin_expect(anyl(gd != 0), 0)) {
              for (unsigned sp = 0; sp < SPIN_CAP; ++sp) {
                __builtin_amdgcn_s_sleep(2);
#pragma unroll
                for (int q = 0; q < 8; ++q) ld128a(lb[q], rgr, lbo[q], 0u);
                waitv<0>();
                if (!anyl(bad())) break;
              }
              cr = (f32x2){0, 0};
#pragma unroll
              for (int q = 0; q < 8; ++q) cr = cfma(l128, cr, (f32x2){u2f(lb[q][0]), u2f(lb[q][2])});
            }
            gd = 0;
            const auto sx_ = __builtin_amdgcn_permlane32_swap(f2u(cr.x), f2u(cr.x), false, false), sy_ = __builtin_amdgcn_permlane32_swap(f2u(cr.y), f2u(cr.y), false, false);
            cr = cfma(l1k, (f32x2){u2f(sx_[1]), u2f(sy_[1])}, (f32x2){u2f(sx_[0]), u2f(sy_[0])});
            PIN(cr);
          }
          if constexpr (s == 6 && u == 9) { st[0] = cmulc(cof, cfma(pw0, cr, Pf[0])); PIN(st[0]); }
          if constexpr (s == 7 && u == 0) { st[1] = cmulc(cof, cfma(pw1, cr, Pf[1])); PIN(st[1]); }
          if constexpr ((s == 7 && u >= 2 && u < 10) || (s == 8 && u >= 1 && u < 9)) {
            constexpr int t = s == 7 ? u - 2 : 7 + u;
            if constexpr (t & 1) { so[0] = st[0]; so[1] = st[1]; }
#pragma unroll
            for (int mt = 0; mt < 2; ++mt) st[mt] = cfma((f32x2){lam.x, -lam.y}, st[mt], (f32x2){acc[mt][0][t], acc[mt][1][t]});
            if constexpr (t & 1) {
#pragma unroll
              for (int mt = 0; mt < 2; ++mt) { ob[mt][0][(t >> 1) & 3] = pk2(so[mt].x, st[mt].x); ob[mt][1][(t >> 1) & 3] = pk2(so[mt].y, st[mt].y); PIN2(ob[mt][0][(t >> 1) & 3], ob[mt][1][(t >> 1) & 3]); }
            }
            PIN2(st[0], st[1]);
          }
          if constexpr ((s == 7 || s == 8) && u == 10) {
            constexpr int hf = s - 7;
#pragma unroll
            for (int q = 0; q < 4; ++q) { const int mt = q >> 1, pt = q & 1;
              st128(rws, (u32x4){ob[mt][pt][0], ob[mt][pt][1], ob[mt][pt][2], ob[mt][pt][3]},
                    (2 * wn + (r31 >> 4)) * 4096 + wm * 2048 + hf * 1024 + (2 * mt + hl) * 256 + ((r15 ^ (hf << 2)) * 16), H_OFF + ci * H_PANEL + (2 * cj + pt) * 16384); }
          }
          if constexpr (s == 9 && u == 0) { if (ci == 15 && w == 1) stflag(rws, epc, lane, EPC_OFF + cj * 128); }
          if constexpr (s == 12 && u == 2) { if (w == 0) ld32a(pv, rws, fvo, flagb + FK_H * 128); }
          if constexpr ((s == 10 || s == 11) && (u & 1) && u < 8) { constexpr int q = 4 * (s - 10) + (u >> 1); ld128a(cg[q], rws, cgo, CG_OFF + (cj * 16 + q) * 2048); }
          if constexpr (s == 13 && u < 4) {
#pragma unroll
            for (int q = 2 * u; q < 2 * u + 2; ++q) { PIN(cg[q]); gd |= (cg[q][1] ^ epc) | (cg[q][3] ^ epc); c0s += u2f(cg[q][0]); c1s += u2f(cg[q][2]); }
            PIN(gd); PIN(c0s); PIN(c1s);
          }
          if constexpr (s == 13 && u == 4) {
            if (__builtin_expect(anyl(gd != 0), 0)) {
              for (unsigned sp = 0; sp < SPIN_CAP; ++sp) { __builtin_amdgcn_s_sleep(2);
#pragma unroll
                for (int q = 0; q < 8; ++q) ld128a(cg[q], rws, cgo, CG_OFF + (cj * 16 + q) * 2048);
                waitv<0>(); if (!anyl(gbad())) break; }
              c0s = 0.f; c1s = 0.f;
#pragma unroll
              for (int q = 0; q < 8; ++q) { c0s += u2f(cg[q][0]); c1s += u2f(cg[q][2]); }
            }
            c0s = sum32(c0s); c1s = sum32(c1s); PIN(c0s); PIN(c1s);
          }
          if constexpr (s == 13 && u == 6) {
            const float v = sel<float>(hl, c1s, c0s), hi = rbf(v), bb = rdf(T_B1 + (2 * cp + hl) * 4);
            *(LAS u32x4*)(size_t)(T_WA + (2 * cp + hl) * 16) = (u32x4){pk2(hi, v - hi), pk2(hi, bb), pk2(bb, 0.f), 0u};
          }
        };
        if constexpr (s < 13) { sfor<11>([&](auto U) AI { G2(IC<s + 3>{}, U); SB(); work(U); SB(); }); if constexpr (s < 12) nexts(); }
        if constexpr (s == 12) { ra = rws;
#pragma unroll
          for (int q = 0; q < 4; ++q) { vb[q] = va[q]; va[q] = lane * 16; }
          ta = t3a; tb = t3b; sl = 32768; sx = 0; next3(); }
        if constexpr (s >= 13) { ival(G, work); sl = nq(sl); next3(); asm volatile("s_nop 4"); }
        if constexpr (s == 14) ival(G, nowork);
      });
      waitl0(); waitv<16>(); bar();
    }
    f32x4 acc[4][4];
#pragma unroll
    for (int a = 0; a < 4; ++a)
#pragma unroll
      for (int b = 0; b < 4; ++b) acc[a][b] = (f32x4){0, 0, 0, 0};
    bf16x8 fa[2][4], fb[2][4];
    const int kap = 4 * (g & 1) + q2, hf = p >> 1;
    const unsigned wb = 16384 + (4 * g + q2) * 256 + p * 8, hb = (g >> 1) * 4096 + wm * 2048 + hf * 1024 + (8 * (g & 1) + q2) * 16 + (p & 1) * 8;
    const unsigned cb3[4] = {hb + 64u * hf, hb + 64u * (1 ^ hf), 16384 + wn * 8192u + r15 * 128 + ((g ^ (r15 >> 1)) * 16), 16384 + wn * 8192u + r15 * 128 + (((4 + g) ^ (r15 >> 1)) * 16)};
    const unsigned cb4[5] = {wm * 4096u + lane * 16, wb + (((4 * wn + 0) ^ kap) * 32), wb + (((4 * wn + 1) ^ kap) * 32), wb + (((4 * wn + 2) ^ kap) * 32), wb + (((4 * wn + 3) ^ kap) * 32)};
    unsigned ba[5];
    sl = 32768;
#pragma unroll
    for (int b = 0; b < 4; ++b) { ba[b] = sl + cb3[b]; PIN(ba[b]); }
    auto rd3 = [&](int r) AI { const int ks = r / 12, xx = r % 12;
      if (xx < 2) sethalf(fa[ks][0], xx, rtr(ba[xx] + ks * 8192));
      else if (xx < 6) fb[ks][xx - 2] = rd128(ba[2 + ks] + (xx - 2) * 2048);
      else { const int a = (xx - 4) >> 1, h = xx & 1; sethalf(fa[ks][a], h, rtr(ba[h] + ks * 8192 + a * 256)); } };
    auto rd4 = [&](int r) AI { const int ks = r / 12, xx = r % 12;
      if (xx == 0) fa[ks][0] = rd128(ba[0] + ks * 8192);
      else if (xx <= 8) { const int b = (xx - 1) >> 1, h = (xx - 1) & 1; sethalf(fb[ks][b], h, rtr(ba[1 + b] + ks * 8192 + h * 4096)); }
      else fa[ks][xx - 8] = rd128(ba[0] + ks * 8192 + (xx - 8) * 1024); };
    auto mm = [&](int ks, int m) AI { acc[m >> 2][m & 3] = MFMA(fb[ks][m & 3], fa[ks][m >> 2], acc[m >> 2][m & 3]); };
    auto up3 = [&](int b) AI { if (b == 0) sl = nq(sl); ba[b] = sl + cb3[b]; PIN(ba[b]); };
    auto up4 = [&](int b) AI { if (b == 0) sl = nq(sl); ba[b] = sl + cb4[b]; PIN(ba[b]); };
    period<16, 4, true, 24, LASTR, 4>(rd3, mm, up3); waitl0(); waitv<8>(); bar();
    for (int s = 1; s < 15; ++s) { period<16, 4, false, 24, LASTR, 4>(rd3, mm, up3); waitl0(); waitv<0>(); bar(); }
    period<16, 4, false, 24, LASTR, 5>(rd3, mm, up4); waitl0(); bar();
    {
      float gr[4]; int seen = 0;
#pragma unroll
      for (int a = 0; a < 4; ++a) gr[a] = rdf(T_ROW + 1536 + (64 * wm + 16 * a + r15) * 4);
      auto mmg = [&](int ks, int m) AI { mm(ks, m); ++seen; const int t = seen - 7;
        if (t >= 0 && t < 16) { acc[t >> 2][t & 3] *= gr[t >> 2]; PIN(acc[t >> 2][t & 3]); } };
      period<16, 4, false, 24, LASTR, 5>(rd4, mmg, up4); waitl0(); bar();
    }
    for (int s = 1; s < n4; ++s) { period<16, 4, false, 24, LASTR, 5>(rd4, mm, up4); waitl0(); bar(); }
#pragma unroll
    for (int m = LAG; m < 16; ++m) mm(1, m);
    if (half) {
      const unsigned pb = ((sl + 65536u + wm * 32768u) & 131071u) + wn * 16384 + g * 256;
#pragma unroll
      for (int a = 0; a < 4; ++a)
#pragma unroll
        for (int b = 0; b < 4; ++b) *(LAS f32x4*)(size_t)(pb + (a * 4 + b) * 1024 + ((r15 ^ (4 * b + g)) * 16)) = acc[a][b];
      waitl0();
    } else {
#pragma unroll
      for (int a = 0; a < 4; ++a)
#pragma unroll
        for (int bp = 0; bp < 2; ++bp)
          st128(rws, (u32x4){pk2(acc[a][2 * bp][0], acc[a][2 * bp][1]), pk2(acc[a][2 * bp][2], acc[a][2 * bp][3]), pk2(acc[a][2 * bp + 1][0], acc[a][2 * bp + 1][1]), pk2(acc[a][2 * bp + 1][2], acc[a][2 * bp + 1][3])},
                (a * 2 + bp) * 1024 + g * 256 + ((r15 ^ (8 * wn + 4 * bp + g)) * 16), PART_OFF + (ci * 8 + cnt) * 32768 + wq * 8192);
      waitv<0>();
    }
    bar();
    if (!half && w == 0) stflag(rws, epr, lane, flagb + FK_P * 128 + cnt * 4);
  } else {
    constexpr bool isx = !PROFX; (void)isx;
    unsigned epr, pv, t3a, t3b;
    {
      u32x4 xr[2][2], gq[2], rgv[2];
      ra = rx; rb = mk(sel<const u16*>(wq & 1, bim, bre), 1024 * 2048);
#pragma unroll
      for (int q = 0; q < 4; ++q) { va[q] = (16 * hl + 4 * q + ((lane >> 3) & 3)) * 2048 + (((lane & 7) ^ ((4 * q + g) & 7)) * 16) - 1024 * q; vb[q] = vnat(q); }
      auto src1 = [&](int s) AI { const int k = s ^ (s < 2 ? 0 : s < 4 ? (rot & 1) : rot); sa = (ci * 128 + 32 * wq) * 2048 + k * 128; sb = (cj * 64 + 32 * (wq >> 1)) * 2048 + k * 128; sl = ((s + 3) & 3) * 32768; };
      src1(0); ival(G, nowork); src1(1); ival(G, nowork);
      waitv<8>(); bar();
      PH(1);
      src1(2); ival(G, nowork);
      unsigned pdt, plr, pli, bq, oq; f32x2 lp; float lr, li;
      const int tc = tq < 224 ? tq : 223;
      ld32a(epr, rws, 0u, flagb + FK_EP * 128); ld128g(gq[0], gw, lane * 16u); ld128g(gq[1], gw + 512, lane * 16u);
      ld16p(pdt, ldt + 64 * cj, lane * 2u); ld16p(plr, lre + 64 * cj, lane * 2u); ld16p(pli, lim + 64 * cj, lane * 2u);
      ld16p(bq, b1, (224 * cj + (tc < 192 ? ((cj & 1) ? 32 : 0) + tc : ((cj & 1) ? -192 : 0) + tc)) * 2u);
      ld16p(oq, sel<const u16*>(wq < 2, b2, dsk), (cnt * 128 + (tq & 127)) * 2u);
      src1(3); ival(G, nowork);
      float sv[6] = {0, 0, 0, 0, 0, 0}, a1, a2, a3, mean, sd, rstd, qv, gg;
      auto E1 = [](int s) constexpr { return (s == 1 || s == 2) ? 2 : s == 8 ? 1 : s == 11 ? 2 : 0; };
      sfor<12>([&](auto S) AI { constexpr int s = S.value;
        if constexpr (s < 2) waitv<16 + 8>(); else waitv<16 + E1(s - 2) + E1(s - 1)>();
        bar();
        src1(s + 4); ival(G, [&](auto U) AI { constexpr int u = U.value;
          if constexpr ((s == 1 || s == 2) && (u == 2 || u == 5)) ld128p(xr[s - 1][u / 3], rx, lane * 16u, (ci * 128 + 8 * cj + 2 * wq + s - 1) * 2048 + (u / 3) * 1024);
          if constexpr (s >= 4 && s < 7 && u < 6 && 6 * (s - 4) + u < 16) {
            constexpr int id = 6 * (s - 4) + u, r = id >> 3, e = id & 7;
            PIN(xr[r][e >> 2]); PIN(gq[e >> 2]);
            const unsigned v = xr[r][e >> 2][e & 3], gv = gq[e >> 2][e & 3];
            const float x0 = bf2f(v & 0xffffu), x1 = u2f(v & 0xffff0000u);
            sv[r] += x0 + x1; sv[2 + r] = __builtin_fmaf(x1, x1, __builtin_fmaf(x0, x0, sv[2 + r]));
            sv[4 + r] = __builtin_fmaf(x1, u2f(gv & 0xffff0000u), __builtin_fmaf(x0, bf2f(gv & 0xffffu), sv[4 + r]));
            PIN(sv[r]); PIN(sv[2 + r]); PIN(sv[4 + r]);
          }
          if constexpr (s == 7 && u < 6) { sv[u] = dppadd<0x140>(dppadd<0x141>(dppadd<0x4E>(dppadd<0xB1>(sv[u])))); PIN(sv[u]); }
          if constexpr (s == 7 && u == 6) { const bool r1 = lane & 4; a1 = sel<float>(r1, sv[1], sv[0]); a2 = sel<float>(r1, sv[3], sv[2]); a3 = sel<float>(r1, sv[5], sv[4]);
                                            a1 = sum32(sum16(a1)); PIN(a1); PIN(a2); PIN(a3); PIN(epr); epr = __builtin_amdgcn_readfirstlane(epr) + 1u; }
          if constexpr (s == 8 && u == 0) { a2 = sum32(sum16(a2)); PIN(a2); }
          if constexpr (s == 8 && u == 1) { a3 = sum32(sum16(a3)); PIN(a3); }
          if constexpr (s == 8 && u == 2) { mean = a1 * (1.f / 1024.f); const float var = __builtin_fmaxf(a2 * (1.f / 1024.f) - mean * mean, 0.f) + 1e-5f; rstd = __builtin_amdgcn_rsqf(var); sd = var * rstd;
                                            PIN(mean); PIN(rstd); PIN(sd); }
          if constexpr (s == 8 && u == 3) { qv = 1.f + fexp(__builtin_fminf(a3, 60.f)); gg = __builtin_amdgcn_rcpf(1.f + fexp(__builtin_fminf(-a3, 80.f))); PIN(qv); PIN(gg); }
          if constexpr (s == 8 && u == 4) { a1 = sel4(lane & 1, lane & 2, -2.302208198f * rstd, -0.1029432397f * rstd * rstd * rstd, qv * sd, gg); PIN(a1); }
          if constexpr (s == 8 && u == 5) { const float mh = rbf(mean), sh = rbf(sd);
                                            a2 = sel4(lane & 1, lane & 2, u2f(pk2(-mh, -mh)), u2f(pk2(mh - mean, sh)), u2f(pk2(sd - sh, 0.f)), 0.f); PIN(a2); }
          if constexpr (s == 8 && u == 6) { if (lane < 16) st64(rws, (u32x2){f2u(sel<float>(lane & 8, a2, a1)), epr}, (lane >> 3) * 4096u + (lane & 7) * 8, rgb + (8 * cj + 2 * wq) * 32); }
          if constexpr (s == 2 && u == 0) { PIN(pdt); PIN(plr); PIN(pli); lr = bf2f(plr); li = bf2f(pli); const float dt = rbf(fexp(bf2f(pdt))); lp = (f32x2){fexp(dt * lr), dt * li}; PIN(lp);
                                            PIN(bq); PIN(oq); wrf(T_B1 + tq * 4, bf2f(bq)); wrf(T_OUT + tq * 4, bf2f(oq)); }
          if constexpr (s == 3 && u == 1) { float sn, cs_; sincos_f(lp.y, sn, cs_); lp = (f32x2){lp.x * cs_, lp.x * sn}; PIN(lp); wrf2(T_CON + lane * 8, lp); }
          if constexpr (s == 3 && u == 5) { const float nr = lp.x - 1.f, ni = lp.y, den = 2.f * __builtin_amdgcn_rcpf(lr * lr + li * li);
                                            wrf2(T_CON + 512 + lane * 8, (f32x2){(nr * lr + ni * li) * den, (ni * lr - nr * li) * den}); }
          if constexpr ((s == 9 || (s == 10 && u < 2))) { constexpr int q = 8 * (s - 9) + u; lp = cmul(lp, lp); PIN(lp);
            if constexpr (q >= 3 && q < 7) wrf2(T_CON + (q - 1) * 512 + lane * 8, lp);
            if constexpr (q == 9) wrf2(T_CON + 3072 + lane * 8, lp); }
          if constexpr (s == 10 && u == 3) table3(t3a, t3b);
          if constexpr (s == 11 && u == 6) { ld128a(rgv[0], rws, tq * 16u, rgb); ld128a(rgv[1], rws, tq * 16u + 4096, rgb); }
        });
      });
      setup2(); sx = 0; nexts();
      const unsigned sb0 = sb;
      waitv<16 + 2>(); bar();
      sfor<4>([&](auto U) AI { G2(IC<0>{}, U); }); nexts();
      const unsigned sb1 = sb; asm volatile("s_nop 4");
      sfor<4>([&](auto U) AI { G2(IC<1>{}, U); }); nexts();
      waitv<16 + 2>(); bar();
      sb = sb0; sfor<7>([&](auto U) AI { G2(IC<0>{}, IC<U.value + 4>{}); });
      sfor<4>([&](auto U) AI { G2(IC<2>{}, U); });
      waitv<19>(); bar();
      sb = sb1; sfor<7>([&](auto U) AI { G2(IC<1>{}, IC<U.value + 4>{}); });
      {
        auto rbad = [&]() AI { PIN(rgv[0]); PIN(rgv[1]); return ((rgv[0][1] ^ epr) | (rgv[0][3] ^ epr) | (rgv[1][1] ^ epr) | (rgv[1][3] ^ epr)) != 0; };
        if (__builtin_expect(anyl(rbad()), 0))
          for (unsigned sp = 0; sp < SPIN_CAP; ++sp) { __builtin_amdgcn_s_sleep(2);
            ld128a(rgv[0], rws, tq * 16u, rgb); ld128a(rgv[1], rws, tq * 16u + 4096, rgb);
            waitv<0>(); if (!anyl(rbad())) break; }
      }
      wrf(T_ROW + (tq & 1) * 1024 + (tq >> 1) * 4, u2f(rgv[0][0])); wrf(T_ROW + (tq & 1) * 1024 + 512 + (tq >> 1) * 4, u2f(rgv[0][2]));
      wrf2(T_XA + tq * 8, (f32x2){u2f(rgv[1][0]), u2f(rgv[1][2])});
      ta = t3a; tb = t3b; sx = 3; next3();
    }
    {
      f32x4 ac1[28];
#pragma unroll
      for (int m = 0; m < 28; ++m) ac1[m] = (f32x4){0, 0, 0, 0};
      {
        bf16x8 fa[2][4], fb[2][8];
        const int f = ((q2 >> 1) & 1) | ((g & 1) << 1);
        const unsigned nb = wm * 8192u + r15 * 128, n0 = (g ^ (r15 >> 1)) * 16, n1 = ((4 + g) ^ (r15 >> 1)) * 16, wk = (g >> 1) * 7168u + p * 8;
        const unsigned cb[8] = {nb + wn * 4096 + n0, nb + wn * 4096 + n1, nb + (1 - wn) * 4096 + n0, nb + (1 - wn) * 4096 + n1,
                                wk + (8 * (g & 1) + q2) * 128 + (((2 * wn) ^ f) * 32), wk + (8 * (g & 1) + q2) * 128 + (((2 * wn + 1) ^ f) * 32),
                                wk + 6144 + (8 * (g & 1) + q2) * 64 + (g & 1) * 32, wk + 6144 + (8 * (g & 1) + q2) * 64 + ((g & 1) ^ 1) * 32};
        unsigned ba[8]; unsigned xn = 98304, wnx = 32768;
        const char* pp0 = lane < 32 ? (const char*)sel<const u16*>(lane & 16, cim, cre) + ((bid * 4 + wq) * 16 + r15) * 128 : (const char*)w2 + ((bid * 4 + wq) * 56 + r15) * 128;
        const char* pp1 = (const char*)w2 + ((bid * 4 + wq) * 56 + (lane < 40 ? 16 + lane : 55)) * 128;
        PIN2(pp0, pp1);
        setm0(T_DUM);
#pragma unroll
        for (int b = 0; b < 8; ++b) { ba[b] = cb[b] + (b < 4 ? 131072u : 0u); PIN(ba[b]); }
        auto rd = [&](int r) AI { const int ks = r / 20, xx = r % 20;
          if (xx == 0) fa[ks][0] = rd128(ba[ks]);
          else if (xx <= 12) { const int b = (xx - 1) >> 1, h = (xx - 1) & 1; sethalf(fb[ks][b], h, rtr(ba[4 + (b & 1)] + (b >> 1) * 2048 + ks * 14336 + h * 512)); }
          else if (xx <= 16) { const int b = (xx - 1) >> 1, h = (xx - 1) & 1; sethalf(fb[ks][b], h, rtr(ba[b] + ks * 14336 + h * 256)); }
          else { const int a = xx - 16; fa[ks][a] = rd128(ba[(a >> 1) * 2 + ks] + (a & 1) * 2048); } };
        auto mm = [&](int ks, int m) AI { const int a = m < 16 ? m >> 3 : (m - 4) / 6, b = m < 16 ? m & 7 : (m - 4) % 6; ac1[m] = MFMA(fb[ks][b], fa[ks][a], ac1[m]); };
        auto up = [&](int b) AI { if (b < 8) { ba[b] = cb[b] + sel<unsigned>(b < 4, xn, wnx); PIN(ba[b]); }
                                  else { wnx = (wnx == 65536u) ? 0u : wnx + 32768u; xn = (xn == 131072u) ? 98304u : xn + 16384u; } };
        waitl0(); waitv<11>(); bar();
        PH(2);
        period<28, 8, true, 40, LASTR + 24, 9>(rd, mm, up); waitl0(); waitv<0>(); bar();
        const int sp = 2 + bid % 5;
        for (int o = 0; o < 3; ++o) {
          for (int s = o == 0 ? 1 : o == 1 ? sp : sp + 7; s < (o == 0 ? sp : o == 1 ? sp + 7 : 16); ++s) { period<28, 8, false, 40, LASTR + 24, 9>(rd, mm, up); waitl0(); bar(); }
          if (o < 2) pfg(pp0);
          pp0 = pp1;
        }
        PH(3);
        setup3(); sl = 0;
        ival(G, [&](auto U) AI { constexpr int u = U.value;
#pragma unroll
          for (int m = LAG + 4 * u; m < LAG + 4 * u + 4; ++m) if (m < 28) mm(1, m); });
#pragma unroll
        for (int a = 0; a < 4; ++a) fa[0][a] = rd128(g ? T_ZERO : T_XA + (64 * wm + 16 * (a ^ (2 * wn)) + r15) * 16);
#pragma unroll
        for (int b = 0; b < 8; ++b) fb[0][b] = rd128(T_WA + ((b < 6 ? (b >> 1) * 64 + wn * 32 + (b & 1) * 16 : 96 + b * 16) + r15) * 16);
        SB();
        sl = nq(sl); next3();
        bar();
        ival(G, [&](auto U) AI { constexpr int u = U.value;
#pragma unroll
          for (int m = 4 * u; m < 4 * u + 4; ++m) if (m < 28) mm(0, m); });
        sl = nq(sl); next3();
      }
      float k0[4], k1[4], qq[4];
#pragma unroll
      for (int a = 0; a < 4; ++a) { const unsigned ro = T_ROW + (64 * wm + 16 * (a ^ (2 * wn)) + r15) * 4; k0[a] = rdf(ro); k1[a] = rdf(ro + 512); qq[a] = rdf(ro + 1024); }
      u32x4 hw[4]; float t[8];
      auto lv0 = [](int s) constexpr { return s < 1 ? 0 : s < 7 ? (6 * (s - 1) < 32 ? 6 * (s - 1) : 32) : (32 + 4 * (s - 7) < 112 ? 32 + 4 * (s - 7) : 112); };
      auto st3 = [](int s) constexpr { return (s == 6 || s == 14 || s == 22) ? 4 : s == 26 ? 2 : 0; };
      sfor<36>([&](auto S1) AI { constexpr int s = S1.value + 1;
        if constexpr (st3(s - 2) > 0) waitv<8>();
        else if constexpr (s >= 2) waitv<16 + st3(s - 1)>();
        if constexpr (s == 12 || s == 20 || s == 28 || s == 36) {
          if (w == 4) { waitv<8>(); check_flags(pv, rws, fvo, flagb + (FK_M1 + (s - 12) / 8) * 128, lane, s == 36 ? 0 : 8 * half, 8 * half + 8, epr); } }
        SB(); bar();
        if constexpr (s == 16) { PH(4); }
        if constexpr (st3(s - 2) > 0) { if (w == 5) stflag(rws, epr, lane, flagb + (FK_M1 + (s - 4) / 8) * 128 + cj * 4); }
        if constexpr (s == 11 || s == 19 || s == 27 || s == 35) { if (w == 4) ld32a(pv, rws, fvo, flagb + (FK_M1 + (s - 11) / 8) * 128); }
#define UU(e) ac1[m0 + ((e) >> 2)][(e) & 3]
#define LVL(expr) do { _Pragma("unroll") for (int e = 0; e < 8; ++e) { t[e] = (expr); PIN(t[e]); } } while (0)
        ival(G, [&](auto U) AI { constexpr int u = U.value, n = lv0(s + 1) - lv0(s), li = n == 6 ? (u < 3 ? u : u - 1) : u >> 1;
          if constexpr ((n == 6 ? (u != 3 && u != 7) : !(u & 1)) && li < n) {
            constexpr int L = lv0(s) + li, v = L >> 3, l = L & 7, a = v & 3, m0 = (a < 2 ? 8 * a : 4 + 6 * a) + 2 * (v >> 2);
            if constexpr (l == 0) LVL(UU(e) * UU(e));
            if constexpr (l == 1) LVL(__builtin_fmaf(t[e], k1[a], k0[a]));
            if constexpr (l == 2) LVL(UU(e) * t[e]);
            if constexpr (l == 3) LVL(__builtin_amdgcn_exp2f(t[e]));
            if constexpr (l == 4) LVL(__builtin_fmaf(t[e], qq[a], qq[a]));
            if constexpr (l == 5) LVL(__builtin_amdgcn_rcpf(t[e]));
            if constexpr (l == 6) LVL(UU(e) * t[e]);
            if constexpr (l == 7) { hw[a] = (u32x4){pk2(t[0], t[1]), pk2(t[2], t[3]), pk2(t[4], t[5]), pk2(t[6], t[7])}; PIN(hw[a]); }
          } });
#pragma unroll
        for (int q = 0; q < st3(s); ++q) st128(rws, hw[q], lane * 16u + (4 * wm + (q ^ (2 * wn))) * 1024, ci * HID_PANEL + kb_of(cj, s < 26 ? 2 * (s >> 3) + wn : 6) * 8192);
        sl = nq(sl);
        if constexpr (s < 11) next3(); else nexts();
        if constexpr (s == 11) {
          rb = mk(w2, 3584 * 2048);
#pragma unroll
          for (int q = 0; q < 4; ++q) vb[q] = (4 * q + g) * 2048 + ((((r15 >> 1) ^ (4 * (q & 1) + g)) * 2) + (lane & 1)) * 16 - 1024 * q;
        }
      });
    }
    static_assert(MOVE >= 0 && MOVE <= 3, "");
    for (int s = 21; s < n4 - 4; ++s) { waitv<16>(); bar(); ival(G, nowork); sl = nq(sl); nexts(); }
    waitv<16>(); bar();
    if (half) {
      sfor<8>([&](auto U) AI { constexpr int u = U.value; if constexpr ((u & 3) == 0) setm0(sl + wq * 8192 + u * 1024);
        dma<(u & 3) * 1024>(rx, (4u * (u & 3) + g) * 2048 + r15 * 16 - (u & 3) * 1024, (ci * 128 + 32 * wq + 16 * (u >> 2)) * 2048 + cnt * 256); });
      waitv<8 + 8>();
    } else waitv<8>();
    bar();
    if (half) waitv<8>(); else waitv<0>();
    bar();
    ld32a(pv, rws, fvo, flagb + FK_P * 128);
    bar();
    waitv<0>();
    if (half) {
      check_flags(pv, rws, fvo, flagb + FK_P * 128, lane, cnt, cnt + 1, epr);
      sfor<8>([&](auto U) AI { constexpr int u = U.value; if constexpr ((u & 3) == 0) setm0(nq(sl) + wq * 8192 + u * 1024);
        dma1<(u & 3) * 1024>(rws, lane * 16u, PART_OFF + (ci * 8 + cnt) * 32768 + wq * 8192 + (u >> 2) * 4096); });
      if (cnt == 0 && w == 4) stflag(rws, epr, lane, flagb + FK_EP * 128);
    }
    PH(5);
    waitv<0>(); bar();
  }
  if (half) {
    const int bg = r31 & 15, sw = (r31 >> 4) * 8 + (bg >> 3) * 4 + (bg & 3);
    const unsigned pa = ((sl + 65536u + (w >> 2) * 32768u) & 131071u) + (r31 >> 4) * 16384 + (wq * 4 + (bg >> 2)) * 1024 + (bg & 3) * 256;
    const unsigned pp = nq(sl) + ((w >> 2) * 2 + (r31 >> 4)) * 8192 + (wq * 2 + (bg >> 3)) * 1024 + (bg & 3) * 256 + ((bg >> 2) & 1) * 8;
    const f32x4 bv = rdf4(T_OUT + r31 * 16), dv = rdf4(T_OUT + 512 + r31 * 16);
#pragma unroll
    for (int u = 0; u < 8; ++u) {
      const int row = 2 * u + hl;
      const f32x4 av = rdf4(pa + ((row ^ bg) * 16));
      const u32x2 pw = *(LAS u32x2*)(size_t)(pp + ((row ^ sw) * 16)), xw = *(LAS u32x2*)(size_t)(sl + (16 * w + row) * 256 + r31 * 8);
      const float gg = rdf(T_ROW + 1536 + (16 * w + row) * 4);
      const f32x4 pf = {bf2f(pw[0] & 0xffffu), u2f(pw[0] & 0xffff0000u), bf2f(pw[1] & 0xffffu), u2f(pw[1] & 0xffff0000u)};
      const f32x4 xf = {bf2f(xw[0] & 0xffffu), u2f(xw[0] & 0xffff0000u), bf2f(xw[1] & 0xffffu), u2f(xw[1] & 0xffff0000u)};
      f32x4 r;
#pragma unroll
      for (int e = 0; e < 4; ++e) r[e] = (av[e] + pf[e]) + __builtin_fmaf(gg, __builtin_fmaf(xf[e], dv[e], -bv[e]), bv[e]);
      st128<OAUX>(mk(out, 8u << 20), __builtin_bit_cast(u32x4, r), (unsigned)((16 * w + hl + 2 * u) * 4096 + r31 * 16), ci * 128 * 4096 + cnt * 512);
    }
  }
}

extern "C" void kernel_launch(void* const* d_in, const int* in_sizes, int n_in, void* d_out, int out_size, void* d_ws, size_t ws_size, hipStream_t stream) {
  (void)in_sizes; (void)n_in; (void)out_size; (void)ws_size;
  (void)hipFuncSetAttribute((const void*)fused_kernel, hipFuncAttributeMaxDynamicSharedMemorySize, LDS_BYTES);
  fused_kernel<<<256, 512, LDS_BYTES, stream>>>((const u16*)d_in[0], (const u16*)d_in[1], (const u16*)d_in[2], (const u16*)d_in[3], (const u16*)d_in[4],
      (const u16*)d_in[5], (const u16*)d_in[6], (const u16*)d_in[7], (const u16*)d_in[8], (const u16*)d_in[10], (const u16*)d_in[11], (const u16*)d_in[12],
      (const u16*)d_in[13], (const u16*)d_in[14], (char*)d_ws, (float*)d_out);
}
```

```cpp
#include <hip/hip_runtime.h>
#include <utility>

typedef unsigned short u16;
typedef __bf16 bf16x2 __attribute__((ext_vector_type(2)));
typedef __bf16 bf16x4 __attribute__((ext_vector_type(4)));
typedef __bf16 bf16x8 __attribute__((ext_vector_type(8)));
typedef float f32x2 __attribute__((ext_vector_type(2)));
typedef float f32x4 __attribute__((ext_vector_type(4)));
typedef float f32x16 __attribute__((ext_vector_type(16)));
typedef unsigned u32x2 __attribute__((ext_vector_type(2)));
typedef unsigned u32x4 __attribute__((ext_vector_type(4)));
typedef __amdgpu_buffer_rsrc_t rsrc_t;
#define LAS __attribute__((address_space(3)))
#define DI __device__ __forceinline__
#define AI __attribute__((always_inline))
#define SB() __builtin_amdgcn_sched_barrier(0)
#define PIN(x) asm volatile("" : "+v"(x))
#define MFMA(a, b, c) __builtin_amdgcn_mfma_f32_16x16x32_bf16(a, b, c, 0, 0, 0)
#define MFMA32(a, b, c) __builtin_amdgcn_mfma_f32_32x32x16_bf16(a, b, c, 0, 0, 0)
template <int V> using IC = std::integral_constant<int, V>;

constexpr unsigned HID_PANEL = 112 * 8192, H_OFF = 16 * HID_PANEL, H_PANEL = 32 * 16384;
constexpr unsigned PART_OFF = H_OFF + 16 * H_PANEL;
constexpr unsigned GRAN_OFF = PART_OFF + 128 * 32768;
constexpr unsigned RG_OFF = GRAN_OFF + 256 * 1024;
constexpr unsigned CG_OFF = RG_OFF + 16 * 8192;
constexpr unsigned FLAG_OFF = CG_OFF + 256 * 2048;
constexpr unsigned EPC_OFF = FLAG_OFF + 16 * 1024;
constexpr unsigned WS_BYTES = EPC_OFF + 16 * 128;
constexpr int FK_M1 = 0, FK_M2 = 1, FK_M3 = 2, FK_M4 = 3, FK_H = 4, FK_P = 5, FK_EP = 6;
constexpr unsigned TAB = 147456, T_ROW = TAB, T_XA = TAB + 2048, T_OUT = TAB + 4096, T_B1 = TAB + 5120, T_CS2 = TAB + 6144, T_WA = TAB + 8192, T_ZERO = TAB + 12288, T_AGG = T_WA, T_DUM = TAB + 12544, T_CON = TAB + 12800;
constexpr unsigned LDS_BYTES = 163840;
#define XMAP 1
#define ROTM 3
#define OAUX 2
#define SP0 4
#define SPN 6
#define PTE 0
#define MOVE 3
#define PROFX false
constexpr int LAG = 2, LASTR = 25;
constexpr unsigned SPIN_CAP = 1u << 16;

DI rsrc_t mk(const void* p, unsigned bytes) { return __builtin_amdgcn_make_buffer_rsrc((void*)p, 0, bytes, 0x00020000); }
DI void setm0(unsigned lds) { asm volatile("s_mov_b32 m0, %0\n\ts_nop 0" ::"s"(lds) : "memory"); }
template <int OFF> DI void dma(rsrc_t r, unsigned voff, unsigned soff) {
  asm volatile("buffer_load_dwordx4 %0, %1, %2 offen offset:%3 lds" ::"v"(voff), "s"(r), "s"(soff), "i"(OFF) : "memory");
}
template <int OFF> DI void dma1(rsrc_t r, unsigned voff, unsigned soff) {
  asm volatile("buffer_load_dwordx4 %0, %1, %2 offen offset:%3 sc1 lds" ::"v"(voff), "s"(r), "s"(soff), "i"(OFF) : "memory");
}
#define PIN2(x, y) asm volatile("" : "+v"(x), "+v"(y))
#define PINS(x) asm volatile("" : "+s"(x))
typedef const void* const __attribute__((address_space(4)))* kargs_t;
DI const char* karg(int i) { kargs_t ka = (kargs_t)__builtin_amdgcn_kernarg_segment_ptr(); PINS(ka); return (const char*)ka[i]; }
enum { A_X, A_LRE, A_LIM, A_BRE, A_BIM, A_CRE, A_CIM, A_LDT, A_DSK, A_W1, A_B1, A_W2, A_B2, A_GW, A_WS, A_OUT };
template <int N> DI void waitv() { asm volatile("s_waitcnt vmcnt(%0)" ::"i"(N) : "memory"); }
DI void waitl0() { SB(); __builtin_amdgcn_s_waitcnt(0xC07F); asm volatile("" ::: "memory"); }
#define PH(k)
DI void bar() { asm volatile("s_barrier" ::: "memory"); SB(); }
DI bf16x8 rd128(unsigned a) { return *(LAS bf16x8*)(size_t)a; }
DI u32x4 rdu4(unsigned a) { return *(LAS u32x4*)(size_t)a; }
DI f32x4 rdf4(unsigned a) { return *(LAS f32x4*)(size_t)a; }
DI f32x2 rdf2(unsigned a) { return *(LAS f32x2*)(size_t)a; }
DI float rdf(unsigned a) { return *(LAS float*)(size_t)a; }
DI void wrf(unsigned a, float v) { *(LAS float*)(size_t)a = v; }
DI void wrf2(unsigned a, f32x2 v) { *(LAS f32x2*)(size_t)a = v; }
DI bf16x4 rtr(unsigned a) { return __builtin_amdgcn_ds_read_tr16_b64_v4bf16((LAS bf16x4*)(size_t)a); }
DI void sethalf(bf16x8& f, int h, bf16x4 v) {
  if (h == 0) f = __builtin_shufflevector(v, v, 0, 1, 2, 3, -1, -1, -1, -1);
  else { bf16x8 t = __builtin_shufflevector(v, v, -1, -1, -1, -1, 0, 1, 2, 3); f = __builtin_shufflevector(f, t, 0, 1, 2, 3, 12, 13, 14, 15); }
}
DI unsigned f2u(float f) { return __builtin_bit_cast(unsigned, f); }
DI float u2f(unsigned u) { return __builtin_bit_cast(float, u); }
DI float shx(int lane, float v, int m) { return u2f(__builtin_amdgcn_ds_bpermute((lane ^ m) * 4, (int)f2u(v))); }
DI float sum16(float v) { const auto r = __builtin_amdgcn_permlane16_swap(f2u(v), f2u(v), false, false); return u2f(r[0]) + u2f(r[1]); }
DI float sum32(float v) { const auto r = __builtin_amdgcn_permlane32_swap(f2u(v), f2u(v), false, false); return u2f(r[0]) + u2f(r[1]); }
DI float fexp(float v) { return __builtin_amdgcn_exp2f(v * 1.4426950408889634f); }
DI float bf2f(unsigned v) { return u2f(v << 16); }
DI unsigned pk2(float lo, float hi) { f32x2 v = {lo, hi}; return __builtin_bit_cast(unsigned, __builtin_convertvector(v, bf16x2)); }
DI float rbf(float f) { return u2f(pk2(f, 0.f) << 16); }
DI float dot2(unsigned a, unsigned b, float c) { return __builtin_amdgcn_fdot2_f32_bf16(__builtin_bit_cast(bf16x2, a), __builtin_bit_cast(bf16x2, b), c, false); }
template <int C> DI float dppadd(float v) { return v + u2f(__builtin_amdgcn_update_dpp(0, (int)f2u(v), C, 0xf, 0xf, true)); }
DI float sel4(bool b0, bool b1, float a, float b, float c, float d) { PIN(a); PIN(b); PIN(c); PIN(d); const float lo = b0 ? b : a, hi = b0 ? d : c; return b1 ? hi : lo; }
DI f32x2 cmul(f32x2 a, f32x2 b) { return (f32x2){a.x * b.x - a.y * b.y, a.x * b.y + a.y * b.x}; }
DI f32x2 cmulc(f32x2 a, f32x2 b) { return (f32x2){__builtin_fmaf(a.x, b.x, -(a.y * b.y)), __builtin_fmaf(-a.x, b.y, -(a.y * b.x))}; }
DI f32x2 cfma(f32x2 a, f32x2 b, f32x2 c) { return (f32x2){__builtin_fmaf(a.x, b.x, __builtin_fmaf(-a.y, b.y, c.x)), __builtin_fmaf(a.x, b.y, __builtin_fmaf(a.y, b.x, c.y))}; }
template <int AUX = 16> DI void st128(rsrc_t r, u32x4 v, unsigned voff, unsigned soff) {
  __builtin_amdgcn_raw_buffer_store_b128(v, r, voff, soff, AUX);
  asm volatile("s_nop 1" ::"v"(v));
}
DI void st64(rsrc_t r, u32x2 v, unsigned voff, unsigned soff) { __builtin_amdgcn_raw_buffer_store_b64(v, r, voff, soff, 16); }
DI void st32(rsrc_t r, unsigned v, unsigned voff, unsigned soff) { __builtin_amdgcn_raw_buffer_store_b32(v, r, voff, soff, 16); }
DI void stflag(rsrc_t r, unsigned v, int lane, unsigned soff) { if (lane == 0) st32(r, v, 0u, soff); }
DI void ld32a(unsigned& d, rsrc_t r, unsigned voff, unsigned soff) { asm volatile("buffer_load_dword %0, %1, %2, %3 offen sc1" : "=v"(d) : "v"(voff), "s"(r), "s"(soff) : "memory"); }
DI void ld16p(unsigned& d, const u16* p, unsigned voff) { asm volatile("global_load_ushort %0, %1, %2" : "=v"(d) : "v"(voff), "s"(p) : "memory"); }
DI void ld64p(u32x2& d, rsrc_t r, unsigned voff, unsigned soff) { asm volatile("buffer_load_dwordx2 %0, %1, %2, %3 offen" : "=v"(d) : "v"(voff), "s"(r), "s"(soff) : "memory"); }
DI void ld64a(u32x2& d, rsrc_t r, unsigned voff, unsigned soff) { asm volatile("buffer_load_dwordx2 %0, %1, %2, %3 offen sc1" : "=v"(d) : "v"(voff), "s"(r), "s"(soff) : "memory"); }
DI void ld128g(u32x4& d, const u16* p, unsigned voff) { asm volatile("global_load_dwordx4 %0, %1, %2" : "=v"(d) : "v"(voff), "s"(p) : "memory"); }
DI void pfl(unsigned& d, rsrc_t r, unsigned voff, unsigned soff) { asm volatile("buffer_load_dword %0, %1, %2, %3 offen" : "+v"(d) : "v"(voff), "s"(r), "s"(soff) : "memory"); }
template <class T> DI T sel(bool c, T a, T b) { return c ? a : b; }
DI void pfg(const char* p) { asm volatile("global_load_lds_dword %0, off" ::"v"(p) : "memory"); }
DI bool anyl(bool b) { return __builtin_amdgcn_ballot_w64(b) != 0; }
DI void ld128p(u32x4& d, rsrc_t r, unsigned voff, unsigned soff) { asm volatile("buffer_load_dwordx4 %0, %1, %2, %3 offen" : "=v"(d) : "v"(voff), "s"(r), "s"(soff) : "memory"); }
DI void sincos_f(float t, float& sn, float& cs) {
  const float k = __builtin_rintf(t * 0.63661977236f);
  float r = __builtin_fmaf(k, -1.5707962513f, t); r = __builtin_fmaf(k, -7.5497894159e-08f, r); r = __builtin_fmaf(k, -5.3903029534e-15f, r);
  const float z = r * r;
  const float sp = __builtin_fmaf(r * z, __builtin_fmaf(z, __builtin_fmaf(z, __builtin_fmaf(z, 2.7183114939e-06f, -1.9839334836e-04f), 8.3333293858e-03f), -1.6666666642e-01f), r);
  const float cp = __builtin_fmaf(z, __builtin_fmaf(z, __builtin_fmaf(z, __builtin_fmaf(z, 2.4390448796e-05f, -1.3886763775e-03f), 4.1666623324e-02f), -4.9999999726e-01f), 1.f);
  const int q = (int)k;
  const float a = (q & 1) ? cp : sp, b = (q & 1) ? sp : cp;
  sn = (q & 2) ? -a : a; cs = ((q + 1) & 2) ? -b : b;
}
DI void ld128a(u32x4& d, rsrc_t r, unsigned voff, unsigned soff) { asm volatile("buffer_load_dwordx4 %0, %1, %2, %3 offen sc1" : "=v"(d) : "v"(voff), "s"(r), "s"(soff) : "memory"); }

DI unsigned x1off(int a) { return a == 0 ? 131072u : (unsigned)(81920 + a * 16384); }
DI unsigned nq(unsigned s) { return (s + 32768u) & 131071u; }

DI int kb_of(int j, int bl) { return 7 * j + ((j & 1) ? (bl == 6 ? 0 : bl + 1) : bl); }

template <int NR, int LAST> constexpr int nread(int sl) { int n = 0; for (int r = 0; r < NR; ++r) n += (r * LAST) / (NR - 1) <= sl; return n; }
template <int NM, int R0, bool FIRST, int NR, int LAST, int NB, int SL, class RD, class MM, class UP>
DI void slot(RD& rd, MM& mm, UP& upd) {
  constexpr int q = SL + LAG;
  if constexpr (!FIRST && (q == NM || q == NM + R0 || q == 2 * NM)) {
    constexpr int cnt = nread<NR, LAST>(SL - 1) - nread<NR, LAST>(SL - 6);
    __builtin_amdgcn_s_waitcnt(0xC07F | (cnt << 8)); SB();
  }
  if constexpr (!(FIRST && q < NM)) mm((q < NM) ? 1 : ((q < 2 * NM) ? 0 : 1), q % NM);
  SB();
  constexpr int r = (SL * (NR - 1) + LAST - 1) / LAST;
  if constexpr (r < NR && (r * LAST) / (NR - 1) == SL) rd(r);
  constexpr int per = NB > 2 * NM - 1 - LAST ? 2 : 1, u0 = (SL - LAST - 1) * per;
  if constexpr (SL > LAST && u0 < NB) { upd(u0); if constexpr (per == 2 && u0 + 1 < NB) upd(u0 + 1); }
  SB();
}
template <int NM, int R0, bool FIRST, int NR, int LAST, int NB, class RD, class MM, class UP, int... I>
DI void period_(RD& rd, MM& mm, UP& upd, std::integer_sequence<int, I...>) { (slot<NM, R0, FIRST, NR, LAST, NB, I>(rd, mm, upd), ...); }
template <int NM, int R0, bool FIRST, int NR, int LAST, int NB, class RD, class MM, class UP>
DI void period(RD& rd, MM& mm, UP& upd) {
  static_assert(NR - 1 <= LAST && NB <= 2 * (2 * NM - 1 - LAST), "");
  period_<NM, R0, FIRST, NR, LAST, NB>(rd, mm, upd, std::make_integer_sequence<int, 2 * NM>{});
}
template <bool FIRST, int SL, class RD, class MM, class UP> DI void slot32(RD& rd, MM& mm, UP& upd) {
  if constexpr (SL == 12) { __builtin_amdgcn_s_waitcnt(0xC07F | (8 << 8)); SB(); }
  if constexpr (!(FIRST && SL < 12)) mm((SL + 4) & 15);
  SB();
  if constexpr (SL < 2) { rd(2 * SL); rd(2 * SL + 1); } else if constexpr (SL < 14) rd(SL + 2);
  if constexpr ((SL & 3) == 2) { upd(SL >> 2); upd(4 + (SL >> 2)); }
  SB();
}
template <bool FIRST, class RD, class MM, class UP, int... I> DI void period32_(RD& rd, MM& mm, UP& upd, std::integer_sequence<int, I...>) { (slot32<FIRST, I>(rd, mm, upd), ...); }
template <bool FIRST, class RD, class MM, class UP> DI void period32(RD& rd, MM& mm, UP& upd) { period32_<FIRST>(rd, mm, upd, std::make_integer_sequence<int, 16>{}); }
template <class G, class W, int... U> DI void ival_(G& g, W& wk, std::integer_sequence<int, U...>) { ((g(IC<U>{}), SB(), wk(IC<U>{}), SB()), ...); }
template <class G, class W> DI void ival(G&& g, W&& wk) { ival_(g, wk, std::make_integer_sequence<int, 8>{}); }
template <int N, class F, int... I> DI void sfor_(F& f, std::integer_sequence<int, I...>) { (f(IC<I>{}), ...); }
template <int N, class F> DI void sfor(F&& f) { sfor_<N>(f, std::make_integer_sequence<int, N>{}); }

DI void check_flags(unsigned& pv, rsrc_t ws, unsigned voff, unsigned soff, int lane, int lo, int hi, unsigned ep) {
  PIN(pv);
  const bool need = lane >= lo && lane < hi;
  if (__builtin_expect(anyl(need && pv != ep), 0)) {
    for (unsigned sp = 0; sp < SPIN_CAP; ++sp) {
      __builtin_amdgcn_s_sleep(2);
      ld32a(pv, ws, voff, soff); asm volatile("s_waitcnt vmcnt(0)" : "+v"(pv)::"memory");
      if (!anyl(need && pv != ep)) break;
    }
  }
}

#if defined(__HIP_DEVICE_COMPILE__) && __HIP_DEVICE_COMPILE__
__attribute__((target("no-packed-fp32-ops")))
#endif
__global__ __launch_bounds__(512, 2) void fused_kernel(const u16* x, const u16* lre, const u16* lim, const u16* bre, const u16* bim, const u16* cre, const u16* cim,
    const u16* ldt, const u16* dsk, const u16* w1, const u16* b1, const u16* w2, const u16* b2, const u16* gw, char* ws, float* out) {
  extern __shared__ __attribute__((aligned(16))) char shm[];
  unsigned long long pc0; asm volatile("s_getpc_b64 %0" : "=s"(pc0));
  const int lane = threadIdx.x & 63, w = __builtin_amdgcn_readfirstlane(threadIdx.x >> 6);
  const int bid = blockIdx.x, xcd = bid & 7, loc = bid >> 3;
  const int ci = (xcd & 3) * 4 + (loc & 3), cj = (loc >> 4) * 8 + (xcd >> 2) * 4 + ((loc >> 2) & 3);
  const int rot = (ci + cj) & ROTM, cnt = cj & 7, half = cj >> 3, wq = w & 3, tq = threadIdx.x & 255;
  const int n4 = half ? 28 + MOVE : 28 - MOVE;
  const rsrc_t rws = mk(ws, WS_BYTES), rx = mk(x, 2048 * 2048);
  const int wm = (w >> 1) & 1, wn = w & 1, g = lane >> 4, r15 = lane & 15, q2 = (lane >> 2) & 3, p = lane & 3, hl = lane >> 5, r31 = lane & 31;
  const unsigned flagb = FLAG_OFF + ci * 1024, fvo = (lane & 15) * 4;
  unsigned rgb = RG_OFF + ci * 8192; PINS(rgb);
  auto nowork = [&](auto) AI {};
  unsigned va[4], vb[4], vw[7], sl, sa, sb, ta, tb; int sx; rsrc_t ra, rb;
  auto vnat = [&](int u) AI { return (8u * u + (lane >> 3)) * 2048 + (((lane & 7) ^ ((4 * u + g) & 7)) * 16) - 1024 * u; };
  auto nexts = [&]() AI { sa = __builtin_amdgcn_readlane(ta, sx); sb = __builtin_amdgcn_readlane(tb, sx); ++sx; PINS(sa); PINS(sb); };
  auto G = [&](auto U) AI { constexpr int u = U.value;
    if constexpr (u == 0) setm0(sl + wq * 4096);
    if constexpr (u == 4) setm0(sl + 16384 + wq * 4096);
    if constexpr (u < 4) dma1<u * 1024>(ra, va[u], sa); else dma<(u - 4) * 1024>(rb, vb[u - 4], sb);
  };
  auto G2 = [&](auto S, auto U) AI { constexpr int a = S.value % 3, u = U.value;
    if constexpr (u == 0) setm0((a == 0 ? 131072u : 81920u + a * 16384u) + wq * 4096);
    if constexpr (u == 4) setm0(a * 32768 + wq * 7168);
    if constexpr (u == 8) setm0(a * 32768 + wq * 7168 + 4096);
    if constexpr (u < 4) dma<u * 1024>(rx, va[u], sa); else dma<(u & 3) * 1024>(rb, vw[u - 4], sb);
  };
  const int c7 = ci & 7;
  auto setup2 = [&]() AI {
#pragma unroll
    for (int q = 0; q < 6; ++q) { const int k = 8 * (q & 1) + (lane >> 3), f = ((k >> 1) & 1) | ((q & 1) << 1);
      vw[q] = 4096 + k * 7168 + ((cj & 1) ? 64 : 0) + (q >> 1) * 128 + (((((lane & 7) >> 1) ^ f) * 2) + (lane & 1)) * 16 - (q & 3) * 1024; }
    vw[6] = 4096 + (lane >> 2) * 7168 + ((cj & 1) ? 0 : 384) + (((((lane & 3) >> 1) ^ hl) * 2) + (lane & 1)) * 16 - 2048;
    int k = lane - 2 + (c7 < 2 ? 3 * c7 : c7 - 2); k = 2 + (k >= 6 ? k - 6 : k);
    k = lane < 2 ? lane : lane < 8 ? k : (lane ^ rot);
    k = (k ^ (ci & 8)) & 15;
    ta = (ci * 128 + 32 * wq) * 2048 + k * 128; tb = (k * 64 + 16 * wq) * 7168 + cj * 448;
    PIN2(ta, tb);
#pragma unroll
    for (int q = 0; q < 7; ++q) PIN(vw[q]);
  };
  auto setup3 = [&]() AI {
    ra = rws;
#pragma unroll
    for (int q = 0; q < 4; ++q) { va[q] = lane * 16; vb[q] = vnat(q); }
  };
  auto table3 = [&](unsigned& ta, unsigned& tb) AI {
    const int ks = 16 * half + (lane ^ rot);
    int n = 2 * (lane - 16) + (wq >> 1), hp = half; if (n >= 56) { n -= 2 * MOVE; hp = 0; }
    const int kb = kb_of(hp * 8 + ((n ^ rot) & 7), n >> 3);
    ta = lane < 16 ? H_OFF + ci * H_PANEL + ks * 16384 + wq * 4096 : ci * HID_PANEL + kb * 8192 + (wq & 1) * 4096;
    tb = lane < 16 ? (cnt * 128 + 32 * wq) * 2048 + (ks >> 1) * 128 + (ks & 1) : (kb * 32 + 16 * (wq & 1)) * 2048 + cnt * 256;
    PIN(ta); PIN(tb);
  };
  auto next3 = [&]() AI { nexts(); rb = mk(sel<const u16*>(sb & 1, cim, cre), 1024 * 2048); sb &= ~1u; };

  if (w < 4) {
    constexpr bool isx = PROFX; (void)isx;
    unsigned epr, epc;
    const unsigned ca = (32 * wn + r31) * 8;
    const int seg0 = 4 * wm + hl;
    f32x2 lam, cof, l16, l128, l1k, pw0, pw1; unsigned t3a, t3b;
    {
      setm0(T_DUM);
      const int row = bid * 8 + (lane & 7);
      const char* pp = (const char*)(hl ? (row < 1024 ? bre : bim - 1024 * 1024) : x) + (size_t)row * 2048 + (4 * wq + ((lane >> 3) & 3)) * 128;
      for (int q = 0; q <= wq; ++q) __builtin_amdgcn_s_sleep(6);
      pfg(pp);
      pfg((const char*)pc0 + (((loc * 4 + wq) % 24) * 16 + (lane & 15)) * 128);
      ld32a(epr, rws, 0u, flagb + FK_EP * 128); ld32a(epc, rws, 0u, EPC_OFF + cj * 128);
      setup2(); sx = 2; nexts(); table3(t3a, t3b);
      rb = mk((const char*)w1 - 4096, 1024 * 7168 + 4096);
#pragma unroll
      for (int q = 0; q < 4; ++q) { va[q] = vnat(q); PIN(va[q]); }
      wrf(T_ZERO + (lane & 3) * 4, 0.f);
    }
    {
      f32x16 acc[2][2];
#pragma unroll
      for (int a = 0; a < 4; ++a)
#pragma unroll
        for (int e = 0; e < 16; ++e) acc[a >> 1][a & 1][e] = 0.f;
      bf16x8 wv[4][2];
      const int sstar = c7 < 2 ? c7 : 2;
      const int fsw = ((q2 >> 1) & 1) | ((g & 1) << 1);
      unsigned cf = sstar * 32768 + (g >> 1) * 7168 + (8 * (g & 1) + q2) * 128 + ((wq ^ fsw) * 32) + p * 8;
      unsigned ch = sstar * 32768 + (g >> 1) * 7168 + 6144 + (8 * (g & 1) + q2) * 64 + (((wq & 1) ^ (g & 1)) * 32) + p * 8;
      PIN(cf); PIN(ch);
      auto wread = [&](int st) AI {
        if (sstar == st) {
#pragma unroll
          for (int e = 0; e < 4; ++e)
#pragma unroll
            for (int t = 0; t < 4; ++t) sethalf(wv[t][e >> 1], e & 1, rtr((e >> 1) * 14336 + (t < 3 ? cf + t * 2048 + (e & 1) * 512 : ch + (e & 1) * 256)));
        } };
      {
        bf16x8 fa[4][2], fb[4][2];
        unsigned cb[8], ba[8], rs = 98304;
#pragma unroll
        for (int b = 0; b < 8; ++b) { cb[b] = (b < 4 ? wm * 8192u : 16384 + wn * 8192u) + r31 * 128 + (((2 * (b & 3) + hl) ^ ((r31 >> 1) & 7)) * 16); ba[b] = rs + cb[b]; PIN(ba[b]); }
        auto rd = [&](int r) AI { const int k16 = r >> 2, xx = r & 3;
          if (xx == 0) fa[k16][0] = rd128(ba[k16]); else if (xx == 3) fa[k16][1] = rd128(ba[k16] + 4096); else fb[k16][xx - 1] = rd128(ba[4 + k16] + (xx - 1) * 4096); };
        auto mm = [&](int m) AI { const int k16 = m >> 2, mt = (m >> 1) & 1, nt = m & 1; acc[mt][nt] = MFMA32(fa[k16][mt], fb[k16][nt], acc[mt][nt]); };
        auto up = [&](int b) AI { if (b == 0) rs = nq(rs); ba[b] = rs + cb[b]; PIN(ba[b]); };
        const char* pp = (const char*)w1 + ((bid * 4 + wq) * 56 + (lane < 56 ? lane : 55)) * 128;
        const int l128k = (lane < 56 ? lane : 55) * 131072;
        const char* pt = wq == 0 ? (const char*)w1 + l128k : wq == 1 ? (const char*)w2 + l128k
                       : wq == 2 ? (lane < 4 ? (const char*)sel<const u16*>(lane & 2, cim, cre) + cnt * 262144 + (lane & 1) * 131072
                                  : lane < 8 ? (const char*)out + ci * 524288 + (lane & 3) * 131072
                                  : lane == 8 ? ws + PART_OFF + (ci * 8 + cnt) * 32768 : lane == 9 ? ws + GRAN_OFF + cj * 16384 : lane == 10 ? ws + CG_OFF + cj * 32768 : ws + rgb)
                       : (lane < 8 ? ws + ci * HID_PANEL + lane * 131008 : ws + H_OFF + ci * H_PANEL + (lane & 3) * 131072);
        PIN(pt);
        PIN(pp);
        if (PTE) pfg(pt);
        waitl0(); bar();
        period32<true>(rd, mm, up); waitl0(); bar();
        const int sp = SP0 + bid % SPN;
        for (int o = 0; o < 2; ++o) {
          for (int s = o ? sp : 1; s < (o ? 16 : sp); ++s) { period32<false>(rd, mm, up); waitl0(); bar(); }
          if (o == 7) { pfg(pp); if (!PTE) pfg(pt); }
        }
        waitv<0>(); PIN(epr); PIN(epc);
        epr = __builtin_amdgcn_readfirstlane(epr) + 1u; epc = __builtin_amdgcn_readfirstlane(epc) + 1u;
        lam = rdf2(T_CON + ca);
        wread(0);
        sfor<7>([&](auto U) AI { G2(IC<2>{}, IC<U.value + 4>{}); SB(); if constexpr (U.value < 6) { mm(4 + 2 * U.value); mm(5 + 2 * U.value); } SB(); });
        nexts();
      }
      f32x2 st[2] = {{0, 0}, {0, 0}}, Pf[2] = {{0, 0}, {0, 0}}, Gt = {0, 0}, cr = {0, 0}, ag[8];
      u32x4 lb[8], cg[8], rgv[2]; unsigned lbo[8], lbe[8], ob[2][2][4], pv, gd = 0;
      f32x4 cs[4]; f32x2 so[2]; float c0s = 0.f, c1s = 0.f;
      const rsrc_t rgr = mk(ws + GRAN_OFF + cj * 16384, 16384);
      const int cp = 28 * wq + (r31 < 28 ? r31 : 27);
      const unsigned cgo = hl * 16384 + cp * 16;
      auto bad = [&]() AI { unsigned d = 0;
#pragma unroll
        for (int q = 0; q < 8; ++q) { PIN(lb[q]); d |= (int)lbo[q] < 0 ? 0u : ((lb[q][1] ^ epc) | (lb[q][3] ^ epc)); }
        return d != 0; };
      auto gbad = [&]() AI { unsigned d = 0;
#pragma unroll
        for (int q = 0; q < 8; ++q) { PIN(cg[q]); d |= (cg[q][1] ^ epc) | (cg[q][3] ^ epc); }
        return d != 0; };
      auto E2 = [](int s) constexpr { return (s == 2 || s == 3) ? 1 : s == 4 ? 8 : s == 9 ? 2 : (s == 10 || s == 11) ? 4 : 0; };
      auto T2 = [](int s) constexpr { return (s == 7 || s == 8) ? 4 : 0; };
      sfor<15>([&](auto S) AI { constexpr int s = S.value;
        if constexpr (s < 3) waitl0();
        if constexpr (s == 10) waitv<11 + E2(9)>();
        else if constexpr (s >= 1) waitv<T2(s - 2) + (s < 14 ? 11 : 8) + E2(s - 1) + T2(s - 1)>();
        if constexpr (s == 13) { if (w == 0) { waitv<8>(); check_flags(pv, rws, fvo, flagb + FK_H * 128, lane, 8 * half, 8 * half + 8, epr); } }
        bar();
        if constexpr (s < 2) wread(s + 1);
        if constexpr (s == 2) {
#pragma unroll
          for (int sg = 0; sg < 8; ++sg) ag[sg] = rdf2(T_AGG + sg * 512 + ca);
          l16 = rdf2(T_CON + 1024 + ca); cof = rdf2(T_CON + 512 + ca);
        }
        if constexpr (s == 5) { pw1 = rdf2(T_CON + 1536 + ca); pw0 = rdf2(T_CON + 2048 + ca); l128 = rdf2(T_CON + 2560 + ca); l1k = rdf2(T_CON + 3072 + ca); }
        if constexpr (s == 10) { if (w == 0) stflag(rws, epr, lane, flagb + FK_H * 128 + cj * 4); }
        SB();
        auto work = [&](auto U) AI { constexpr int u = U.value;
          if constexpr (s < 2 && u >= 1 && u < 9) {
            constexpr int t = 8 * s + u - 1;
#pragma unroll
            for (int mt = 0; mt < 2; ++mt) st[mt] = cfma(lam, st[mt], (f32x2){acc[mt][0][t], acc[mt][1][t]});
            PIN2(st[0], st[1]);
          }
          if constexpr (s == 1 && u == 9) { wrf2(T_AGG + seg0 * 512 + ca, st[0]); wrf2(T_AGG + (seg0 + 2) * 512 + ca, st[1]); }
          if constexpr (s == 2 && u >= 1 && u < 9) {
            constexpr int sg = u - 1, mt = (sg >> 1) & 1; const bool me = sg == seg0 + 2 * mt;
            Pf[mt].x = sel<float>(me, Gt.x, Pf[mt].x); Pf[mt].y = sel<float>(me, Gt.y, Pf[mt].y); Gt = cfma(l16, Gt, ag[sg]);
            PIN2(Gt, Pf[mt]);
          }
          if constexpr (s == 2 && u == 9) st64(rgr, (u32x2){f2u(sel<float>(hl, Gt.y, Gt.x)), epc}, ca * 2 + hl * 8, ci * 1024);
          if constexpr (s == 3 && u < 4) {
            const bf16x8 one = __builtin_bit_cast(bf16x8, (u32x4){0x3F803F80u, 0x3F803F80u, 0x3F803F80u, 0x3F803F80u});
            cs[u] = MFMA(one, wv[u][1], MFMA(one, wv[u][0], ((f32x4){0, 0, 0, 0}))); PIN(cs[u]);
          }
          if constexpr (s == 3 && u == 9)
            st64(rws, (u32x2){f2u(sel4(g & 1, g & 2, cs[0][0], cs[1][0], cs[2][0], cs[3][0])), epc}, (g < 3 ? g * 64 + wq * 16 + r15 : 192 + (wq & 1) * 16 + r15) * 8u, CG_OFF + (cj * 16 + ci) * 2048);
          if constexpr ((s == 3 && u >= 4 && u < 9) || (s == 4 && u < 5) || (s == 5 && u >= 1 && u < 7)) {
            constexpr int t = s == 3 ? u - 4 : s == 4 ? 5 + u : 9 + u;
            f32x2 v0 = cmulc(cof, (f32x2){acc[0][0][t], acc[0][1][t]}), v1 = cmulc(cof, (f32x2){acc[1][0][t], acc[1][1][t]}); PIN2(v0, v1);
            acc[0][0][t] = v0.x; acc[0][1][t] = v0.y; acc[1][0][t] = v1.x; acc[1][1][t] = v1.y;
          }
          if constexpr (s == 4 && u >= 6 && u < 10) {
#pragma unroll
            for (int q = 2 * u - 12; q < 2 * u - 10; ++q) { lbo[q] = (ci - 8 - 8 * hl + q) * 1024 + ca * 2; ld128a(lb[q], rgr, lbo[q], 0u); }
          }
          if constexpr (s == 5 && u == 8) {
            if (!wm) pw0 = (f32x2){1.f, 0.f};
            pw0 = cmul(pw0, (f32x2){sel<float>(hl, l16.x, 1.f), sel<float>(hl, l16.y, 0.f)}); pw1 = cmul(pw0, pw1); PIN2(pw0, pw1);
          }
          if constexpr (s == 5 && (u == 7 || u == 9)) {
#pragma unroll
            for (int q = 2 * u - 14; q < 2 * u - 10; ++q) { lbe[q] = (int)lbo[q] < 0 ? 0u : epc; PIN(lbe[q]); }
          }
          if constexpr (s == 6 && u < 8) {
            PIN(lb[u]); gd |= (lb[u][1] ^ lbe[u]) | (lb[u][3] ^ lbe[u]); cr = cfma(l128, cr, (f32x2){u2f(lb[u][0]), u2f(lb[u][2])});
            PIN2(cr, gd);
          }
          if constexpr (s == 6 && u == 8) {
            if (__builtin_expect(anyl(gd != 0), 0)) {
              for (unsigned sp = 0; sp < SPIN_CAP; ++sp) {
                __builtin_amdgcn_s_sleep(2);
#pragma unroll
                for (int q = 0; q < 8; ++q) ld128a(lb[q], rgr, lbo[q], 0u);
                waitv<0>();
                if (!anyl(bad())) break;
              }
              cr = (f32x2){0, 0};
#pragma unroll
              for (int q = 0; q < 8; ++q) cr = cfma(l128, cr, (f32x2){u2f(lb[q][0]), u2f(lb[q][2])});
            }
            gd = 0;
            const auto sx_ = __builtin_amdgcn_permlane32_swap(f2u(cr.x), f2u(cr.x), false, false), sy_ = __builtin_amdgcn_permlane32_swap(f2u(cr.y), f2u(cr.y), false, false);
            cr = cfma(l1k, (f32x2){u2f(sx_[1]), u2f(sy_[1])}, (f32x2){u2f(sx_[0]), u2f(sy_[0])});
            PIN(cr);
          }
          if constexpr (s == 6 && u == 9) { st[0] = cmulc(cof, cfma(pw0, cr, Pf[0])); PIN(st[0]); }
          if constexpr (s == 7 && u == 0) { st[1] = cmulc(cof, cfma(pw1, cr, Pf[1])); PIN(st[1]); }
          if constexpr ((s == 7 && u >= 2 && u < 10) || (s == 8 && u >= 1 && u < 9)) {
            constexpr int t = s == 7 ? u - 2 : 7 + u;
            if constexpr (t & 1) { so[0] = st[0]; so[1] = st[1]; }
#pragma unroll
            for (int mt = 0; mt < 2; ++mt) st[mt] = cfma((f32x2){lam.x, -lam.y}, st[mt], (f32x2){acc[mt][0][t], acc[mt][1][t]});
            if constexpr (t & 1) {
#pragma unroll
              for (int mt = 0; mt < 2; ++mt) { ob[mt][0][(t >> 1) & 3] = pk2(so[mt].x, st[mt].x); ob[mt][1][(t >> 1) & 3] = pk2(so[mt].y, st[mt].y); PIN2(ob[mt][0][(t >> 1) & 3], ob[mt][1][(t >> 1) & 3]); }
            }
            PIN2(st[0], st[1]);
          }
          if constexpr ((s == 7 || s == 8) && u == 10) {
            constexpr int hf = s - 7;
#pragma unroll
            for (int q = 0; q < 4; ++q) { const int mt = q >> 1, pt = q & 1;
              st128(rws, (u32x4){ob[mt][pt][0], ob[mt][pt][1], ob[mt][pt][2], ob[mt][pt][3]},
                    (2 * wn + (r31 >> 4)) * 4096 + wm * 2048 + hf * 1024 + (2 * mt + hl) * 256 + ((r15 ^ (hf << 2)) * 16), H_OFF + ci * H_PANEL + (2 * cj + pt) * 16384); }
          }
          if constexpr (s == 9 && u == 0) { if (ci == 15 && w == 1) stflag(rws, epc, lane, EPC_OFF + cj * 128); }
          if constexpr (s == 9 && (u == 3 || u == 7)) ld128a(rgv[u >> 2], rws, tq * 16u + (u >> 2) * 4096, rgb);
          if constexpr (s == 12 && u == 4) {
            auto rbad = [&]() AI { PIN2(rgv[0], rgv[1]); return ((rgv[0][1] ^ epr) | (rgv[0][3] ^ epr) | (rgv[1][1] ^ epr) | (rgv[1][3] ^ epr)) != 0; };
            if (__builtin_expect(anyl(rbad()), 0))
              for (unsigned sp = 0; sp < SPIN_CAP; ++sp) { __builtin_amdgcn_s_sleep(2);
                ld128a(rgv[0], rws, tq * 16u, rgb); ld128a(rgv[1], rws, tq * 16u + 4096, rgb);
                waitv<0>(); if (!anyl(rbad())) break; }
          }
          if constexpr (s == 12 && u == 6) {
            wrf(T_ROW + (tq & 1) * 1024 + (tq >> 1) * 4, u2f(rgv[0][0])); wrf(T_ROW + (tq & 1) * 1024 + 512 + (tq >> 1) * 4, u2f(rgv[0][2]));
            wrf2(T_XA + tq * 8, (f32x2){u2f(rgv[1][0]), u2f(rgv[1][2])});
          }
          if constexpr (s == 12 && u == 2) { if (w == 0) ld32a(pv, rws, fvo, flagb + FK_H * 128); }
          if constexpr ((s == 10 || s == 11) && (u & 1) && u < 8) { constexpr int q = 4 * (s - 10) + (u >> 1); ld128a(cg[q], rws, cgo, CG_OFF + (cj * 16 + q) * 2048); }
          if constexpr (s == 13 && u < 4) {
#pragma unroll
            for (int q = 2 * u; q < 2 * u + 2; ++q) { PIN(cg[q]); gd |= (cg[q][1] ^ epc) | (cg[q][3] ^ epc); c0s += u2f(cg[q][0]); c1s += u2f(cg[q][2]); }
            PIN(gd); PIN(c0s); PIN(c1s);
          }
          if constexpr (s == 13 && u == 4) {
            if (__builtin_expect(anyl(gd != 0), 0)) {
              for (unsigned sp = 0; sp < SPIN_CAP; ++sp) { __builtin_amdgcn_s_sleep(2);
#pragma unroll
                for (int q = 0; q < 8; ++q) ld128a(cg[q], rws, cgo, CG_OFF + (cj * 16 + q) * 2048);
                waitv<0>(); if (!anyl(gbad())) break; }
              c0s = 0.f; c1s = 0.f;
#pragma unroll
              for (int q = 0; q < 8; ++q) { c0s += u2f(cg[q][0]); c1s += u2f(cg[q][2]); }
            }
            c0s = sum32(c0s); c1s = sum32(c1s); PIN(c0s); PIN(c1s);
          }
          if constexpr (s == 13 && u == 6) {
            const float v = sel<float>(hl, c1s, c0s), hi = rbf(v), bb = rdf(T_B1 + (2 * cp + hl) * 4);
            *(LAS u32x4*)(size_t)(T_WA + (2 * cp + hl) * 16) = (u32x4){pk2(hi, v - hi), pk2(hi, bb), pk2(bb, 0.f), 0u};
          }
        };
        if constexpr (s < 13) { sfor<11>([&](auto U) AI { G2(IC<s + 3>{}, U); SB(); work(U); SB(); }); if constexpr (s < 12) nexts(); }
        if constexpr (s == 12) { ra = rws;
#pragma unroll
          for (int q = 0; q < 4; ++q) { vb[q] = va[q]; va[q] = lane * 16; }
          ta = t3a; tb = t3b; sl = 32768; sx = 0; next3(); }
        if constexpr (s >= 13) { ival(G, work); sl = nq(sl); next3(); asm volatile("s_nop 4"); }
        if constexpr (s == 14) ival(G, nowork);
      });
      waitl0(); waitv<16>(); bar();
    }
    f32x4 acc[4][4];
#pragma unroll
    for (int a = 0; a < 4; ++a)
#pragma unroll
      for (int b = 0; b < 4; ++b) acc[a][b] = (f32x4){0, 0, 0, 0};
    bf16x8 fa[2][4], fb[2][4];
    const int kap = 4 * (g & 1) + q2, hf = p >> 1;
    const unsigned wb = 16384 + (4 * g + q2) * 256 + p * 8, hb = (g >> 1) * 4096 + wm * 2048 + hf * 1024 + (8 * (g & 1) + q2) * 16 + (p & 1) * 8;
    const unsigned cb3[4] = {hb + 64u * hf, hb + 64u * (1 ^ hf), 16384 + wn * 8192u + r15 * 128 + ((g ^ (r15 >> 1)) * 16), 16384 + wn * 8192u + r15 * 128 + (((4 + g) ^ (r15 >> 1)) * 16)};
    const unsigned cb4[5] = {wm * 4096u + lane * 16, wb + (((4 * wn + 0) ^ kap) * 32), wb + (((4 * wn + 1) ^ kap) * 32), wb + (((4 * wn + 2) ^ kap) * 32), wb + (((4 * wn + 3) ^ kap) * 32)};
    unsigned ba[5];
    sl = 32768;
#pragma unroll
    for (int b = 0; b < 4; ++b) { ba[b] = sl + cb3[b]; PIN(ba[b]); }
    auto rd3 = [&](int r) AI { const int ks = r / 12, xx = r % 12;
      if (xx < 2) sethalf(fa[ks][0], xx, rtr(ba[xx] + ks * 8192));
      else if (xx < 6) fb[ks][xx - 2] = rd128(ba[2 + ks] + (xx - 2) * 2048);
      else { const int a = (xx - 4) >> 1, h = xx & 1; sethalf(fa[ks][a], h, rtr(ba[h] + ks * 8192 + a * 256)); } };
    auto rd4 = [&](int r) AI { const int ks = r / 12, xx = r % 12;
      if (xx == 0) fa[ks][0] = rd128(ba[0] + ks * 8192);
      else if (xx <= 8) { const int b = (xx - 1) >> 1, h = (xx - 1) & 1; sethalf(fb[ks][b], h, rtr(ba[1 + b] + ks * 8192 + h * 4096)); }
      else fa[ks][xx - 8] = rd128(ba[0] + ks * 8192 + (xx - 8) * 1024); };
    auto mm = [&](int ks, int m) AI { acc[m >> 2][m & 3] = MFMA(fb[ks][m & 3], fa[ks][m >> 2], acc[m >> 2][m & 3]); };
    auto up3 = [&](int b) AI { if (b == 0) sl = nq(sl); ba[b] = sl + cb3[b]; PIN(ba[b]); };
    auto up4 = [&](int b) AI { if (b == 0) sl = nq(sl); ba[b] = sl + cb4[b]; PIN(ba[b]); };
    period<16, 4, true, 24, LASTR, 4>(rd3, mm, up3); waitl0(); waitv<8>(); bar();
    for (int s = 1; s < 15; ++s) { period<16, 4, false, 24, LASTR, 4>(rd3, mm, up3); waitl0(); waitv<0>(); bar(); }
    period<16, 4, false, 24, LASTR, 5>(rd3, mm, up4); waitl0(); bar();
    {
      float gr[4]; int seen = 0;
#pragma unroll
      for (int a = 0; a < 4; ++a) gr[a] = rdf(T_ROW + 1536 + (64 * wm + 16 * a + r15) * 4);
      auto mmg = [&](int ks, int m) AI { mm(ks, m); ++seen; const int t = seen - 7;
        if (t >= 0 && t < 16) { acc[t >> 2][t & 3] *= gr[t >> 2]; PIN(acc[t >> 2][t & 3]); } };
      period<16, 4, false, 24, LASTR, 5>(rd4, mmg, up4); waitl0(); bar();
    }
    for (int s = 1; s < n4; ++s) { period<16, 4, false, 24, LASTR, 5>(rd4, mm, up4); waitl0(); bar(); }
#pragma unroll
    for (int m = LAG; m < 16; ++m) mm(1, m);
    if (half) {
      const unsigned pb = ((sl + 65536u + wm * 32768u) & 131071u) + wn * 16384 + g * 256;
#pragma unroll
      for (int a = 0; a < 4; ++a)
#pragma unroll
        for (int b = 0; b < 4; ++b) *(LAS f32x4*)(size_t)(pb + (a * 4 + b) * 1024 + ((r15 ^ (4 * b + g)) * 16)) = acc[a][b];
      waitl0();
    } else {
#pragma unroll
      for (int a = 0; a < 4; ++a)
#pragma unroll
        for (int bp = 0; bp < 2; ++bp)
          st128(rws, (u32x4){pk2(acc[a][2 * bp][0], acc[a][2 * bp][1]), pk2(acc[a][2 * bp][2], acc[a][2 * bp][3]), pk2(acc[a][2 * bp + 1][0], acc[a][2 * bp + 1][1]), pk2(acc[a][2 * bp + 1][2], acc[a][2 * bp + 1][3])},
                (a * 2 + bp) * 1024 + g * 256 + ((r15 ^ (8 * wn + 4 * bp + g)) * 16), PART_OFF + (ci * 8 + cnt) * 32768 + wq * 8192);
      waitv<0>();
    }
    bar();
    if (!half && w == 0) stflag(rws, epr, lane, flagb + FK_P * 128 + cnt * 4);
  } else {
    constexpr bool isx = !PROFX; (void)isx;
    unsigned epr, pv, t3a, t3b, cb[8], ba[8]; const char *pp0, *pp1;
    {
      u32x4 xr[2][2], gq[2];
      ra = rx; rb = mk(sel<const u16*>(wq & 1, bim, bre), 1024 * 2048);
#pragma unroll
      for (int q = 0; q < 4; ++q) { va[q] = (16 * hl + 4 * q + ((lane >> 3) & 3)) * 2048 + (((lane & 7) ^ ((4 * q + g) & 7)) * 16) - 1024 * q; vb[q] = vnat(q); }
      auto src1 = [&](int s) AI { const int k = s ^ (s < 2 ? 0 : s < 4 ? (rot & 1) : rot); sa = (ci * 128 + 32 * wq) * 2048 + k * 128; sb = (cj * 64 + 32 * (wq >> 1)) * 2048 + k * 128; sl = ((s + 3) & 3) * 32768; };
      src1(0); ival(G, nowork); src1(1); ival(G, nowork);
      unsigned pdt, plr, pli, bq, oq; f32x2 lp; float lr, li;
      const int tc = tq < 224 ? tq : 223;
      ld32a(epr, rws, 0u, flagb + FK_EP * 128); ld128g(gq[0], gw, lane * 16u); ld128g(gq[1], gw + 512, lane * 16u);
      ld16p(pdt, ldt + 64 * cj, lane * 2u); ld16p(plr, lre + 64 * cj, lane * 2u); ld16p(pli, lim + 64 * cj, lane * 2u);
      ld16p(bq, b1, (224 * cj + (tc < 192 ? ((cj & 1) ? 32 : 0) + tc : ((cj & 1) ? -192 : 0) + tc)) * 2u);
      ld16p(oq, sel<const u16*>(wq < 2, b2, dsk), (cnt * 128 + (tq & 127)) * 2u);
      SB();
      setup2(); table3(t3a, t3b);
      {
        const int f = ((q2 >> 1) & 1) | ((g & 1) << 1);
        const unsigned nb = wm * 8192u + r15 * 128, n0 = (g ^ (r15 >> 1)) * 16, n1 = ((4 + g) ^ (r15 >> 1)) * 16, wk = (g >> 1) * 7168u + p * 8;
        cb[0] = nb + wn * 4096 + n0; cb[1] = nb + wn * 4096 + n1; cb[2] = nb + (1 - wn) * 4096 + n0; cb[3] = nb + (1 - wn) * 4096 + n1;
        cb[4] = wk + (8 * (g & 1) + q2) * 128 + (((2 * wn) ^ f) * 32); cb[5] = wk + (8 * (g & 1) + q2) * 128 + (((2 * wn + 1) ^ f) * 32);
        cb[6] = wk + 6144 + (8 * (g & 1) + q2) * 64 + (g & 1) * 32; cb[7] = wk + 6144 + (8 * (g & 1) + q2) * 64 + ((g & 1) ^ 1) * 32;
#pragma unroll
        for (int b = 0; b < 8; ++b) { ba[b] = cb[b] + (b < 4 ? 131072u : 0u); PIN2(cb[b], ba[b]); }
        pp0 = lane < 32 ? (const char*)sel<const u16*>(lane & 16, cim, cre) + ((bid * 4 + wq) * 16 + r15) * 128 : (const char*)w2 + ((bid * 4 + wq) * 56 + r15) * 128;
        pp1 = (const char*)w2 + ((bid * 4 + wq) * 56 + (lane < 40 ? 16 + lane : 55)) * 128;
        PIN2(pp0, pp1);
      }
      waitv<8 + 8>(); bar();
      PH(1);
      src1(2); ival(G, nowork);
      src1(3); ival(G, nowork);
      float sv[6] = {0, 0, 0, 0, 0, 0}, a1, a2, a3, mean, sd, rstd, qv, gg, kq, rr;
      auto E1 = [](int s) constexpr { return (s == 1 || s == 2) ? 2 : s == 11 ? 1 : 0; };
      sfor<12>([&](auto S) AI { constexpr int s = S.value;
        if constexpr (s == 0) waitv<16 + 8>(); else waitv<16 + E1(s - 2) + E1(s - 1)>();
        bar();
        src1(s + 4); ival(G, [&](auto U) AI { constexpr int u = U.value;
          if constexpr ((s == 1 || s == 2) && (u == 2 || u == 5)) ld128p(xr[s - 1][u / 3], rx, lane * 16u, (ci * 128 + 8 * cj + 2 * wq + s - 1) * 2048 + (u / 3) * 1024);
          if constexpr (s == 1 && u == 0) { PIN2(pdt, plr); PIN(pli); lr = bf2f(plr); li = bf2f(pli); const float dt = rbf(fexp(bf2f(pdt))); lp = (f32x2){fexp(dt * lr), dt * li}; PIN(lp); }
          if constexpr (s == 1 && u == 3) { PIN2(bq, oq); wrf(T_B1 + tq * 4, bf2f(bq)); wrf(T_OUT + tq * 4, bf2f(oq)); PIN(epr); epr = __builtin_amdgcn_readfirstlane(epr) + 1u; }
          if constexpr (s == 1 && u == 6) { kq = __builtin_rintf(lp.y * 0.63661977236f);
            rr = __builtin_fmaf(kq, -1.5707962513f, lp.y); rr = __builtin_fmaf(kq, -7.5497894159e-08f, rr); rr = __builtin_fmaf(kq, -5.3903029534e-15f, rr); PIN2(kq, rr); }
          if constexpr (s == 2 && u == 0) { const float z = rr * rr;
            a1 = __builtin_fmaf(rr * z, __builtin_fmaf(z, __builtin_fmaf(z, __builtin_fmaf(z, 2.7183114939e-06f, -1.9839334836e-04f), 8.3333293858e-03f), -1.6666666642e-01f), rr); PIN(a1); }
          if constexpr (s == 2 && u == 3) { const float z = rr * rr;
            a2 = __builtin_fmaf(z, __builtin_fmaf(z, __builtin_fmaf(z, __builtin_fmaf(z, 2.4390448796e-05f, -1.3886763775e-03f), 4.1666623324e-02f), -4.9999999726e-01f), 1.f); PIN(a2); }
          if constexpr (s == 2 && u == 6) { const int q = (int)kq; const float sa_ = sel<float>(q & 1, a2, a1), ca_ = sel<float>(q & 1, a1, a2);
            lp = (f32x2){lp.x * (((q + 1) & 2) ? -ca_ : ca_), lp.x * ((q & 2) ? -sa_ : sa_)}; PIN(lp); wrf2(T_CON + lane * 8, lp); }
          if constexpr (s == 3 && u == 0) { const float nr = lp.x - 1.f, ni = lp.y, den = 2.f * __builtin_amdgcn_rcpf(lr * lr + li * li);
                                            wrf2(T_CON + 512 + lane * 8, (f32x2){(nr * lr + ni * li) * den, (ni * lr - nr * li) * den}); }
          if constexpr ((s == 3 && u >= 2) || (s == 4 && u >= 4)) { constexpr int q = s == 3 ? u - 2 : u + 2; lp = cmul(lp, lp); PIN(lp);
            if constexpr (q >= 3 && q < 7) wrf2(T_CON + (q - 1) * 512 + lane * 8, lp);
            if constexpr (q == 9) wrf2(T_CON + 3072 + lane * 8, lp); }
          if constexpr ((s == 4 && u < 2) || (s >= 5 && s < 9 && !(u & 1) && 4 * (s - 5) + 2 + (u >> 1) < 16)) {
            constexpr int id = s == 4 ? u : 4 * (s - 5) + 2 + (u >> 1), r = id >> 3, e = id & 7;
            PIN2(xr[r][e >> 2], gq[e >> 2]);
            const unsigned v = xr[r][e >> 2][e & 3], gv = gq[e >> 2][e & 3];
            const float x0 = bf2f(v & 0xffffu), x1 = u2f(v & 0xffff0000u);
            sv[r] += x0 + x1; sv[2 + r] = __builtin_fmaf(x1, x1, __builtin_fmaf(x0, x0, sv[2 + r]));
            sv[4 + r] = __builtin_fmaf(x1, u2f(gv & 0xffff0000u), __builtin_fmaf(x0, bf2f(gv & 0xffffu), sv[4 + r]));
            PIN2(sv[r], sv[2 + r]); PIN(sv[4 + r]);
          }
          if constexpr (s == 9 && u < 4) {
#pragma unroll
            for (int q = 0; q < 6; ++q) sv[q] = u == 0 ? dppadd<0xB1>(sv[q]) : u == 1 ? dppadd<0x4E>(sv[q]) : u == 2 ? dppadd<0x141>(sv[q]) : dppadd<0x140>(sv[q]);
            PIN2(sv[0], sv[1]); PIN2(sv[2], sv[3]); PIN2(sv[4], sv[5]);
          }
          if constexpr (s == 9 && u == 5) { const bool r1 = lane & 4; a1 = sum16(sel<float>(r1, sv[1], sv[0])); a2 = sum16(sel<float>(r1, sv[3], sv[2])); a3 = sum16(sel<float>(r1, sv[5], sv[4])); PIN2(a1, a2); PIN(a3); }
          if constexpr (s == 10 && u == 0) { a1 = sum32(a1); a2 = sum32(a2); a3 = sum32(a3); PIN2(a1, a2); PIN(a3); }
          if constexpr (s == 10 && u == 2) { mean = a1 * (1.f / 1024.f); const float var = __builtin_fmaxf(a2 * (1.f / 1024.f) - mean * mean, 0.f) + 1e-5f; rstd = __builtin_amdgcn_rsqf(var); sd = var * rstd;
                                             PIN2(mean, rstd); PIN(sd); }
          if constexpr (s == 10 && u == 4) { qv = 1.f + fexp(__builtin_fminf(a3, 60.f)); gg = __builtin_amdgcn_rcpf(1.f + fexp(__builtin_fminf(-a3, 80.f))); PIN2(qv, gg); }
          if constexpr (s == 11 && u == 0) { a1 = sel4(lane & 1, lane & 2, -2.302208198f * rstd, -0.1029432397f * rstd * rstd * rstd, qv * sd, gg); PIN(a1); }
          if constexpr (s == 11 && u == 2) { const float mh = rbf(mean), sh = rbf(sd);
                                             a2 = sel4(lane & 1, lane & 2, u2f(pk2(-mh, -mh)), u2f(pk2(mh - mean, sh)), u2f(pk2(sd - sh, 0.f)), 0.f); PIN(a2); }
          if constexpr (s == 11 && u == 5) { if (lane < 16) st64(rws, (u32x2){f2u(sel<float>(lane & 8, a2, a1)), epr}, (lane >> 3) * 4096u + (lane & 7) * 8, rgb + (8 * cj + 2 * wq) * 32); }
        });
      });
      rb = mk((const char*)w1 - 4096, 1024 * 7168 + 4096);
#pragma unroll
      for (int q = 0; q < 4; ++q) va[q] = vb[q];
      sx = 0; nexts();
      const unsigned sb0 = sb, sa0 = sa; nexts();
      const unsigned sb1 = sb, sa1 = sa; nexts();
      const unsigned sa2 = sa;
      waitv<16 + 1>(); bar();
      sa = sa0; sfor<4>([&](auto U) AI { G2(IC<0>{}, U); });
      sa = sa1; sfor<4>([&](auto U) AI { G2(IC<1>{}, U); });
      waitv<16 + 1>(); bar();
      sb = sb0; sfor<7>([&](auto U) AI { G2(IC<0>{}, IC<U.value + 4>{}); });
      sa = sa2; sfor<4>([&](auto U) AI { G2(IC<2>{}, U); });
      waitv<19>(); bar();
      sb = sb1; sfor<7>([&](auto U) AI { G2(IC<1>{}, IC<U.value + 4>{}); });
      ta = t3a; tb = t3b; sx = 3; next3();
    }
    {
      f32x4 ac1[28];
#pragma unroll
      for (int m = 0; m < 28; ++m) ac1[m] = (f32x4){0, 0, 0, 0};
      {
        bf16x8 fa[2][4], fb[2][8];
        unsigned xn = 98304, wnx = 32768;
        setm0(T_DUM);
        auto rd = [&](int r) AI { const int ks = r / 20, xx = r % 20;
          if (xx == 0) fa[ks][0] = rd128(ba[ks]);
          else if (xx <= 12) { const int b = (xx - 1) >> 1, h = (xx - 1) & 1; sethalf(fb[ks][b], h, rtr(ba[4 + (b & 1)] + (b >> 1) * 2048 + ks * 14336 + h * 512)); }
          else if (xx <= 16) { const int b = (xx - 1) >> 1, h = (xx - 1) & 1; sethalf(fb[ks][b], h, rtr(ba[b] + ks * 14336 + h * 256)); }
          else { const int a = xx - 16; fa[ks][a] = rd128(ba[(a >> 1) * 2 + ks] + (a & 1) * 2048); } };
        auto mm = [&](int ks, int m) AI { const int a = m < 16 ? m >> 3 : (m - 4) / 6, b = m < 16 ? m & 7 : (m - 4) % 6; ac1[m] = MFMA(fb[ks][b], fa[ks][a], ac1[m]); };
        auto up = [&](int b) AI { if (b < 8) { ba[b] = cb[b] + sel<unsigned>(b < 4, xn, wnx); PIN(ba[b]); }
                                  else { wnx = (wnx == 65536u) ? 0u : wnx + 32768u; xn = (xn == 131072u) ? 98304u : xn + 16384u; } };
        waitl0(); waitv<11>(); bar();
        PH(2);
        period<28, 8, true, 40, LASTR + 24, 9>(rd, mm, up); waitl0(); waitv<0>(); bar();
        const int sp = 2 + bid % 7;
        for (int o = 0; o < 2; ++o) {
          for (int s = o ? sp : 1; s < (o ? 16 : sp); ++s) { period<28, 8, false, 40, LASTR + 24, 9>(rd, mm, up); waitl0(); bar(); }
          if (o == 0) { pfg(pp0); pfg(pp1); }
        }
        PH(3);
        setup3(); sl = 0;
        ival(G, [&](auto U) AI { constexpr int u = U.value;
#pragma unroll
          for (int m = LAG + 4 * u; m < LAG + 4 * u + 4; ++m) if (m < 28) mm(1, m); });
#pragma unroll
        for (int a = 0; a < 4; ++a) fa[0][a] = rd128(g ? T_ZERO : T_XA + (64 * wm + 16 * (a ^ (2 * wn)) + r15) * 16);
#pragma unroll
        for (int b = 0; b < 8; ++b) fb[0][b] = rd128(T_WA + ((b < 6 ? (b >> 1) * 64 + wn * 32 + (b & 1) * 16 : 96 + b * 16) + r15) * 16);
        SB();
        sl = nq(sl); next3();
        bar();
        ival(G, [&](auto U) AI { constexpr int u = U.value;
#pragma unroll
          for (int m = 4 * u; m < 4 * u + 4; ++m) if (m < 28) mm(0, m); });
        sl = nq(sl); next3();
      }
      float k0[4], k1[4], qq[4];
#pragma unroll
      for (int a = 0; a < 4; ++a) { const unsigned ro = T_ROW + (64 * wm + 16 * (a ^ (2 * wn)) + r15) * 4; k0[a] = rdf(ro); k1[a] = rdf(ro + 512); qq[a] = rdf(ro + 1024); }
      u32x4 hw[4]; float t[8];
      auto lv0 = [](int s) constexpr { return s < 1 ? 0 : s < 7 ? (6 * (s - 1) < 32 ? 6 * (s - 1) : 32) : (32 + 4 * (s - 7) < 112 ? 32 + 4 * (s - 7) : 112); };
      auto st3 = [](int s) constexpr { return (s == 6 || s == 14 || s == 22) ? 4 : s == 26 ? 2 : 0; };
      sfor<36>([&](auto S1) AI { constexpr int s = S1.value + 1;
        if constexpr (st3(s - 2) > 0) waitv<8>();
        else if constexpr (s >= 2) waitv<16 + st3(s - 1)>();
        if constexpr (s == 12 || s == 20 || s == 28 || s == 36) {
          if (w == 4) { waitv<8>(); check_flags(pv, rws, fvo, flagb + (FK_M1 + (s - 12) / 8) * 128, lane, s == 36 ? 0 : 8 * half, 8 * half + 8, epr); } }
        SB(); bar();
        if constexpr (s == 16) { PH(4); }
        if constexpr (st3(s - 2) > 0) { if (w == 5) stflag(rws, epr, lane, flagb + (FK_M1 + (s - 4) / 8) * 128 + cj * 4); }
        if constexpr (s == 11 || s == 19 || s == 27 || s == 35) { if (w == 4) ld32a(pv, rws, fvo, flagb + (FK_M1 + (s - 11) / 8) * 128); }
#define UU(e) ac1[m0 + ((e) >> 2)][(e) & 3]
#define LVL(expr) do { _Pragma("unroll") for (int e = 0; e < 8; ++e) { t[e] = (expr); PIN(t[e]); } } while (0)
        ival(G, [&](auto U) AI { constexpr int u = U.value, n = lv0(s + 1) - lv0(s), li = n == 6 ? (u < 3 ? u : u - 1) : u >> 1;
          if constexpr ((n == 6 ? (u != 3 && u != 7) : !(u & 1)) && li < n) {
            constexpr int L = lv0(s) + li, v = L >> 3, l = L & 7, a = v & 3, m0 = (a < 2 ? 8 * a : 4 + 6 * a) + 2 * (v >> 2);
            if constexpr (l == 0) LVL(UU(e) * UU(e));
            if constexpr (l == 1) LVL(__builtin_fmaf(t[e], k1[a], k0[a]));
            if constexpr (l == 2) LVL(UU(e) * t[e]);
            if constexpr (l == 3) LVL(__builtin_amdgcn_exp2f(t[e]));
            if constexpr (l == 4) LVL(__builtin_fmaf(t[e], qq[a], qq[a]));
            if constexpr (l == 5) LVL(__builtin_amdgcn_rcpf(t[e]));
            if constexpr (l == 6) LVL(UU(e) * t[e]);
            if constexpr (l == 7) { hw[a] = (u32x4){pk2(t[0], t[1]), pk2(t[2], t[3]), pk2(t[4], t[5]), pk2(t[6], t[7])}; PIN(hw[a]); }
          } });
#pragma unroll
        for (int q = 0; q < st3(s); ++q) st128(rws, hw[q], lane * 16u + (4 * wm + (q ^ (2 * wn))) * 1024, ci * HID_PANEL + kb_of(cj, s < 26 ? 2 * (s >> 3) + wn : 6) * 8192);
        sl = nq(sl);
        if constexpr (s < 11) next3(); else nexts();
        if constexpr (s == 11) {
          rb = mk(w2, 3584 * 2048);
#pragma unroll
          for (int q = 0; q < 4; ++q) vb[q] = (4 * q + g) * 2048 + ((((r15 >> 1) ^ (4 * (q & 1) + g)) * 2) + (lane & 1)) * 16 - 1024 * q;
        }
      });
    }
    static_assert(MOVE >= 0 && MOVE <= 3, "");
    for (int s = 21; s < n4 - 4; ++s) { waitv<16>(); bar(); ival(G, nowork); sl = nq(sl); nexts(); }
    waitv<16>(); bar();
    if (half) {
      sfor<8>([&](auto U) AI { constexpr int u = U.value; if constexpr ((u & 3) == 0) setm0(sl + wq * 8192 + u * 1024);
        dma<(u & 3) * 1024>(rx, (4u * (u & 3) + g) * 2048 + r15 * 16 - (u & 3) * 1024, (ci * 128 + 32 * wq + 16 * (u >> 2)) * 2048 + cnt * 256); });
      waitv<8 + 8>();
    } else waitv<8>();
    bar();
    if (half) waitv<8>(); else waitv<0>();
    bar();
    ld32a(pv, rws, fvo, flagb + FK_P * 128);
    bar();
    waitv<0>();
    if (half) {
      check_flags(pv, rws, fvo, flagb + FK_P * 128, lane, cnt, cnt + 1, epr);
      sfor<8>([&](auto U) AI { constexpr int u = U.value; if constexpr ((u & 3) == 0) setm0(nq(sl) + wq * 8192 + u * 1024);
        dma1<(u & 3) * 1024>(rws, lane * 16u, PART_OFF + (ci * 8 + cnt) * 32768 + wq * 8192 + (u >> 2) * 4096); });
      if (cnt == 0 && w == 4) stflag(rws, epr, lane, flagb + FK_EP * 128);
    }
    PH(5);
    waitv<0>(); bar();
  }
  if (half) {
    const int bg = r31 & 15, sw = (r31 >> 4) * 8 + (bg >> 3) * 4 + (bg & 3);
    const unsigned pa = ((sl + 65536u + (w >> 2) * 32768u) & 131071u) + (r31 >> 4) * 16384 + (wq * 4 + (bg >> 2)) * 1024 + (bg & 3) * 256;
    const unsigned pp = nq(sl) + ((w >> 2) * 2 + (r31 >> 4)) * 8192 + (wq * 2 + (bg >> 3)) * 1024 + (bg & 3) * 256 + ((bg >> 2) & 1) * 8;
    const f32x4 bv = rdf4(T_OUT + r31 * 16), dv = rdf4(T_OUT + 512 + r31 * 16);
    f32x4 av[8]; u32x2 pw[8], xw[8]; float gg[8];
#pragma unroll
    for (int u = 0; u < 8; ++u) {
      const int row = 2 * u + hl;
      av[u] = rdf4(pa + ((row ^ bg) * 16)); pw[u] = *(LAS u32x2*)(size_t)(pp + ((row ^ sw) * 16));
      xw[u] = *(LAS u32x2*)(size_t)(sl + (16 * w + hl) * 256 + r31 * 8 + u * 512); gg[u] = rdf(T_ROW + 1536 + (16 * w + hl) * 4 + u * 8);
    }
    SB();
#pragma unroll
    for (int u = 0; u < 8; ++u) {
      const f32x4 pf = {bf2f(pw[u][0] & 0xffffu), u2f(pw[u][0] & 0xffff0000u), bf2f(pw[u][1] & 0xffffu), u2f(pw[u][1] & 0xffff0000u)};
      const f32x4 xf = {bf2f(xw[u][0] & 0xffffu), u2f(xw[u][0] & 0xffff0000u), bf2f(xw[u][1] & 0xffffu), u2f(xw[u][1] & 0xffff0000u)};
      f32x4 r;
#pragma unroll
      for (int e = 0; e < 4; ++e) r[e] = (av[u][e] + pf[e]) + __builtin_fmaf(gg[u], __builtin_fmaf(xf[e], dv[e], -bv[e]), bv[e]);
      st128<OAUX>(mk(out, 8u << 20), __builtin_bit_cast(u32x4, r), (unsigned)((16 * w + hl + 2 * u) * 4096 + r31 * 16), ci * 128 * 4096 + cnt * 512);
    }
  }
}

extern "C" void kernel_launch(void* const* d_in, const int* in_sizes, int n_in, void* d_out, int out_size, void* d_ws, size_t ws_size, hipStream_t stream) {
  (void)in_sizes; (void)n_in; (void)out_size; (void)ws_size;
  (void)hipFuncSetAttribute((const void*)fused_kernel, hipFuncAttributeMaxDynamicSharedMemorySize, LDS_BYTES);
  fused_kernel<<<256, 512, LDS_BYTES, stream>>>((const u16*)d_in[0], (const u16*)d_in[1], (const u16*)d_in[2], (const u16*)d_in[3], (const u16*)d_in[4],
      (const u16*)d_in[5], (const u16*)d_in[6], (const u16*)d_in[7], (const u16*)d_in[8], (const u16*)d_in[10], (const u16*)d_in[11], (const u16*)d_in[12],
      (const u16*)d_in[13], (const u16*)d_in[14], (char*)d_ws, (float*)d_out);
}
```

```cpp
#include <hip/hip_runtime.h>
#include <utility>

typedef unsigned short u16;
typedef __bf16 bf16x2 __attribute__((ext_vector_type(2)));
typedef __bf16 bf16x4 __attribute__((ext_vector_type(4)));
typedef __bf16 bf16x8 __attribute__((ext_vector_type(8)));
typedef float f32x2 __attribute__((ext_vector_type(2)));
typedef float f32x4 __attribute__((ext_vector_type(4)));
typedef float f32x16 __attribute__((ext_vector_type(16)));
typedef unsigned u32x2 __attribute__((ext_vector_type(2)));
typedef unsigned u32x4 __attribute__((ext_vector_type(4)));
typedef __amdgpu_buffer_rsrc_t rsrc_t;
#define LAS __attribute__((address_space(3)))
#define DI __device__ __forceinline__
#define AI __attribute__((always_inline))
#define SB() __builtin_amdgcn_sched_barrier(0)
#define PIN(x) asm volatile("" : "+v"(x))
#define MFMA(a, b, c) __builtin_amdgcn_mfma_f32_16x16x32_bf16(a, b, c, 0, 0, 0)
#define MFMA32(a, b, c) __builtin_amdgcn_mfma_f32_32x32x16_bf16(a, b, c, 0, 0, 0)
template <int V> using IC = std::integral_constant<int, V>;

constexpr unsigned HID_PANEL = 112 * 8192, H_OFF = 16 * HID_PANEL, H_PANEL = 32 * 16384;
constexpr unsigned PART_OFF = H_OFF + 16 * H_PANEL;
constexpr unsigned GRAN_OFF = PART_OFF + 128 * 32768;
constexpr unsigned RG_OFF = GRAN_OFF + 256 * 1024;
constexpr unsigned CG_OFF = RG_OFF + 16 * 8192;
constexpr unsigned FLAG_OFF = CG_OFF + 256 * 2048;
constexpr unsigned EPC_OFF = FLAG_OFF + 16 * 1024;
constexpr unsigned WS_BYTES = EPC_OFF + 16 * 128;
constexpr int FK_M1 = 0, FK_M2 = 1, FK_M3 = 2, FK_M4 = 3, FK_H = 4, FK_P = 5, FK_EP = 6;
constexpr unsigned TAB = 147456, T_ROW = TAB, T_XA = TAB + 2048, T_OUT = TAB + 4096, T_B1 = TAB + 5120, T_CS2 = TAB + 6144, T_WA = TAB + 8192, T_ZERO = TAB + 12288, T_AGG = T_WA, T_DUM = TAB + 12544, T_CON = TAB + 12800;
constexpr unsigned LDS_BYTES = 163840;
#define XMAP 1
#define ROTM 3
#define OAUX 2
#define SP0 4
#define SPN 6
#define PTE 0
#define MOVE 3
#define PROFX false
constexpr int LAG = 2, LASTR = 25;
constexpr unsigned SPIN_CAP = 1u << 16;

DI rsrc_t mk(const void* p, unsigned bytes) { return __builtin_amdgcn_make_buffer_rsrc((void*)p, 0, bytes, 0x00020000); }
DI void setm0(unsigned lds) { asm volatile("s_mov_b32 m0, %0\n\ts_nop 0" ::"s"(lds) : "memory"); }
template <int OFF> DI void dma(rsrc_t r, unsigned voff, unsigned soff) {
  asm volatile("buffer_load_dwordx4 %0, %1, %2 offen offset:%3 lds" ::"v"(voff), "s"(r), "s"(soff), "i"(OFF) : "memory");
}
template <int OFF> DI void dma1(rsrc_t r, unsigned voff, unsigned soff) {
  asm volatile("buffer_load_dwordx4 %0, %1, %2 offen offset:%3 sc1 lds" ::"v"(voff), "s"(r), "s"(soff), "i"(OFF) : "memory");
}
#define PIN2(x, y) asm volatile("" : "+v"(x), "+v"(y))
#define PINS(x) asm volatile("" : "+s"(x))
typedef const void* const __attribute__((address_space(4)))* kargs_t;
DI const char* karg(int i) { kargs_t ka = (kargs_t)__builtin_amdgcn_kernarg_segment_ptr(); PINS(ka); return (const char*)ka[i]; }
enum { A_X, A_LRE, A_LIM, A_BRE, A_BIM, A_CRE, A_CIM, A_LDT, A_DSK, A_W1, A_B1, A_W2, A_B2, A_GW, A_WS, A_OUT };
template <int N> DI void waitv() { asm volatile("s_waitcnt vmcnt(%0)" ::"i"(N) : "memory"); }
DI void waitl0() { SB(); __builtin_amdgcn_s_waitcnt(0xC07F); asm volatile("" ::: "memory"); }
#define PH(k)
DI void bar() { asm volatile("s_barrier" ::: "memory"); SB(); }
DI bf16x8 rd128(unsigned a) { return *(LAS bf16x8*)(size_t)a; }
DI u32x4 rdu4(unsigned a) { return *(LAS u32x4*)(size_t)a; }
DI f32x4 rdf4(unsigned a) { return *(LAS f32x4*)(size_t)a; }
DI f32x2 rdf2(unsigned a) { return *(LAS f32x2*)(size_t)a; }
DI float rdf(unsigned a) { return *(LAS float*)(size_t)a; }
DI void wrf(unsigned a, float v) { *(LAS float*)(size_t)a = v; }
DI void wrf2(unsigned a, f32x2 v) { *(LAS f32x2*)(size_t)a = v; }
DI bf16x4 rtr(unsigned a) { return __builtin_amdgcn_ds_read_tr16_b64_v4bf16((LAS bf16x4*)(size_t)a); }
DI void sethalf(bf16x8& f, int h, bf16x4 v) {
  if (h == 0) f = __builtin_shufflevector(v, v, 0, 1, 2, 3, -1, -1, -1, -1);
  else { bf16x8 t = __builtin_shufflevector(v, v, -1, -1, -1, -1, 0, 1, 2, 3); f = __builtin_shufflevector(f, t, 0, 1, 2, 3, 12, 13, 14, 15); }
}
DI unsigned f2u(float f) { return __builtin_bit_cast(unsigned, f); }
DI float u2f(unsigned u) { return __builtin_bit_cast(float, u); }
DI float shx(int lane, float v, int m) { return u2f(__builtin_amdgcn_ds_bpermute((lane ^ m) * 4, (int)f2u(v))); }
DI float sum16(float v) { const auto r = __builtin_amdgcn_permlane16_swap(f2u(v), f2u(v), false, false); return u2f(r[0]) + u2f(r[1]); }
DI float sum32(float v) { const auto r = __builtin_amdgcn_permlane32_swap(f2u(v), f2u(v), false, false); return u2f(r[0]) + u2f(r[1]); }
DI float fexp(float v) { return __builtin_amdgcn_exp2f(v * 1.4426950408889634f); }
DI float bf2f(unsigned v) { return u2f(v << 16); }
DI unsigned pk2(float lo, float hi) { f32x2 v = {lo, hi}; return __builtin_bit_cast(unsigned, __builtin_convertvector(v, bf16x2)); }
DI float rbf(float f) { return u2f(pk2(f, 0.f) << 16); }
DI float dot2(unsigned a, unsigned b, float c) { return __builtin_amdgcn_fdot2_f32_bf16(__builtin_bit_cast(bf16x2, a), __builtin_bit_cast(bf16x2, b), c, false); }
template <int C> DI float dppadd(float v) { return v + u2f(__builtin_amdgcn_update_dpp(0, (int)f2u(v), C, 0xf, 0xf, true)); }
DI float sel4(bool b0, bool b1, float a, float b, float c, float d) { PIN(a); PIN(b); PIN(c); PIN(d); const float lo = b0 ? b : a, hi = b0 ? d : c; return b1 ? hi : lo; }
DI f32x2 cmul(f32x2 a, f32x2 b) { return (f32x2){a.x * b.x - a.y * b.y, a.x * b.y + a.y * b.x}; }
DI f32x2 cmulc(f32x2 a, f32x2 b) { return (f32x2){__builtin_fmaf(a.x, b.x, -(a.y * b.y)), __builtin_fmaf(-a.x, b.y, -(a.y * b.x))}; }
DI f32x2 cfma(f32x2 a, f32x2 b, f32x2 c) { return (f32x2){__builtin_fmaf(a.x, b.x, __builtin_fmaf(-a.y, b.y, c.x)), __builtin_fmaf(a.x, b.y, __builtin_fmaf(a.y, b.x, c.y))}; }
template <int AUX = 16> DI void st128(rsrc_t r, u32x4 v, unsigned voff, unsigned soff) {
  __builtin_amdgcn_raw_buffer_store_b128(v, r, voff, soff, AUX);
  asm volatile("s_nop 1" ::"v"(v));
}
DI void st64(rsrc_t r, u32x2 v, unsigned voff, unsigned soff) { __builtin_amdgcn_raw_buffer_store_b64(v, r, voff, soff, 16); }
DI void st32(rsrc_t r, unsigned v, unsigned voff, unsigned soff) { __builtin_amdgcn_raw_buffer_store_b32(v, r, voff, soff, 16); }
DI void stflag(rsrc_t r, unsigned v, int lane, unsigned soff) { if (lane == 0) st32(r, v, 0u, soff); }
DI void ld32a(unsigned& d, rsrc_t r, unsigned voff, unsigned soff) { asm volatile("buffer_load_dword %0, %1, %2, %3 offen sc1" : "=v"(d) : "v"(voff), "s"(r), "s"(soff) : "memory"); }
DI void ld16p(unsigned& d, const u16* p, unsigned voff) { asm volatile("global_load_ushort %0, %1, %2" : "=v"(d) : "v"(voff), "s"(p) : "memory"); }
DI void ld64p(u32x2& d, rsrc_t r, unsigned voff, unsigned soff) { asm volatile("buffer_load_dwordx2 %0, %1, %2, %3 offen" : "=v"(d) : "v"(voff), "s"(r), "s"(soff) : "memory"); }
DI void ld64a(u32x2& d, rsrc_t r, unsigned voff, unsigned soff) { asm volatile("buffer_load_dwordx2 %0, %1, %2, %3 offen sc1" : "=v"(d) : "v"(voff), "s"(r), "s"(soff) : "memory"); }
DI void ld128g(u32x4& d, const u16* p, unsigned voff) { asm volatile("global_load_dwordx4 %0, %1, %2" : "=v"(d) : "v"(voff), "s"(p) : "memory"); }
DI void pfl(unsigned& d, rsrc_t r, unsigned voff, unsigned soff) { asm volatile("buffer_load_dword %0, %1, %2, %3 offen" : "+v"(d) : "v"(voff), "s"(r), "s"(soff) : "memory"); }
template <class T> DI T sel(bool c, T a, T b) { return c ? a : b; }
DI void pfg(const char* p) { asm volatile("global_load_lds_dword %0, off" ::"v"(p) : "memory"); }
DI bool anyl(bool b) { return __builtin_amdgcn_ballot_w64(b) != 0; }
DI void ld128p(u32x4& d, rsrc_t r, unsigned voff, unsigned soff) { asm volatile("buffer_load_dwordx4 %0, %1, %2, %3 offen" : "=v"(d) : "v"(voff), "s"(r), "s"(soff) : "memory"); }
DI void sincos_f(float t, float& sn, float& cs) {
  const float k = __builtin_rintf(t * 0.63661977236f);
  float r = __builtin_fmaf(k, -1.5707962513f, t); r = __builtin_fmaf(k, -7.5497894159e-08f, r); r = __builtin_fmaf(k, -5.3903029534e-15f, r);
  const float z = r * r;
  const float sp = __builtin_fmaf(r * z, __builtin_fmaf(z, __builtin_fmaf(z, __builtin_fmaf(z, 2.7183114939e-06f, -1.9839334836e-04f), 8.3333293858e-03f), -1.6666666642e-01f), r);
  const float cp = __builtin_fmaf(z, __builtin_fmaf(z, __builtin_fmaf(z, __builtin_fmaf(z, 2.4390448796e-05f, -1.3886763775e-03f), 4.1666623324e-02f), -4.9999999726e-01f), 1.f);
  const int q = (int)k;
  const float a = (q & 1) ? cp : sp, b = (q & 1) ? sp : cp;
  sn = (q & 2) ? -a : a; cs = ((q + 1) & 2) ? -b : b;
}
DI void ld128a(u32x4& d, rsrc_t r, unsigned voff, unsigned soff) { asm volatile("buffer_load_dwordx4 %0, %1, %2, %3 offen sc1" : "=v"(d) : "v"(voff), "s"(r), "s"(soff) : "memory"); }

DI unsigned x1off(int a) { return a == 0 ? 131072u : (unsigned)(81920 + a * 16384); }
DI unsigned nq(unsigned s) { return (s + 32768u) & 131071u; }

DI int kb_of(int j, int bl) { return 7 * j + ((j & 1) ? (bl == 6 ? 0 : bl + 1) : bl); }

template <int NR, int LAST> constexpr int nread(int sl) { int n = 0; for (int r = 0; r < NR; ++r) n += (r * LAST) / (NR - 1) <= sl; return n; }
template <int NM, int R0, bool FIRST, int NR, int LAST, int NB, int SL, class RD, class MM, class UP>
DI void slot(RD& rd, MM& mm, UP& upd) {
  constexpr int q = SL + LAG;
  if constexpr (!FIRST && (q == NM || q == NM + R0 || q == 2 * NM)) {
    constexpr int cnt = nread<NR, LAST>(SL - 1) - nread<NR, LAST>(SL - 6);
    __builtin_amdgcn_s_waitcnt(0xC07F | (cnt << 8)); SB();
  }
  if constexpr (!(FIRST && q < NM)) mm((q < NM) ? 1 : ((q < 2 * NM) ? 0 : 1), q % NM);
  SB();
  constexpr int r = (SL * (NR - 1) + LAST - 1) / LAST;
  if constexpr (r < NR && (r * LAST) / (NR - 1) == SL) rd(r);
  constexpr int per = NB > 2 * NM - 1 - LAST ? 2 : 1, u0 = (SL - LAST - 1) * per;
  if constexpr (SL > LAST && u0 < NB) { upd(u0); if constexpr (per == 2 && u0 + 1 < NB) upd(u0 + 1); }
  SB();
}
template <int NM, int R0, bool FIRST, int NR, int LAST, int NB, class RD, class MM, class UP, int... I>
DI void period_(RD& rd, MM& mm, UP& upd, std::integer_sequence<int, I...>) { (slot<NM, R0, FIRST, NR, LAST, NB, I>(rd, mm, upd), ...); }
template <int NM, int R0, bool FIRST, int NR, int LAST, int NB, class RD, class MM, class UP>
DI void period(RD& rd, MM& mm, UP& upd) {
  static_assert(NR - 1 <= LAST && NB <= 2 * (2 * NM - 1 - LAST), "");
  period_<NM, R0, FIRST, NR, LAST, NB>(rd, mm, upd, std::make_integer_sequence<int, 2 * NM>{});
}
template <bool FIRST, int SL, class RD, class MM, class UP> DI void slot32(RD& rd, MM& mm, UP& upd) {
  if constexpr (SL == 12) { __builtin_amdgcn_s_waitcnt(0xC07F | (8 << 8)); SB(); }
  if constexpr (!(FIRST && SL < 12)) mm((SL + 4) & 15);
  SB();
  if constexpr (SL < 2) { rd(2 * SL); rd(2 * SL + 1); } else if constexpr (SL < 14) rd(SL + 2);
  if constexpr ((SL & 3) == 2) { upd(SL >> 2); upd(4 + (SL >> 2)); }
  SB();
}
template <bool FIRST, class RD, class MM, class UP, int... I> DI void period32_(RD& rd, MM& mm, UP& upd, std::integer_sequence<int, I...>) { (slot32<FIRST, I>(rd, mm, upd), ...); }
template <bool FIRST, class RD, class MM, class UP> DI void period32(RD& rd, MM& mm, UP& upd) { period32_<FIRST>(rd, mm, upd, std::make_integer_sequence<int, 16>{}); }
template <class G, class W, int... U> DI void ival_(G& g, W& wk, std::integer_sequence<int, U...>) { ((g(IC<U>{}), SB(), wk(IC<U>{}), SB()), ...); }
template <class G, class W> DI void ival(G&& g, W&& wk) { ival_(g, wk, std::make_integer_sequence<int, 8>{}); }
template <int N, class F, int... I> DI void sfor_(F& f, std::integer_sequence<int, I...>) { (f(IC<I>{}), ...); }
template <int N, class F> DI void sfor(F&& f) { sfor_<N>(f, std::make_integer_sequence<int, N>{}); }

DI void check_flags(unsigned& pv, rsrc_t ws, unsigned voff, unsigned soff, int lane, int lo, int hi, unsigned ep) {
  PIN(pv);
  const bool need = lane >= lo && lane < hi;
  if (__builtin_expect(anyl(need && pv != ep), 0)) {
    for (unsigned sp = 0; sp < SPIN_CAP; ++sp) {
      __builtin_amdgcn_s_sleep(2);
      ld32a(pv, ws, voff, soff); asm volatile("s_waitcnt vmcnt(0)" : "+v"(pv)::"memory");
      if (!anyl(need && pv != ep)) break;
    }
  }
}

#if defined(__HIP_DEVICE_COMPILE__) && __HIP_DEVICE_COMPILE__
__attribute__((target("no-packed-fp32-ops")))
#endif
__global__ __launch_bounds__(512, 2) void fused_kernel(const u16* x, const u16* lre, const u16* lim, const u16* bre, const u16* bim, const u16* cre, const u16* cim,
    const u16* ldt, const u16* dsk, const u16* w1, const u16* b1, const u16* w2, const u16* b2, const u16* gw, char* ws, float* out) {
  extern __shared__ __attribute__((aligned(16))) char shm[];
  unsigned long long pc0; asm volatile("s_getpc_b64 %0" : "=s"(pc0));
  const int lane = threadIdx.x & 63, w = __builtin_amdgcn_readfirstlane(threadIdx.x >> 6);
  const int bid = blockIdx.x, xcd = bid & 7, loc = bid >> 3;
  const int ci = (xcd & 3) * 4 + (loc & 3), cj = (loc >> 4) * 8 + (xcd >> 2) * 4 + ((loc >> 2) & 3);
  const int rot = (ci + cj) & ROTM, cnt = cj & 7, half = cj >> 3, wq = w & 3, tq = threadIdx.x & 255;
  const int n4 = half ? 28 + MOVE : 28 - MOVE;
  const rsrc_t rws = mk(ws, WS_BYTES), rx = mk(x, 2048 * 2048);
  const int wm = (w >> 1) & 1, wn = w & 1, g = lane >> 4, r15 = lane & 15, q2 = (lane >> 2) & 3, p = lane & 3, hl = lane >> 5, r31 = lane & 31;
  const unsigned flagb = FLAG_OFF + ci * 1024, fvo = (lane & 15) * 4;
  unsigned rgb = RG_OFF + ci * 8192; PINS(rgb);
  auto nowork = [&](auto) AI {};
  unsigned va[4], vb[4], vw[7], sl, sa, sb, ta, tb; int sx; rsrc_t ra, rb;
  auto vnat = [&](int u) AI { return (8u * u + (lane >> 3)) * 2048 + (((lane & 7) ^ ((4 * u + g) & 7)) * 16) - 1024 * u; };
  auto nexts = [&]() AI { sa = __builtin_amdgcn_readlane(ta, sx); sb = __builtin_amdgcn_readlane(tb, sx); ++sx; PINS(sa); PINS(sb); };
  auto G = [&](auto U) AI { constexpr int u = U.value;
    if constexpr (u == 0) setm0(sl + wq * 4096);
    if constexpr (u == 4) setm0(sl + 16384 + wq * 4096);
    if constexpr (u < 4) dma1<u * 1024>(ra, va[u], sa); else dma<(u - 4) * 1024>(rb, vb[u - 4], sb);
  };
  auto G2 = [&](auto S, auto U) AI { constexpr int a = S.value % 3, u = U.value;
    if constexpr (u == 0) setm0((a == 0 ? 131072u : 81920u + a * 16384u) + wq * 4096);
    if constexpr (u == 4) setm0(a * 32768 + wq * 7168);
    if constexpr (u == 8) setm0(a * 32768 + wq * 7168 + 4096);
    if constexpr (u < 4) dma<u * 1024>(rx, va[u], sa); else dma<(u & 3) * 1024>(rb, vw[u - 4], sb);
  };
  const int c7 = ci & 7;
  auto setup2 = [&]() AI {
#pragma unroll
    for (int q = 0; q < 6; ++q) { const int k = 8 * (q & 1) + (lane >> 3), f = ((k >> 1) & 1) | ((q & 1) << 1);
      vw[q] = 4096 + k * 7168 + ((cj & 1) ? 64 : 0) + (q >> 1) * 128 + (((((lane & 7) >> 1) ^ f) * 2) + (lane & 1)) * 16 - (q & 3) * 1024; }
    vw[6] = 4096 + (lane >> 2) * 7168 + ((cj & 1) ? 0 : 384) + (((((lane & 3) >> 1) ^ hl) * 2) + (lane & 1)) * 16 - 2048;
    int k = lane - 2 + (c7 < 2 ? 3 * c7 : c7 - 2); k = 2 + (k >= 6 ? k - 6 : k);
    k = lane < 2 ? lane : lane < 8 ? k : (lane ^ rot);
    k = (k ^ (ci & 8)) & 15;
    ta = (ci * 128 + 32 * wq) * 2048 + k * 128; tb = (k * 64 + 16 * wq) * 7168 + cj * 448;
    PIN2(ta, tb);
#pragma unroll
    for (int q = 0; q < 7; ++q) PIN(vw[q]);
  };
  auto setup3 = [&]() AI {
    ra = rws;
#pragma unroll
    for (int q = 0; q < 4; ++q) { va[q] = lane * 16; vb[q] = vnat(q); }
  };
  auto table3 = [&](unsigned& ta, unsigned& tb) AI {
    const int ks = 16 * half + ((lane ^ (2 * cnt)) & 15);
    int n = 2 * (lane - 16) + (wq >> 1), hp = half; if (n >= 56) { n -= 2 * MOVE; hp = 0; }
    const int kb = kb_of(hp * 8 + ((n ^ rot) & 7), n >> 3);
    ta = lane < 16 ? H_OFF + ci * H_PANEL + ks * 16384 + wq * 4096 : ci * HID_PANEL + kb * 8192 + (wq & 1) * 4096;
    tb = lane < 16 ? (cnt * 128 + 32 * wq) * 2048 + (ks >> 1) * 128 + (ks & 1) : (kb * 32 + 16 * (wq & 1)) * 2048 + cnt * 256;
    PIN(ta); PIN(tb);
  };
  auto next3 = [&]() AI { nexts(); rb = mk(sel<const u16*>(sb & 1, cim, cre), 1024 * 2048); sb &= ~1u; };

  if (w < 4) {
    constexpr bool isx = PROFX; (void)isx;
    unsigned epr, epc;
    const unsigned ca = (32 * wn + r31) * 8;
    const int seg0 = 4 * wm + hl;
    f32x2 lam, cof, l16, l128, l1k, pw0, pw1; unsigned t3a, t3b;
    {
      setm0(T_DUM);
      const int row = bid * 8 + (lane & 7);
      const char* pp = (const char*)(hl ? (row < 1024 ? bre : bim - 1024 * 1024) : x) + (size_t)row * 2048 + (4 * wq + ((lane >> 3) & 3)) * 128;
      for (int q = 0; q <= wq; ++q) __builtin_amdgcn_s_sleep(6);
      pfg(pp);
      pfg((const char*)pc0 + (((loc * 4 + wq) % 20) * 16 + (lane & 15)) * 128);
      ld32a(epr, rws, 0u, flagb + FK_EP * 128); ld32a(epc, rws, 0u, EPC_OFF + cj * 128);
      setup2(); sx = 2; nexts(); table3(t3a, t3b);
      rb = mk((const char*)w1 - 4096, 1024 * 7168 + 4096);
#pragma unroll
      for (int q = 0; q < 4; ++q) { va[q] = vnat(q); PIN(va[q]); }
      wrf(T_ZERO + (lane & 3) * 4, 0.f);
    }
    {
      f32x16 acc[2][2];
#pragma unroll
      for (int a = 0; a < 4; ++a)
#pragma unroll
        for (int e = 0; e < 16; ++e) acc[a >> 1][a & 1][e] = 0.f;
      bf16x8 wv[4][2];
      const int sstar = c7 < 2 ? c7 : 2;
      const int fsw = ((q2 >> 1) & 1) | ((g & 1) << 1);
      unsigned cf = sstar * 32768 + (g >> 1) * 7168 + (8 * (g & 1) + q2) * 128 + ((wq ^ fsw) * 32) + p * 8;
      unsigned ch = sstar * 32768 + (g >> 1) * 7168 + 6144 + (8 * (g & 1) + q2) * 64 + (((wq & 1) ^ (g & 1)) * 32) + p * 8;
      PIN(cf); PIN(ch);
      auto wread = [&](int st) AI {
        if (sstar == st) {
#pragma unroll
          for (int e = 0; e < 4; ++e)
#pragma unroll
            for (int t = 0; t < 4; ++t) sethalf(wv[t][e >> 1], e & 1, rtr((e >> 1) * 14336 + (t < 3 ? cf + t * 2048 + (e & 1) * 512 : ch + (e & 1) * 256)));
        } };
      {
        bf16x8 fa[4][2], fb[4][2];
        unsigned cb[8], ba[8], rs = 98304;
#pragma unroll
        for (int b = 0; b < 8; ++b) { cb[b] = (b < 4 ? wm * 8192u : 16384 + wn * 8192u) + r31 * 128 + (((2 * (b & 3) + hl) ^ ((r31 >> 1) & 7)) * 16); ba[b] = rs + cb[b]; PIN(ba[b]); }
        auto rd = [&](int r) AI { const int k16 = r >> 2, xx = r & 3;
          if (xx == 0) fa[k16][0] = rd128(ba[k16]); else if (xx == 3) fa[k16][1] = rd128(ba[k16] + 4096); else fb[k16][xx - 1] = rd128(ba[4 + k16] + (xx - 1) * 4096); };
        auto mm = [&](int m) AI { const int k16 = m >> 2, mt = (m >> 1) & 1, nt = m & 1; acc[mt][nt] = MFMA32(fa[k16][mt], fb[k16][nt], acc[mt][nt]); };
        auto up = [&](int b) AI { if (b == 0) rs = nq(rs); ba[b] = rs + cb[b]; PIN(ba[b]); };
        waitl0(); bar();
        period32<true>(rd, mm, up); waitl0(); bar();
        for (int s = 1; s < 16; ++s) { period32<false>(rd, mm, up); waitl0(); bar(); }
        waitv<0>(); PIN(epr); PIN(epc);
        epr = __builtin_amdgcn_readfirstlane(epr) + 1u; epc = __builtin_amdgcn_readfirstlane(epc) + 1u;
        lam = rdf2(T_CON + ca);
        wread(0);
        sfor<7>([&](auto U) AI { G2(IC<2>{}, IC<U.value + 4>{}); SB(); if constexpr (U.value < 6) { mm(4 + 2 * U.value); mm(5 + 2 * U.value); } SB(); });
        nexts();
      }
      f32x2 st[2] = {{0, 0}, {0, 0}}, Pf[2] = {{0, 0}, {0, 0}}, Gt = {0, 0}, cr = {0, 0}, ag[8];
      u32x4 lb[8], cg[8], rgv[2]; unsigned lbo[8], lbe[8], pv, gd = 0; u32x4 ob[2][2][2];
      f32x4 cs[4]; f32x2 so[2]; float c0s = 0.f, c1s = 0.f;
      const rsrc_t rgr = mk(ws + GRAN_OFF + cj * 16384, 16384);
      const int cp = 28 * wq + (r31 < 28 ? r31 : 27);
      const unsigned cgo = hl * 16384 + cp * 16;
      auto hofs = [&](int hf, int mt) AI { return (2u * wn + (r31 >> 4)) * 4096 + wm * 2048 + hf * 1024 + (2 * mt + hl) * 256 + ((r15 ^ (hf << 2)) * 16); };
      auto bad = [&]() AI { unsigned d = 0;
#pragma unroll
        for (int q = 0; q < 8; ++q) { PIN(lb[q]); d |= (int)lbo[q] < 0 ? 0u : ((lb[q][1] ^ epc) | (lb[q][3] ^ epc)); }
        return d != 0; };
      auto gbad = [&]() AI { unsigned d = 0;
#pragma unroll
        for (int q = 0; q < 8; ++q) { PIN(cg[q]); d |= (cg[q][1] ^ epc) | (cg[q][3] ^ epc); }
        return d != 0; };
      auto E2 = [](int s) constexpr { return (s == 2 || s == 3) ? 1 : s == 4 ? 8 : (s == 11 || s == 12) ? 5 : 0; };
      auto T2 = [](int s) constexpr { return (s >= 7 && s < 11) ? 2 : 0; };
      sfor<15>([&](auto S) AI { constexpr int s = S.value;
        if constexpr (s < 3) waitl0();
        if constexpr (s == 12) waitv<11 + E2(11)>();
        else if constexpr (s >= 1) waitv<T2(s - 2) + (s < 14 ? 11 : 4) + E2(s - 1) + T2(s - 1)>();
        bar();
        if constexpr (s < 2) wread(s + 1);
        if constexpr (s == 2) {
#pragma unroll
          for (int sg = 0; sg < 8; ++sg) ag[sg] = rdf2(T_AGG + sg * 512 + ca);
          l16 = rdf2(T_CON + 1024 + ca); cof = rdf2(T_CON + 512 + ca);
        }
        if constexpr (s == 5) { pw1 = rdf2(T_CON + 1536 + ca); pw0 = rdf2(T_CON + 2048 + ca); l128 = rdf2(T_CON + 2560 + ca); l1k = rdf2(T_CON + 3072 + ca); }
        if constexpr (s == 12) { if (w == 0) stflag(rws, epr, lane, flagb + FK_H * 128 + cj * 4); }
        if constexpr (s == 14) { if (w == 0) ld32a(pv, rws, fvo, flagb + FK_H * 128); }
        SB();
        auto work = [&](auto U) AI { constexpr int u = U.value;
          if constexpr (s < 2 && u >= 1 && u < 9) {
            constexpr int t = 8 * s + u - 1;
#pragma unroll
            for (int mt = 0; mt < 2; ++mt) st[mt] = cfma(lam, st[mt], (f32x2){acc[mt][0][t], acc[mt][1][t]});
            PIN2(st[0], st[1]);
          }
          if constexpr (s == 1 && u == 9) { wrf2(T_AGG + seg0 * 512 + ca, st[0]); wrf2(T_AGG + (seg0 + 2) * 512 + ca, st[1]); }
          if constexpr (s == 2 && u >= 1 && u < 9) {
            constexpr int sg = u - 1, mt = (sg >> 1) & 1; const bool me = sg == seg0 + 2 * mt;
            Pf[mt].x = sel<float>(me, Gt.x, Pf[mt].x); Pf[mt].y = sel<float>(me, Gt.y, Pf[mt].y); Gt = cfma(l16, Gt, ag[sg]);
            PIN2(Gt, Pf[mt]);
          }
          if constexpr (s == 2 && u == 9) st64(rgr, (u32x2){f2u(sel<float>(hl, Gt.y, Gt.x)), epc}, ca * 2 + hl * 8, ci * 1024);
          if constexpr (s == 3 && u < 4) {
            const bf16x8 one = __builtin_bit_cast(bf16x8, (u32x4){0x3F803F80u, 0x3F803F80u, 0x3F803F80u, 0x3F803F80u});
            cs[u] = MFMA(one, wv[u][1], MFMA(one, wv[u][0], ((f32x4){0, 0, 0, 0}))); PIN(cs[u]);
          }
          if constexpr (s == 3 && u == 9)
            st64(rws, (u32x2){f2u(sel4(g & 1, g & 2, cs[0][0], cs[1][0], cs[2][0], cs[3][0])), epc}, (g < 3 ? g * 64 + wq * 16 + r15 : 192 + (wq & 1) * 16 + r15) * 8u, CG_OFF + (cj * 16 + ci) * 2048);
          if constexpr ((s == 3 && u >= 4 && u < 9) || (s == 4 && u < 5) || (s == 5 && u >= 1 && u < 7)) {
            constexpr int t = s == 3 ? u - 4 : s == 4 ? 5 + u : 9 + u;
            f32x2 v0 = cmulc(cof, (f32x2){acc[0][0][t], acc[0][1][t]}), v1 = cmulc(cof, (f32x2){acc[1][0][t], acc[1][1][t]}); PIN2(v0, v1);
            acc[0][0][t] = v0.x; acc[0][1][t] = v0.y; acc[1][0][t] = v1.x; acc[1][1][t] = v1.y;
          }
          if constexpr (s == 4 && u >= 6 && u < 10) {
#pragma unroll
            for (int q = 2 * u - 12; q < 2 * u - 10; ++q) { lbo[q] = (ci - 8 - 8 * hl + q) * 1024 + ca * 2; ld128a(lb[q], rgr, lbo[q], 0u); }
          }
          if constexpr (s == 5 && u == 8) {
            if (!wm) pw0 = (f32x2){1.f, 0.f};
            if (ci <= 8) l1k = (f32x2){0.f, 0.f};
            pw0 = cmul(pw0, (f32x2){sel<float>(hl, l16.x, 1.f), sel<float>(hl, l16.y, 0.f)}); pw1 = cmul(pw0, pw1); PIN2(pw0, pw1);
          }
          if constexpr (s == 5 && (u == 7 || u == 9)) {
#pragma unroll
            for (int q = 2 * u - 14; q < 2 * u - 10; ++q) { lbe[q] = (int)lbo[q] < 0 ? 0u : epc; PIN(lbe[q]); }
          }
          if constexpr (s == 6 && u < 8) {
            PIN(lb[u]); gd |= (lb[u][1] ^ lbe[u]) | (lb[u][3] ^ lbe[u]); cr = cfma(l128, cr, (f32x2){u2f(lb[u][0]), u2f(lb[u][2])});
            PIN2(cr, gd);
          }
          if constexpr (s == 6 && u == 8) {
            if (__builtin_expect(anyl(gd != 0), 0)) {
              for (unsigned sp = 0; sp < SPIN_CAP; ++sp) {
                __builtin_amdgcn_s_sleep(2);
#pragma unroll
                for (int q = 0; q < 8; ++q) ld128a(lb[q], rgr, lbo[q], 0u);
                waitv<0>();
                if (!anyl(bad())) break;
              }
              cr = (f32x2){0, 0};
#pragma unroll
              for (int q = 0; q < 8; ++q) cr = cfma(l128, cr, (f32x2){u2f(lb[q][0]), u2f(lb[q][2])});
            }
            gd = 0;
            const auto sx_ = __builtin_amdgcn_permlane32_swap(f2u(cr.x), f2u(cr.x), false, false), sy_ = __builtin_amdgcn_permlane32_swap(f2u(cr.y), f2u(cr.y), false, false);
            cr = cfma(l1k, (f32x2){u2f(sx_[1]), u2f(sy_[1])}, (f32x2){u2f(sx_[0]), u2f(sy_[0])});
            PIN(cr);
          }
          if constexpr (s == 6 && u == 9) { st[0] = cmulc(cof, cfma(pw0, cr, Pf[0])); PIN(st[0]); }
          if constexpr (s == 7 && u == 0) { st[1] = cmulc(cof, cfma(pw1, cr, Pf[1])); PIN(st[1]); }
          if constexpr ((s == 7 && u >= 2 && u < 10) || (s == 8 && u >= 1 && u < 9)) {
            constexpr int t = s == 7 ? u - 2 : 7 + u;
            if constexpr (t & 1) { so[0] = st[0]; so[1] = st[1]; }
#pragma unroll
            for (int mt = 0; mt < 2; ++mt) st[mt] = cfma((f32x2){lam.x, -lam.y}, st[mt], (f32x2){acc[mt][0][t], acc[mt][1][t]});
            if constexpr (t & 1) {
#pragma unroll
              for (int mt = 0; mt < 2; ++mt) { ob[t >> 3][mt][0][(t >> 1) & 3] = pk2(so[mt].x, st[mt].x); ob[t >> 3][mt][1][(t >> 1) & 3] = pk2(so[mt].y, st[mt].y); PIN2(ob[t >> 3][mt][0], ob[t >> 3][mt][1]); }
            }
            PIN2(st[0], st[1]);
          }
          if constexpr (s >= 7 && s < 11 && u == 10) {
            constexpr int hf = (s - 7) >> 1, mt = (s - 7) & 1;
#pragma unroll
            for (int pt = 0; pt < 2; ++pt) st128(rws, ob[hf][mt][pt], hofs(hf, mt), H_OFF + ci * H_PANEL + (2 * cj + pt) * 16384);
          }
          if constexpr (s >= 13 && u < 4) *(LAS u32x4*)(size_t)((s - 12) * 32768 + hofs(u >> 1, u & 1)) = ob[u >> 1][u & 1][s - 13];
          if constexpr (s == 9 && u == 0) { if (ci == 15 && w == 1) stflag(rws, epc, lane, EPC_OFF + cj * 128); }
          if constexpr ((s == 11 || s == 12) && u == 9) ld128a(rgv[s - 11], rws, tq * 16u + (s - 11) * 4096, rgb);
          if constexpr (s == 14 && u == 5) {
            auto rbad = [&]() AI { PIN2(rgv[0], rgv[1]); return ((rgv[0][1] ^ epr) | (rgv[0][3] ^ epr) | (rgv[1][1] ^ epr) | (rgv[1][3] ^ epr)) != 0; };
            if (__builtin_expect(anyl(rbad()), 0))
              for (unsigned sp = 0; sp < SPIN_CAP; ++sp) { __builtin_amdgcn_s_sleep(2);
                ld128a(rgv[0], rws, tq * 16u, rgb); ld128a(rgv[1], rws, tq * 16u + 4096, rgb);
                waitv<0>(); if (!anyl(rbad())) break; }
          }
          if constexpr (s == 14 && u == 7) {
            wrf(T_ROW + (tq & 1) * 1024 + (tq >> 1) * 4, u2f(rgv[0][0])); wrf(T_ROW + (tq & 1) * 1024 + 512 + (tq >> 1) * 4, u2f(rgv[0][2]));
            wrf2(T_XA + tq * 8, (f32x2){u2f(rgv[1][0]), u2f(rgv[1][2])});
          }
          if constexpr ((s == 11 || s == 12) && (u & 1) && u < 8) { constexpr int q = 4 * (s - 11) + (u >> 1); ld128a(cg[q], rws, cgo, CG_OFF + (cj * 16 + q) * 2048); }
          if constexpr (s == 14 && u < 4) {
#pragma unroll
            for (int q = 2 * u; q < 2 * u + 2; ++q) { PIN(cg[q]); gd |= (cg[q][1] ^ epc) | (cg[q][3] ^ epc); c0s += u2f(cg[q][0]); c1s += u2f(cg[q][2]); }
            PIN(gd); PIN(c0s); PIN(c1s);
          }
          if constexpr (s == 14 && u == 4) {
            if (__builtin_expect(anyl(gd != 0), 0)) {
              for (unsigned sp = 0; sp < SPIN_CAP; ++sp) { __builtin_amdgcn_s_sleep(2);
#pragma unroll
                for (int q = 0; q < 8; ++q) ld128a(cg[q], rws, cgo, CG_OFF + (cj * 16 + q) * 2048);
                waitv<0>(); if (!anyl(gbad())) break; }
              c0s = 0.f; c1s = 0.f;
#pragma unroll
              for (int q = 0; q < 8; ++q) { c0s += u2f(cg[q][0]); c1s += u2f(cg[q][2]); }
            }
            c0s = sum32(c0s); c1s = sum32(c1s); PIN(c0s); PIN(c1s);
          }
          if constexpr (s == 14 && u == 6) {
            const float v = sel<float>(hl, c1s, c0s), hi = rbf(v), bb = rdf(T_B1 + (2 * cp + hl) * 4);
            *(LAS u32x4*)(size_t)(T_WA + (2 * cp + hl) * 16) = (u32x4){pk2(hi, v - hi), pk2(hi, bb), pk2(bb, 0.f), 0u};
          }
        };
        if constexpr (s < 13) { sfor<11>([&](auto U) AI { G2(IC<s + 3>{}, U); SB(); work(U); SB(); }); if constexpr (s < 12) nexts(); }
        if constexpr (s == 12) {
#pragma unroll
          for (int q = 0; q < 4; ++q) vb[q] = va[q];
          ta = t3a; tb = t3b; sl = 32768; sx = 0; next3(); }
        if constexpr (s >= 13) { sfor<4>([&](auto U) AI { G(IC<U.value + 4>{}); SB(); work(U); SB(); }); sl = nq(sl); next3(); asm volatile("s_nop 4"); }
        if constexpr (s == 14) sfor<4>([&](auto U) AI { G(IC<U.value + 4>{}); SB(); work(IC<U.value + 4>{}); SB(); });
      });
      waitl0(); waitv<8>();
      if (w == 0) check_flags(pv, rws, fvo, flagb + FK_H * 128, lane, 8 * half, 8 * half + 8, epr);
      bar();
    }
    f32x4 acc[4][4];
#pragma unroll
    for (int a = 0; a < 4; ++a)
#pragma unroll
      for (int b = 0; b < 4; ++b) acc[a][b] = (f32x4){0, 0, 0, 0};
    bf16x8 fa[2][4], fb[2][4];
    const int kap = 4 * (g & 1) + q2, hf = p >> 1;
    const unsigned wb = 16384 + (4 * g + q2) * 256 + p * 8, hb = (g >> 1) * 4096 + wm * 2048 + hf * 1024 + (8 * (g & 1) + q2) * 16 + (p & 1) * 8;
    const unsigned cb3[4] = {hb + 64u * hf, hb + 64u * (1 ^ hf), 16384 + wn * 8192u + r15 * 128 + ((g ^ (r15 >> 1)) * 16), 16384 + wn * 8192u + r15 * 128 + (((4 + g) ^ (r15 >> 1)) * 16)};
    const unsigned cb4[5] = {wm * 4096u + lane * 16, wb + (((4 * wn + 0) ^ kap) * 32), wb + (((4 * wn + 1) ^ kap) * 32), wb + (((4 * wn + 2) ^ kap) * 32), wb + (((4 * wn + 3) ^ kap) * 32)};
    unsigned ba[5];
    sl = 32768;
#pragma unroll
    for (int b = 0; b < 4; ++b) { ba[b] = sl + cb3[b]; PIN(ba[b]); }
    auto rd3 = [&](int r) AI { const int ks = r / 12, xx = r % 12;
      if (xx < 2) sethalf(fa[ks][0], xx, rtr(ba[xx] + ks * 8192));
      else if (xx < 6) fb[ks][xx - 2] = rd128(ba[2 + ks] + (xx - 2) * 2048);
      else { const int a = (xx - 4) >> 1, h = xx & 1; sethalf(fa[ks][a], h, rtr(ba[h] + ks * 8192 + a * 256)); } };
    auto rd4 = [&](int r) AI { const int ks = r / 12, xx = r % 12;
      if (xx == 0) fa[ks][0] = rd128(ba[0] + ks * 8192);
      else if (xx <= 8) { const int b = (xx - 1) >> 1, h = (xx - 1) & 1; sethalf(fb[ks][b], h, rtr(ba[1 + b] + ks * 8192 + h * 4096)); }
      else fa[ks][xx - 8] = rd128(ba[0] + ks * 8192 + (xx - 8) * 1024); };
    auto mm = [&](int ks, int m) AI { acc[m >> 2][m & 3] = MFMA(fb[ks][m & 3], fa[ks][m >> 2], acc[m >> 2][m & 3]); };
    auto up3 = [&](int b) AI { if (b == 0) sl = nq(sl); ba[b] = sl + cb3[b]; PIN(ba[b]); };
    auto up4 = [&](int b) AI { if (b == 0) sl = nq(sl); ba[b] = sl + cb4[b]; PIN(ba[b]); };
    period<16, 4, true, 24, LASTR, 4>(rd3, mm, up3); waitl0(); waitv<4>(); bar();
    for (int s = 1; s < 15; ++s) { period<16, 4, false, 24, LASTR, 4>(rd3, mm, up3); waitl0(); waitv<0>(); bar(); }
    period<16, 4, false, 24, LASTR, 5>(rd3, mm, up4); waitl0(); bar();
    {
      float gr[4]; int seen = 0;
#pragma unroll
      for (int a = 0; a < 4; ++a) gr[a] = rdf(T_ROW + 1536 + (64 * wm + 16 * a + r15) * 4);
      auto mmg = [&](int ks, int m) AI { mm(ks, m); ++seen; const int t = seen - 7;
        if (t >= 0 && t < 16) { acc[t >> 2][t & 3] *= gr[t >> 2]; PIN(acc[t >> 2][t & 3]); } };
      period<16, 4, false, 24, LASTR, 5>(rd4, mmg, up4); waitl0(); bar();
    }
    for (int s = 1; s < n4; ++s) { period<16, 4, false, 24, LASTR, 5>(rd4, mm, up4); waitl0(); bar(); }
    if (half) {
      const unsigned pb = ((sl + 65536u + wm * 32768u) & 131071u) + wn * 16384 + g * 256;
      sfor<18>([&](auto M) AI { constexpr int m = M.value;
        if constexpr (m + LAG < 16) mm(1, m + LAG);
        SB();
        if constexpr (m >= 2) *(LAS f32x4*)(size_t)(pb + (m - 2) * 1024 + ((r15 ^ (4 * ((m - 2) & 3) + g)) * 16)) = acc[(m - 2) >> 2][(m - 2) & 3];
        SB(); });
      waitl0();
    } else {
#pragma unroll
      for (int m = LAG; m < 16; ++m) mm(1, m);
#pragma unroll
      for (int a = 0; a < 4; ++a)
#pragma unroll
        for (int bp = 0; bp < 2; ++bp)
          st128(rws, (u32x4){pk2(acc[a][2 * bp][0], acc[a][2 * bp][1]), pk2(acc[a][2 * bp][2], acc[a][2 * bp][3]), pk2(acc[a][2 * bp + 1][0], acc[a][2 * bp + 1][1]), pk2(acc[a][2 * bp + 1][2], acc[a][2 * bp + 1][3])},
                (a * 2 + bp) * 1024 + g * 256 + ((r15 ^ (8 * wn + 4 * bp + g)) * 16), PART_OFF + (ci * 8 + cnt) * 32768 + wq * 8192);
      waitv<0>();
    }
    bar();
    if (!half && w == 0) stflag(rws, epr, lane, flagb + FK_P * 128 + cnt * 4);
  } else {
    constexpr bool isx = !PROFX; (void)isx;
    unsigned epr, pv, t3a, t3b, sa2h, cb[8], ba[8]; const char *pp0, *pp1;
    {
      u32x4 xr[2][2], gq[2];
      ra = rx; rb = mk(sel<const u16*>(wq & 1, bim, bre), 1024 * 2048);
#pragma unroll
      for (int q = 0; q < 4; ++q) { va[q] = (16 * hl + 4 * q + ((lane >> 3) & 3)) * 2048 + (((lane & 7) ^ ((4 * q + g) & 7)) * 16) - 1024 * q; vb[q] = vnat(q); }
      auto src1 = [&](int s) AI { const int k = s ^ (s < 2 ? 0 : s < 4 ? (rot & 1) : rot); sa = (ci * 128 + 32 * wq) * 2048 + k * 128; sb = (cj * 64 + 32 * (wq >> 1)) * 2048 + k * 128; sl = ((s + 3) & 3) * 32768; };
      src1(0); ival(G, nowork);
      SB();
      setup2(); table3(t3a, t3b);
      {
        const int f = ((q2 >> 1) & 1) | ((g & 1) << 1);
        const unsigned nb = wm * 8192u + r15 * 128, n0 = (g ^ (r15 >> 1)) * 16, n1 = ((4 + g) ^ (r15 >> 1)) * 16, wk = (g >> 1) * 7168u + p * 8;
        cb[0] = nb + wn * 4096 + n0; cb[1] = nb + wn * 4096 + n1; cb[2] = nb + (1 - wn) * 4096 + n0; cb[3] = nb + (1 - wn) * 4096 + n1;
        cb[4] = wk + (8 * (g & 1) + q2) * 128 + (((2 * wn) ^ f) * 32); cb[5] = wk + (8 * (g & 1) + q2) * 128 + (((2 * wn + 1) ^ f) * 32);
        cb[6] = wk + 6144 + (8 * (g & 1) + q2) * 64 + (g & 1) * 32; cb[7] = wk + 6144 + (8 * (g & 1) + q2) * 64 + ((g & 1) ^ 1) * 32;
#pragma unroll
        for (int b = 0; b < 8; ++b) { ba[b] = cb[b] + (b < 4 ? 131072u : 0u); PIN2(cb[b], ba[b]); }
        pp0 = lane < 32 ? (const char*)sel<const u16*>(lane & 16, cim, cre) + ((bid * 4 + wq) * 16 + r15) * 128 : (const char*)w2 + ((bid * 4 + wq) * 56 + r15) * 128;
        pp1 = (const char*)w2 + ((bid * 4 + wq) * 56 + (lane < 40 ? 16 + lane : 55)) * 128;
        PIN2(pp0, pp1);
      }
      src1(1); ival(G, nowork);
      waitv<8>(); bar();
      unsigned pdt, plr, pli, bq, oq; f32x2 lp; float lr, li;
      const int tc = tq < 224 ? tq : 223;
      ld32a(epr, rws, 0u, flagb + FK_EP * 128); ld128g(gq[0], gw, lane * 16u); ld128g(gq[1], gw + 512, lane * 16u);
      ld16p(pdt, ldt + 64 * cj, lane * 2u); ld16p(plr, lre + 64 * cj, lane * 2u); ld16p(pli, lim + 64 * cj, lane * 2u);
      ld16p(bq, b1, (224 * cj + (tc < 192 ? ((cj & 1) ? 32 : 0) + tc : ((cj & 1) ? -192 : 0) + tc)) * 2u);
      ld16p(oq, sel<const u16*>(wq < 2, b2, dsk), (cnt * 128 + (tq & 127)) * 2u);
      PH(1);
      src1(2); ival(G, nowork);
      src1(3); ival(G, nowork);
      float sv[6] = {0, 0, 0, 0, 0, 0}, a1, a2, a3, mean, sd, rstd, qv, gg, kq, rr;
      auto E1 = [](int s) constexpr { return (s == 1 || s == 2) ? 2 : s == 11 ? 1 : 0; };
      sfor<12>([&](auto S) AI { constexpr int s = S.value;
        if constexpr (s == 0) waitv<16 + 8>(); else waitv<16 + E1(s - 2) + E1(s - 1)>();
        bar();
        src1(s + 4); ival(G, [&](auto U) AI { constexpr int u = U.value;
          if constexpr ((s == 1 || s == 2) && (u == 2 || u == 5)) ld128p(xr[s - 1][u / 3], rx, lane * 16u, (ci * 128 + 8 * cj + 2 * wq + s - 1) * 2048 + (u / 3) * 1024);
          if constexpr (s == 1 && u == 0) { PIN2(pdt, plr); PIN(pli); lr = bf2f(plr); li = bf2f(pli); const float dt = rbf(fexp(bf2f(pdt))); lp = (f32x2){fexp(dt * lr), dt * li}; PIN(lp); }
          if constexpr (s == 1 && u == 3) { PIN2(bq, oq); wrf(T_B1 + tq * 4, bf2f(bq)); wrf(T_OUT + tq * 4, bf2f(oq)); PIN(epr); epr = __builtin_amdgcn_readfirstlane(epr) + 1u; }
          if constexpr (s == 1 && u == 6) { kq = __builtin_rintf(lp.y * 0.63661977236f);
            rr = __builtin_fmaf(kq, -1.5707962513f, lp.y); rr = __builtin_fmaf(kq, -7.5497894159e-08f, rr); rr = __builtin_fmaf(kq, -5.3903029534e-15f, rr); PIN2(kq, rr); }
          if constexpr (s == 2 && u == 0) { const float z = rr * rr;
            a1 = __builtin_fmaf(rr * z, __builtin_fmaf(z, __builtin_fmaf(z, __builtin_fmaf(z, 2.7183114939e-06f, -1.9839334836e-04f), 8.3333293858e-03f), -1.6666666642e-01f), rr); PIN(a1); }
          if constexpr (s == 2 && u == 3) { const float z = rr * rr;
            a2 = __builtin_fmaf(z, __builtin_fmaf(z, __builtin_fmaf(z, __builtin_fmaf(z, 2.4390448796e-05f, -1.3886763775e-03f), 4.1666623324e-02f), -4.9999999726e-01f), 1.f); PIN(a2); }
          if constexpr (s == 2 && u == 6) { const int q = (int)kq; const float sa_ = sel<float>(q & 1, a2, a1), ca_ = sel<float>(q & 1, a1, a2);
            lp = (f32x2){lp.x * (((q + 1) & 2) ? -ca_ : ca_), lp.x * ((q & 2) ? -sa_ : sa_)}; PIN(lp); wrf2(T_CON + lane * 8, lp); }
          if constexpr (s == 3 && u == 0) { const float nr = lp.x - 1.f, ni = lp.y, den = 2.f * __builtin_amdgcn_rcpf(lr * lr + li * li);
                                            wrf2(T_CON + 512 + lane * 8, (f32x2){(nr * lr + ni * li) * den, (ni * lr - nr * li) * den}); }
          if constexpr ((s == 3 && u >= 2) || (s == 4 && u >= 4)) { constexpr int q = s == 3 ? u - 2 : u + 2; lp = cmul(lp, lp); PIN(lp);
            if constexpr (q >= 3 && q < 7) wrf2(T_CON + (q - 1) * 512 + lane * 8, lp);
            if constexpr (q == 9) wrf2(T_CON + 3072 + lane * 8, lp); }
          if constexpr ((s == 4 && u < 2) || (s >= 5 && s < 9 && !(u & 1) && 4 * (s - 5) + 2 + (u >> 1) < 16)) {
            constexpr int id = s == 4 ? u : 4 * (s - 5) + 2 + (u >> 1), r = id >> 3, e = id & 7;
            PIN2(xr[r][e >> 2], gq[e >> 2]);
            const unsigned v = xr[r][e >> 2][e & 3], gv = gq[e >> 2][e & 3];
            const float x0 = bf2f(v & 0xffffu), x1 = u2f(v & 0xffff0000u);
            sv[r] += x0 + x1; sv[2 + r] = __builtin_fmaf(x1, x1, __builtin_fmaf(x0, x0, sv[2 + r]));
            sv[4 + r] = __builtin_fmaf(x1, u2f(gv & 0xffff0000u), __builtin_fmaf(x0, bf2f(gv & 0xffffu), sv[4 + r]));
            PIN2(sv[r], sv[2 + r]); PIN(sv[4 + r]);
          }
          if constexpr (s == 9 && u < 4) {
#pragma unroll
            for (int q = 0; q < 6; ++q) sv[q] = u == 0 ? dppadd<0xB1>(sv[q]) : u == 1 ? dppadd<0x4E>(sv[q]) : u == 2 ? dppadd<0x141>(sv[q]) : dppadd<0x140>(sv[q]);
            PIN2(sv[0], sv[1]); PIN2(sv[2], sv[3]); PIN2(sv[4], sv[5]);
          }
          if constexpr (s == 9 && u == 5) { const bool r1 = lane & 4; a1 = sum16(sel<float>(r1, sv[1], sv[0])); a2 = sum16(sel<float>(r1, sv[3], sv[2])); a3 = sum16(sel<float>(r1, sv[5], sv[4])); PIN2(a1, a2); PIN(a3); }
          if constexpr (s == 10 && u == 0) { a1 = sum32(a1); a2 = sum32(a2); a3 = sum32(a3); PIN2(a1, a2); PIN(a3); }
          if constexpr (s == 10 && u == 2) { mean = a1 * (1.f / 1024.f); const float var = __builtin_fmaxf(a2 * (1.f / 1024.f) - mean * mean, 0.f) + 1e-5f; rstd = __builtin_amdgcn_rsqf(var); sd = var * rstd;
                                             PIN2(mean, rstd); PIN(sd); }
          if constexpr (s == 10 && u == 4) { qv = 1.f + fexp(__builtin_fminf(a3, 60.f)); gg = __builtin_amdgcn_rcpf(1.f + fexp(__builtin_fminf(-a3, 80.f))); PIN2(qv, gg); }
          if constexpr (s == 11 && u == 0) { a1 = sel4(lane & 1, lane & 2, -2.302208198f * rstd, -0.1029432397f * rstd * rstd * rstd, qv * sd, gg); PIN(a1); }
          if constexpr (s == 11 && u == 2) { const float mh = rbf(mean), sh = rbf(sd);
                                             a2 = sel4(lane & 1, lane & 2, u2f(pk2(-mh, -mh)), u2f(pk2(mh - mean, sh)), u2f(pk2(sd - sh, 0.f)), 0.f); PIN(a2); }
          if constexpr (s == 11 && u == 5) { if (lane < 16) st64(rws, (u32x2){f2u(sel<float>(lane & 8, a2, a1)), epr}, (lane >> 3) * 4096u + (lane & 7) * 8, rgb + (8 * cj + 2 * wq) * 32); }
        });
      });
      rb = mk((const char*)w1 - 4096, 1024 * 7168 + 4096);
#pragma unroll
      for (int q = 0; q < 4; ++q) va[q] = vb[q];
      sx = 0; nexts();
      const unsigned sb0 = sb, sa0 = sa; nexts();
      const unsigned sb1 = sb, sa1 = sa; nexts();
      const unsigned sa2 = sa;
      waitv<16 + 1>(); bar();
      sa = sa0; sfor<4>([&](auto U) AI { G2(IC<0>{}, U); });
      sa = sa1; sfor<4>([&](auto U) AI { G2(IC<1>{}, U); });
      waitv<16 + 1>(); bar();
      sb = sb0; sfor<7>([&](auto U) AI { G2(IC<0>{}, IC<U.value + 4>{}); });
      sa = sa2; sfor<4>([&](auto U) AI { G2(IC<2>{}, U); });
      waitv<19>(); bar();
      sb = sb1; sfor<7>([&](auto U) AI { G2(IC<1>{}, IC<U.value + 4>{}); });
      ta = t3a; tb = t3b; sx = 2; nexts(); sa2h = sa; next3();
    }
    {
      f32x4 ac1[28];
#pragma unroll
      for (int m = 0; m < 28; ++m) ac1[m] = (f32x4){0, 0, 0, 0};
      {
        bf16x8 fa[2][4], fb[2][8];
        unsigned xn = 98304, wnx = 32768;
        setm0(T_DUM);
        auto rd = [&](int r) AI { const int ks = r / 20, xx = r % 20;
          if (xx == 0) fa[ks][0] = rd128(ba[ks]);
          else if (xx <= 12) { const int b = (xx - 1) >> 1, h = (xx - 1) & 1; sethalf(fb[ks][b], h, rtr(ba[4 + (b & 1)] + (b >> 1) * 2048 + ks * 14336 + h * 512)); }
          else if (xx <= 16) { const int b = (xx - 1) >> 1, h = (xx - 1) & 1; sethalf(fb[ks][b], h, rtr(ba[b] + ks * 14336 + h * 256)); }
          else { const int a = xx - 16; fa[ks][a] = rd128(ba[(a >> 1) * 2 + ks] + (a & 1) * 2048); } };
        auto mm = [&](int ks, int m) AI { const int a = m < 16 ? m >> 3 : (m - 4) / 6, b = m < 16 ? m & 7 : (m - 4) % 6; ac1[m] = MFMA(fb[ks][b], fa[ks][a], ac1[m]); };
        auto up = [&](int b) AI { if (b < 8) { ba[b] = cb[b] + sel<unsigned>(b < 4, xn, wnx); PIN(ba[b]); }
                                  else { wnx = (wnx == 65536u) ? 0u : wnx + 32768u; xn = (xn == 131072u) ? 98304u : xn + 16384u; } };
        waitl0(); waitv<11>(); bar();
        PH(2);
        period<28, 8, true, 40, LASTR + 24, 9>(rd, mm, up); waitl0(); waitv<0>(); bar();
        const int sp = 2 + bid % 7;
        for (int o = 0; o < 2; ++o) {
          for (int s = o ? sp : 1; s < (o ? 16 : sp); ++s) { period<28, 8, false, 40, LASTR + 24, 9>(rd, mm, up); waitl0(); bar(); }
          if (o == 0) { pfg(pp0); pfg(pp1); }
        }
        PH(3);
        setup3(); sl = 0;
        setm0(98304 + wq * 4096); sfor<4>([&](auto U) AI { dma1<U.value * 1024>(rws, va[0], sa2h); });
        ival(G, [&](auto U) AI { constexpr int u = U.value;
#pragma unroll
          for (int m = LAG + 4 * u; m < LAG + 4 * u + 4; ++m) if (m < 28) mm(1, m); });
#pragma unroll
        for (int a = 0; a < 4; ++a) fa[0][a] = rd128(g ? T_ZERO : T_XA + (64 * wm + 16 * (a ^ (2 * wn)) + r15) * 16);
#pragma unroll
        for (int b = 0; b < 8; ++b) fb[0][b] = rd128(T_WA + ((b < 6 ? (b >> 1) * 64 + wn * 32 + (b & 1) * 16 : 96 + b * 16) + r15) * 16);
        SB();
        sl = nq(sl); next3();
        bar();
        ival(G, [&](auto U) AI { constexpr int u = U.value;
#pragma unroll
          for (int m = 4 * u; m < 4 * u + 4; ++m) if (m < 28) mm(0, m); });
        sl = nq(sl); next3();
      }
      float k0[4], k1[4], qq[4];
#pragma unroll
      for (int a = 0; a < 4; ++a) { const unsigned ro = T_ROW + (64 * wm + 16 * (a ^ (2 * wn)) + r15) * 4; k0[a] = rdf(ro); k1[a] = rdf(ro + 512); qq[a] = rdf(ro + 1024); }
      u32x4 hw[4]; float t[8];
      auto lv0 = [](int s) constexpr { return s < 1 ? 0 : s < 6 ? 13 * (s - 1) : (64 + 8 * (s - 6) < 224 ? 64 + 8 * (s - 6) : 224); };
      auto st3 = [](int s) constexpr { return (s == 5 || s == 13 || s == 21) ? 4 : s == 25 ? 2 : 0; };
      sfor<36>([&](auto S1) AI { constexpr int s = S1.value + 1;
        if constexpr (s == 7) waitv<8>();
        else waitv<16 + st3(s - 2) + st3(s - 1)>();
        if constexpr (s == 12 || s == 20 || s == 28 || s == 36) {
          if (w == 4) check_flags(pv, rws, fvo, flagb + (FK_M1 + (s - 12) / 8) * 128, lane, s == 36 ? 0 : 8 * half, 8 * half + 8, epr); }
        SB(); bar();
        if constexpr (s == 16) { PH(4); }
        if constexpr (s == 7 || s == 16 || s == 24 || s == 28) { if (w == 5) stflag(rws, epr, lane, flagb + (FK_M1 + (s - 4) / 8) * 128 + cj * 4); }
        if constexpr (s == 10 || s == 18 || s == 26 || s == 34) { if (w == 4) ld32a(pv, rws, fvo, flagb + (FK_M1 + (s - 10) / 8) * 128); }
#define UU(e) ac1[m0 + ((e) >> 2)][(e) & 3]
#define LVL(expr) do { _Pragma("unroll") for (int e = 4 * hh; e < 4 * hh + 4; ++e) t[e] = (expr); PIN2(t[4 * hh], t[4 * hh + 1]); PIN2(t[4 * hh + 2], t[4 * hh + 3]); } while (0)
        auto hlev = [&](auto LL) AI { constexpr int L = LL.value, v = L >> 4, l = (L >> 1) & 7, hh = L & 1, a = v & 3, m0 = (a < 2 ? 8 * a : 4 + 6 * a) + 2 * (v >> 2);
          if constexpr (l == 0) LVL(UU(e) * UU(e));
          if constexpr (l == 1) LVL(__builtin_fmaf(t[e], k1[a], k0[a]));
          if constexpr (l == 2) LVL(UU(e) * t[e]);
          if constexpr (l == 3) LVL(__builtin_amdgcn_exp2f(t[e]));
          if constexpr (l == 4) LVL(__builtin_fmaf(t[e], qq[a], qq[a]));
          if constexpr (l == 5) LVL(__builtin_amdgcn_rcpf(t[e]));
          if constexpr (l == 6) LVL(UU(e) * t[e]);
          if constexpr (l == 7) { hw[a][2 * hh] = pk2(t[4 * hh], t[4 * hh + 1]); hw[a][2 * hh + 1] = pk2(t[4 * hh + 2], t[4 * hh + 3]); PIN(hw[a]); }
        };
        ival(G, [&](auto U) AI { constexpr int u = U.value, n = lv0(s + 1) - lv0(s), i0 = (n * u + 7) / 8, i1 = (n * (u + 1) + 7) / 8;
          if constexpr (i0 < i1) hlev(IC<lv0(s) + i0>{});
          if constexpr (i0 + 1 < i1) hlev(IC<lv0(s) + i0 + 1>{});
        });
#pragma unroll
        for (int q = 0; q < st3(s); ++q) st128(rws, hw[q], lane * 16u + (4 * wm + (q ^ (2 * wn))) * 1024, ci * HID_PANEL + kb_of(cj, s < 25 ? 2 * (s >> 3) + wn : 6) * 8192);
        sl = nq(sl);
        if constexpr (s < 11) next3(); else nexts();
        if constexpr (s == 11) {
          rb = mk(w2, 3584 * 2048);
#pragma unroll
          for (int q = 0; q < 4; ++q) vb[q] = (4 * q + g) * 2048 + ((((r15 >> 1) ^ (4 * (q & 1) + g)) * 2) + (lane & 1)) * 16 - 1024 * q;
        }
      });
    }
    static_assert(MOVE >= 0 && MOVE <= 3, "");
    sfor<3 - MOVE>([&](auto) AI { waitv<16>(); bar(); ival(G, nowork); sl = nq(sl); nexts(); });
    if (half) sfor<2 * MOVE>([&](auto) AI { waitv<16>(); bar(); ival(G, nowork); sl = nq(sl); nexts(); });
    waitv<16>(); bar();
    if (half) {
      sfor<8>([&](auto U) AI { constexpr int u = U.value; if constexpr ((u & 3) == 0) setm0(sl + wq * 8192 + u * 1024);
        dma<(u & 3) * 1024>(rx, (4u * (u & 3) + g) * 2048 + r15 * 16 - (u & 3) * 1024, (ci * 128 + 32 * wq + 16 * (u >> 2)) * 2048 + cnt * 256); });
      waitv<8 + 8>();
    } else waitv<8>();
    bar();
    ld32a(pv, rws, fvo, flagb + FK_P * 128);
    if (half) waitv<9>(); else waitv<0>();
    bar();
    auto getp = [&]() AI {
      sfor<8>([&](auto U) AI { constexpr int u = U.value; if constexpr ((u & 3) == 0) setm0(nq(sl) + wq * 8192 + u * 1024);
        dma1<(u & 3) * 1024>(rws, lane * 16u, PART_OFF + (ci * 8 + cnt) * 32768 + wq * 8192 + (u >> 2) * 4096); }); };
    waitv<0>(); PIN(pv);
    const bool early = half && !anyl(lane == cnt && pv != epr);
    if (early) getp(); else ld32a(pv, rws, fvo, flagb + FK_P * 128);
    bar();
    if (half) {
      if (!early) { waitv<0>(); check_flags(pv, rws, fvo, flagb + FK_P * 128, lane, cnt, cnt + 1, epr); getp(); }
      if (cnt == 0 && w == 4) stflag(rws, epr, lane, flagb + FK_EP * 128);
    }
    PH(5);
    waitv<0>(); bar();
  }
  if (half) {
    const int bg = r31 & 15, sw = (r31 >> 4) * 8 + (bg >> 3) * 4 + (bg & 3);
    const unsigned pa = ((sl + 65536u + (w >> 2) * 32768u) & 131071u) + (r31 >> 4) * 16384 + (wq * 4 + (bg >> 2)) * 1024 + (bg & 3) * 256;
    const unsigned pp = nq(sl) + ((w >> 2) * 2 + (r31 >> 4)) * 8192 + (wq * 2 + (bg >> 3)) * 1024 + (bg & 3) * 256 + ((bg >> 2) & 1) * 8;
    const f32x4 bv = rdf4(T_OUT + r31 * 16), dv = rdf4(T_OUT + 512 + r31 * 16);
    f32x4 av[8]; u32x2 pw[8], xw[8]; float gg[8];
#pragma unroll
    for (int u = 0; u < 8; ++u) {
      const int row = 2 * u + hl;
      av[u] = rdf4(pa + ((row ^ bg) * 16)); pw[u] = *(LAS u32x2*)(size_t)(pp + ((row ^ sw) * 16));
      xw[u] = *(LAS u32x2*)(size_t)(sl + (16 * w + hl) * 256 + r31 * 8 + u * 512); gg[u] = rdf(T_ROW + 1536 + (16 * w + hl) * 4 + u * 8);
    }
    SB();
#pragma unroll
    for (int u = 0; u < 8; ++u) {
      const f32x4 pf = {bf2f(pw[u][0] & 0xffffu), u2f(pw[u][0] & 0xffff0000u), bf2f(pw[u][1] & 0xffffu), u2f(pw[u][1] & 0xffff0000u)};
      const f32x4 xf = {bf2f(xw[u][0] & 0xffffu), u2f(xw[u][0] & 0xffff0000u), bf2f(xw[u][1] & 0xffffu), u2f(xw[u][1] & 0xffff0000u)};
      f32x4 r;
#pragma unroll
      for (int e = 0; e < 4; ++e) r[e] = (av[u][e] + pf[e]) + __builtin_fmaf(gg[u], __builtin_fmaf(xf[e], dv[e], -bv[e]), bv[e]);
      st128<OAUX>(mk(out, 8u << 20), __builtin_bit_cast(u32x4, r), (unsigned)((16 * w + hl + 2 * u) * 4096 + r31 * 16), ci * 128 * 4096 + cnt * 512);
    }
  }
}

extern "C" void kernel_launch(void* const* d_in, const int* in_sizes, int n_in, void* d_out, int out_size, void* d_ws, size_t ws_size, hipStream_t stream) {
  (void)in_sizes; (void)n_in; (void)out_size; (void)ws_size;
  (void)hipFuncSetAttribute((const void*)fused_kernel, hipFuncAttributeMaxDynamicSharedMemorySize, LDS_BYTES);
  fused_kernel<<<256, 512, LDS_BYTES, stream>>>((const u16*)d_in[0], (const u16*)d_in[1], (const u16*)d_in[2], (const u16*)d_in[3], (const u16*)d_in[4],
      (const u16*)d_in[5], (const u16*)d_in[6], (const u16*)d_in[7], (const u16*)d_in[8], (const u16*)d_in[10], (const u16*)d_in[11], (const u16*)d_in[12],
      (const u16*)d_in[13], (const u16*)d_in[14], (char*)d_ws, (float*)d_out);
}
```

```cpp
#include <hip/hip_runtime.h>
#include <utility>

typedef unsigned short u16;
typedef __bf16 bf16x2 __attribute__((ext_vector_type(2)));
typedef __bf16 bf16x4 __attribute__((ext_vector_type(4)));
typedef __bf16 bf16x8 __attribute__((ext_vector_type(8)));
typedef float f32x2 __attribute__((ext_vector_type(2)));
typedef float f32x4 __attribute__((ext_vector_type(4)));
typedef float f32x16 __attribute__((ext_vector_type(16)));
typedef unsigned u32x2 __attribute__((ext_vector_type(2)));
typedef unsigned u32x4 __attribute__((ext_vector_type(4)));
typedef __amdgpu_buffer_rsrc_t rsrc_t;
#define LAS __attribute__((address_space(3)))
#define DI __device__ __forceinline__
#define AI __attribute__((always_inline))
#define SB() __builtin_amdgcn_sched_barrier(0)
#define PIN(x) asm volatile("" : "+v"(x))
#define MFMA(a, b, c) __builtin_amdgcn_mfma_f32_16x16x32_bf16(a, b, c, 0, 0, 0)
#define MMA(c, a, b) asm volatile("v_mfma_f32_16x16x32_bf16 %0, %1, %2, %0" : "+v"(c) : "v"(a), "v"(b))
#define MPAD() asm volatile("s_nop 7")
template <int V> using IC = std::integral_constant<int, V>;

constexpr unsigned HID_PANEL = 112 * 8192, H_OFF = 16 * HID_PANEL, H_PANEL = 32 * 16384;
constexpr unsigned PART_OFF = H_OFF + 16 * H_PANEL;
constexpr unsigned GRAN_OFF = PART_OFF + 128 * 32768;
constexpr unsigned RG_OFF = GRAN_OFF + 256 * 1024;
constexpr unsigned CG_OFF = RG_OFF + 16 * 8192;
constexpr unsigned FLAG_OFF = CG_OFF + 256 * 2048;
constexpr unsigned EPC_OFF = FLAG_OFF + 16 * 1024;
constexpr unsigned WS_BYTES = EPC_OFF + 16 * 128;
constexpr int FK_M1 = 0, FK_M2 = 1, FK_M3 = 2, FK_M4 = 3, FK_H = 4, FK_P = 5, FK_EP = 6;
constexpr unsigned TAB = 147456, T_ROW = TAB, T_XA = TAB + 2048, T_OUT = TAB + 4096, T_B1 = TAB + 5120, T_CS2 = TAB + 6144, T_WA = TAB + 8192, T_ZERO = TAB + 12288, T_AGG = T_WA, T_DUM = TAB + 12544, T_CON = TAB + 12800;
constexpr unsigned LDS_BYTES = 163840;
#define XMAP 1
#define ROTM 3
#define OAUX 2
#define SP0 4
#define SPN 6
#define PTE 0
#define MOVE 3
#define PROFX false
constexpr int LASTR = 25;
constexpr unsigned SPIN_CAP = 1u << 16;

DI rsrc_t mk(const void* p, unsigned bytes) { return __builtin_amdgcn_make_buffer_rsrc((void*)p, 0, bytes, 0x00020000); }
DI void setm0(unsigned lds) { asm volatile("s_mov_b32 m0, %0\n\ts_nop 0" ::"s"(lds) : "memory"); }
template <int OFF> DI void dma(rsrc_t r, unsigned voff, unsigned soff) {
  asm volatile("buffer_load_dwordx4 %0, %1, %2 offen offset:%3 lds" ::"v"(voff), "s"(r), "s"(soff), "i"(OFF) : "memory");
}
template <int OFF> DI void dma1(rsrc_t r, unsigned voff, unsigned soff) {
  asm volatile("buffer_load_dwordx4 %0, %1, %2 offen offset:%3 sc1 lds" ::"v"(voff), "s"(r), "s"(soff), "i"(OFF) : "memory");
}
#define PIN2(x, y) asm volatile("" : "+v"(x), "+v"(y))
#define PINS(x) asm volatile("" : "+s"(x))
typedef const void* const __attribute__((address_space(4)))* kargs_t;
DI const char* karg(int i) { kargs_t ka = (kargs_t)__builtin_amdgcn_kernarg_segment_ptr(); PINS(ka); return (const char*)ka[i]; }
enum { A_X, A_LRE, A_LIM, A_BRE, A_BIM, A_CRE, A_CIM, A_LDT, A_DSK, A_W1, A_B1, A_W2, A_B2, A_GW, A_WS, A_OUT };
template <int N> DI void waitv() { asm volatile("s_waitcnt vmcnt(%0)" ::"i"(N) : "memory"); }
DI void waitl0() { SB(); __builtin_amdgcn_s_waitcnt(0xC07F); asm volatile("" ::: "memory"); }
#define PH(k)
DI void bar() { asm volatile("s_barrier" ::: "memory"); SB(); }
DI bf16x8 rd128(unsigned a) { return *(LAS bf16x8*)(size_t)a; }
DI u32x4 rdu4(unsigned a) { return *(LAS u32x4*)(size_t)a; }
DI f32x4 rdf4(unsigned a) { return *(LAS f32x4*)(size_t)a; }
DI f32x2 rdf2(unsigned a) { return *(LAS f32x2*)(size_t)a; }
DI float rdf(unsigned a) { return *(LAS float*)(size_t)a; }
DI void wrf(unsigned a, float v) { *(LAS float*)(size_t)a = v; }
DI void wrf2(unsigned a, f32x2 v) { *(LAS f32x2*)(size_t)a = v; }
DI bf16x4 rtr(unsigned a) { return __builtin_amdgcn_ds_read_tr16_b64_v4bf16((LAS bf16x4*)(size_t)a); }
DI void sethalf(bf16x8& f, int h, bf16x4 v) {
  if (h == 0) f = __builtin_shufflevector(v, v, 0, 1, 2, 3, -1, -1, -1, -1);
  else { bf16x8 t = __builtin_shufflevector(v, v, -1, -1, -1, -1, 0, 1, 2, 3); f = __builtin_shufflevector(f, t, 0, 1, 2, 3, 12, 13, 14, 15); }
}
DI unsigned f2u(float f) { return __builtin_bit_cast(unsigned, f); }
DI float u2f(unsigned u) { return __builtin_bit_cast(float, u); }
DI float shx(int lane, float v, int m) { return u2f(__builtin_amdgcn_ds_bpermute((lane ^ m) * 4, (int)f2u(v))); }
DI float sum16(float v) { const auto r = __builtin_amdgcn_permlane16_swap(f2u(v), f2u(v), false, false); return u2f(r[0]) + u2f(r[1]); }
DI float sum32(float v) { const auto r = __builtin_amdgcn_permlane32_swap(f2u(v), f2u(v), false, false); return u2f(r[0]) + u2f(r[1]); }
DI float fexp(float v) { return __builtin_amdgcn_exp2f(v * 1.4426950408889634f); }
DI float bf2f(unsigned v) { return u2f(v << 16); }
DI unsigned pk2(float lo, float hi) { f32x2 v = {lo, hi}; return __builtin_bit_cast(unsigned, __builtin_convertvector(v, bf16x2)); }
DI float rbf(float f) { return u2f(pk2(f, 0.f) << 16); }
DI float dot2(unsigned a, unsigned b, float c) { return __builtin_amdgcn_fdot2_f32_bf16(__builtin_bit_cast(bf16x2, a), __builtin_bit_cast(bf16x2, b), c, false); }
template <int C> DI float dppadd(float v) { return v + u2f(__builtin_amdgcn_update_dpp(0, (int)f2u(v), C, 0xf, 0xf, true)); }
DI float sel4(bool b0, bool b1, float a, float b, float c, float d) { PIN(a); PIN(b); PIN(c); PIN(d); const float lo = b0 ? b : a, hi = b0 ? d : c; return b1 ? hi : lo; }
DI f32x2 cmul(f32x2 a, f32x2 b) { return (f32x2){a.x * b.x - a.y * b.y, a.x * b.y + a.y * b.x}; }
DI f32x2 cmulc(f32x2 a, f32x2 b) { return (f32x2){__builtin_fmaf(a.x, b.x, -(a.y * b.y)), __builtin_fmaf(-a.x, b.y, -(a.y * b.x))}; }
DI f32x2 cfma(f32x2 a, f32x2 b, f32x2 c) { return (f32x2){__builtin_fmaf(a.x, b.x, __builtin_fmaf(-a.y, b.y, c.x)), __builtin_fmaf(a.x, b.y, __builtin_fmaf(a.y, b.x, c.y))}; }
template <int AUX = 16> DI void st128(rsrc_t r, u32x4 v, unsigned voff, unsigned soff) {
  __builtin_amdgcn_raw_buffer_store_b128(v, r, voff, soff, AUX);
  asm volatile("s_nop 1" ::"v"(v));
}
DI void st64(rsrc_t r, u32x2 v, unsigned voff, unsigned soff) { __builtin_amdgcn_raw_buffer_store_b64(v, r, voff, soff, 16); }
DI void st32(rsrc_t r, unsigned v, unsigned voff, unsigned soff) { __builtin_amdgcn_raw_buffer_store_b32(v, r, voff, soff, 16); }
DI void stflag(rsrc_t r, unsigned v, int lane, unsigned soff) { if (lane == 0) st32(r, v, 0u, soff); }
DI void ld32a(unsigned& d, rsrc_t r, unsigned voff, unsigned soff) { asm volatile("buffer_load_dword %0, %1, %2, %3 offen sc1" : "=v"(d) : "v"(voff), "s"(r), "s"(soff) : "memory"); }
DI void ld16p(unsigned& d, const u16* p, unsigned voff) { asm volatile("global_load_ushort %0, %1, %2" : "=v"(d) : "v"(voff), "s"(p) : "memory"); }
DI void ld64p(u32x2& d, rsrc_t r, unsigned voff, unsigned soff) { asm volatile("buffer_load_dwordx2 %0, %1, %2, %3 offen" : "=v"(d) : "v"(voff), "s"(r), "s"(soff) : "memory"); }
DI void ld64a(u32x2& d, rsrc_t r, unsigned voff, unsigned soff) { asm volatile("buffer_load_dwordx2 %0, %1, %2, %3 offen sc1" : "=v"(d) : "v"(voff), "s"(r), "s"(soff) : "memory"); }
DI void ld128g(u32x4& d, const u16* p, unsigned voff) { asm volatile("global_load_dwordx4 %0, %1, %2" : "=v"(d) : "v"(voff), "s"(p) : "memory"); }
DI void pfl(unsigned& d, rsrc_t r, unsigned voff, unsigned soff) { asm volatile("buffer_load_dword %0, %1, %2, %3 offen" : "+v"(d) : "v"(voff), "s"(r), "s"(soff) : "memory"); }
template <class T> DI T sel(bool c, T a, T b) { return c ? a : b; }
DI void pfg(const char* p) { asm volatile("global_load_lds_dword %0, off" ::"v"(p) : "memory"); }
DI bool anyl(bool b) { return __builtin_amdgcn_ballot_w64(b) != 0; }
DI void ld128p(u32x4& d, rsrc_t r, unsigned voff, unsigned soff) { asm volatile("buffer_load_dwordx4 %0, %1, %2, %3 offen" : "=v"(d) : "v"(voff), "s"(r), "s"(soff) : "memory"); }
DI void sincos_f(float t, float& sn, float& cs) {
  const float k = __builtin_rintf(t * 0.63661977236f);
  float r = __builtin_fmaf(k, -1.5707962513f, t); r = __builtin_fmaf(k, -7.5497894159e-08f, r); r = __builtin_fmaf(k, -5.3903029534e-15f, r);
  const float z = r * r;
  const float sp = __builtin_fmaf(r * z, __builtin_fmaf(z, __builtin_fmaf(z, __builtin_fmaf(z, 2.7183114939e-06f, -1.9839334836e-04f), 8.3333293858e-03f), -1.6666666642e-01f), r);
  const float cp = __builtin_fmaf(z, __builtin_fmaf(z, __builtin_fmaf(z, __builtin_fmaf(z, 2.4390448796e-05f, -1.3886763775e-03f), 4.1666623324e-02f), -4.9999999726e-01f), 1.f);
  const int q = (int)k;
  const float a = (q & 1) ? cp : sp, b = (q & 1) ? sp : cp;
  sn = (q & 2) ? -a : a; cs = ((q + 1) & 2) ? -b : b;
}
DI void ld128a(u32x4& d, rsrc_t r, unsigned voff, unsigned soff) { asm volatile("buffer_load_dwordx4 %0, %1, %2, %3 offen sc1" : "=v"(d) : "v"(voff), "s"(r), "s"(soff) : "memory"); }

DI unsigned x1off(int a) { return a == 0 ? 131072u : (unsigned)(81920 + a * 16384); }
DI unsigned nq(unsigned s) { return (s + 32768u) & 131071u; }

DI int kb_of(int j, int bl) { return 7 * j + ((j & 1) ? (bl == 6 ? 0 : bl + 1) : bl); }

template <int N, int L, int X, bool FIRST, int NR, int LAST, int NB, int SL, class RD, class MM, class UP>
DI void slot(RD& rd, MM& mm, UP& upd) {
  if constexpr (SL >= L) mm(X, SL - L, SL); else if constexpr (!FIRST) mm(X ^ 1, SL + N - L, SL);
  SB();
  constexpr int r = (SL * (NR - 1) + LAST - 1) / LAST;
  if constexpr (r < NR && (r * LAST) / (NR - 1) == SL) rd(X, r);
  constexpr int per = NB > N - 1 - LAST ? 2 : 1, u0 = (SL - LAST - 1) * per;
  if constexpr (SL > LAST && u0 < NB) { upd(u0); if constexpr (per == 2 && u0 + 1 < NB) upd(u0 + 1); }
  SB();
}
template <int N, int L, int X, bool FIRST, int NR, int LAST, int NB, class RD, class MM, class UP, int... I>
DI void period_(RD& rd, MM& mm, UP& upd, std::integer_sequence<int, I...>) { (slot<N, L, X, FIRST, NR, LAST, NB, I>(rd, mm, upd), ...); }
template <int N, int L, int X, bool FIRST, int NR, int LAST, int NB, class RD, class MM, class UP>
DI void period(RD& rd, MM& mm, UP& upd) {
  static_assert(NR - 1 <= LAST && NB <= 2 * (N - 1 - LAST), "");
  period_<N, L, X, FIRST, NR, LAST, NB>(rd, mm, upd, std::make_integer_sequence<int, N>{});
  waitl0();
}
template <class G, class W, int... U> DI void ival_(G& g, W& wk, std::integer_sequence<int, U...>) { ((g(IC<U>{}), SB(), wk(IC<U>{}), SB()), ...); }
template <class G, class W> DI void ival(G&& g, W&& wk) { ival_(g, wk, std::make_integer_sequence<int, 8>{}); }
template <int N, class F, int... I> DI void sfor_(F& f, std::integer_sequence<int, I...>) { (f(IC<I>{}), ...); }
template <int N, class F> DI void sfor(F&& f) { sfor_<N>(f, std::make_integer_sequence<int, N>{}); }

DI void check_flags(unsigned& pv, rsrc_t ws, unsigned voff, unsigned soff, int lane, int lo, int hi, unsigned ep) {
  PIN(pv);
  const bool need = lane >= lo && lane < hi;
  if (__builtin_expect(anyl(need && pv != ep), 0)) {
    for (unsigned sp = 0; sp < SPIN_CAP; ++sp) {
      __builtin_amdgcn_s_sleep(2);
      ld32a(pv, ws, voff, soff); asm volatile("s_waitcnt vmcnt(0)" : "+v"(pv)::"memory");
      if (!anyl(need && pv != ep)) break;
    }
  }
}

#if defined(__HIP_DEVICE_COMPILE__) && __HIP_DEVICE_COMPILE__
__attribute__((target("no-packed-fp32-ops")))
#endif
__global__ __launch_bounds__(512, 2) void fused_kernel(const u16* x, const u16* lre, const u16* lim, const u16* bre, const u16* bim, const u16* cre, const u16* cim,
    const u16* ldt, const u16* dsk, const u16* w1, const u16* b1, const u16* w2, const u16* b2, const u16* gw, char* ws, float* out) {
  extern __shared__ __attribute__((aligned(16))) char shm[];
  unsigned long long pc0; asm volatile("s_getpc_b64 %0" : "=s"(pc0));
  const int lane = threadIdx.x & 63, w = __builtin_amdgcn_readfirstlane(threadIdx.x >> 6);
  const int bid = blockIdx.x, xcd = bid & 7, loc = bid >> 3;
  const int ci = (xcd & 3) * 4 + (loc & 3), cj = (loc >> 4) * 8 + (xcd >> 2) * 4 + ((loc >> 2) & 3);
  const int rot = (ci + cj) & ROTM, cnt = cj & 7, half = cj >> 3, wq = w & 3, tq = threadIdx.x & 255;
  const int n4 = half ? 28 + MOVE : 28 - MOVE;
  const rsrc_t rws = mk(ws, WS_BYTES), rx = mk(x, 2048 * 2048);
  const int wm = (w >> 1) & 1, wn = w & 1, g = lane >> 4, r15 = lane & 15, q2 = (lane >> 2) & 3, p = lane & 3, hl = lane >> 5, r31 = lane & 31;
  const unsigned flagb = FLAG_OFF + ci * 1024, fvo = (lane & 15) * 4;
  unsigned rgb = RG_OFF + ci * 8192; PINS(rgb);
  auto nowork = [&](auto) AI {};
  unsigned va[4], vb[4], vw[7], sl, sa, sb, ta, tb; int sx; rsrc_t ra, rb;
  auto vnat = [&](int u) AI { return (8u * u + (lane >> 3)) * 2048 + (((lane & 7) ^ ((4 * u + g) & 7)) * 16) - 1024 * u; };
  auto nexts = [&]() AI { sa = __builtin_amdgcn_readlane(ta, sx); sb = __builtin_amdgcn_readlane(tb, sx); ++sx; PINS(sa); PINS(sb); };
  auto G = [&](auto U) AI { constexpr int u = U.value;
    if constexpr (u == 0) setm0(sl + wq * 4096);
    if constexpr (u == 4) setm0(sl + 16384 + wq * 4096);
    if constexpr (u < 4) dma1<u * 1024>(ra, va[u], sa); else dma<(u - 4) * 1024>(rb, vb[u - 4], sb);
  };
  auto G2 = [&](auto S, auto U) AI { constexpr int a = S.value % 3, u = U.value;
    if constexpr (u == 0) setm0((a == 0 ? 131072u : 81920u + a * 16384u) + wq * 4096);
    if constexpr (u == 4) setm0(a * 32768 + wq * 7168);
    if constexpr (u == 8) setm0(a * 32768 + wq * 7168 + 4096);
    if constexpr (u < 4) dma<u * 1024>(rx, va[u], sa); else dma<(u & 3) * 1024>(rb, vw[u - 4], sb);
  };
  const int c7 = ci & 7;
  auto setup2 = [&]() AI {
#pragma unroll
    for (int q = 0; q < 6; ++q) { const int k = 8 * (q & 1) + (lane >> 3), f = ((k >> 1) & 1) | ((q & 1) << 1);
      vw[q] = 4096 + k * 7168 + ((cj & 1) ? 64 : 0) + (q >> 1) * 128 + (((((lane & 7) >> 1) ^ f) * 2) + (lane & 1)) * 16 - (q & 3) * 1024; }
    vw[6] = 4096 + (lane >> 2) * 7168 + ((cj & 1) ? 0 : 384) + (((((lane & 3) >> 1) ^ hl) * 2) + (lane & 1)) * 16 - 2048;
    int k = lane - 2 + (c7 < 2 ? 3 * c7 : c7 - 2); k = 2 + (k >= 6 ? k - 6 : k);
    k = lane < 2 ? lane : lane < 8 ? k : (lane ^ rot);
    k = (k ^ (ci & 8)) & 15;
    ta = (ci * 128 + 32 * wq) * 2048 + k * 128; tb = (k * 64 + 16 * wq) * 7168 + cj * 448;
    PIN2(ta, tb);
#pragma unroll
    for (int q = 0; q < 7; ++q) PIN(vw[q]);
  };
  auto setup3 = [&]() AI {
    ra = rws;
#pragma unroll
    for (int q = 0; q < 4; ++q) { va[q] = lane * 16; vb[q] = vnat(q); }
  };
  auto table3 = [&](unsigned& ta, unsigned& tb) AI {
    const int ks = 16 * half + ((lane ^ (2 * cnt)) & 15);
    int n = 2 * (lane - 16) + (wq >> 1), hp = half; if (n >= 56) { n -= 2 * MOVE; hp = 0; }
    const int kb = kb_of(hp * 8 + ((n ^ rot) & 7), n >> 3);
    ta = lane < 16 ? H_OFF + ci * H_PANEL + ks * 16384 + wq * 4096 : ci * HID_PANEL + kb * 8192 + (wq & 1) * 4096;
    tb = lane < 16 ? (cnt * 128 + 32 * wq) * 2048 + (ks >> 1) * 128 + (ks & 1) : (kb * 32 + 16 * (wq & 1)) * 2048 + cnt * 256;
    PIN(ta); PIN(tb);
  };
  auto next3 = [&]() AI { nexts(); rb = mk(sel<const u16*>(sb & 1, cim, cre), 1024 * 2048); sb &= ~1u; };

  if (w < 4) {
    constexpr bool isx = PROFX; (void)isx;
    unsigned epr, epc;
    const unsigned ca = (32 * wn + r31) * 8;
    const unsigned c0 = (32 * wn + r15) * 8;
    const int seg0 = 4 * wm + 2 * hl;
    f32x2 lam[2], cof[2], cfa, l16, l128, l1k, pw0, pw1; unsigned t3a, t3b;
    f32x4 ac2[4][4];
    float zero = 0.f; PIN(zero);
    {
      setm0(T_DUM);
      const int row = bid * 8 + (lane & 7);
      const char* pp = (const char*)(hl ? (row < 1024 ? bre : bim - 1024 * 1024) : x) + (size_t)row * 2048 + (4 * wq + ((lane >> 3) & 3)) * 128;
      for (int q = 0; q <= wq; ++q) __builtin_amdgcn_s_sleep(6);
      pfg(pp);
      pfg((const char*)pc0 + (((loc * 4 + wq) % 20) * 16 + (lane & 15)) * 128);
      ld32a(epr, rws, 0u, flagb + FK_EP * 128); ld32a(epc, rws, 0u, EPC_OFF + cj * 128);
      setup2(); sx = 2; nexts(); table3(t3a, t3b);
      rb = mk((const char*)w1 - 4096, 1024 * 7168 + 4096);
#pragma unroll
      for (int q = 0; q < 4; ++q) { va[q] = vnat(q); PIN(va[q]); }
      wrf(T_ZERO + (lane & 3) * 4, 0.f);
    }
    {
      f32x4 acc[4][4];
#pragma unroll
      for (int a = 0; a < 16; ++a) { acc[a >> 2][a & 3] = (f32x4){0, 0, 0, 0}; PIN(acc[a >> 2][a & 3]); }
      bf16x8 wv[4][2];
      const int sstar = c7 < 2 ? c7 : 2;
      const int fsw = ((q2 >> 1) & 1) | ((g & 1) << 1);
      unsigned cf = sstar * 32768 + (g >> 1) * 7168 + (8 * (g & 1) + q2) * 128 + ((wq ^ fsw) * 32) + p * 8;
      unsigned ch = sstar * 32768 + (g >> 1) * 7168 + 6144 + (8 * (g & 1) + q2) * 64 + (((wq & 1) ^ (g & 1)) * 32) + p * 8;
      PIN(cf); PIN(ch);
      auto wread = [&](int st) AI {
        if (sstar == st) {
#pragma unroll
          for (int e = 0; e < 4; ++e)
#pragma unroll
            for (int t = 0; t < 4; ++t) sethalf(wv[t][e >> 1], e & 1, rtr((e >> 1) * 14336 + (t < 3 ? cf + t * 2048 + (e & 1) * 512 : ch + (e & 1) * 256)));
        } };
      {
        bf16x8 fa[4][2], fb[2][4][2];
        const unsigned n0 = r15 * 128 + ((g ^ (r15 >> 1)) * 16), n1 = r15 * 128 + (((4 + g) ^ (r15 >> 1)) * 16);
        const unsigned cb[4] = {wm * 8192u + n0, wm * 8192u + n1, 16384 + wn * 8192u + n0, 16384 + wn * 8192u + n1};
        unsigned ba[4], rs = 98304;
#pragma unroll
        for (int b = 0; b < 4; ++b) { ba[b] = rs + cb[b]; PIN(ba[b]); }
        auto rd = [&](int X, int r) AI { const int ks = r & 1, f = r >> 1;
          if (f >= 1 && f < 5) fb[X][f - 1][ks] = rd128(ba[2 + ks] + (f - 1) * 2048); else { const int a = f ? f - 4 : 0; fa[a][ks] = rd128(ba[ks] + a * 2048); } };
        auto mm = [&](int X, int q, int) AI { const int a = q >> 3, b = (q >> 1) & 3, ks = q & 1; MMA(acc[a][b], fa[a][ks], fb[X][b][ks]); };
        auto up = [&](int b) AI { if (b == 0) rs = nq(rs); ba[b] = rs + cb[b]; PIN(ba[b]); };
        waitl0(); bar();
        period<32, 16, 0, true, 16, LASTR, 4>(rd, mm, up); bar();
        for (int s = 0; s < 7; ++s) { period<32, 16, 1, false, 16, LASTR, 4>(rd, mm, up); bar(); period<32, 16, 0, false, 16, LASTR, 4>(rd, mm, up); bar(); }
        period<32, 16, 1, false, 16, LASTR, 4>(rd, mm, up); bar();
        waitv<0>(); PIN(epr); PIN(epc);
        epr = __builtin_amdgcn_readfirstlane(epr) + 1u; epc = __builtin_amdgcn_readfirstlane(epc) + 1u;
        lam[0] = rdf2(T_CON + c0); lam[1] = rdf2(T_CON + c0 + 128);
        wread(0);
        sfor<7>([&](auto U) AI { G2(IC<2>{}, IC<U.value + 4>{}); SB();
#pragma unroll
          for (int q = 16 + (16 * U.value) / 7 / 2 * 2; q < 16 + (16 * (U.value + 1)) / 7 / 2 * 2; ++q) mm(1, q, 0);
          SB(); });
        nexts();
      }
      f32x2 st[2] = {{0, 0}, {0, 0}}, Pf[2] = {{0, 0}, {0, 0}}, Gt = {0, 0}, cr = {0, 0}, ag[8];
      u32x4 lb[8], cg[8], rgv[2]; unsigned lbo[8], lbe[8], pv, gd = 0; u32x4 ob[2][2][2];
      f32x4 cs[4]; f32x2 so[2]; float c0s = 0.f, c1s = 0.f;
      const rsrc_t rgr = mk(ws + GRAN_OFF + cj * 16384, 16384);
      const int cp = 28 * wq + (r31 < 28 ? r31 : 27);
      const unsigned cgo = hl * 16384 + cp * 16;
      auto hofs = [&](int hf, int mt) AI { return (2u * wn + mt) * 4096 + wm * 2048 + hf * 1024 + g * 256 + ((r15 ^ (hf << 2)) * 16); };
      auto bad = [&]() AI { unsigned d = 0;
#pragma unroll
        for (int q = 0; q < 8; ++q) { PIN(lb[q]); d |= (int)lbo[q] < 0 ? 0u : ((lb[q][1] ^ epc) | (lb[q][3] ^ epc)); }
        return d != 0; };
      auto gbad = [&]() AI { unsigned d = 0;
#pragma unroll
        for (int q = 0; q < 8; ++q) { PIN(cg[q]); d |= (cg[q][1] ^ epc) | (cg[q][3] ^ epc); }
        return d != 0; };
      auto E2 = [](int s) constexpr { return (s == 2 || s == 3) ? 1 : s == 4 ? 8 : (s == 11 || s == 12) ? 5 : 0; };
      auto T2 = [](int s) constexpr { return (s >= 7 && s < 11) ? 2 : 0; };
      sfor<15>([&](auto S) AI { constexpr int s = S.value;
        if constexpr (s < 3) waitl0();
        if constexpr (s == 12) waitv<11 + E2(11)>();
        else if constexpr (s >= 1) waitv<T2(s - 2) + (s < 14 ? 11 : 4) + E2(s - 1) + T2(s - 1)>();
        bar();
        if constexpr (s < 2) wread(s + 1);
        if constexpr (s == 2) {
#pragma unroll
          for (int sg = 0; sg < 8; ++sg) ag[sg] = rdf2(T_AGG + sg * 512 + ca);
          l16 = rdf2(T_CON + 1024 + ca); cof[0] = rdf2(T_CON + 512 + c0); cof[1] = rdf2(T_CON + 512 + c0 + 128);
        }
        if constexpr (s == 5) { pw1 = rdf2(T_CON + 1536 + ca); pw0 = rdf2(T_CON + 2048 + ca); l128 = rdf2(T_CON + 2560 + ca); l1k = rdf2(T_CON + 3072 + ca); cfa = rdf2(T_CON + 512 + ca); }
        if constexpr (s == 12) { if (w == 0) stflag(rws, epr, lane, flagb + FK_H * 128 + cj * 4); }
        if constexpr (s == 14) { if (w == 0) ld32a(pv, rws, fvo, flagb + FK_H * 128); }
        SB();
        auto work = [&](auto U) AI { constexpr int u = U.value;
          if constexpr (s < 2 && u >= 1 && u < 9) {
            constexpr int t = 8 * s + u - 1;
#pragma unroll
            for (int mt = 0; mt < 2; ++mt) st[mt] = cfma(lam[mt], st[mt], (f32x2){acc[t >> 2][mt][t & 3], acc[t >> 2][2 + mt][t & 3]});
            PIN2(st[0], st[1]);
          }
          if constexpr (s == 1 && u == 9) { wrf2(T_AGG + (4 * wm + g) * 512 + c0, st[0]); wrf2(T_AGG + (4 * wm + g) * 512 + c0 + 128, st[1]); }
          if constexpr (s == 2 && u >= 1 && u < 9) {
            constexpr int sg = u - 1, mt = sg & 1; const bool me = sg == seg0 + mt;
            Pf[mt].x = sel<float>(me, Gt.x, Pf[mt].x); Pf[mt].y = sel<float>(me, Gt.y, Pf[mt].y); Gt = cfma(l16, Gt, ag[sg]);
            PIN2(Gt, Pf[mt]);
          }
          if constexpr (s == 2 && u == 9) st64(rgr, (u32x2){f2u(sel<float>(hl, Gt.y, Gt.x)), epc}, ca * 2 + hl * 8, ci * 1024);
          if constexpr (s == 3 && u < 4) {
            const bf16x8 one = __builtin_bit_cast(bf16x8, (u32x4){0x3F803F80u, 0x3F803F80u, 0x3F803F80u, 0x3F803F80u});
            cs[u] = MFMA(one, wv[u][1], MFMA(one, wv[u][0], ((f32x4){0, 0, 0, 0}))); PIN(cs[u]);
          }
          if constexpr (s == 3 && u == 9)
            st64(rws, (u32x2){f2u(sel4(g & 1, g & 2, cs[0][0], cs[1][0], cs[2][0], cs[3][0])), epc}, (g < 3 ? g * 64 + wq * 16 + r15 : 192 + (wq & 1) * 16 + r15) * 8u, CG_OFF + (cj * 16 + ci) * 2048);
          if constexpr ((s == 3 && u >= 4 && u < 9) || (s == 4 && u < 5) || (s == 5 && u >= 1 && u < 7)) {
            constexpr int t = s == 3 ? u - 4 : s == 4 ? 5 + u : 9 + u;
            f32x2 v0 = cmulc(cof[0], (f32x2){acc[t >> 2][0][t & 3], acc[t >> 2][2][t & 3]}), v1 = cmulc(cof[1], (f32x2){acc[t >> 2][1][t & 3], acc[t >> 2][3][t & 3]}); PIN2(v0, v1);
            acc[t >> 2][0][t & 3] = v0.x; acc[t >> 2][2][t & 3] = v0.y; acc[t >> 2][1][t & 3] = v1.x; acc[t >> 2][3][t & 3] = v1.y;
          }
          if constexpr (s == 4 && u >= 6 && u < 10) {
#pragma unroll
            for (int q = 2 * u - 12; q < 2 * u - 10; ++q) { lbo[q] = (ci - 8 - 8 * hl + q) * 1024 + ca * 2; ld128a(lb[q], rgr, lbo[q], 0u); }
          }
          if constexpr (s == 5 && u == 8) {
            if (!wm) pw0 = (f32x2){1.f, 0.f};
            if (ci <= 8) l1k = (f32x2){0.f, 0.f};
            pw0 = cmul(pw0, (f32x2){sel<float>(hl, pw1.x, 1.f), sel<float>(hl, pw1.y, 0.f)}); pw1 = cmul(pw0, l16); PIN2(pw0, pw1);
          }
          if constexpr (s == 5 && (u == 7 || u == 9)) {
#pragma unroll
            for (int q = 2 * u - 14; q < 2 * u - 10; ++q) { lbe[q] = (int)lbo[q] < 0 ? 0u : epc; PIN(lbe[q]); }
          }
          if constexpr (s == 6 && u < 8) {
            PIN(lb[u]); gd |= (lb[u][1] ^ lbe[u]) | (lb[u][3] ^ lbe[u]); cr = cfma(l128, cr, (f32x2){u2f(lb[u][0]), u2f(lb[u][2])});
            PIN2(cr, gd);
          }
          if constexpr (s == 6 && u == 8) {
            if (__builtin_expect(anyl(gd != 0), 0)) {
              for (unsigned sp = 0; sp < SPIN_CAP; ++sp) {
                __builtin_amdgcn_s_sleep(2);
#pragma unroll
                for (int q = 0; q < 8; ++q) ld128a(lb[q], rgr, lbo[q], 0u);
                waitv<0>();
                if (!anyl(bad())) break;
              }
              cr = (f32x2){0, 0};
#pragma unroll
              for (int q = 0; q < 8; ++q) cr = cfma(l128, cr, (f32x2){u2f(lb[q][0]), u2f(lb[q][2])});
            }
            gd = 0;
            const auto sx_ = __builtin_amdgcn_permlane32_swap(f2u(cr.x), f2u(cr.x), false, false), sy_ = __builtin_amdgcn_permlane32_swap(f2u(cr.y), f2u(cr.y), false, false);
            cr = cfma(l1k, (f32x2){u2f(sx_[1]), u2f(sy_[1])}, (f32x2){u2f(sx_[0]), u2f(sy_[0])});
            PIN(cr);
          }
          if constexpr (s == 6 && u == 9) { st[0] = cmulc(cfa, cfma(pw0, cr, Pf[0])); PIN(st[0]); }
          if constexpr (s == 7 && u == 0) { st[1] = cmulc(cfa, cfma(pw1, cr, Pf[1]));
            const auto ex = __builtin_amdgcn_permlane16_swap(f2u(st[0].x), f2u(st[1].x), false, false), ey = __builtin_amdgcn_permlane16_swap(f2u(st[0].y), f2u(st[1].y), false, false);
            st[0] = (f32x2){u2f(ex[0]), u2f(ey[0])}; st[1] = (f32x2){u2f(ex[1]), u2f(ey[1])}; PIN2(st[0], st[1]); }
          if constexpr ((s == 7 && u >= 2 && u < 10) || (s == 8 && u >= 1 && u < 9)) {
            constexpr int t = s == 7 ? u - 2 : 7 + u;
            if constexpr (t & 1) { so[0] = st[0]; so[1] = st[1]; }
#pragma unroll
            for (int mt = 0; mt < 2; ++mt) st[mt] = cfma((f32x2){lam[mt].x, -lam[mt].y}, st[mt], (f32x2){acc[t >> 2][mt][t & 3], acc[t >> 2][2 + mt][t & 3]});
            if constexpr (t & 1) {
#pragma unroll
              for (int mt = 0; mt < 2; ++mt) { ob[t >> 3][mt][0][(t >> 1) & 3] = pk2(so[mt].x, st[mt].x); ob[t >> 3][mt][1][(t >> 1) & 3] = pk2(so[mt].y, st[mt].y); PIN2(ob[t >> 3][mt][0], ob[t >> 3][mt][1]); }
            }
            PIN2(st[0], st[1]);
          }
          if constexpr (s >= 7 && s < 11 && u == 10) {
            constexpr int hf = (s - 7) >> 1, mt = (s - 7) & 1;
#pragma unroll
            for (int pt = 0; pt < 2; ++pt) st128(rws, ob[hf][mt][pt], hofs(hf, mt), H_OFF + ci * H_PANEL + (2 * cj + pt) * 16384);
          }
          if constexpr (s >= 13 && u < 4) *(LAS u32x4*)(size_t)((s - 12) * 32768 + hofs(u >> 1, u & 1)) = ob[u >> 1][u & 1][s - 13];
          if constexpr ((s == 9 || s == 10) && u >= 1 && u < 9) { constexpr int t = 8 * (s - 9) + u - 1; ac2[t >> 2][t & 3] = (f32x4){zero, zero, zero, zero}; PIN(ac2[t >> 2][t & 3]); }
          if constexpr (s == 9 && u == 0) { if (ci == 15 && w == 1) stflag(rws, epc, lane, EPC_OFF + cj * 128); }
          if constexpr ((s == 11 || s == 12) && u == 9) ld128a(rgv[s - 11], rws, tq * 16u + (s - 11) * 4096, rgb);
          if constexpr (s == 14 && u == 5) {
            auto rbad = [&]() AI { PIN2(rgv[0], rgv[1]); return ((rgv[0][1] ^ epr) | (rgv[0][3] ^ epr) | (rgv[1][1] ^ epr) | (rgv[1][3] ^ epr)) != 0; };
            if (__builtin_expect(anyl(rbad()), 0))
              for (unsigned sp = 0; sp < SPIN_CAP; ++sp) { __builtin_amdgcn_s_sleep(2);
                ld128a(rgv[0], rws, tq * 16u, rgb); ld128a(rgv[1], rws, tq * 16u + 4096, rgb);
                waitv<0>(); if (!anyl(rbad())) break; }
          }
          if constexpr (s == 14 && u == 7) {
            wrf(T_ROW + (tq & 1) * 1024 + (tq >> 1) * 4, u2f(rgv[0][0])); wrf(T_ROW + (tq & 1) * 1024 + 512 + (tq >> 1) * 4, u2f(rgv[0][2]));
            wrf2(T_XA + tq * 8, (f32x2){u2f(rgv[1][0]), u2f(rgv[1][2])});
          }
          if constexpr ((s == 11 || s == 12) && (u & 1) && u < 8) { constexpr int q = 4 * (s - 11) + (u >> 1); ld128a(cg[q], rws, cgo, CG_OFF + (cj * 16 + q) * 2048); }
          if constexpr (s == 14 && u < 4) {
#pragma unroll
            for (int q = 2 * u; q < 2 * u + 2; ++q) { PIN(cg[q]); gd |= (cg[q][1] ^ epc) | (cg[q][3] ^ epc); c0s += u2f(cg[q][0]); c1s += u2f(cg[q][2]); }
            PIN(gd); PIN(c0s); PIN(c1s);
          }
          if constexpr (s == 14 && u == 4) {
            if (__builtin_expect(anyl(gd != 0), 0)) {
              for (unsigned sp = 0; sp < SPIN_CAP; ++sp) { __builtin_amdgcn_s_sleep(2);
#pragma unroll
                for (int q = 0; q < 8; ++q) ld128a(cg[q], rws, cgo, CG_OFF + (cj * 16 + q) * 2048);
                waitv<0>(); if (!anyl(gbad())) break; }
              c0s = 0.f; c1s = 0.f;
#pragma unroll
              for (int q = 0; q < 8; ++q) { c0s += u2f(cg[q][0]); c1s += u2f(cg[q][2]); }
            }
            c0s = sum32(c0s); c1s = sum32(c1s); PIN(c0s); PIN(c1s);
          }
          if constexpr (s == 14 && u == 6) {
            const float v = sel<float>(hl, c1s, c0s), hi = rbf(v), bb = rdf(T_B1 + (2 * cp + hl) * 4);
            *(LAS u32x4*)(size_t)(T_WA + (2 * cp + hl) * 16) = (u32x4){pk2(hi, v - hi), pk2(hi, bb), pk2(bb, 0.f), 0u};
          }
        };
        if constexpr (s < 13) { sfor<11>([&](auto U) AI { G2(IC<s + 3>{}, U); SB(); work(U); SB(); }); if constexpr (s < 12) nexts(); }
        if constexpr (s == 12) {
#pragma unroll
          for (int q = 0; q < 4; ++q) vb[q] = va[q];
          ta = t3a; tb = t3b; sl = 32768; sx = 0; next3(); }
        if constexpr (s >= 13) { sfor<4>([&](auto U) AI { G(IC<U.value + 4>{}); SB(); work(U); SB(); }); sl = nq(sl); next3(); asm volatile("s_nop 4"); }
        if constexpr (s == 14) sfor<4>([&](auto U) AI { G(IC<U.value + 4>{}); SB(); work(IC<U.value + 4>{}); SB(); });
      });
      waitl0(); waitv<8>();
      if (w == 0) check_flags(pv, rws, fvo, flagb + FK_H * 128, lane, 8 * half, 8 * half + 8, epr);
      bar();
    }
    f32x4 (&acc)[4][4] = ac2;
    bf16x8 fs[4][2], fr[2][4][2];
    const int kap = 4 * (g & 1) + q2, hf = p >> 1;
    const unsigned wb = 16384 + (4 * g + q2) * 256 + p * 8, hb = (g >> 1) * 4096 + wm * 2048 + hf * 1024 + (8 * (g & 1) + q2) * 16 + (p & 1) * 8;
    const unsigned cb3[4] = {hb + 64u * hf, hb + 64u * (1 ^ hf), 16384 + wn * 8192u + r15 * 128 + ((g ^ (r15 >> 1)) * 16), 16384 + wn * 8192u + r15 * 128 + (((4 + g) ^ (r15 >> 1)) * 16)};
    const unsigned cb4[5] = {wm * 4096u + lane * 16, wb + (((4 * wn + 0) ^ kap) * 32), wb + (((4 * wn + 1) ^ kap) * 32), wb + (((4 * wn + 2) ^ kap) * 32), wb + (((4 * wn + 3) ^ kap) * 32)};
    unsigned ba[5];
    sl = 32768;
#pragma unroll
    for (int b = 0; b < 4; ++b) { ba[b] = sl + cb3[b]; PIN(ba[b]); }
    auto rd3 = [&](int X, int r) AI {
      if (r >= 4 && r < 12) fr[X][(r - 4) >> 1][r & 1] = rd128(ba[2 + (r & 1)] + ((r - 4) >> 1) * 2048);
      else { const int a = r < 4 ? 0 : (r - 8) >> 2, ks = (r >> 1) & 1, h = r & 1; sethalf(fs[a][ks], h, rtr(ba[h] + ks * 8192 + a * 256)); } };
    auto rd4 = [&](int X, int r) AI {
      if (r >= 4 && r < 12) fr[X][(r - 4) >> 1][r & 1] = rd128(ba[0] + (r & 1) * 8192 + ((r - 4) >> 1) * 1024);
      else { const int b = r < 4 ? 0 : (r - 8) >> 2, ks = (r >> 1) & 1, h = r & 1; sethalf(fs[b][ks], h, rtr(ba[1 + b] + ks * 8192 + h * 4096)); } };
    auto mm3 = [&](int X, int q, int) AI { const int a = q >> 3, b = (q >> 1) & 3, ks = q & 1; MMA(acc[a][b], fr[X][b][ks], fs[a][ks]); };
    auto mm4 = [&](int X, int q, int SL) AI { const int b = q >> 3, a = (q >> 1) & 3, ks = q & 1;
      if (SL < 0) acc[a][b] = MFMA(fs[b][ks], fr[X][a][ks], acc[a][b]); else MMA(acc[a][b], fs[b][ks], fr[X][a][ks]); };
    auto up3 = [&](int b) AI { if (b == 0) sl = nq(sl); ba[b] = sl + cb3[b]; PIN(ba[b]); };
    auto up4 = [&](int b) AI { if (b == 0) sl = nq(sl); ba[b] = sl + cb4[b]; PIN(ba[b]); };
    period<32, 16, 0, true, 24, LASTR, 4>(rd3, mm3, up3); waitv<4>(); bar();
    for (int s = 0; s < 7; ++s) { period<32, 16, 1, false, 24, LASTR, 4>(rd3, mm3, up3); waitv<0>(); bar(); period<32, 16, 0, false, 24, LASTR, 4>(rd3, mm3, up3); bar(); }
    float gr[4];
#pragma unroll
    for (int a = 0; a < 4; ++a) gr[a] = rdf(T_ROW + 1536 + (64 * wm + 16 * a + r15) * 4);
    period<32, 16, 1, false, 24, LASTR, 5>(rd3, mm3, up4); bar();
    {
      auto mmg = [&](int X, int q, int SL) AI { if (SL < 16) mm3(X, q, SL); else mm4(X, q, SL);
        constexpr int ord[16] = {0, 4, 1, 5, 8, 9, 2, 12, 13, 6, 10, 14, 3, 7, 11, 15};
        const int t = ord[SL >> 1], e = 2 * (SL & 1); SB();
        acc[t >> 2][t & 3][e] *= gr[t >> 2]; acc[t >> 2][t & 3][e + 1] *= gr[t >> 2]; PIN(acc[t >> 2][t & 3]); };
      period<32, 16, 0, false, 24, LASTR, 5>(rd4, mmg, up4); bar();
    }
    static_assert(MOVE & 1, "the remaining Q4 periods run in pairs");
    for (int s = 1; s < n4; s += 2) { period<32, 16, 1, false, 24, LASTR, 5>(rd4, mm4, up4); bar(); period<32, 16, 0, false, 24, LASTR, 5>(rd4, mm4, up4); bar(); }
    MPAD();
    if (half) {
      const unsigned pb = ((sl + 65536u + wm * 32768u) & 131071u) + wn * 16384 + g * 256;
      sfor<16>([&](auto M) AI { constexpr int m = M.value, a = m & 3, b = m >> 2;
        if constexpr (m < 8) { mm4(0, 16 + 2 * m, -1); mm4(0, 17 + 2 * m, -1); }
        SB();
        *(LAS f32x4*)(size_t)(pb + (a * 4 + b) * 1024 + ((r15 ^ (4 * b + g)) * 16)) = acc[a][b];
        SB(); });
      waitl0();
    } else {
#pragma unroll
      for (int q = 16; q < 32; ++q) mm4(0, q, -1);
#pragma unroll
      for (int bp = 0; bp < 2; ++bp)
#pragma unroll
        for (int a = 0; a < 4; ++a)
          st128(rws, (u32x4){pk2(acc[a][2 * bp][0], acc[a][2 * bp][1]), pk2(acc[a][2 * bp][2], acc[a][2 * bp][3]), pk2(acc[a][2 * bp + 1][0], acc[a][2 * bp + 1][1]), pk2(acc[a][2 * bp + 1][2], acc[a][2 * bp + 1][3])},
                (a * 2 + bp) * 1024 + g * 256 + ((r15 ^ (8 * wn + 4 * bp + g)) * 16), PART_OFF + (ci * 8 + cnt) * 32768 + wq * 8192);
      waitv<0>();
    }
    bar();
    if (!half && w == 0) stflag(rws, epr, lane, flagb + FK_P * 128 + cnt * 4);
  } else {
    constexpr bool isx = !PROFX; (void)isx;
    unsigned epr, pv, t3a, t3b, sa2h, cb[8], ba[8]; const char *pp0, *pp1;
    f32x4 ac1[28];
    {
      u32x4 xr[2][2], gq[2];
      ra = rx; rb = mk(sel<const u16*>(wq & 1, bim, bre), 1024 * 2048);
#pragma unroll
      for (int q = 0; q < 4; ++q) { va[q] = (32 * (q & 1) + 16 * hl + 4 * (q >> 1) + ((lane >> 3) & 3)) * 2048 + (((lane & 7) ^ ((4 * q + g) & 7)) * 16) - 1024 * q; vb[q] = vnat(q); }
      auto src1 = [&](int s) AI { const int k = s ^ (s < 2 ? 0 : s < 4 ? (rot & 1) : rot); sa = (ci * 128 + 64 * (wq >> 1) + 8 * (wq & 1)) * 2048 + k * 128; sb = (cj * 64 + 32 * (wq >> 1)) * 2048 + k * 128; sl = ((s + 3) & 3) * 32768; };
      src1(0); ival(G, nowork);
      SB();
      setup2(); table3(t3a, t3b);
      {
        const int f = ((q2 >> 1) & 1) | ((g & 1) << 1);
        const unsigned nb = wm * 8192u + r15 * 128, n0 = (g ^ (r15 >> 1)) * 16, n1 = ((4 + g) ^ (r15 >> 1)) * 16, wk = (g >> 1) * 7168u + p * 8;
        cb[0] = nb + wn * 4096 + n0; cb[1] = nb + wn * 4096 + n1; cb[2] = nb + (1 - wn) * 4096 + n0; cb[3] = nb + (1 - wn) * 4096 + n1;
        cb[4] = wk + (8 * (g & 1) + q2) * 128 + (((2 * wn) ^ f) * 32); cb[5] = wk + (8 * (g & 1) + q2) * 128 + (((2 * wn + 1) ^ f) * 32);
        cb[6] = wk + 6144 + (8 * (g & 1) + q2) * 64 + (g & 1) * 32; cb[7] = wk + 6144 + (8 * (g & 1) + q2) * 64 + ((g & 1) ^ 1) * 32;
#pragma unroll
        for (int b = 0; b < 8; ++b) { ba[b] = cb[b] + (b < 4 ? 131072u : 0u); PIN2(cb[b], ba[b]); }
        pp0 = lane < 32 ? (const char*)sel<const u16*>(lane & 16, cim, cre) + ((bid * 4 + wq) * 16 + r15) * 128 : (const char*)w2 + ((bid * 4 + wq) * 56 + r15) * 128;
        pp1 = (const char*)w2 + ((bid * 4 + wq) * 56 + (lane < 40 ? 16 + lane : 55)) * 128;
        PIN2(pp0, pp1);
#pragma unroll
        for (int m = 0; m < 28; ++m) { ac1[m] = (f32x4){0, 0, 0, 0}; PIN(ac1[m]); }
      }
      src1(1); ival(G, nowork);
      waitv<8>(); bar();
      unsigned pdt, plr, pli, bq, oq; f32x2 lp; float lr, li;
      const int tc = tq < 224 ? tq : 223;
      ld32a(epr, rws, 0u, flagb + FK_EP * 128); ld128g(gq[0], gw, lane * 16u); ld128g(gq[1], gw + 512, lane * 16u);
      ld16p(pdt, ldt + 64 * cj, lane * 2u); ld16p(plr, lre + 64 * cj, lane * 2u); ld16p(pli, lim + 64 * cj, lane * 2u);
      ld16p(bq, b1, (224 * cj + (tc < 192 ? ((cj & 1) ? 32 : 0) + tc : ((cj & 1) ? -192 : 0) + tc)) * 2u);
      ld16p(oq, sel<const u16*>(wq < 2, b2, dsk), (cnt * 128 + (tq & 127)) * 2u);
      PH(1);
      src1(2); ival(G, nowork);
      src1(3); ival(G, nowork);
      float sv[6] = {0, 0, 0, 0, 0, 0}, a1, a2, a3, mean, sd, rstd, qv, gg, kq, rr;
      auto E1 = [](int s) constexpr { return (s == 1 || s == 2) ? 2 : s == 11 ? 1 : 0; };
      sfor<12>([&](auto S) AI { constexpr int s = S.value;
        if constexpr (s == 0) waitv<16 + 8>(); else waitv<16 + E1(s - 2) + E1(s - 1)>();
        bar();
        src1(s + 4); ival(G, [&](auto U) AI { constexpr int u = U.value;
          if constexpr ((s == 1 || s == 2) && (u == 2 || u == 5)) ld128p(xr[s - 1][u / 3], rx, lane * 16u, (ci * 128 + 8 * cj + 2 * wq + s - 1) * 2048 + (u / 3) * 1024);
          if constexpr (s == 1 && u == 0) { PIN2(pdt, plr); PIN(pli); lr = bf2f(plr); li = bf2f(pli); const float dt = rbf(fexp(bf2f(pdt))); lp = (f32x2){fexp(dt * lr), dt * li}; PIN(lp); }
          if constexpr (s == 1 && u == 3) { PIN2(bq, oq); wrf(T_B1 + tq * 4, bf2f(bq)); wrf(T_OUT + tq * 4, bf2f(oq)); PIN(epr); epr = __builtin_amdgcn_readfirstlane(epr) + 1u; }
          if constexpr (s == 1 && u == 6) { kq = __builtin_rintf(lp.y * 0.63661977236f);
            rr = __builtin_fmaf(kq, -1.5707962513f, lp.y); rr = __builtin_fmaf(kq, -7.5497894159e-08f, rr); rr = __builtin_fmaf(kq, -5.3903029534e-15f, rr); PIN2(kq, rr); }
          if constexpr (s == 2 && u == 0) { const float z = rr * rr;
            a1 = __builtin_fmaf(rr * z, __builtin_fmaf(z, __builtin_fmaf(z, __builtin_fmaf(z, 2.7183114939e-06f, -1.9839334836e-04f), 8.3333293858e-03f), -1.6666666642e-01f), rr); PIN(a1); }
          if constexpr (s == 2 && u == 3) { const float z = rr * rr;
            a2 = __builtin_fmaf(z, __builtin_fmaf(z, __builtin_fmaf(z, __builtin_fmaf(z, 2.4390448796e-05f, -1.3886763775e-03f), 4.1666623324e-02f), -4.9999999726e-01f), 1.f); PIN(a2); }
          if constexpr (s == 2 && u == 6) { const int q = (int)kq; const float sa_ = sel<float>(q & 1, a2, a1), ca_ = sel<float>(q & 1, a1, a2);
            lp = (f32x2){lp.x * (((q + 1) & 2) ? -ca_ : ca_), lp.x * ((q & 2) ? -sa_ : sa_)}; PIN(lp); wrf2(T_CON + lane * 8, lp); }
          if constexpr (s == 3 && u == 0) { const float nr = lp.x - 1.f, ni = lp.y, den = 2.f * __builtin_amdgcn_rcpf(lr * lr + li * li);
                                            wrf2(T_CON + 512 + lane * 8, (f32x2){(nr * lr + ni * li) * den, (ni * lr - nr * li) * den}); }
          if constexpr ((s == 3 && u >= 2) || (s == 4 && u >= 4)) { constexpr int q = s == 3 ? u - 2 : u + 2; lp = cmul(lp, lp); PIN(lp);
            if constexpr (q >= 3 && q < 7) wrf2(T_CON + (q - 1) * 512 + lane * 8, lp);
            if constexpr (q == 9) wrf2(T_CON + 3072 + lane * 8, lp); }
          if constexpr ((s == 4 && u < 2) || (s >= 5 && s < 9 && !(u & 1) && 4 * (s - 5) + 2 + (u >> 1) < 16)) {
            constexpr int id = s == 4 ? u : 4 * (s - 5) + 2 + (u >> 1), r = id >> 3, e = id & 7;
            PIN2(xr[r][e >> 2], gq[e >> 2]);
            const unsigned v = xr[r][e >> 2][e & 3], gv = gq[e >> 2][e & 3];
            const float x0 = bf2f(v & 0xffffu), x1 = u2f(v & 0xffff0000u);
            sv[r] += x0 + x1; sv[2 + r] = __builtin_fmaf(x1, x1, __builtin_fmaf(x0, x0, sv[2 + r]));
            sv[4 + r] = __builtin_fmaf(x1, u2f(gv & 0xffff0000u), __builtin_fmaf(x0, bf2f(gv & 0xffffu), sv[4 + r]));
            PIN2(sv[r], sv[2 + r]); PIN(sv[4 + r]);
          }
          if constexpr (s == 9 && u < 4) {
#pragma unroll
            for (int q = 0; q < 6; ++q) sv[q] = u == 0 ? dppadd<0xB1>(sv[q]) : u == 1 ? dppadd<0x4E>(sv[q]) : u == 2 ? dppadd<0x141>(sv[q]) : dppadd<0x140>(sv[q]);
            PIN2(sv[0], sv[1]); PIN2(sv[2], sv[3]); PIN2(sv[4], sv[5]);
          }
          if constexpr (s == 9 && u == 5) { const bool r1 = lane & 4; a1 = sum16(sel<float>(r1, sv[1], sv[0])); a2 = sum16(sel<float>(r1, sv[3], sv[2])); a3 = sum16(sel<float>(r1, sv[5], sv[4])); PIN2(a1, a2); PIN(a3); }
          if constexpr (s == 10 && u == 0) { a1 = sum32(a1); a2 = sum32(a2); a3 = sum32(a3); PIN2(a1, a2); PIN(a3); }
          if constexpr (s == 10 && u == 2) { mean = a1 * (1.f / 1024.f); const float var = __builtin_fmaxf(a2 * (1.f / 1024.f) - mean * mean, 0.f) + 1e-5f; rstd = __builtin_amdgcn_rsqf(var); sd = var * rstd;
                                             PIN2(mean, rstd); PIN(sd); }
          if constexpr (s == 10 && u == 4) { qv = 1.f + fexp(__builtin_fminf(a3, 60.f)); gg = __builtin_amdgcn_rcpf(1.f + fexp(__builtin_fminf(-a3, 80.f))); PIN2(qv, gg); }
          if constexpr (s == 11 && u == 0) { a1 = sel4(lane & 1, lane & 2, -2.302208198f * rstd, -0.1029432397f * rstd * rstd * rstd, qv * sd, gg); PIN(a1); }
          if constexpr (s == 11 && u == 2) { const float mh = rbf(mean), sh = rbf(sd);
                                             a2 = sel4(lane & 1, lane & 2, u2f(pk2(-mh, -mh)), u2f(pk2(mh - mean, sh)), u2f(pk2(sd - sh, 0.f)), 0.f); PIN(a2); }
          if constexpr (s == 11 && u == 5) { if (lane < 16) st64(rws, (u32x2){f2u(sel<float>(lane & 8, a2, a1)), epr}, (lane >> 3) * 4096u + (lane & 7) * 8, rgb + (8 * cj + 2 * wq) * 32); }
        });
      });
      rb = mk((const char*)w1 - 4096, 1024 * 7168 + 4096);
#pragma unroll
      for (int q = 0; q < 4; ++q) va[q] = vb[q];
      sx = 0; nexts();
      const unsigned sb0 = sb, sa0 = sa; nexts();
      const unsigned sb1 = sb, sa1 = sa; nexts();
      const unsigned sa2 = sa;
      waitv<16 + 1>(); bar();
      sa = sa0; sfor<4>([&](auto U) AI { G2(IC<0>{}, U); });
      sa = sa1; sfor<4>([&](auto U) AI { G2(IC<1>{}, U); });
      waitv<16 + 1>(); bar();
      sb = sb0; sfor<7>([&](auto U) AI { G2(IC<0>{}, IC<U.value + 4>{}); });
      sa = sa2; sfor<4>([&](auto U) AI { G2(IC<2>{}, U); });
      waitv<19>(); bar();
      sb = sb1; sfor<7>([&](auto U) AI { G2(IC<1>{}, IC<U.value + 4>{}); });
      ta = t3a; tb = t3b; sx = 2; nexts(); sa2h = sa; next3();
    }
    {
      {
        bf16x8 fa[2][4][2], fb[8][2];
        unsigned xn = 98304, wnx = 32768;
        setm0(T_DUM);
        auto rd = [&](int X, int r) AI {
          if (r >= 4 && r < 12) { const int a = (r - 4) >> 1, ks = r & 1; fa[X][a][ks] = rd128(ba[(a >> 1) * 2 + ks] + (a & 1) * 2048); }
          else { const int b = r < 4 ? 0 : (r - 8) >> 2, ks = (r >> 1) & 1, h = r & 1;
            sethalf(fb[b][ks], h, rtr(b < 6 ? ba[4 + (b & 1)] + (b >> 1) * 2048 + ks * 14336 + h * 512 : ba[b] + ks * 14336 + h * 256)); } };
        auto mm = [&](int X, int q, int) AI { const int b = q < 48 ? q >> 3 : 6 + ((q - 48) >> 2), a = (q >> 1) & (q < 48 ? 3 : 1), ks = q & 1, m = (a < 2 ? 8 * a : 4 + 6 * a) + b;
          MMA(ac1[m], fb[b][ks], fa[X][a][ks]); };
        auto up = [&](int b) AI { if (b < 8) { ba[b] = cb[b] + sel<unsigned>(b < 4, xn, wnx); PIN(ba[b]); }
                                  else { wnx = (wnx == 65536u) ? 0u : wnx + 32768u; xn = (xn == 131072u) ? 98304u : xn + 16384u; } };
        waitl0(); waitv<11>(); bar();
        PH(2);
        period<56, 16, 0, true, 40, 49, 9>(rd, mm, up); waitv<0>(); bar();
        const int sp = bid % 4;
        for (int s = 0; s < 7; ++s) {
          period<56, 16, 1, false, 40, 49, 9>(rd, mm, up); bar();
          if (s == sp) { pfg(pp0); pfg(pp1); }
          period<56, 16, 0, false, 40, 49, 9>(rd, mm, up); bar();
        }
        period<56, 16, 1, false, 40, 49, 9>(rd, mm, up); bar();
        PH(3);
        setup3(); sl = 0;
        setm0(98304 + wq * 4096); sfor<4>([&](auto U) AI { dma1<U.value * 1024>(rws, va[0], sa2h); });
        ival(G, [&](auto U) AI { mm(1, 40 + 2 * U.value, 0); mm(1, 41 + 2 * U.value, 0); });
#pragma unroll
        for (int a = 0; a < 4; ++a) fa[0][a][0] = rd128(g ? T_ZERO : T_XA + (64 * wm + 16 * (a ^ (2 * wn)) + r15) * 16);
#pragma unroll
        for (int b = 0; b < 8; ++b) fb[b][0] = rd128(T_WA + ((b < 6 ? (b >> 1) * 64 + wn * 32 + (b & 1) * 16 : 96 + b * 16) + r15) * 16);
        SB();
        sl = nq(sl); next3();
        bar();
        ival(G, [&](auto U) AI { constexpr int u = U.value;
#pragma unroll
          for (int q = 8 * u; q < 8 * u + 8; q += 2) if (q < 56) mm(0, q, 0); });
        sl = nq(sl); next3();
      }
      float k0[4], k1[4], qq[4];
#pragma unroll
      for (int a = 0; a < 4; ++a) { const unsigned ro = T_ROW + (64 * wm + 16 * (a ^ (2 * wn)) + r15) * 4; k0[a] = rdf(ro); k1[a] = rdf(ro + 512); qq[a] = rdf(ro + 1024); }
      u32x4 hw[4]; float t[8];
      auto lv0 = [](int s) constexpr { return s < 1 ? 0 : s < 6 ? 13 * (s - 1) : (64 + 8 * (s - 6) < 224 ? 64 + 8 * (s - 6) : 224); };
      auto st3 = [](int s) constexpr { return (s == 5 || s == 13 || s == 21) ? 4 : s == 25 ? 2 : 0; };
      sfor<36>([&](auto S1) AI { constexpr int s = S1.value + 1;
        if constexpr (s == 7) waitv<8>();
        else waitv<16 + st3(s - 2) + st3(s - 1)>();
        if constexpr (s == 12 || s == 20 || s == 28 || s == 36) {
          if (w == 4) check_flags(pv, rws, fvo, flagb + (FK_M1 + (s - 12) / 8) * 128, lane, s == 36 ? 0 : 8 * half, 8 * half + 8, epr); }
        SB(); bar();
        if constexpr (s == 16) { PH(4); }
        if constexpr (s == 7 || s == 16 || s == 24 || s == 28) { if (w == 5) stflag(rws, epr, lane, flagb + (FK_M1 + (s - 4) / 8) * 128 + cj * 4); }
        if constexpr (s == 10 || s == 18 || s == 26 || s == 34) { if (w == 4) ld32a(pv, rws, fvo, flagb + (FK_M1 + (s - 10) / 8) * 128); }
#define UU(e) ac1[m0 + ((e) >> 2)][(e) & 3]
#define LVL(expr) do { _Pragma("unroll") for (int e = 4 * hh; e < 4 * hh + 4; ++e) t[e] = (expr); PIN2(t[4 * hh], t[4 * hh + 1]); PIN2(t[4 * hh + 2], t[4 * hh + 3]); } while (0)
        auto hlev = [&](auto LL) AI { constexpr int L = LL.value, v = L >> 4, l = (L >> 1) & 7, hh = L & 1, a = v & 3, m0 = (a < 2 ? 8 * a : 4 + 6 * a) + 2 * (v >> 2);
          if constexpr (l == 0) LVL(UU(e) * UU(e));
          if constexpr (l == 1) LVL(__builtin_fmaf(t[e], k1[a], k0[a]));
          if constexpr (l == 2) LVL(UU(e) * t[e]);
          if constexpr (l == 3) LVL(__builtin_amdgcn_exp2f(t[e]));
          if constexpr (l == 4) LVL(__builtin_fmaf(t[e], qq[a], qq[a]));
          if constexpr (l == 5) LVL(__builtin_amdgcn_rcpf(t[e]));
          if constexpr (l == 6) LVL(UU(e) * t[e]);
          if constexpr (l == 7) { hw[a][2 * hh] = pk2(t[4 * hh], t[4 * hh + 1]); hw[a][2 * hh + 1] = pk2(t[4 * hh + 2], t[4 * hh + 3]); PIN(hw[a]); }
        };
        ival(G, [&](auto U) AI { constexpr int u = U.value, n = lv0(s + 1) - lv0(s), i0 = (n * u + 7) / 8, i1 = (n * (u + 1) + 7) / 8;
          if constexpr (i0 < i1) hlev(IC<lv0(s) + i0>{});
          if constexpr (i0 + 1 < i1) hlev(IC<lv0(s) + i0 + 1>{});
        });
#pragma unroll
        for (int q = 0; q < st3(s); ++q) st128(rws, hw[q], lane * 16u + (4 * wm + (q ^ (2 * wn))) * 1024, ci * HID_PANEL + kb_of(cj, s < 25 ? 2 * (s >> 3) + wn : 6) * 8192);
        sl = nq(sl);
        if constexpr (s < 11) next3(); else nexts();
        if constexpr (s == 11) {
          rb = mk(w2, 3584 * 2048);
#pragma unroll
          for (int q = 0; q < 4; ++q) vb[q] = (4 * q + g) * 2048 + ((((r15 >> 1) ^ (4 * (q & 1) + g)) * 2) + (lane & 1)) * 16 - 1024 * q;
        }
      });
    }
    static_assert(MOVE >= 0 && MOVE <= 3, "");
    sfor<3 - MOVE>([&](auto) AI { waitv<16>(); bar(); ival(G, nowork); sl = nq(sl); nexts(); });
    if (half) sfor<2 * MOVE>([&](auto) AI { waitv<16>(); bar(); ival(G, nowork); sl = nq(sl); nexts(); });
    waitv<16>(); bar();
    if (half) {
      sfor<8>([&](auto U) AI { constexpr int u = U.value; if constexpr ((u & 3) == 0) setm0(sl + wq * 8192 + u * 1024);
        dma<(u & 3) * 1024>(rx, (4u * (u & 3) + g) * 2048 + r15 * 16 - (u & 3) * 1024, (ci * 128 + 32 * wq + 16 * (u >> 2)) * 2048 + cnt * 256); });
      waitv<8 + 8>();
    } else waitv<8>();
    bar();
    ld32a(pv, rws, fvo, flagb + FK_P * 128);
    if (half) waitv<9>(); else waitv<0>();
    bar();
    auto getp = [&]() AI {
      sfor<8>([&](auto U) AI { constexpr int u = U.value; if constexpr ((u & 3) == 0) setm0(nq(sl) + wq * 8192 + u * 1024);
        dma1<(u & 3) * 1024>(rws, lane * 16u, PART_OFF + (ci * 8 + cnt) * 32768 + wq * 8192 + (u >> 2) * 4096); }); };
    waitv<0>(); PIN(pv);
    const bool early = half && !anyl(lane == cnt && pv != epr);
    if (early) getp(); else ld32a(pv, rws, fvo, flagb + FK_P * 128);
    bar();
    if (half) {
      if (!early) { waitv<0>(); check_flags(pv, rws, fvo, flagb + FK_P * 128, lane, cnt, cnt + 1, epr); getp(); }
      if (cnt == 0 && w == 4) stflag(rws, epr, lane, flagb + FK_EP * 128);
    }
    PH(5);
    waitv<0>(); bar();
  }
  if (half) {
    const int bg = r31 & 15, sw = (r31 >> 4) * 8 + (bg >> 3) * 4 + (bg & 3);
    const unsigned pa = ((sl + 65536u + (w >> 2) * 32768u) & 131071u) + (r31 >> 4) * 16384 + (wq * 4 + (bg >> 2)) * 1024 + (bg & 3) * 256;
    const unsigned pp = nq(sl) + ((w >> 2) * 2 + (r31 >> 4)) * 8192 + (wq * 2 + (bg >> 3)) * 1024 + (bg & 3) * 256 + ((bg >> 2) & 1) * 8;
    const f32x4 bv = rdf4(T_OUT + r31 * 16), dv = rdf4(T_OUT + 512 + r31 * 16);
    f32x4 av[8]; u32x2 pw[8], xw[8]; float gg[8];
#pragma unroll
    for (int u = 0; u < 8; ++u) {
      const int row = 2 * u + hl;
      av[u] = rdf4(pa + ((row ^ bg) * 16)); pw[u] = *(LAS u32x2*)(size_t)(pp + ((row ^ sw) * 16));
      xw[u] = *(LAS u32x2*)(size_t)(sl + (16 * w + hl) * 256 + r31 * 8 + u * 512); gg[u] = rdf(T_ROW + 1536 + (16 * w + hl) * 4 + u * 8);
    }
    SB();
#pragma unroll
    for (int u = 0; u < 8; ++u) {
      const f32x4 pf = {bf2f(pw[u][0] & 0xffffu), u2f(pw[u][0] & 0xffff0000u), bf2f(pw[u][1] & 0xffffu), u2f(pw[u][1] & 0xffff0000u)};
      const f32x4 xf = {bf2f(xw[u][0] & 0xffffu), u2f(xw[u][0] & 0xffff0000u), bf2f(xw[u][1] & 0xffffu), u2f(xw[u][1] & 0xffff0000u)};
      f32x4 r;
#pragma unroll
      for (int e = 0; e < 4; ++e) r[e] = (av[u][e] + pf[e]) + __builtin_fmaf(gg[u], __builtin_fmaf(xf[e], dv[e], -bv[e]), bv[e]);
      st128<OAUX>(mk(out, 8u << 20), __builtin_bit_cast(u32x4, r), (unsigned)((16 * w + hl + 2 * u) * 4096 + r31 * 16), ci * 128 * 4096 + cnt * 512);
    }
  }
}

extern "C" void kernel_launch(void* const* d_in, const int* in_sizes, int n_in, void* d_out, int out_size, void* d_ws, size_t ws_size, hipStream_t stream) {
  (void)in_sizes; (void)n_in; (void)out_size; (void)ws_size;
  (void)hipFuncSetAttribute((const void*)fused_kernel, hipFuncAttributeMaxDynamicSharedMemorySize, LDS_BYTES);
  fused_kernel<<<256, 512, LDS_BYTES, stream>>>((const u16*)d_in[0], (const u16*)d_in[1], (const u16*)d_in[2], (const u16*)d_in[3], (const u16*)d_in[4],
      (const u16*)d_in[5], (const u16*)d_in[6], (const u16*)d_in[7], (const u16*)d_in[8], (const u16*)d_in[10], (const u16*)d_in[11], (const u16*)d_in[12],
      (const u16*)d_in[13], (const u16*)d_in[14], (char*)d_ws, (float*)d_out);
}
```

```cpp
#include <hip/hip_runtime.h>
#include <utility>

typedef unsigned short u16;
typedef __bf16 bf16x2 __attribute__((ext_vector_type(2)));
typedef __bf16 bf16x4 __attribute__((ext_vector_type(4)));
typedef __bf16 bf16x8 __attribute__((ext_vector_type(8)));
typedef float f32x2 __attribute__((ext_vector_type(2)));
typedef float f32x4 __attribute__((ext_vector_type(4)));
typedef float f32x16 __attribute__((ext_vector_type(16)));
typedef unsigned u32x2 __attribute__((ext_vector_type(2)));
typedef unsigned u32x4 __attribute__((ext_vector_type(4)));
typedef __amdgpu_buffer_rsrc_t rsrc_t;
#define LAS __attribute__((address_space(3)))
#define DI __device__ __forceinline__
#define AI __attribute__((always_inline))
#define SB() __builtin_amdgcn_sched_barrier(0)
#define PIN(x) asm volatile("" : "+v"(x))
#define MFMA(a, b, c) __builtin_amdgcn_mfma_f32_16x16x32_bf16(a, b, c, 0, 0, 0)
#define MMA(c, a, b) asm volatile("v_mfma_f32_16x16x32_bf16 %0, %1, %2, %0" : "+v"(c) : "v"(a), "v"(b))
#define MPAD() asm volatile("s_nop 7")
template <int V> using IC = std::integral_constant<int, V>;

constexpr unsigned HID_PANEL = 112 * 8192, H_OFF = 16 * HID_PANEL, H_PANEL = 32 * 16384;
constexpr unsigned PART_OFF = H_OFF + 16 * H_PANEL;
constexpr unsigned GRAN_OFF = PART_OFF + 128 * 32768;
constexpr unsigned RG_OFF = GRAN_OFF + 256 * 1024;
constexpr unsigned CG_OFF = RG_OFF + 16 * 8192;
constexpr unsigned FLAG_OFF = CG_OFF + 256 * 2048;
constexpr unsigned EPC_OFF = FLAG_OFF + 16 * 1024;
constexpr unsigned WS_BYTES = EPC_OFF + 16 * 128;
constexpr int FK_M1 = 0, FK_M2 = 1, FK_M3 = 2, FK_M4 = 3, FK_H = 4, FK_P = 5, FK_EP = 6;
constexpr unsigned TAB = 147456, T_ROW = TAB, T_XA = TAB + 2048, T_OUT = TAB + 4096, T_B1 = TAB + 5120, T_CS2 = TAB + 6144, T_WA = TAB + 8192, T_ZERO = TAB + 12288, T_AGG = T_WA, T_DUM = TAB + 12544, T_CON = TAB + 12800;
constexpr unsigned LDS_BYTES = 163840;
#define XMAP 1
#define ROTM 3
#define OAUX 2
#define SP0 4
#define SPN 6
#define PTE 0
#define MOVE 3
#define PROFX false
#define WD 8
constexpr int LASTR = 24;
constexpr unsigned SPIN_CAP = 1u << 16;

DI rsrc_t mk(const void* p, unsigned bytes) { return __builtin_amdgcn_make_buffer_rsrc((void*)p, 0, bytes, 0x00020000); }
DI void setm0(unsigned lds) { asm volatile("s_mov_b32 m0, %0\n\ts_nop 0" ::"s"(lds) : "memory"); }
template <int OFF> DI void dma(rsrc_t r, unsigned voff, unsigned soff) {
  asm volatile("buffer_load_dwordx4 %0, %1, %2 offen offset:%3 lds" ::"v"(voff), "s"(r), "s"(soff), "i"(OFF) : "memory");
}
template <int OFF> DI void dma1(rsrc_t r, unsigned voff, unsigned soff) {
  asm volatile("buffer_load_dwordx4 %0, %1, %2 offen offset:%3 sc1 lds" ::"v"(voff), "s"(r), "s"(soff), "i"(OFF) : "memory");
}
#define PIN2(x, y) asm volatile("" : "+v"(x), "+v"(y))
#define PINS(x) asm volatile("" : "+s"(x))
typedef const void* const __attribute__((address_space(4)))* kargs_t;
DI const char* karg(int i) { kargs_t ka = (kargs_t)__builtin_amdgcn_kernarg_segment_ptr(); PINS(ka); return (const char*)ka[i]; }
enum { A_X, A_LRE, A_LIM, A_BRE, A_BIM, A_CRE, A_CIM, A_LDT, A_DSK, A_W1, A_B1, A_W2, A_B2, A_GW, A_WS, A_OUT };
template <int N> DI void waitv() { asm volatile("s_waitcnt vmcnt(%0)" ::"i"(N) : "memory"); }
DI void waitl0() { SB(); __builtin_amdgcn_s_waitcnt(0xC07F); asm volatile("" ::: "memory"); }
#define PH(k)
DI void bar() { asm volatile("s_barrier" ::: "memory"); SB(); }
DI bf16x8 rd128(unsigned a) { return *(LAS bf16x8*)(size_t)a; }
DI u32x4 rdu4(unsigned a) { return *(LAS u32x4*)(size_t)a; }
DI f32x4 rdf4(unsigned a) { return *(LAS f32x4*)(size_t)a; }
DI f32x2 rdf2(unsigned a) { return *(LAS f32x2*)(size_t)a; }
DI float rdf(unsigned a) { return *(LAS float*)(size_t)a; }
DI void wrf(unsigned a, float v) { *(LAS float*)(size_t)a = v; }
DI void wrf2(unsigned a, f32x2 v) { *(LAS f32x2*)(size_t)a = v; }
DI bf16x4 rtr(unsigned a) { return __builtin_amdgcn_ds_read_tr16_b64_v4bf16((LAS bf16x4*)(size_t)a); }
DI void sethalf(bf16x8& f, int h, bf16x4 v) {
  if (h == 0) f = __builtin_shufflevector(v, v, 0, 1, 2, 3, -1, -1, -1, -1);
  else { bf16x8 t = __builtin_shufflevector(v, v, -1, -1, -1, -1, 0, 1, 2, 3); f = __builtin_shufflevector(f, t, 0, 1, 2, 3, 12, 13, 14, 15); }
}
DI unsigned f2u(float f) { return __builtin_bit_cast(unsigned, f); }
DI float u2f(unsigned u) { return __builtin_bit_cast(float, u); }
DI float shx(int lane, float v, int m) { return u2f(__builtin_amdgcn_ds_bpermute((lane ^ m) * 4, (int)f2u(v))); }
DI float sum16(float v) { const auto r = __builtin_amdgcn_permlane16_swap(f2u(v), f2u(v), false, false); return u2f(r[0]) + u2f(r[1]); }
DI float sum32(float v) { const auto r = __builtin_amdgcn_permlane32_swap(f2u(v), f2u(v), false, false); return u2f(r[0]) + u2f(r[1]); }
DI float fexp(float v) { return __builtin_amdgcn_exp2f(v * 1.4426950408889634f); }
DI float bf2f(unsigned v) { return u2f(v << 16); }
DI unsigned pk2(float lo, float hi) { f32x2 v = {lo, hi}; return __builtin_bit_cast(unsigned, __builtin_convertvector(v, bf16x2)); }
DI float rbf(float f) { return u2f(pk2(f, 0.f) << 16); }
DI float dot2(unsigned a, unsigned b, float c) { return __builtin_amdgcn_fdot2_f32_bf16(__builtin_bit_cast(bf16x2, a), __builtin_bit_cast(bf16x2, b), c, false); }
template <int C> DI float dppadd(float v) { return v + u2f(__builtin_amdgcn_update_dpp(0, (int)f2u(v), C, 0xf, 0xf, true)); }
DI float sel4(bool b0, bool b1, float a, float b, float c, float d) { PIN(a); PIN(b); PIN(c); PIN(d); const float lo = b0 ? b : a, hi = b0 ? d : c; return b1 ? hi : lo; }
DI f32x2 cmul(f32x2 a, f32x2 b) { return (f32x2){a.x * b.x - a.y * b.y, a.x * b.y + a.y * b.x}; }
DI f32x2 cmulc(f32x2 a, f32x2 b) { return (f32x2){__builtin_fmaf(a.x, b.x, -(a.y * b.y)), __builtin_fmaf(-a.x, b.y, -(a.y * b.x))}; }
DI f32x2 cfma(f32x2 a, f32x2 b, f32x2 c) { return (f32x2){__builtin_fmaf(a.x, b.x, __builtin_fmaf(-a.y, b.y, c.x)), __builtin_fmaf(a.x, b.y, __builtin_fmaf(a.y, b.x, c.y))}; }
template <int AUX = 16> DI void st128(rsrc_t r, u32x4 v, unsigned voff, unsigned soff) {
  __builtin_amdgcn_raw_buffer_store_b128(v, r, voff, soff, AUX);
  asm volatile("s_nop 1" ::"v"(v));
}
DI void st64(rsrc_t r, u32x2 v, unsigned voff, unsigned soff) { __builtin_amdgcn_raw_buffer_store_b64(v, r, voff, soff, 16); }
DI void st32(rsrc_t r, unsigned v, unsigned voff, unsigned soff) { __builtin_amdgcn_raw_buffer_store_b32(v, r, voff, soff, 16); }
DI void stflag(rsrc_t r, unsigned v, int lane, unsigned soff) { if (lane == 0) st32(r, v, 0u, soff); }
DI void ld32a(unsigned& d, rsrc_t r, unsigned voff, unsigned soff) { asm volatile("buffer_load_dword %0, %1, %2, %3 offen sc1" : "=v"(d) : "v"(voff), "s"(r), "s"(soff) : "memory"); }
DI void ld16p(unsigned& d, const u16* p, unsigned voff) { asm volatile("global_load_ushort %0, %1, %2" : "=v"(d) : "v"(voff), "s"(p) : "memory"); }
DI void ld64p(u32x2& d, rsrc_t r, unsigned voff, unsigned soff) { asm volatile("buffer_load_dwordx2 %0, %1, %2, %3 offen" : "=v"(d) : "v"(voff), "s"(r), "s"(soff) : "memory"); }
DI void ld64a(u32x2& d, rsrc_t r, unsigned voff, unsigned soff) { asm volatile("buffer_load_dwordx2 %0, %1, %2, %3 offen sc1" : "=v"(d) : "v"(voff), "s"(r), "s"(soff) : "memory"); }
DI void ld128g(u32x4& d, const u16* p, unsigned voff) { asm volatile("global_load_dwordx4 %0, %1, %2" : "=v"(d) : "v"(voff), "s"(p) : "memory"); }
DI void pfl(unsigned& d, rsrc_t r, unsigned voff, unsigned soff) { asm volatile("buffer_load_dword %0, %1, %2, %3 offen" : "+v"(d) : "v"(voff), "s"(r), "s"(soff) : "memory"); }
template <class T> DI T sel(bool c, T a, T b) { return c ? a : b; }
DI void pfg(const char* p) { asm volatile("global_load_lds_dword %0, off" ::"v"(p) : "memory"); }
DI bool anyl(bool b) { return __builtin_amdgcn_ballot_w64(b) != 0; }
DI void ld128p(u32x4& d, rsrc_t r, unsigned voff, unsigned soff) { asm volatile("buffer_load_dwordx4 %0, %1, %2, %3 offen" : "=v"(d) : "v"(voff), "s"(r), "s"(soff) : "memory"); }
DI void sincos_f(float t, float& sn, float& cs) {
  const float k = __builtin_rintf(t * 0.63661977236f);
  float r = __builtin_fmaf(k, -1.5707962513f, t); r = __builtin_fmaf(k, -7.5497894159e-08f, r); r = __builtin_fmaf(k, -5.3903029534e-15f, r);
  const float z = r * r;
  const float sp = __builtin_fmaf(r * z, __builtin_fmaf(z, __builtin_fmaf(z, __builtin_fmaf(z, 2.7183114939e-06f, -1.9839334836e-04f), 8.3333293858e-03f), -1.6666666642e-01f), r);
  const float cp = __builtin_fmaf(z, __builtin_fmaf(z, __builtin_fmaf(z, __builtin_fmaf(z, 2.4390448796e-05f, -1.3886763775e-03f), 4.1666623324e-02f), -4.9999999726e-01f), 1.f);
  const int q = (int)k;
  const float a = (q & 1) ? cp : sp, b = (q & 1) ? sp : cp;
  sn = (q & 2) ? -a : a; cs = ((q + 1) & 2) ? -b : b;
}
DI void ld128a(u32x4& d, rsrc_t r, unsigned voff, unsigned soff) { asm volatile("buffer_load_dwordx4 %0, %1, %2, %3 offen sc1" : "=v"(d) : "v"(voff), "s"(r), "s"(soff) : "memory"); }

DI unsigned x1off(int a) { return a == 0 ? 131072u : (unsigned)(81920 + a * 16384); }
DI unsigned nq(unsigned s) { return (s + 32768u) & 131071u; }

DI int kb_of(int j, int bl) { return 7 * j + ((j & 1) ? (bl == 6 ? 0 : bl + 1) : bl); }

template <int NR, int LAST> constexpr int nread(int sl) {
  constexpr int DENSE = 2 * NR - LAST - 2;
  return sl < 0 ? 0 : sl < DENSE ? sl + 1 : sl >= LAST ? NR : DENSE + (sl - DENSE) / 2 + 1;
}
template <int N, int L, int X, bool FIRST, int NR, int LAST, int NB, int SL, class RD, class MM, class UP>
DI void slot(RD& rd, MM& mm, UP& upd) {
  if constexpr (!FIRST && SL >= L && (SL == L + 4 || (SL - L) % 8 == 0)) {
    __builtin_amdgcn_s_waitcnt(0xC07F | ((nread<NR, LAST>(SL - 1) - nread<NR, LAST>(SL - WD)) << 8)); SB();
  }
  if constexpr (SL >= L) mm(X, SL - L, SL); else if constexpr (!FIRST) mm(X ^ 1, SL + N - L, SL);
  SB();
  if constexpr (nread<NR, LAST>(SL) > nread<NR, LAST>(SL - 1)) rd(X, nread<NR, LAST>(SL) - 1);
  constexpr int per = NB > N - 1 - LAST ? 2 : 1, u0 = (SL - LAST - 1) * per;
  if constexpr (SL > LAST && u0 < NB) { upd(u0); if constexpr (per == 2 && u0 + 1 < NB) upd(u0 + 1); }
  SB();
}
template <int N, int L, int X, bool FIRST, int NR, int LAST, int NB, class RD, class MM, class UP, int... I>
DI void period_(RD& rd, MM& mm, UP& upd, std::integer_sequence<int, I...>) { (slot<N, L, X, FIRST, NR, LAST, NB, I>(rd, mm, upd), ...); }
template <int N, int L, int X, bool FIRST, int NR, int LAST, int NB, class RD, class MM, class UP>
DI void period(RD& rd, MM& mm, UP& upd) {
  static_assert(NR - 1 <= LAST && !(LAST & 1) && NB <= 2 * (N - 1 - LAST), "");
  period_<N, L, X, FIRST, NR, LAST, NB>(rd, mm, upd, std::make_integer_sequence<int, N>{});
  waitl0();
}
template <class G, class W, int... U> DI void ival_(G& g, W& wk, std::integer_sequence<int, U...>) { ((g(IC<U>{}), SB(), wk(IC<U>{}), SB()), ...); }
template <class G, class W> DI void ival(G&& g, W&& wk) { ival_(g, wk, std::make_integer_sequence<int, 8>{}); }
template <int N, class F, int... I> DI void sfor_(F& f, std::integer_sequence<int, I...>) { (f(IC<I>{}), ...); }
template <int N, class F> DI void sfor(F&& f) { sfor_<N>(f, std::make_integer_sequence<int, N>{}); }

DI void check_flags(unsigned& pv, rsrc_t ws, unsigned voff, unsigned soff, int lane, int lo, int hi, unsigned ep) {
  PIN(pv);
  const bool need = lane >= lo && lane < hi;
  if (__builtin_expect(anyl(need && pv != ep), 0)) {
    for (unsigned sp = 0; sp < SPIN_CAP; ++sp) {
      __builtin_amdgcn_s_sleep(2);
      ld32a(pv, ws, voff, soff); asm volatile("s_waitcnt vmcnt(0)" : "+v"(pv)::"memory");
      if (!anyl(need && pv != ep)) break;
    }
  }
}

#if defined(__HIP_DEVICE_COMPILE__) && __HIP_DEVICE_COMPILE__
__attribute__((target("no-packed-fp32-ops")))
#endif
__global__ __launch_bounds__(512, 2) void fused_kernel(const u16* x, const u16* lre, const u16* lim, const u16* bre, const u16* bim, const u16* cre, const u16* cim,
    const u16* ldt, const u16* dsk, const u16* w1, const u16* b1, const u16* w2, const u16* b2, const u16* gw, char* ws, float* out) {
  extern __shared__ __attribute__((aligned(16))) char shm[];
  unsigned long long pc0; asm volatile("s_getpc_b64 %0" : "=s"(pc0));
  const int lane = threadIdx.x & 63, w = __builtin_amdgcn_readfirstlane(threadIdx.x >> 6);
  const int bid = blockIdx.x, xcd = bid & 7, loc = bid >> 3;
  const int ci = (xcd & 3) * 4 + (loc & 3), cj = (loc >> 4) * 8 + (xcd >> 2) * 4 + ((loc >> 2) & 3);
  const int rot = (ci + cj) & ROTM, cnt = cj & 7, half = cj >> 3, wq = w & 3, tq = threadIdx.x & 255;
  const int n4 = half ? 28 + MOVE : 28 - MOVE;
  const rsrc_t rws = mk(ws, WS_BYTES), rx = mk(x, 2048 * 2048);
  const int wm = (w >> 1) & 1, wn = w & 1, g = lane >> 4, r15 = lane & 15, q2 = (lane >> 2) & 3, p = lane & 3, hl = lane >> 5, r31 = lane & 31;
  const unsigned flagb = FLAG_OFF + ci * 1024, fvo = (lane & 15) * 4;
  unsigned rgb = RG_OFF + ci * 8192; PINS(rgb);
  auto nowork = [&](auto) AI {};
  unsigned va[4], vb[4], vw[7], sl, sa, sb, ta, tb; int sx; rsrc_t ra, rb;
  auto vnat = [&](int u) AI { return (8u * u + (lane >> 3)) * 2048 + (((lane & 7) ^ ((4 * u + g) & 7)) * 16) - 1024 * u; };
  auto nexts = [&]() AI { sa = __builtin_amdgcn_readlane(ta, sx); sb = __builtin_amdgcn_readlane(tb, sx); ++sx; PINS(sa); PINS(sb); };
  auto G = [&](auto U) AI { constexpr int u = U.value;
    if constexpr (u == 0) setm0(sl + wq * 4096);
    if constexpr (u == 4) setm0(sl + 16384 + wq * 4096);
    if constexpr (u < 4) dma1<u * 1024>(ra, va[u], sa); else dma<(u - 4) * 1024>(rb, vb[u - 4], sb);
  };
  auto G2 = [&](auto S, auto U) AI { constexpr int a = S.value % 3, u = U.value;
    if constexpr (u == 0) setm0((a == 0 ? 131072u : 81920u + a * 16384u) + wq * 4096);
    if constexpr (u == 4) setm0(a * 32768 + wq * 7168);
    if constexpr (u == 8) setm0(a * 32768 + wq * 7168 + 4096);
    if constexpr (u < 4) dma<u * 1024>(rx, va[u], sa); else dma<(u & 3) * 1024>(rb, vw[u - 4], sb);
  };
  const int c7 = ci & 7;
  auto setup2 = [&]() AI {
#pragma unroll
    for (int q = 0; q < 6; ++q) { const int k = 8 * (q & 1) + (lane >> 3), f = ((k >> 1) & 1) | ((q & 1) << 1);
      vw[q] = 4096 + k * 7168 + ((cj & 1) ? 64 : 0) + (q >> 1) * 128 + (((((lane & 7) >> 1) ^ f) * 2) + (lane & 1)) * 16 - (q & 3) * 1024; }
    vw[6] = 4096 + (lane >> 2) * 7168 + ((cj & 1) ? 0 : 384) + (((((lane & 3) >> 1) ^ hl) * 2) + (lane & 1)) * 16 - 2048;
    int k = lane - 2 + (c7 < 2 ? 3 * c7 : c7 - 2); k = 2 + (k >= 6 ? k - 6 : k);
    k = lane < 2 ? lane : lane < 8 ? k : (lane ^ rot);
    k = (k ^ (ci & 8)) & 15;
    ta = (ci * 128 + 32 * wq) * 2048 + k * 128; tb = (k * 64 + 16 * wq) * 7168 + cj * 448;
    PIN2(ta, tb);
#pragma unroll
    for (int q = 0; q < 7; ++q) PIN(vw[q]);
  };
  auto setup3 = [&]() AI {
    ra = rws;
#pragma unroll
    for (int q = 0; q < 4; ++q) { va[q] = lane * 16; vb[q] = vnat(q); }
  };
  auto table3 = [&](unsigned& ta, unsigned& tb) AI {
    const int ks = 16 * half + ((lane ^ (2 * cnt)) & 15);
    int n = 2 * (lane - 16) + (wq >> 1), hp = half; if (n >= 56) { n -= 2 * MOVE; hp = 0; }
    const int kb = kb_of(hp * 8 + ((n ^ rot) & 7), n >> 3);
    ta = lane < 16 ? H_OFF + ci * H_PANEL + ks * 16384 + wq * 4096 : ci * HID_PANEL + kb * 8192 + (wq & 1) * 4096;
    tb = lane < 16 ? (cnt * 128 + 32 * wq) * 2048 + (ks >> 1) * 128 + (ks & 1) : (kb * 32 + 16 * (wq & 1)) * 2048 + cnt * 256;
    PIN(ta); PIN(tb);
  };
  auto next3 = [&]() AI { nexts(); rb = mk(sel<const u16*>(sb & 1, cim, cre), 1024 * 2048); sb &= ~1u; };

  if (w < 4) {
    constexpr bool isx = PROFX; (void)isx;
    unsigned epr, epc;
    const unsigned ca = (32 * wn + r31) * 8;
    const unsigned c0 = (32 * wn + r15) * 8;
    const int seg0 = 4 * wm + 2 * hl;
    f32x2 lam[2], cof[2], cfa, l16, l128, l1k, pw0, pw1; unsigned t3a, t3b;
    f32x4 ac2[4][4];
    {
      setm0(T_DUM);
      const int row = bid * 8 + (lane & 7);
      const char* pp = (const char*)(hl ? (row < 1024 ? bre : bim - 1024 * 1024) : x) + (size_t)row * 2048 + (4 * wq + ((lane >> 3) & 3)) * 128;
      for (int q = 0; q <= wq; ++q) __builtin_amdgcn_s_sleep(6);
      pfg(pp);
      pfg((const char*)pc0 + (((loc * 4 + wq) % 20) * 16 + (lane & 15)) * 128);
      ld32a(epr, rws, 0u, flagb + FK_EP * 128); ld32a(epc, rws, 0u, EPC_OFF + cj * 128);
      setup2(); sx = 2; nexts(); table3(t3a, t3b);
      rb = mk((const char*)w1 - 4096, 1024 * 7168 + 4096);
#pragma unroll
      for (int q = 0; q < 4; ++q) { va[q] = vnat(q); PIN(va[q]); }
      wrf(T_ZERO + (lane & 3) * 4, 0.f);
    }
    {
      f32x4 acc[4][4];
#pragma unroll
      for (int a = 0; a < 16; ++a) { acc[a >> 2][a & 3] = (f32x4){0, 0, 0, 0}; PIN(acc[a >> 2][a & 3]); }
      bf16x8 wv[4][2];
      const int sstar = c7 < 2 ? c7 : 2;
      const int fsw = ((q2 >> 1) & 1) | ((g & 1) << 1);
      unsigned cf = sstar * 32768 + (g >> 1) * 7168 + (8 * (g & 1) + q2) * 128 + ((wq ^ fsw) * 32) + p * 8;
      unsigned ch = sstar * 32768 + (g >> 1) * 7168 + 6144 + (8 * (g & 1) + q2) * 64 + (((wq & 1) ^ (g & 1)) * 32) + p * 8;
      PIN(cf); PIN(ch);
      auto wread = [&](int st) AI {
        if (sstar == st) {
#pragma unroll
          for (int e = 0; e < 4; ++e)
#pragma unroll
            for (int t = 0; t < 4; ++t) sethalf(wv[t][e >> 1], e & 1, rtr((e >> 1) * 14336 + (t < 3 ? cf + t * 2048 + (e & 1) * 512 : ch + (e & 1) * 256)));
        } };
      {
        bf16x8 fa[4][2], fb[2][4][2];
        const unsigned n0 = r15 * 128 + ((g ^ (r15 >> 1)) * 16), n1 = r15 * 128 + (((4 + g) ^ (r15 >> 1)) * 16);
        const unsigned cb[4] = {wm * 8192u + n0, wm * 8192u + n1, 16384 + wn * 8192u + n0, 16384 + wn * 8192u + n1};
        unsigned ba[4], rs = 98304;
#pragma unroll
        for (int b = 0; b < 4; ++b) { ba[b] = rs + cb[b]; PIN(ba[b]); }
        auto rd = [&](int X, int r) AI { const int ks = r & 1, f = r >> 1;
          if (f >= 1 && f < 5) fb[X][f - 1][ks] = rd128(ba[2 + ks] + (f - 1) * 2048); else { const int a = f ? f - 4 : 0; fa[a][ks] = rd128(ba[ks] + a * 2048); } };
        auto mm = [&](int X, int q, int) AI { const int a = q >> 3, b = (q >> 1) & 3, ks = q & 1; MMA(acc[a][b], fa[a][ks], fb[X][b][ks]); };
        auto up = [&](int b) AI { if (b == 0) { rs = nq(rs); PINS(rs); } else { ba[b - 1] = rs + cb[b - 1]; PIN(ba[b - 1]); } };
        waitl0(); bar();
        period<32, 16, 0, true, 16, LASTR, 5>(rd, mm, up); bar();
        for (int s = 0; s < 7; ++s) { period<32, 16, 1, false, 16, LASTR, 5>(rd, mm, up); bar(); period<32, 16, 0, false, 16, LASTR, 5>(rd, mm, up); bar(); }
        period<32, 16, 1, false, 16, LASTR, 5>(rd, mm, up); bar();
        waitv<0>(); PIN(epr); PIN(epc);
        epr = __builtin_amdgcn_readfirstlane(epr) + 1u; epc = __builtin_amdgcn_readfirstlane(epc) + 1u;
        lam[0] = rdf2(T_CON + c0); lam[1] = rdf2(T_CON + c0 + 128);
        wread(0);
        sfor<7>([&](auto U) AI { G2(IC<2>{}, IC<U.value + 4>{}); SB();
#pragma unroll
          for (int q = 16 + (16 * U.value) / 7 / 2 * 2; q < 16 + (16 * (U.value + 1)) / 7 / 2 * 2; ++q) mm(1, q, 0);
          SB(); });
        nexts();
      }
      f32x2 st[2] = {{0, 0}, {0, 0}}, Pf[2] = {{0, 0}, {0, 0}}, Gt = {0, 0}, cr = {0, 0}, ag[8];
      u32x4 lb[8], cg[8], rgv[2]; unsigned lbo[8], lbe[8], pv, gd = 0; unsigned obs[2][2][2][4];
      f32x4 cs[4]; f32x2 so[2]; float c0s = 0.f, c1s = 0.f;
      const rsrc_t rgr = mk(ws + GRAN_OFF + cj * 16384, 16384);
      const int cp = 28 * wq + (r31 < 28 ? r31 : 27);
      const unsigned cgo = hl * 16384 + cp * 16;
#define OB(a, b, c) ((u32x4){obs[a][b][c][0], obs[a][b][c][1], obs[a][b][c][2], obs[a][b][c][3]})
      auto hofs = [&](int hf, int mt) AI { return (2u * wn + mt) * 4096 + wm * 2048 + hf * 1024 + g * 256 + ((r15 ^ (hf << 2)) * 16); };
      auto bad = [&]() AI { unsigned d = 0;
#pragma unroll
        for (int q = 0; q < 8; ++q) { PIN(lb[q]); d |= (int)lbo[q] < 0 ? 0u : ((lb[q][1] ^ epc) | (lb[q][3] ^ epc)); }
        return d != 0; };
      auto gbad = [&]() AI { unsigned d = 0;
#pragma unroll
        for (int q = 0; q < 8; ++q) { PIN(cg[q]); d |= (cg[q][1] ^ epc) | (cg[q][3] ^ epc); }
        return d != 0; };
      auto E2 = [](int s) constexpr { return (s == 2 || s == 3) ? 1 : 0; };
      auto T2 = [](int s) constexpr { return s == 4 ? 8 : (s == 7 || s == 8) ? 4 : s == 11 ? 10 : 0; };
      sfor<15>([&](auto S) AI { constexpr int s = S.value;
        if constexpr (s < 3) waitl0();
        if constexpr (s == 6) waitv<11>();
        else if constexpr (s >= 1) waitv<T2(s - 2) + (s < 14 ? 11 : 4) + E2(s - 1) + T2(s - 1)>();
        bar();
        if constexpr (s < 2) wread(s + 1);
        if constexpr (s == 2) {
#pragma unroll
          for (int sg = 0; sg < 8; ++sg) ag[sg] = rdf2(T_AGG + sg * 512 + ca);
          l16 = rdf2(T_CON + 1024 + ca); cof[0] = rdf2(T_CON + 512 + c0); cof[1] = rdf2(T_CON + 512 + c0 + 128);
        }
        if constexpr (s == 5) { pw1 = rdf2(T_CON + 1536 + ca); pw0 = rdf2(T_CON + 2048 + ca); l128 = rdf2(T_CON + 2560 + ca); l1k = rdf2(T_CON + 3072 + ca); cfa = rdf2(T_CON + 512 + ca); }
        if constexpr (s == 11) { if (w == 0) stflag(rws, epr, lane, flagb + FK_H * 128 + cj * 4); }
        if constexpr (s == 14) { if (w == 0) ld32a(pv, rws, fvo, flagb + FK_H * 128); }
        SB();
        auto work = [&](auto U) AI { constexpr int u = U.value;
          if constexpr (s < 2 && u >= 1 && u < 9) {
            constexpr int t = 8 * s + u - 1;
#pragma unroll
            for (int mt = 0; mt < 2; ++mt) st[mt] = cfma(lam[mt], st[mt], (f32x2){acc[t >> 2][mt][t & 3], acc[t >> 2][2 + mt][t & 3]});
            PIN2(st[0], st[1]);
          }
          if constexpr (s == 1 && u == 9) { wrf2(T_AGG + (4 * wm + g) * 512 + c0, st[0]); wrf2(T_AGG + (4 * wm + g) * 512 + c0 + 128, st[1]); }
          if constexpr (s == 2 && u >= 1 && u < 9) {
            constexpr int sg = u - 1, mt = sg & 1; const bool me = sg == seg0 + mt;
            Pf[mt].x = sel<float>(me, Gt.x, Pf[mt].x); Pf[mt].y = sel<float>(me, Gt.y, Pf[mt].y); Gt = cfma(l16, Gt, ag[sg]);
            PIN2(Gt, Pf[mt]);
          }
          if constexpr (s == 2 && u == 9) st64(rgr, (u32x2){f2u(sel<float>(hl, Gt.y, Gt.x)), epc}, ca * 2 + hl * 8, ci * 1024);
          if constexpr (s == 3 && u < 4) {
            const bf16x8 one = __builtin_bit_cast(bf16x8, (u32x4){0x3F803F80u, 0x3F803F80u, 0x3F803F80u, 0x3F803F80u});
            cs[u] = MFMA(one, wv[u][1], MFMA(one, wv[u][0], ((f32x4){0, 0, 0, 0}))); PIN(cs[u]);
          }
          if constexpr (s == 3 && u == 9)
            st64(rws, (u32x2){f2u(sel4(g & 1, g & 2, cs[0][0], cs[1][0], cs[2][0], cs[3][0])), epc}, (g < 3 ? g * 64 + wq * 16 + r15 : 192 + (wq & 1) * 16 + r15) * 8u, CG_OFF + (cj * 16 + ci) * 2048);
          if constexpr ((s == 3 && u >= 4 && u < 9) || (s == 4 && u < 5) || (s == 5 && u >= 1 && u < 7)) {
            constexpr int t = s == 3 ? u - 4 : s == 4 ? 5 + u : 9 + u;
            f32x2 v0 = cmulc(cof[0], (f32x2){acc[t >> 2][0][t & 3], acc[t >> 2][2][t & 3]}), v1 = cmulc(cof[1], (f32x2){acc[t >> 2][1][t & 3], acc[t >> 2][3][t & 3]}); PIN2(v0, v1);
            acc[t >> 2][0][t & 3] = v0.x; acc[t >> 2][2][t & 3] = v0.y; acc[t >> 2][1][t & 3] = v1.x; acc[t >> 2][3][t & 3] = v1.y;
          }
          if constexpr (s == 4 && (u == 6 || u == 8)) {
#pragma unroll
            for (int q = 2 * u - 12; q < 2 * u - 8; ++q) { lbo[q] = (ci - 8 - 8 * hl + q) * 1024 + ca * 2; PIN(lbo[q]); }
          }
          if constexpr (s == 4 && u == 10) {
#pragma unroll
            for (int q = 0; q < 8; ++q) ld128a(lb[q], rgr, lbo[q], 0u);
          }
          if constexpr (s == 5 && u == 8) {
            if (!wm) pw0 = (f32x2){1.f, 0.f};
            if (ci <= 8) l1k = (f32x2){0.f, 0.f};
            pw0 = cmul(pw0, (f32x2){sel<float>(hl, pw1.x, 1.f), sel<float>(hl, pw1.y, 0.f)}); pw1 = cmul(pw0, l16); PIN2(pw0, pw1);
          }
          if constexpr (s == 5 && (u == 7 || u == 9)) {
#pragma unroll
            for (int q = 2 * u - 14; q < 2 * u - 10; ++q) { lbe[q] = (int)lbo[q] < 0 ? 0u : epc; PIN(lbe[q]); }
          }
          if constexpr (s == 6 && u < 8) {
            PIN(lb[u]); gd |= (lb[u][1] ^ lbe[u]) | (lb[u][3] ^ lbe[u]); cr = cfma(l128, cr, (f32x2){u2f(lb[u][0]), u2f(lb[u][2])});
            PIN2(cr, gd);
          }
          if constexpr (s == 6 && u == 8) {
            if (__builtin_expect(anyl(gd != 0), 0)) {
              for (unsigned sp = 0; sp < SPIN_CAP; ++sp) {
                __builtin_amdgcn_s_sleep(2);
#pragma unroll
                for (int q = 0; q < 8; ++q) ld128a(lb[q], rgr, lbo[q], 0u);
                waitv<0>();
                if (!anyl(bad())) break;
              }
              cr = (f32x2){0, 0};
#pragma unroll
              for (int q = 0; q < 8; ++q) cr = cfma(l128, cr, (f32x2){u2f(lb[q][0]), u2f(lb[q][2])});
            }
            gd = 0;
            const auto sx_ = __builtin_amdgcn_permlane32_swap(f2u(cr.x), f2u(cr.x), false, false), sy_ = __builtin_amdgcn_permlane32_swap(f2u(cr.y), f2u(cr.y), false, false);
            cr = cfma(l1k, (f32x2){u2f(sx_[1]), u2f(sy_[1])}, (f32x2){u2f(sx_[0]), u2f(sy_[0])});
            PIN(cr);
          }
          if constexpr (s == 6 && u == 9) { st[0] = cmulc(cfa, cfma(pw0, cr, Pf[0])); PIN(st[0]); }
          if constexpr (s == 7 && u == 0) { st[1] = cmulc(cfa, cfma(pw1, cr, Pf[1]));
            const auto ex = __builtin_amdgcn_permlane16_swap(f2u(st[0].x), f2u(st[1].x), false, false), ey = __builtin_amdgcn_permlane16_swap(f2u(st[0].y), f2u(st[1].y), false, false);
            st[0] = (f32x2){u2f(ex[0]), u2f(ey[0])}; st[1] = (f32x2){u2f(ex[1]), u2f(ey[1])}; PIN2(st[0], st[1]); }
          if constexpr ((s == 7 && u >= 2 && u < 10) || (s == 8 && u >= 1 && u < 9)) {
            constexpr int t = s == 7 ? u - 2 : 7 + u;
            if constexpr (t & 1) { so[0] = st[0]; so[1] = st[1]; }
#pragma unroll
            for (int mt = 0; mt < 2; ++mt) st[mt] = cfma((f32x2){lam[mt].x, -lam[mt].y}, st[mt], (f32x2){acc[t >> 2][mt][t & 3], acc[t >> 2][2 + mt][t & 3]});
            if constexpr (t & 1) {
#pragma unroll
              for (int mt = 0; mt < 2; ++mt) { obs[t >> 3][mt][0][(t >> 1) & 3] = pk2(so[mt].x, st[mt].x); obs[t >> 3][mt][1][(t >> 1) & 3] = pk2(so[mt].y, st[mt].y); PIN2(obs[t >> 3][mt][0][(t >> 1) & 3], obs[t >> 3][mt][1][(t >> 1) & 3]); }
            }
            PIN2(st[0], st[1]);
          }
          if constexpr ((s == 7 || s == 8) && u == 10) {
#pragma unroll
            for (int q = 0; q < 4; ++q) st128(rws, OB(s - 7, q >> 1, q & 1), hofs(s - 7, q >> 1), H_OFF + ci * H_PANEL + (2 * cj + (q & 1)) * 16384);
          }
          if constexpr (s >= 13 && u < 4) *(LAS u32x4*)(size_t)((s - 12) * 32768 + hofs(u >> 1, u & 1)) = OB(u >> 1, u & 1, s - 13);
          if constexpr ((s == 9 || s == 10) && u >= 1 && u < 9) { constexpr int t = 8 * (s - 9) + u - 1; asm volatile("v_mov_b32 %0, 0\n\tv_mov_b32 %1, 0\n\tv_mov_b32 %2, 0\n\tv_mov_b32 %3, 0" : "=v"(ac2[t >> 2][t & 3][0]), "=v"(ac2[t >> 2][t & 3][1]), "=v"(ac2[t >> 2][t & 3][2]), "=v"(ac2[t >> 2][t & 3][3])); PIN(ac2[t >> 2][t & 3]); }
          if constexpr (s == 9 && u == 0) { if (ci == 15 && w == 1) stflag(rws, epc, lane, EPC_OFF + cj * 128); }
          if constexpr (s == 14 && u == 5) {
            auto rbad = [&]() AI { PIN2(rgv[0], rgv[1]); return ((rgv[0][1] ^ epr) | (rgv[0][3] ^ epr) | (rgv[1][1] ^ epr) | (rgv[1][3] ^ epr)) != 0; };
            if (__builtin_expect(anyl(rbad()), 0))
              for (unsigned sp = 0; sp < SPIN_CAP; ++sp) { __builtin_amdgcn_s_sleep(2);
                ld128a(rgv[0], rws, tq * 16u, rgb); ld128a(rgv[1], rws, tq * 16u + 4096, rgb);
                waitv<0>(); if (!anyl(rbad())) break; }
          }
          if constexpr (s == 14 && u == 7) {
            wrf(T_ROW + (tq & 1) * 1024 + (tq >> 1) * 4, u2f(rgv[0][0])); wrf(T_ROW + (tq & 1) * 1024 + 512 + (tq >> 1) * 4, u2f(rgv[0][2]));
            wrf2(T_XA + tq * 8, (f32x2){u2f(rgv[1][0]), u2f(rgv[1][2])});
          }
          if constexpr (s == 11 && u == 10) {
#pragma unroll
            for (int q = 0; q < 8; ++q) ld128a(cg[q], rws, cgo, CG_OFF + (cj * 16 + q) * 2048);
            ld128a(rgv[0], rws, tq * 16u, rgb); ld128a(rgv[1], rws, tq * 16u + 4096, rgb);
          }
          if constexpr (s == 14 && u < 4) {
#pragma unroll
            for (int q = 2 * u; q < 2 * u + 2; ++q) { PIN(cg[q]); gd |= (cg[q][1] ^ epc) | (cg[q][3] ^ epc); c0s += u2f(cg[q][0]); c1s += u2f(cg[q][2]); }
            PIN(gd); PIN(c0s); PIN(c1s);
          }
          if constexpr (s == 14 && u == 4) {
            if (__builtin_expect(anyl(gd != 0), 0)) {
              for (unsigned sp = 0; sp < SPIN_CAP; ++sp) { __builtin_amdgcn_s_sleep(2);
#pragma unroll
                for (int q = 0; q < 8; ++q) ld128a(cg[q], rws, cgo, CG_OFF + (cj * 16 + q) * 2048);
                waitv<0>(); if (!anyl(gbad())) break; }
              c0s = 0.f; c1s = 0.f;
#pragma unroll
              for (int q = 0; q < 8; ++q) { c0s += u2f(cg[q][0]); c1s += u2f(cg[q][2]); }
            }
            c0s = sum32(c0s); c1s = sum32(c1s); PIN(c0s); PIN(c1s);
          }
          if constexpr (s == 14 && u == 6) {
            const float v = sel<float>(hl, c1s, c0s), hi = rbf(v), bb = rdf(T_B1 + (2 * cp + hl) * 4);
            *(LAS u32x4*)(size_t)(T_WA + (2 * cp + hl) * 16) = (u32x4){pk2(hi, v - hi), pk2(hi, bb), pk2(bb, 0.f), 0u};
          }
        };
        if constexpr (s < 13) { sfor<11>([&](auto U) AI { G2(IC<s + 3>{}, U); SB(); work(U); SB(); }); if constexpr (s < 12) nexts(); }
        if constexpr (s == 12) {
#pragma unroll
          for (int q = 0; q < 4; ++q) vb[q] = va[q];
          ta = t3a; tb = t3b; sl = 32768; sx = 0; next3(); }
        if constexpr (s >= 13) { sfor<4>([&](auto U) AI { G(IC<U.value + 4>{}); SB(); work(U); SB(); }); sl = nq(sl); next3(); asm volatile("s_nop 4"); }
        if constexpr (s == 14) sfor<4>([&](auto U) AI { G(IC<U.value + 4>{}); SB(); work(IC<U.value + 4>{}); SB(); });
      });
      waitl0(); waitv<8>();
      if (w == 0) check_flags(pv, rws, fvo, flagb + FK_H * 128, lane, 8 * half, 8 * half + 8, epr);
      bar();
    }
    f32x4 (&acc)[4][4] = ac2;
    bf16x8 fs[4][2], fr[2][4][2];
    const int kap = 4 * (g & 1) + q2, hf = p >> 1;
    const unsigned wb = 16384 + (4 * g + q2) * 256 + p * 8, hb = (g >> 1) * 4096 + wm * 2048 + hf * 1024 + (8 * (g & 1) + q2) * 16 + (p & 1) * 8;
    const unsigned cb3[4] = {hb + 64u * hf, hb + 64u * (1 ^ hf), 16384 + wn * 8192u + r15 * 128 + ((g ^ (r15 >> 1)) * 16), 16384 + wn * 8192u + r15 * 128 + (((4 + g) ^ (r15 >> 1)) * 16)};
    const unsigned cb4[5] = {wm * 4096u + lane * 16, wb + (((4 * wn + 0) ^ kap) * 32), wb + (((4 * wn + 1) ^ kap) * 32), wb + (((4 * wn + 2) ^ kap) * 32), wb + (((4 * wn + 3) ^ kap) * 32)};
    unsigned ba[5];
    sl = 32768;
#pragma unroll
    for (int b = 0; b < 4; ++b) { ba[b] = sl + cb3[b]; PIN(ba[b]); }
    auto rd3 = [&](int X, int r) AI {
      if (r >= 4 && r < 12) fr[X][(r - 4) >> 1][r & 1] = rd128(ba[2 + (r & 1)] + ((r - 4) >> 1) * 2048);
      else { const int a = r < 4 ? 0 : (r - 8) >> 2, ks = (r >> 1) & 1, h = r & 1; sethalf(fs[a][ks], h, rtr(ba[h] + ks * 8192 + a * 256)); } };
    auto rd4 = [&](int X, int r) AI {
      if (r >= 4 && r < 12) fr[X][(r - 4) >> 1][r & 1] = rd128(ba[0] + (r & 1) * 8192 + ((r - 4) >> 1) * 1024);
      else { const int b = r < 4 ? 0 : (r - 8) >> 2, ks = (r >> 1) & 1, h = r & 1; sethalf(fs[b][ks], h, rtr(ba[1 + b] + ks * 8192 + h * 4096)); } };
    auto mm3 = [&](int X, int q, int) AI { const int a = q >> 3, b = (q >> 1) & 3, ks = q & 1; MMA(acc[a][b], fr[X][b][ks], fs[a][ks]); };
    auto mm4 = [&](int X, int q, int SL) AI { const int b = q >> 3, a = (q >> 1) & 3, ks = q & 1;
      if (SL < 0) acc[a][b] = MFMA(fs[b][ks], fr[X][a][ks], acc[a][b]); else MMA(acc[a][b], fs[b][ks], fr[X][a][ks]); };
    auto up3 = [&](int b) AI { if (b == 0) { sl = nq(sl); PINS(sl); } else { ba[b - 1] = sl + cb3[b - 1]; PIN(ba[b - 1]); } };
    auto up4 = [&](int b) AI { if (b == 0) { sl = nq(sl); PINS(sl); } else { ba[b - 1] = sl + cb4[b - 1]; PIN(ba[b - 1]); } };
    period<32, 16, 0, true, 24, LASTR, 5>(rd3, mm3, up3); waitv<4>(); bar();
    for (int s = 0; s < 7; ++s) { period<32, 16, 1, false, 24, LASTR, 5>(rd3, mm3, up3); waitv<0>(); bar(); period<32, 16, 0, false, 24, LASTR, 5>(rd3, mm3, up3); bar(); }
    float gr[4];
#pragma unroll
    for (int a = 0; a < 4; ++a) gr[a] = rdf(T_ROW + 1536 + (64 * wm + 16 * a + r15) * 4);
    period<32, 16, 1, false, 24, LASTR, 6>(rd3, mm3, up4); bar();
    {
      auto mmg = [&](int X, int q, int SL) AI { if (SL < 16) mm3(X, q, SL); else mm4(X, q, SL);
        constexpr int ord[16] = {0, 4, 1, 5, 8, 9, 2, 12, 13, 6, 10, 14, 3, 7, 11, 15};
        const int t = ord[SL >> 1], e = 2 * (SL & 1); SB();
        acc[t >> 2][t & 3][e] *= gr[t >> 2]; acc[t >> 2][t & 3][e + 1] *= gr[t >> 2]; PIN(acc[t >> 2][t & 3]); };
      period<32, 16, 0, false, 24, LASTR, 6>(rd4, mmg, up4); bar();
    }
    static_assert(MOVE & 1, "the remaining Q4 periods run in pairs");
    for (int s = 1; s < n4; s += 2) { period<32, 16, 1, false, 24, LASTR, 6>(rd4, mm4, up4); bar(); period<32, 16, 0, false, 24, LASTR, 6>(rd4, mm4, up4); bar(); }
    MPAD();
    if (half) {
      const unsigned pb = ((sl + 65536u + wm * 32768u) & 131071u) + wn * 16384 + g * 256;
      sfor<16>([&](auto M) AI { constexpr int m = M.value, a = m & 3, b = m >> 2;
        if constexpr (m < 8) { mm4(0, 16 + 2 * m, -1); mm4(0, 17 + 2 * m, -1); }
        SB();
        *(LAS f32x4*)(size_t)(pb + (a * 4 + b) * 1024 + ((r15 ^ (4 * b + g)) * 16)) = acc[a][b];
        SB(); });
      waitl0();
    } else {
#pragma unroll
      for (int q = 16; q < 32; ++q) mm4(0, q, -1);
#pragma unroll
      for (int bp = 0; bp < 2; ++bp)
#pragma unroll
        for (int a = 0; a < 4; ++a)
          st128(rws, (u32x4){pk2(acc[a][2 * bp][0], acc[a][2 * bp][1]), pk2(acc[a][2 * bp][2], acc[a][2 * bp][3]), pk2(acc[a][2 * bp + 1][0], acc[a][2 * bp + 1][1]), pk2(acc[a][2 * bp + 1][2], acc[a][2 * bp + 1][3])},
                (a * 2 + bp) * 1024 + g * 256 + ((r15 ^ (8 * wn + 4 * bp + g)) * 16), PART_OFF + (ci * 8 + cnt) * 32768 + wq * 8192);
      waitv<0>();
    }
    bar();
    if (!half && w == 0) stflag(rws, epr, lane, flagb + FK_P * 128 + cnt * 4);
  } else {
    constexpr bool isx = !PROFX; (void)isx;
    unsigned epr, pv, t3a, t3b, sa2h, cb[8], ba[8]; const char *pp0, *pp1;
    f32x4 ac1[28];
    {
      u32x4 xr[2][2], gq[2];
      ra = rx; rb = mk(sel<const u16*>(wq & 1, bim, bre), 1024 * 2048);
#pragma unroll
      for (int q = 0; q < 4; ++q) { va[q] = (32 * (q & 1) + 16 * hl + 4 * (q >> 1) + ((lane >> 3) & 3)) * 2048 + (((lane & 7) ^ ((4 * q + g) & 7)) * 16) - 1024 * q; vb[q] = vnat(q); }
      auto src1 = [&](int s) AI { const int k = s ^ (s < 2 ? 0 : s < 4 ? (rot & 1) : rot); sa = (ci * 128 + 64 * (wq >> 1) + 8 * (wq & 1)) * 2048 + k * 128; sb = (cj * 64 + 32 * (wq >> 1)) * 2048 + k * 128; sl = ((s + 3) & 3) * 32768; };
      src1(0); ival(G, nowork);
      SB();
      setup2(); table3(t3a, t3b);
      {
        const int f = ((q2 >> 1) & 1) | ((g & 1) << 1);
        const unsigned nb = wm * 8192u + r15 * 128, n0 = (g ^ (r15 >> 1)) * 16, n1 = ((4 + g) ^ (r15 >> 1)) * 16, wk = (g >> 1) * 7168u + p * 8;
        cb[0] = nb + wn * 4096 + n0; cb[1] = nb + wn * 4096 + n1; cb[2] = nb + (1 - wn) * 4096 + n0; cb[3] = nb + (1 - wn) * 4096 + n1;
        cb[4] = wk + (8 * (g & 1) + q2) * 128 + (((2 * wn) ^ f) * 32); cb[5] = wk + (8 * (g & 1) + q2) * 128 + (((2 * wn + 1) ^ f) * 32);
        cb[6] = wk + 6144 + (8 * (g & 1) + q2) * 64 + (g & 1) * 32; cb[7] = wk + 6144 + (8 * (g & 1) + q2) * 64 + ((g & 1) ^ 1) * 32;
#pragma unroll
        for (int b = 0; b < 8; ++b) { ba[b] = cb[b] + (b < 4 ? 131072u : 0u); PIN2(cb[b], ba[b]); }
        pp0 = lane < 32 ? (const char*)sel<const u16*>(lane & 16, cim, cre) + ((bid * 4 + wq) * 16 + r15) * 128 : (const char*)w2 + ((bid * 4 + wq) * 56 + r15) * 128;
        pp1 = (const char*)w2 + ((bid * 4 + wq) * 56 + (lane < 40 ? 16 + lane : 55)) * 128;
        PIN2(pp0, pp1);
#pragma unroll
        for (int m = 0; m < 28; ++m) { ac1[m] = (f32x4){0, 0, 0, 0}; PIN(ac1[m]); }
      }
      src1(1); ival(G, nowork);
      waitv<8>(); bar();
      unsigned pdt, plr, pli, bq, oq; f32x2 lp; float lr, li;
      const int tc = tq < 224 ? tq : 223;
      ld32a(epr, rws, 0u, flagb + FK_EP * 128); ld128g(gq[0], gw, lane * 16u); ld128g(gq[1], gw + 512, lane * 16u);
      ld16p(pdt, ldt + 64 * cj, lane * 2u); ld16p(plr, lre + 64 * cj, lane * 2u); ld16p(pli, lim + 64 * cj, lane * 2u);
      ld16p(bq, b1, (224 * cj + (tc < 192 ? ((cj & 1) ? 32 : 0) + tc : ((cj & 1) ? -192 : 0) + tc)) * 2u);
      ld16p(oq, sel<const u16*>(wq < 2, b2, dsk), (cnt * 128 + (tq & 127)) * 2u);
      PH(1);
      src1(2); ival(G, nowork);
      src1(3); ival(G, nowork);
      float sv[6] = {0, 0, 0, 0, 0, 0}, a1, a2, a3, mean, sd, rstd, qv, gg, kq, rr;
      auto E1 = [](int s) constexpr { return (s == 1 || s == 2) ? 2 : s == 11 ? 1 : 0; };
      sfor<12>([&](auto S) AI { constexpr int s = S.value;
        if constexpr (s == 0) waitv<16 + 8>(); else waitv<16 + E1(s - 2) + E1(s - 1)>();
        bar();
        src1(s + 4); ival(G, [&](auto U) AI { constexpr int u = U.value;
          if constexpr ((s == 1 || s == 2) && (u == 2 || u == 5)) ld128p(xr[s - 1][u / 3], rx, lane * 16u, (ci * 128 + 8 * cj + 2 * wq + s - 1) * 2048 + (u / 3) * 1024);
          if constexpr (s == 1 && u == 0) { PIN2(pdt, plr); PIN(pli); lr = bf2f(plr); li = bf2f(pli); const float dt = rbf(fexp(bf2f(pdt))); lp = (f32x2){fexp(dt * lr), dt * li}; PIN(lp); }
          if constexpr (s == 1 && u == 3) { PIN2(bq, oq); wrf(T_B1 + tq * 4, bf2f(bq)); wrf(T_OUT + tq * 4, bf2f(oq)); PIN(epr); epr = __builtin_amdgcn_readfirstlane(epr) + 1u; }
          if constexpr (s == 1 && u == 6) { kq = __builtin_rintf(lp.y * 0.63661977236f);
            rr = __builtin_fmaf(kq, -1.5707962513f, lp.y); rr = __builtin_fmaf(kq, -7.5497894159e-08f, rr); rr = __builtin_fmaf(kq, -5.3903029534e-15f, rr); PIN2(kq, rr); }
          if constexpr (s == 2 && u == 0) { const float z = rr * rr;
            a1 = __builtin_fmaf(rr * z, __builtin_fmaf(z, __builtin_fmaf(z, __builtin_fmaf(z, 2.7183114939e-06f, -1.9839334836e-04f), 8.3333293858e-03f), -1.6666666642e-01f), rr); PIN(a1); }
          if constexpr (s == 2 && u == 3) { const float z = rr * rr;
            a2 = __builtin_fmaf(z, __builtin_fmaf(z, __builtin_fmaf(z, __builtin_fmaf(z, 2.4390448796e-05f, -1.3886763775e-03f), 4.1666623324e-02f), -4.9999999726e-01f), 1.f); PIN(a2); }
          if constexpr (s == 2 && u == 6) { const int q = (int)kq; const float sa_ = sel<float>(q & 1, a2, a1), ca_ = sel<float>(q & 1, a1, a2);
            lp = (f32x2){lp.x * (((q + 1) & 2) ? -ca_ : ca_), lp.x * ((q & 2) ? -sa_ : sa_)}; PIN(lp); wrf2(T_CON + lane * 8, lp); }
          if constexpr (s == 3 && u == 0) { const float nr = lp.x - 1.f, ni = lp.y, den = 2.f * __builtin_amdgcn_rcpf(lr * lr + li * li);
                                            wrf2(T_CON + 512 + lane * 8, (f32x2){(nr * lr + ni * li) * den, (ni * lr - nr * li) * den}); }
          if constexpr ((s == 3 && u >= 2) || (s == 4 && u >= 4)) { constexpr int q = s == 3 ? u - 2 : u + 2; lp = cmul(lp, lp); PIN(lp);
            if constexpr (q >= 3 && q < 7) wrf2(T_CON + (q - 1) * 512 + lane * 8, lp);
            if constexpr (q == 9) wrf2(T_CON + 3072 + lane * 8, lp); }
          if constexpr ((s == 4 && u < 2) || (s >= 5 && s < 9 && !(u & 1) && 4 * (s - 5) + 2 + (u >> 1) < 16)) {
            constexpr int id = s == 4 ? u : 4 * (s - 5) + 2 + (u >> 1), r = id >> 3, e = id & 7;
            PIN2(xr[r][e >> 2], gq[e >> 2]);
            const unsigned v = xr[r][e >> 2][e & 3], gv = gq[e >> 2][e & 3];
            const float x0 = bf2f(v & 0xffffu), x1 = u2f(v & 0xffff0000u);
            sv[r] += x0 + x1; sv[2 + r] = __builtin_fmaf(x1, x1, __builtin_fmaf(x0, x0, sv[2 + r]));
            sv[4 + r] = __builtin_fmaf(x1, u2f(gv & 0xffff0000u), __builtin_fmaf(x0, bf2f(gv & 0xffffu), sv[4 + r]));
            PIN2(sv[r], sv[2 + r]); PIN(sv[4 + r]);
          }
          if constexpr (s == 9 && u < 4) {
#pragma unroll
            for (int q = 0; q < 6; ++q) sv[q] = u == 0 ? dppadd<0xB1>(sv[q]) : u == 1 ? dppadd<0x4E>(sv[q]) : u == 2 ? dppadd<0x141>(sv[q]) : dppadd<0x140>(sv[q]);
            PIN2(sv[0], sv[1]); PIN2(sv[2], sv[3]); PIN2(sv[4], sv[5]);
          }
          if constexpr (s == 9 && u == 5) { const bool r1 = lane & 4; a1 = sum16(sel<float>(r1, sv[1], sv[0])); a2 = sum16(sel<float>(r1, sv[3], sv[2])); a3 = sum16(sel<float>(r1, sv[5], sv[4])); PIN2(a1, a2); PIN(a3); }
          if constexpr (s == 10 && u == 0) { a1 = sum32(a1); a2 = sum32(a2); a3 = sum32(a3); PIN2(a1, a2); PIN(a3); }
          if constexpr (s == 10 && u == 2) { mean = a1 * (1.f / 1024.f); const float var = __builtin_fmaxf(a2 * (1.f / 1024.f) - mean * mean, 0.f) + 1e-5f; rstd = __builtin_amdgcn_rsqf(var); sd = var * rstd;
                                             PIN2(mean, rstd); PIN(sd); }
          if constexpr (s == 10 && u == 4) { qv = 1.f + fexp(__builtin_fminf(a3, 60.f)); gg = __builtin_amdgcn_rcpf(1.f + fexp(__builtin_fminf(-a3, 80.f))); PIN2(qv, gg); }
          if constexpr (s == 11 && u == 0) { a1 = sel4(lane & 1, lane & 2, -2.302208198f * rstd, -0.1029432397f * rstd * rstd * rstd, qv * sd, gg); PIN(a1); }
          if constexpr (s == 11 && u == 2) { const float mh = rbf(mean), sh = rbf(sd);
                                             a2 = sel4(lane & 1, lane & 2, u2f(pk2(-mh, -mh)), u2f(pk2(mh - mean, sh)), u2f(pk2(sd - sh, 0.f)), 0.f); PIN(a2); }
          if constexpr (s == 11 && u == 5) { if (lane < 16) st64(rws, (u32x2){f2u(sel<float>(lane & 8, a2, a1)), epr}, (lane >> 3) * 4096u + (lane & 7) * 8, rgb + (8 * cj + 2 * wq) * 32); }
        });
      });
      rb = mk((const char*)w1 - 4096, 1024 * 7168 + 4096);
#pragma unroll
      for (int q = 0; q < 4; ++q) va[q] = vb[q];
      sx = 0; nexts();
      const unsigned sb0 = sb, sa0 = sa; nexts();
      const unsigned sb1 = sb, sa1 = sa; nexts();
      const unsigned sa2 = sa;
      waitv<16 + 1>(); bar();
      sa = sa0; sfor<4>([&](auto U) AI { G2(IC<0>{}, U); });
      sa = sa1; sfor<4>([&](auto U) AI { G2(IC<1>{}, U); });
      waitv<16 + 1>(); bar();
      sb = sb0; sfor<7>([&](auto U) AI { G2(IC<0>{}, IC<U.value + 4>{}); });
      sa = sa2; sfor<4>([&](auto U) AI { G2(IC<2>{}, U); });
      waitv<19>(); bar();
      sb = sb1; sfor<7>([&](auto U) AI { G2(IC<1>{}, IC<U.value + 4>{}); });
      ta = t3a; tb = t3b; sx = 2; nexts(); sa2h = sa; next3();
    }
    {
      {
        bf16x8 fa[2][4][2], fb[8][2];
        unsigned xn = 98304, wnx = 32768;
        setm0(T_DUM);
        auto rd = [&](int X, int r) AI {
          if (r >= 4 && r < 12) { const int a = (r - 4) >> 1, ks = r & 1; fa[X][a][ks] = rd128(ba[(a >> 1) * 2 + ks] + (a & 1) * 2048); }
          else { const int b = r < 4 ? 0 : (r - 8) >> 2, ks = (r >> 1) & 1, h = r & 1;
            sethalf(fb[b][ks], h, rtr(b < 6 ? ba[4 + (b & 1)] + (b >> 1) * 2048 + ks * 14336 + h * 512 : ba[b] + ks * 14336 + h * 256)); } };
        auto mm = [&](int X, int q, int) AI { const int b = q < 48 ? q >> 3 : 6 + ((q - 48) >> 2), a = (q >> 1) & (q < 48 ? 3 : 1), ks = q & 1, m = (a < 2 ? 8 * a : 4 + 6 * a) + b;
          MMA(ac1[m], fb[b][ks], fa[X][a][ks]); };
        auto up = [&](int b) AI { if (b < 8) { ba[b] = cb[b] + sel<unsigned>(b < 4, xn, wnx); PIN(ba[b]); }
                                  else if (b == 8) { xn = (xn == 131072u) ? 98304u : xn + 16384u; PINS(xn); }
                                  else if (b == 10) { wnx = (wnx == 65536u) ? 0u : wnx + 32768u; PINS(wnx); } };
        waitl0(); waitv<11>(); bar();
        PH(2);
        period<56, 16, 0, true, 40, 48, 12>(rd, mm, up); waitv<0>(); bar();
        const int sp = (loc + xcd) & 7;
        for (int s = 0; s < 14; s += 2) {
          period<56, 16, 1, false, 40, 48, 12>(rd, mm, up); bar();
          if (s == sp) { pfg(pp0); pfg(pp1); }
          period<56, 16, 0, false, 40, 48, 12>(rd, mm, up); bar();
          if (s + 1 == sp) { pfg(pp0); pfg(pp1); }
        }
        period<56, 16, 1, false, 40, 48, 12>(rd, mm, up); bar();
        PH(3);
        setup3(); sl = 0;
        setm0(98304 + wq * 4096); sfor<4>([&](auto U) AI { dma1<U.value * 1024>(rws, va[0], sa2h); });
        ival(G, [&](auto U) AI { mm(1, 40 + 2 * U.value, 0); mm(1, 41 + 2 * U.value, 0); });
#pragma unroll
        for (int a = 0; a < 4; ++a) fa[0][a][0] = rd128(g ? T_ZERO : T_XA + (64 * wm + 16 * (a ^ (2 * wn)) + r15) * 16);
#pragma unroll
        for (int b = 0; b < 8; ++b) fb[b][0] = rd128(T_WA + ((b < 6 ? (b >> 1) * 64 + wn * 32 + (b & 1) * 16 : 96 + b * 16) + r15) * 16);
        SB();
        sl = nq(sl); next3();
        bar();
        ival(G, [&](auto U) AI { constexpr int u = U.value;
#pragma unroll
          for (int q = 8 * u; q < 8 * u + 8; q += 2) if (q < 56) mm(0, q, 0); });
        sl = nq(sl); next3();
      }
      float k0[4], k1[4], qq[4];
#pragma unroll
      for (int a = 0; a < 4; ++a) { const unsigned ro = T_ROW + (64 * wm + 16 * (a ^ (2 * wn)) + r15) * 4; k0[a] = rdf(ro); k1[a] = rdf(ro + 512); qq[a] = rdf(ro + 1024); }
      u32x4 hw[4]; float t[8];
      auto lv0 = [](int s) constexpr { return s < 1 ? 0 : s < 6 ? 13 * (s - 1) : (64 + 8 * (s - 6) < 224 ? 64 + 8 * (s - 6) : 224); };
      auto st3 = [](int s) constexpr { return (s == 5 || s == 13 || s == 21) ? 4 : s == 25 ? 2 : 0; };
      sfor<36>([&](auto S1) AI { constexpr int s = S1.value + 1;
        if constexpr (s == 7) waitv<8>();
        else waitv<16 + st3(s - 2) + st3(s - 1)>();
        if constexpr (s == 12 || s == 20 || s == 28 || s == 36) {
          if (w == 4) check_flags(pv, rws, fvo, flagb + (FK_M1 + (s - 12) / 8) * 128, lane, s == 36 ? 0 : 8 * half, 8 * half + 8, epr); }
        SB(); bar();
        if constexpr (s == 16) { PH(4); }
        if constexpr (s == 7 || s == 16 || s == 24 || s == 28) { if (w == 5) stflag(rws, epr, lane, flagb + (FK_M1 + (s - 4) / 8) * 128 + cj * 4); }
        if constexpr (s == 10 || s == 18 || s == 26 || s == 34) { if (w == 4) ld32a(pv, rws, fvo, flagb + (FK_M1 + (s - 10) / 8) * 128); }
#define UU(e) ac1[m0 + ((e) >> 2)][(e) & 3]
#define LVL(expr) do { _Pragma("unroll") for (int e = 4 * hh; e < 4 * hh + 4; ++e) t[e] = (expr); PIN2(t[4 * hh], t[4 * hh + 1]); PIN2(t[4 * hh + 2], t[4 * hh + 3]); } while (0)
        auto hlev = [&](auto LL) AI { constexpr int L = LL.value, v = L >> 4, l = (L >> 1) & 7, hh = L & 1, a = v & 3, m0 = (a < 2 ? 8 * a : 4 + 6 * a) + 2 * (v >> 2);
          if constexpr (l == 0) LVL(UU(e) * UU(e));
          if constexpr (l == 1) LVL(__builtin_fmaf(t[e], k1[a], k0[a]));
          if constexpr (l == 2) LVL(UU(e) * t[e]);
          if constexpr (l == 3) LVL(__builtin_amdgcn_exp2f(t[e]));
          if constexpr (l == 4) LVL(__builtin_fmaf(t[e], qq[a], qq[a]));
          if constexpr (l == 5) LVL(__builtin_amdgcn_rcpf(t[e]));
          if constexpr (l == 6) LVL(UU(e) * t[e]);
          if constexpr (l == 7) { hw[a][2 * hh] = pk2(t[4 * hh], t[4 * hh + 1]); hw[a][2 * hh + 1] = pk2(t[4 * hh + 2], t[4 * hh + 3]); PIN(hw[a]); }
        };
        ival(G, [&](auto U) AI { constexpr int u = U.value, n = lv0(s + 1) - lv0(s), i0 = (n * u + 7) / 8, i1 = (n * (u + 1) + 7) / 8;
          if constexpr (i0 < i1) hlev(IC<lv0(s) + i0>{});
          if constexpr (i0 + 1 < i1) hlev(IC<lv0(s) + i0 + 1>{});
        });
#pragma unroll
        for (int q = 0; q < st3(s); ++q) st128(rws, hw[q], lane * 16u + (4 * wm + (q ^ (2 * wn))) * 1024, ci * HID_PANEL + kb_of(cj, s < 25 ? 2 * (s >> 3) + wn : 6) * 8192);
        sl = nq(sl);
        if constexpr (s < 11) next3(); else nexts();
        if constexpr (s == 11) {
          rb = mk(w2, 3584 * 2048);
#pragma unroll
          for (int q = 0; q < 4; ++q) vb[q] = (4 * q + g) * 2048 + ((((r15 >> 1) ^ (4 * (q & 1) + g)) * 2) + (lane & 1)) * 16 - 1024 * q;
        }
      });
    }
    static_assert(MOVE >= 0 && MOVE <= 3, "");
    sfor<3 - MOVE>([&](auto) AI { waitv<16>(); bar(); ival(G, nowork); sl = nq(sl); nexts(); });
    if (half) sfor<2 * MOVE>([&](auto) AI { waitv<16>(); bar(); ival(G, nowork); sl = nq(sl); nexts(); });
    waitv<16>(); bar();
    if (half) {
      sfor<8>([&](auto U) AI { constexpr int u = U.value; if constexpr ((u & 3) == 0) setm0(sl + wq * 8192 + u * 1024);
        dma<(u & 3) * 1024>(rx, (4u * (u & 3) + g) * 2048 + r15 * 16 - (u & 3) * 1024, (ci * 128 + 32 * wq + 16 * (u >> 2)) * 2048 + cnt * 256); });
      waitv<8 + 8>();
    } else waitv<8>();
    bar();
    ld32a(pv, rws, fvo, flagb + FK_P * 128);
    if (half) waitv<9>(); else waitv<0>();
    bar();
    auto getp = [&]() AI {
      sfor<8>([&](auto U) AI { constexpr int u = U.value; if constexpr ((u & 3) == 0) setm0(nq(sl) + wq * 8192 + u * 1024);
        dma1<(u & 3) * 1024>(rws, lane * 16u, PART_OFF + (ci * 8 + cnt) * 32768 + wq * 8192 + (u >> 2) * 4096); }); };
    waitv<0>(); PIN(pv);
    const bool early = half && !anyl(lane == cnt && pv != epr);
    if (early) getp(); else ld32a(pv, rws, fvo, flagb + FK_P * 128);
    bar();
    if (half) {
      if (!early) { waitv<0>(); check_flags(pv, rws, fvo, flagb + FK_P * 128, lane, cnt, cnt + 1, epr); getp(); }
      if (cnt == 0 && w == 4) stflag(rws, epr, lane, flagb + FK_EP * 128);
    }
    PH(5);
    waitv<0>(); bar();
  }
  if (half) {
    const int bg = r31 & 15, sw = (r31 >> 4) * 8 + (bg >> 3) * 4 + (bg & 3);
    const unsigned pa = ((sl + 65536u + (w >> 2) * 32768u) & 131071u) + (r31 >> 4) * 16384 + (wq * 4 + (bg >> 2)) * 1024 + (bg & 3) * 256;
    const unsigned pp = nq(sl) + ((w >> 2) * 2 + (r31 >> 4)) * 8192 + (wq * 2 + (bg >> 3)) * 1024 + (bg & 3) * 256 + ((bg >> 2) & 1) * 8;
    const f32x4 bv = rdf4(T_OUT + r31 * 16), dv = rdf4(T_OUT + 512 + r31 * 16);
    f32x4 av[8]; u32x2 pw[8], xw[8]; float gg[8];
#pragma unroll
    for (int u = 0; u < 8; ++u) {
      const int row = 2 * u + hl;
      av[u] = rdf4(pa + ((row ^ bg) * 16)); pw[u] = *(LAS u32x2*)(size_t)(pp + ((row ^ sw) * 16));
      xw[u] = *(LAS u32x2*)(size_t)(sl + (16 * w + hl) * 256 + r31 * 8 + u * 512); gg[u] = rdf(T_ROW + 1536 + (16 * w + hl) * 4 + u * 8);
    }
    SB();
#pragma unroll
    for (int u = 0; u < 8; ++u) {
      const f32x4 pf = {bf2f(pw[u][0] & 0xffffu), u2f(pw[u][0] & 0xffff0000u), bf2f(pw[u][1] & 0xffffu), u2f(pw[u][1] & 0xffff0000u)};
      const f32x4 xf = {bf2f(xw[u][0] & 0xffffu), u2f(xw[u][0] & 0xffff0000u), bf2f(xw[u][1] & 0xffffu), u2f(xw[u][1] & 0xffff0000u)};
      f32x4 r;
#pragma unroll
      for (int e = 0; e < 4; ++e) r[e] = (av[u][e] + pf[e]) + __builtin_fmaf(gg[u], __builtin_fmaf(xf[e], dv[e], -bv[e]), bv[e]);
      st128<OAUX>(mk(out, 8u << 20), __builtin_bit_cast(u32x4, r), (unsigned)((16 * w + hl + 2 * u) * 4096 + r31 * 16), ci * 128 * 4096 + cnt * 512);
    }
  }
}

extern "C" void kernel_launch(void* const* d_in, const int* in_sizes, int n_in, void* d_out, int out_size, void* d_ws, size_t ws_size, hipStream_t stream) {
  (void)in_sizes; (void)n_in; (void)out_size; (void)ws_size;
  (void)hipFuncSetAttribute((const void*)fused_kernel, hipFuncAttributeMaxDynamicSharedMemorySize, LDS_BYTES);
  fused_kernel<<<256, 512, LDS_BYTES, stream>>>((const u16*)d_in[0], (const u16*)d_in[1], (const u16*)d_in[2], (const u16*)d_in[3], (const u16*)d_in[4],
      (const u16*)d_in[5], (const u16*)d_in[6], (const u16*)d_in[7], (const u16*)d_in[8], (const u16*)d_in[10], (const u16*)d_in[11], (const u16*)d_in[12],
      (const u16*)d_in[13], (const u16*)d_in[14], (char*)d_ws, (float*)d_out);
}
```

```cpp
#include <hip/hip_runtime.h>
#include <utility>

typedef unsigned short u16;
typedef __bf16 bf16x2 __attribute__((ext_vector_type(2)));
typedef __bf16 bf16x4 __attribute__((ext_vector_type(4)));
typedef __bf16 bf16x8 __attribute__((ext_vector_type(8)));
typedef float f32x2 __attribute__((ext_vector_type(2)));
typedef float f32x4 __attribute__((ext_vector_type(4)));
typedef float f32x16 __attribute__((ext_vector_type(16)));
typedef unsigned u32x2 __attribute__((ext_vector_type(2)));
typedef unsigned u32x4 __attribute__((ext_vector_type(4)));
typedef __amdgpu_buffer_rsrc_t rsrc_t;
#define LAS __attribute__((address_space(3)))
#define DI __device__ __forceinline__
#define AI __attribute__((always_inline))
#define SB() __builtin_amdgcn_sched_barrier(0)
#define PIN(x) asm volatile("" : "+v"(x))
#define MFMA(a, b, c) __builtin_amdgcn_mfma_f32_16x16x32_bf16(a, b, c, 0, 0, 0)
#define MMA(c, a, b) asm volatile("v_mfma_f32_16x16x32_bf16 %0, %1, %2, %0" : "+v"(c) : "v"(a), "v"(b))
#define MPAD() asm volatile("s_nop 7")
template <int V> using IC = std::integral_constant<int, V>;

constexpr unsigned HID_PANEL = 112 * 8192, H_OFF = 16 * HID_PANEL, H_PANEL = 32 * 16384;
constexpr unsigned PART_OFF = H_OFF + 16 * H_PANEL;
constexpr unsigned GRAN_OFF = PART_OFF + 128 * 32768;
constexpr unsigned RG_OFF = GRAN_OFF + 256 * 1024;
constexpr unsigned CG_OFF = RG_OFF + 16 * 8192;
constexpr unsigned FLAG_OFF = CG_OFF + 256 * 2048;
constexpr unsigned EPC_OFF = FLAG_OFF + 16 * 1024;
constexpr unsigned WS_BYTES = EPC_OFF + 16 * 128;
constexpr int FK_M1 = 0, FK_M2 = 1, FK_M3 = 2, FK_M4 = 3, FK_H = 4, FK_P = 5, FK_EP = 6;
constexpr unsigned TAB = 147456, T_ROW = TAB, T_XA = TAB + 2048, T_OUT = TAB + 4096, T_B1 = TAB + 5120, T_CS2 = TAB + 6144, T_WA = TAB + 8192, T_ZERO = TAB + 12288, T_AGG = T_WA, T_DUM = TAB + 12544, T_CON = TAB + 12800;
constexpr unsigned LDS_BYTES = 163840;
#define XMAP 1
#define ROTM 3
#define OAUX 2
#define SP0 4
#define SPN 6
#define PTE 0
#define PFX 1
#define W1B 1
#define MOVE 3
#define PROFX false
#define WD 8
constexpr int LASTR = 24;
constexpr unsigned SPIN_CAP = 1u << 16;

DI rsrc_t mk(const void* p, unsigned bytes) { return __builtin_amdgcn_make_buffer_rsrc((void*)p, 0, bytes, 0x00020000); }
DI void setm0(unsigned lds) { asm volatile("s_mov_b32 m0, %0\n\ts_nop 0" ::"s"(lds) : "memory"); }
template <int OFF> DI void dma(rsrc_t r, unsigned voff, unsigned soff) {
  asm volatile("buffer_load_dwordx4 %0, %1, %2 offen offset:%3 lds" ::"v"(voff), "s"(r), "s"(soff), "i"(OFF) : "memory");
}
template <int OFF> DI void dma1(rsrc_t r, unsigned voff, unsigned soff) {
  asm volatile("buffer_load_dwordx4 %0, %1, %2 offen offset:%3 sc1 lds" ::"v"(voff), "s"(r), "s"(soff), "i"(OFF) : "memory");
}
#define PIN2(x, y) asm volatile("" : "+v"(x), "+v"(y))
#define PINS(x) asm volatile("" : "+s"(x))
typedef const void* const __attribute__((address_space(4)))* kargs_t;
DI const char* karg(int i) { kargs_t ka = (kargs_t)__builtin_amdgcn_kernarg_segment_ptr(); PINS(ka); return (const char*)ka[i]; }
enum { A_X, A_LRE, A_LIM, A_BRE, A_BIM, A_CRE, A_CIM, A_LDT, A_DSK, A_W1, A_B1, A_W2, A_B2, A_GW, A_WS, A_OUT };
template <int N> DI void waitv() { asm volatile("s_waitcnt vmcnt(%0)" ::"i"(N) : "memory"); }
DI void waitl0() { SB(); __builtin_amdgcn_s_waitcnt(0xC07F); asm volatile("" ::: "memory"); }
#define PH(k)
DI void bar() { asm volatile("s_barrier" ::: "memory"); SB(); }
DI bf16x8 rd128(unsigned a) { return *(LAS bf16x8*)(size_t)a; }
DI u32x4 rdu4(unsigned a) { return *(LAS u32x4*)(size_t)a; }
DI f32x4 rdf4(unsigned a) { return *(LAS f32x4*)(size_t)a; }
DI f32x2 rdf2(unsigned a) { return *(LAS f32x2*)(size_t)a; }
DI float rdf(unsigned a) { return *(LAS float*)(size_t)a; }
DI void wrf(unsigned a, float v) { *(LAS float*)(size_t)a = v; }
DI void wrf2(unsigned a, f32x2 v) { *(LAS f32x2*)(size_t)a = v; }
DI bf16x4 rtr(unsigned a) { return __builtin_amdgcn_ds_read_tr16_b64_v4bf16((LAS bf16x4*)(size_t)a); }
DI void sethalf(bf16x8& f, int h, bf16x4 v) {
  if (h == 0) f = __builtin_shufflevector(v, v, 0, 1, 2, 3, -1, -1, -1, -1);
  else { bf16x8 t = __builtin_shufflevector(v, v, -1, -1, -1, -1, 0, 1, 2, 3); f = __builtin_shufflevector(f, t, 0, 1, 2, 3, 12, 13, 14, 15); }
}
DI unsigned f2u(float f) { return __builtin_bit_cast(unsigned, f); }
DI float u2f(unsigned u) { return __builtin_bit_cast(float, u); }
DI float shx(int lane, float v, int m) { return u2f(__builtin_amdgcn_ds_bpermute((lane ^ m) * 4, (int)f2u(v))); }
DI float sum16(float v) { const auto r = __builtin_amdgcn_permlane16_swap(f2u(v), f2u(v), false, false); return u2f(r[0]) + u2f(r[1]); }
DI float sum32(float v) { const auto r = __builtin_amdgcn_permlane32_swap(f2u(v), f2u(v), false, false); return u2f(r[0]) + u2f(r[1]); }
DI float fexp(float v) { return __builtin_amdgcn_exp2f(v * 1.4426950408889634f); }
DI float bf2f(unsigned v) { return u2f(v << 16); }
DI unsigned pk2(float lo, float hi) { f32x2 v = {lo, hi}; return __builtin_bit_cast(unsigned, __builtin_convertvector(v, bf16x2)); }
DI float rbf(float f) { return u2f(pk2(f, 0.f) << 16); }
DI float dot2(unsigned a, unsigned b, float c) { return __builtin_amdgcn_fdot2_f32_bf16(__builtin_bit_cast(bf16x2, a), __builtin_bit_cast(bf16x2, b), c, false); }
template <int C> DI float dppadd(float v) { return v + u2f(__builtin_amdgcn_update_dpp(0, (int)f2u(v), C, 0xf, 0xf, true)); }
DI float sel4(bool b0, bool b1, float a, float b, float c, float d) { PIN(a); PIN(b); PIN(c); PIN(d); const float lo = b0 ? b : a, hi = b0 ? d : c; return b1 ? hi : lo; }
DI f32x2 cmul(f32x2 a, f32x2 b) { return (f32x2){a.x * b.x - a.y * b.y, a.x * b.y + a.y * b.x}; }
DI f32x2 cmulc(f32x2 a, f32x2 b) { return (f32x2){__builtin_fmaf(a.x, b.x, -(a.y * b.y)), __builtin_fmaf(-a.x, b.y, -(a.y * b.x))}; }
DI f32x2 cfma(f32x2 a, f32x2 b, f32x2 c) { return (f32x2){__builtin_fmaf(a.x, b.x, __builtin_fmaf(-a.y, b.y, c.x)), __builtin_fmaf(a.x, b.y, __builtin_fmaf(a.y, b.x, c.y))}; }
template <int AUX = 16> DI void st128(rsrc_t r, u32x4 v, unsigned voff, unsigned soff) {
  __builtin_amdgcn_raw_buffer_store_b128(v, r, voff, soff, AUX);
  asm volatile("s_nop 1" ::"v"(v));
}
DI void st64(rsrc_t r, u32x2 v, unsigned voff, unsigned soff) { __builtin_amdgcn_raw_buffer_store_b64(v, r, voff, soff, 16); }
DI void st32(rsrc_t r, unsigned v, unsigned voff, unsigned soff) { __builtin_amdgcn_raw_buffer_store_b32(v, r, voff, soff, 16); }
DI void stflag(rsrc_t r, unsigned v, int lane, unsigned soff) { if (lane == 0) st32(r, v, 0u, soff); }
DI void ld32a(unsigned& d, rsrc_t r, unsigned voff, unsigned soff) { asm volatile("buffer_load_dword %0, %1, %2, %3 offen sc1" : "=v"(d) : "v"(voff), "s"(r), "s"(soff) : "memory"); }
DI void ld16p(unsigned& d, const u16* p, unsigned voff) { asm volatile("global_load_ushort %0, %1, %2" : "=v"(d) : "v"(voff), "s"(p) : "memory"); }
DI void ld64p(u32x2& d, rsrc_t r, unsigned voff, unsigned soff) { asm volatile("buffer_load_dwordx2 %0, %1, %2, %3 offen" : "=v"(d) : "v"(voff), "s"(r), "s"(soff) : "memory"); }
DI void ld64a(u32x2& d, rsrc_t r, unsigned voff, unsigned soff) { asm volatile("buffer_load_dwordx2 %0, %1, %2, %3 offen sc1" : "=v"(d) : "v"(voff), "s"(r), "s"(soff) : "memory"); }
DI void ld128g(u32x4& d, const u16* p, unsigned voff) { asm volatile("global_load_dwordx4 %0, %1, %2" : "=v"(d) : "v"(voff), "s"(p) : "memory"); }
DI void pfl(unsigned& d, rsrc_t r, unsigned voff, unsigned soff) { asm volatile("buffer_load_dword %0, %1, %2, %3 offen" : "+v"(d) : "v"(voff), "s"(r), "s"(soff) : "memory"); }
template <class T> DI T sel(bool c, T a, T b) { return c ? a : b; }
DI void pfg(const char* p) { asm volatile("global_load_lds_dword %0, off" ::"v"(p) : "memory"); }
DI bool anyl(bool b) { return __builtin_amdgcn_ballot_w64(b) != 0; }
DI void ld128p(u32x4& d, rsrc_t r, unsigned voff, unsigned soff) { asm volatile("buffer_load_dwordx4 %0, %1, %2, %3 offen" : "=v"(d) : "v"(voff), "s"(r), "s"(soff) : "memory"); }
DI void sincos_f(float t, float& sn, float& cs) {
  const float k = __builtin_rintf(t * 0.63661977236f);
  float r = __builtin_fmaf(k, -1.5707962513f, t); r = __builtin_fmaf(k, -7.5497894159e-08f, r); r = __builtin_fmaf(k, -5.3903029534e-15f, r);
  const float z = r * r;
  const float sp = __builtin_fmaf(r * z, __builtin_fmaf(z, __builtin_fmaf(z, __builtin_fmaf(z, 2.7183114939e-06f, -1.9839334836e-04f), 8.3333293858e-03f), -1.6666666642e-01f), r);
  const float cp = __builtin_fmaf(z, __builtin_fmaf(z, __builtin_fmaf(z, __builtin_fmaf(z, 2.4390448796e-05f, -1.3886763775e-03f), 4.1666623324e-02f), -4.9999999726e-01f), 1.f);
  const int q = (int)k;
  const float a = (q & 1) ? cp : sp, b = (q & 1) ? sp : cp;
  sn = (q & 2) ? -a : a; cs = ((q + 1) & 2) ? -b : b;
}
DI void ld128a(u32x4& d, rsrc_t r, unsigned voff, unsigned soff) { asm volatile("buffer_load_dwordx4 %0, %1, %2, %3 offen sc1" : "=v"(d) : "v"(voff), "s"(r), "s"(soff) : "memory"); }

DI unsigned x1off(int a) { return a == 0 ? 131072u : (unsigned)(81920 + a * 16384); }
DI unsigned nq(unsigned s) { return (s + 32768u) & 131071u; }

DI int kb_of(int j, int bl) { return 7 * j + ((j & 1) ? (bl == 6 ? 0 : bl + 1) : bl); }

template <int NR, int LAST> constexpr int nread(int sl) {
  constexpr int DENSE = 2 * NR - LAST - 2;
  return sl < 0 ? 0 : sl < DENSE ? sl + 1 : sl >= LAST ? NR : DENSE + (sl - DENSE) / 2 + 1;
}
template <int N, int L, int X, bool FIRST, int NR, int LAST, int NB, int SL, class RD, class MM, class UP>
DI void slot(RD& rd, MM& mm, UP& upd) {
  if constexpr (!FIRST && SL >= L && (SL == L + 4 || (SL - L) % 8 == 0)) {
    __builtin_amdgcn_s_waitcnt(0xC07F | ((nread<NR, LAST>(SL - 1) - nread<NR, LAST>(SL - WD)) << 8)); SB();
  }
  if constexpr (SL >= L) mm(X, SL - L, SL); else if constexpr (!FIRST) mm(X ^ 1, SL + N - L, SL);
  SB();
  if constexpr (nread<NR, LAST>(SL) > nread<NR, LAST>(SL - 1)) rd(X, nread<NR, LAST>(SL) - 1);
  constexpr int per = NB > N - 1 - LAST ? 2 : 1, u0 = (SL - LAST - 1) * per;
  if constexpr (SL > LAST && u0 < NB) { upd(u0); if constexpr (per == 2 && u0 + 1 < NB) upd(u0 + 1); }
  SB();
}
template <int N, int L, int X, bool FIRST, int NR, int LAST, int NB, class RD, class MM, class UP, int... I>
DI void period_(RD& rd, MM& mm, UP& upd, std::integer_sequence<int, I...>) { (slot<N, L, X, FIRST, NR, LAST, NB, I>(rd, mm, upd), ...); }
template <int N, int L, int X, bool FIRST, int NR, int LAST, int NB, class RD, class MM, class UP>
DI void period(RD& rd, MM& mm, UP& upd) {
  static_assert(NR - 1 <= LAST && !(LAST & 1) && NB <= 2 * (N - 1 - LAST), "");
  period_<N, L, X, FIRST, NR, LAST, NB>(rd, mm, upd, std::make_integer_sequence<int, N>{});
  waitl0();
}
template <class G, class W, int... U> DI void ival_(G& g, W& wk, std::integer_sequence<int, U...>) { ((g(IC<U>{}), SB(), wk(IC<U>{}), SB()), ...); }
template <class G, class W> DI void ival(G&& g, W&& wk) { ival_(g, wk, std::make_integer_sequence<int, 8>{}); }
template <int N, class F, int... I> DI void sfor_(F& f, std::integer_sequence<int, I...>) { (f(IC<I>{}), ...); }
template <int N, class F> DI void sfor(F&& f) { sfor_<N>(f, std::make_integer_sequence<int, N>{}); }

DI void check_flags(unsigned& pv, rsrc_t ws, unsigned voff, unsigned soff, int lane, int lo, int hi, unsigned ep) {
  PIN(pv);
  const bool need = lane >= lo && lane < hi;
  if (__builtin_expect(anyl(need && pv != ep), 0)) {
    for (unsigned sp = 0; sp < SPIN_CAP; ++sp) {
      __builtin_amdgcn_s_sleep(2);
      ld32a(pv, ws, voff, soff); asm volatile("s_waitcnt vmcnt(0)" : "+v"(pv)::"memory");
      if (!anyl(need && pv != ep)) break;
    }
  }
}

#if defined(__HIP_DEVICE_COMPILE__) && __HIP_DEVICE_COMPILE__
__attribute__((target("no-packed-fp32-ops")))
#endif
__global__ __launch_bounds__(512, 2) void fused_kernel(const u16* x, const u16* lre, const u16* lim, const u16* bre, const u16* bim, const u16* cre, const u16* cim,
    const u16* ldt, const u16* dsk, const u16* w1, const u16* b1, const u16* w2, const u16* b2, const u16* gw, char* ws, float* out) {
  extern __shared__ __attribute__((aligned(16))) char shm[];
  unsigned long long pc0; asm volatile("s_getpc_b64 %0" : "=s"(pc0));
  const int lane = threadIdx.x & 63, w = __builtin_amdgcn_readfirstlane(threadIdx.x >> 6);
  const int bid = blockIdx.x, xcd = bid & 7, loc = bid >> 3;
  const int ci = (xcd & 3) * 4 + (loc & 3), cj = (loc >> 4) * 8 + (xcd >> 2) * 4 + ((loc >> 2) & 3);
  const int rot = (ci + cj) & ROTM, cnt = cj & 7, half = cj >> 3, wq = w & 3, tq = threadIdx.x & 255;
  const int n4 = half ? 28 + MOVE : 28 - MOVE;
  const rsrc_t rws = mk(ws, WS_BYTES), rx = mk(x, 2048 * 2048);
  const int wm = (w >> 1) & 1, wn = w & 1, g = lane >> 4, r15 = lane & 15, q2 = (lane >> 2) & 3, p = lane & 3, hl = lane >> 5, r31 = lane & 31;
  const unsigned flagb = FLAG_OFF + ci * 1024, fvo = (lane & 15) * 4;
  const int os = ((xcd >> 2) ^ PFX) & 1, sid = (loc * 4 + (xcd & 3)) * 4 + wq;
  unsigned rgb = RG_OFF + ci * 8192; PINS(rgb);
  auto nowork = [&](auto) AI {};
  unsigned va[4], vb[4], vw[7], sl, sa, sb, ta, tb; int sx; rsrc_t ra, rb;
  auto vnat = [&](int u) AI { return (8u * u + (lane >> 3)) * 2048 + (((lane & 7) ^ ((4 * u + g) & 7)) * 16) - 1024 * u; };
  auto nexts = [&]() AI { sa = __builtin_amdgcn_readlane(ta, sx); sb = __builtin_amdgcn_readlane(tb, sx); ++sx; PINS(sa); PINS(sb); };
  auto G = [&](auto U) AI { constexpr int u = U.value;
    if constexpr (u == 0) setm0(sl + wq * 4096);
    if constexpr (u == 4) setm0(sl + 16384 + wq * 4096);
    if constexpr (u < 4) dma1<u * 1024>(ra, va[u], sa); else dma<(u - 4) * 1024>(rb, vb[u - 4], sb);
  };
  auto G2 = [&](auto S, auto U) AI { constexpr int a = S.value % 3, u = U.value;
    if constexpr (u == 0) setm0((a == 0 ? 131072u : 81920u + a * 16384u) + wq * 4096);
    if constexpr (u == 4) setm0(a * 32768 + wq * 7168);
    if constexpr (u == 8) setm0(a * 32768 + wq * 7168 + 4096);
    if constexpr (u < 4) dma<u * 1024>(rx, va[u], sa); else dma<(u & 3) * 1024>(rb, vw[u - 4], sb);
  };
  const int c7 = ci & 7;
  auto setup2 = [&]() AI {
#pragma unroll
    for (int q = 0; q < 6; ++q) { const int k = 8 * (q & 1) + (lane >> 3), f = ((k >> 1) & 1) | ((q & 1) << 1);
      vw[q] = 4096 + k * 7168 + ((cj & 1) ? 64 : 0) + (q >> 1) * 128 + (((((lane & 7) >> 1) ^ f) * 2) + (lane & 1)) * 16 - (q & 3) * 1024; }
    vw[6] = 4096 + (lane >> 2) * 7168 + ((cj & 1) ? 0 : 384) + (((((lane & 3) >> 1) ^ hl) * 2) + (lane & 1)) * 16 - 2048;
    int k = lane - 2 + (c7 < 2 ? 3 * c7 : c7 - 2); k = 2 + (k >= 6 ? k - 6 : k);
    k = lane < 2 ? lane : lane < 8 ? k : (lane ^ rot);
    k = (k ^ (ci & 8)) & 15;
    ta = (ci * 128 + 32 * wq) * 2048 + k * 128; tb = (k * 64 + 16 * wq) * 7168 + cj * 448;
    PIN2(ta, tb);
#pragma unroll
    for (int q = 0; q < 7; ++q) PIN(vw[q]);
  };
  auto setup3 = [&]() AI {
    ra = rws;
#pragma unroll
    for (int q = 0; q < 4; ++q) { va[q] = lane * 16; vb[q] = vnat(q); }
  };
  auto table3 = [&](unsigned& ta, unsigned& tb) AI {
    const int ks = 16 * half + ((lane ^ (2 * cnt)) & 15);
    int n = 2 * (lane - 16) + (wq >> 1), hp = half; if (n >= 56) { n -= 2 * MOVE; hp = 0; }
    const int kb = kb_of(hp * 8 + ((n ^ rot) & 7), n >> 3);
    ta = lane < 16 ? H_OFF + ci * H_PANEL + ks * 16384 + wq * 4096 : ci * HID_PANEL + kb * 8192 + (wq & 1) * 4096;
    tb = lane < 16 ? (cnt * 128 + 32 * wq) * 2048 + (ks >> 1) * 128 + (ks & 1) : (kb * 32 + 16 * (wq & 1)) * 2048 + cnt * 256;
    PIN(ta); PIN(tb);
  };
  auto next3 = [&]() AI { nexts(); rb = mk(sel<const u16*>(sb & 1, cim, cre), 1024 * 2048); sb &= ~1u; };

  if (w < 4) {
    constexpr bool isx = PROFX; (void)isx;
    unsigned epr, epc;
    const unsigned ca = (32 * wn + r31) * 8;
    const unsigned c0 = (32 * wn + r15) * 8;
    const int seg0 = 4 * wm + 2 * hl;
    f32x2 lam[2], cof[2], cfa, l16, l128, l1k, pw0, pw1; unsigned t3a, t3b;
    f32x4 ac2[4][4];
    {
      setm0(T_DUM);
      const int br = (sid >> 2) * 8 + (lane & 7), brow = (br & 255) + os * 256 + ((br >> 8) & 1) * 512;
      const char* pp = (hl ? (const char*)sel<const u16*>(br >> 9, bim, bre) + brow * 2048 : (const char*)x + (size_t)(bid * 8 + (lane & 7)) * 2048) + (4 * wq + ((lane >> 3) & 3)) * 128;
      for (int q = 0; q <= wq; ++q) __builtin_amdgcn_s_sleep(6);
      pfg(pp);
      pfg((const char*)pc0 + (((loc * 4 + wq) % 20) * 16 + (lane & 15)) * 128);
      ld32a(epr, rws, 0u, flagb + FK_EP * 128); ld32a(epc, rws, 0u, EPC_OFF + cj * 128);
      setup2(); sx = 2; nexts(); table3(t3a, t3b);
      rb = mk((const char*)w1 - 4096, 1024 * 7168 + 4096);
#pragma unroll
      for (int q = 0; q < 4; ++q) { va[q] = vnat(q); PIN(va[q]); }
      wrf(T_ZERO + (lane & 3) * 4, 0.f);
    }
    {
      f32x4 acc[4][4];
#pragma unroll
      for (int a = 0; a < 16; ++a) { acc[a >> 2][a & 3] = (f32x4){0, 0, 0, 0}; PIN(acc[a >> 2][a & 3]); }
      bf16x8 wv[4][2];
      const int sstar = c7 < 2 ? c7 : 2;
      const int fsw = ((q2 >> 1) & 1) | ((g & 1) << 1);
      unsigned cf = sstar * 32768 + (g >> 1) * 7168 + (8 * (g & 1) + q2) * 128 + ((wq ^ fsw) * 32) + p * 8;
      unsigned ch = sstar * 32768 + (g >> 1) * 7168 + 6144 + (8 * (g & 1) + q2) * 64 + (((wq & 1) ^ (g & 1)) * 32) + p * 8;
      PIN(cf); PIN(ch);
      auto wread = [&](int st) AI {
        if (sstar == st) {
#pragma unroll
          for (int e = 0; e < 4; ++e)
#pragma unroll
            for (int t = 0; t < 4; ++t) sethalf(wv[t][e >> 1], e & 1, rtr((e >> 1) * 14336 + (t < 3 ? cf + t * 2048 + (e & 1) * 512 : ch + (e & 1) * 256)));
        } };
      {
        bf16x8 fa[4][2], fb[2][4][2];
        const unsigned n0 = r15 * 128 + ((g ^ (r15 >> 1)) * 16), n1 = r15 * 128 + (((4 + g) ^ (r15 >> 1)) * 16);
        const unsigned cb[4] = {wm * 8192u + n0, wm * 8192u + n1, 16384 + wn * 8192u + n0, 16384 + wn * 8192u + n1};
        unsigned ba[4], rs = 98304;
#pragma unroll
        for (int b = 0; b < 4; ++b) { ba[b] = rs + cb[b]; PIN(ba[b]); }
        auto rd = [&](int X, int r) AI { const int ks = r & 1, f = r >> 1;
          if (f >= 1 && f < 5) fb[X][f - 1][ks] = rd128(ba[2 + ks] + (f - 1) * 2048); else { const int a = f ? f - 4 : 0; fa[a][ks] = rd128(ba[ks] + a * 2048); } };
        auto mm = [&](int X, int q, int) AI { const int a = q >> 3, b = (q >> 1) & 3, ks = q & 1; MMA(acc[a][b], fa[a][ks], fb[X][b][ks]); };
        auto up = [&](int b) AI { if (b == 0) { rs = nq(rs); PINS(rs); } else { ba[b - 1] = rs + cb[b - 1]; PIN(ba[b - 1]); } };
        const int wl = r31 < 28 ? r31 : 27;
        const char* pw = (const char*)w1 + (sid * 2 + hl) * 7168 + os * 1792 + (wl >= 14 ? 3584 - 1792 : 0) + wl * 128; PIN(pw);
        waitl0(); bar();
        period<32, 16, 0, true, 16, LASTR, 5>(rd, mm, up); bar();
        for (int s = 0; s < 7; ++s) { period<32, 16, 1, false, 16, LASTR, 5>(rd, mm, up); bar();
          if (W1B && s == 1 + loc % 3) pfg(pw);
          period<32, 16, 0, false, 16, LASTR, 5>(rd, mm, up); bar(); }
        period<32, 16, 1, false, 16, LASTR, 5>(rd, mm, up); bar();
        waitv<0>(); PIN(epr); PIN(epc);
        epr = __builtin_amdgcn_readfirstlane(epr) + 1u; epc = __builtin_amdgcn_readfirstlane(epc) + 1u;
        lam[0] = rdf2(T_CON + c0); lam[1] = rdf2(T_CON + c0 + 128);
        wread(0);
        sfor<7>([&](auto U) AI { G2(IC<2>{}, IC<U.value + 4>{}); SB();
#pragma unroll
          for (int q = 16 + (16 * U.value) / 7 / 2 * 2; q < 16 + (16 * (U.value + 1)) / 7 / 2 * 2; ++q) mm(1, q, 0);
          SB(); });
        nexts();
      }
      f32x2 st[2] = {{0, 0}, {0, 0}}, Pf[2] = {{0, 0}, {0, 0}}, Gt = {0, 0}, cr = {0, 0}, ag[8];
      u32x4 lb[8], cg[8], rgv[2]; unsigned lbo[8], lbe[8], pv, gd = 0; unsigned obs[2][2][2][4];
      f32x4 cs[4]; f32x2 so[2]; float c0s = 0.f, c1s = 0.f;
      const rsrc_t rgr = mk(ws + GRAN_OFF + cj * 16384, 16384);
      const int cp = 28 * wq + (r31 < 28 ? r31 : 27);
      const unsigned cgo = hl * 16384 + cp * 16;
#define OB(a, b, c) ((u32x4){obs[a][b][c][0], obs[a][b][c][1], obs[a][b][c][2], obs[a][b][c][3]})
      auto hofs = [&](int hf, int mt) AI { return (2u * wn + mt) * 4096 + wm * 2048 + hf * 1024 + g * 256 + ((r15 ^ (hf << 2)) * 16); };
      auto bad = [&]() AI { unsigned d = 0;
#pragma unroll
        for (int q = 0; q < 8; ++q) { PIN(lb[q]); d |= (int)lbo[q] < 0 ? 0u : ((lb[q][1] ^ epc) | (lb[q][3] ^ epc)); }
        return d != 0; };
      auto gbad = [&]() AI { unsigned d = 0;
#pragma unroll
        for (int q = 0; q < 8; ++q) { PIN(cg[q]); d |= (cg[q][1] ^ epc) | (cg[q][3] ^ epc); }
        return d != 0; };
      auto E2 = [](int s) constexpr { return (s == 2 || s == 3) ? 1 : 0; };
      auto T2 = [](int s) constexpr { return s == 4 ? 8 : (s == 7 || s == 8) ? 4 : s == 11 ? 10 : 0; };
      sfor<15>([&](auto S) AI { constexpr int s = S.value;
        if constexpr (s < 3) waitl0();
        if constexpr (s == 6) waitv<11>();
        else if constexpr (s >= 1) waitv<T2(s - 2) + (s < 14 ? 11 : 4) + E2(s - 1) + T2(s - 1)>();
        bar();
        if constexpr (s < 2) wread(s + 1);
        if constexpr (s == 2) {
#pragma unroll
          for (int sg = 0; sg < 8; ++sg) ag[sg] = rdf2(T_AGG + sg * 512 + ca);
          l16 = rdf2(T_CON + 1024 + ca); cof[0] = rdf2(T_CON + 512 + c0); cof[1] = rdf2(T_CON + 512 + c0 + 128);
        }
        if constexpr (s == 5) { pw1 = rdf2(T_CON + 1536 + ca); pw0 = rdf2(T_CON + 2048 + ca); l128 = rdf2(T_CON + 2560 + ca); l1k = rdf2(T_CON + 3072 + ca); cfa = rdf2(T_CON + 512 + ca); }
        if constexpr (s == 11) { if (w == 0) stflag(rws, epr, lane, flagb + FK_H * 128 + cj * 4); }
        if constexpr (s == 14) { if (w == 0) ld32a(pv, rws, fvo, flagb + FK_H * 128); }
        SB();
        auto work = [&](auto U) AI { constexpr int u = U.value;
          if constexpr (s < 2 && u >= 1 && u < 9) {
            constexpr int t = 8 * s + u - 1;
#pragma unroll
            for (int mt = 0; mt < 2; ++mt) st[mt] = cfma(lam[mt], st[mt], (f32x2){acc[t >> 2][mt][t & 3], acc[t >> 2][2 + mt][t & 3]});
            PIN2(st[0], st[1]);
          }
          if constexpr (s == 1 && u == 9) { wrf2(T_AGG + (4 * wm + g) * 512 + c0, st[0]); wrf2(T_AGG + (4 * wm + g) * 512 + c0 + 128, st[1]); }
          if constexpr (s == 2 && u >= 1 && u < 9) {
            constexpr int sg = u - 1, mt = sg & 1; const bool me = sg == seg0 + mt;
            Pf[mt].x = sel<float>(me, Gt.x, Pf[mt].x); Pf[mt].y = sel<float>(me, Gt.y, Pf[mt].y); Gt = cfma(l16, Gt, ag[sg]);
            PIN2(Gt, Pf[mt]);
          }
          if constexpr (s == 2 && u == 9) st64(rgr, (u32x2){f2u(sel<float>(hl, Gt.y, Gt.x)), epc}, ca * 2 + hl * 8, ci * 1024);
          if constexpr (s == 3 && u < 4) {
            const bf16x8 one = __builtin_bit_cast(bf16x8, (u32x4){0x3F803F80u, 0x3F803F80u, 0x3F803F80u, 0x3F803F80u});
            cs[u] = MFMA(one, wv[u][1], MFMA(one, wv[u][0], ((f32x4){0, 0, 0, 0}))); PIN(cs[u]);
          }
          if constexpr (s == 3 && u == 9)
            st64(rws, (u32x2){f2u(sel4(g & 1, g & 2, cs[0][0], cs[1][0], cs[2][0], cs[3][0])), epc}, (g < 3 ? g * 64 + wq * 16 + r15 : 192 + (wq & 1) * 16 + r15) * 8u, CG_OFF + (cj * 16 + ci) * 2048);
          if constexpr ((s == 3 && u >= 4 && u < 9) || (s == 4 && u < 5) || (s == 5 && u >= 1 && u < 7)) {
            constexpr int t = s == 3 ? u - 4 : s == 4 ? 5 + u : 9 + u;
            f32x2 v0 = cmulc(cof[0], (f32x2){acc[t >> 2][0][t & 3], acc[t >> 2][2][t & 3]}), v1 = cmulc(cof[1], (f32x2){acc[t >> 2][1][t & 3], acc[t >> 2][3][t & 3]}); PIN2(v0, v1);
            acc[t >> 2][0][t & 3] = v0.x; acc[t >> 2][2][t & 3] = v0.y; acc[t >> 2][1][t & 3] = v1.x; acc[t >> 2][3][t & 3] = v1.y;
          }
          if constexpr (s == 4 && (u == 6 || u == 8)) {
#pragma unroll
            for (int q = 2 * u - 12; q < 2 * u - 8; ++q) { lbo[q] = (ci - 8 - 8 * hl + q) * 1024 + ca * 2; PIN(lbo[q]); }
          }
          if constexpr (s == 4 && u == 10) {
#pragma unroll
            for (int q = 0; q < 8; ++q) ld128a(lb[q], rgr, lbo[q], 0u);
          }
          if constexpr (s == 5 && u == 8) {
            if (!wm) pw0 = (f32x2){1.f, 0.f};
            if (ci <= 8) l1k = (f32x2){0.f, 0.f};
            pw0 = cmul(pw0, (f32x2){sel<float>(hl, pw1.x, 1.f), sel<float>(hl, pw1.y, 0.f)}); pw1 = cmul(pw0, l16); PIN2(pw0, pw1);
          }
          if constexpr (s == 5 && (u == 7 || u == 9)) {
#pragma unroll
            for (int q = 2 * u - 14; q < 2 * u - 10; ++q) { lbe[q] = (int)lbo[q] < 0 ? 0u : epc; PIN(lbe[q]); }
          }
          if constexpr (s == 6 && u < 8) {
            PIN(lb[u]); gd |= (lb[u][1] ^ lbe[u]) | (lb[u][3] ^ lbe[u]); cr = cfma(l128, cr, (f32x2){u2f(lb[u][0]), u2f(lb[u][2])});
            PIN2(cr, gd);
          }
          if constexpr (s == 6 && u == 8) {
            if (__builtin_expect(anyl(gd != 0), 0)) {
              for (unsigned sp = 0; sp < SPIN_CAP; ++sp) {
                __builtin_amdgcn_s_sleep(2);
#pragma unroll
                for (int q = 0; q < 8; ++q) ld128a(lb[q], rgr, lbo[q], 0u);
                waitv<0>();
                if (!anyl(bad())) break;
              }
              cr = (f32x2){0, 0};
#pragma unroll
              for (int q = 0; q < 8; ++q) cr = cfma(l128, cr, (f32x2){u2f(lb[q][0]), u2f(lb[q][2])});
            }
            gd = 0;
            const auto sx_ = __builtin_amdgcn_permlane32_swap(f2u(cr.x), f2u(cr.x), false, false), sy_ = __builtin_amdgcn_permlane32_swap(f2u(cr.y), f2u(cr.y), false, false);
            cr = cfma(l1k, (f32x2){u2f(sx_[1]), u2f(sy_[1])}, (f32x2){u2f(sx_[0]), u2f(sy_[0])});
            PIN(cr);
          }
          if constexpr (s == 6 && u == 9) { st[0] = cmulc(cfa, cfma(pw0, cr, Pf[0])); PIN(st[0]); }
          if constexpr (s == 7 && u == 0) { st[1] = cmulc(cfa, cfma(pw1, cr, Pf[1]));
            const auto ex = __builtin_amdgcn_permlane16_swap(f2u(st[0].x), f2u(st[1].x), false, false), ey = __builtin_amdgcn_permlane16_swap(f2u(st[0].y), f2u(st[1].y), false, false);
            st[0] = (f32x2){u2f(ex[0]), u2f(ey[0])}; st[1] = (f32x2){u2f(ex[1]), u2f(ey[1])}; PIN2(st[0], st[1]); }
          if constexpr ((s == 7 && u >= 2 && u < 10) || (s == 8 && u >= 1 && u < 9)) {
            constexpr int t = s == 7 ? u - 2 : 7 + u;
            if constexpr (t & 1) { so[0] = st[0]; so[1] = st[1]; }
#pragma unroll
            for (int mt = 0; mt < 2; ++mt) st[mt] = cfma((f32x2){lam[mt].x, -lam[mt].y}, st[mt], (f32x2){acc[t >> 2][mt][t & 3], acc[t >> 2][2 + mt][t & 3]});
            if constexpr (t & 1) {
#pragma unroll
              for (int mt = 0; mt < 2; ++mt) { obs[t >> 3][mt][0][(t >> 1) & 3] = pk2(so[mt].x, st[mt].x); obs[t >> 3][mt][1][(t >> 1) & 3] = pk2(so[mt].y, st[mt].y); PIN2(obs[t >> 3][mt][0][(t >> 1) & 3], obs[t >> 3][mt][1][(t >> 1) & 3]); }
            }
            PIN2(st[0], st[1]);
          }
          if constexpr ((s == 7 || s == 8) && u == 10) {
#pragma unroll
            for (int q = 0; q < 4; ++q) st128(rws, OB(s - 7, q >> 1, q & 1), hofs(s - 7, q >> 1), H_OFF + ci * H_PANEL + (2 * cj + (q & 1)) * 16384);
          }
          if constexpr (s >= 13 && u < 4) *(LAS u32x4*)(size_t)((s - 12) * 32768 + hofs(u >> 1, u & 1)) = OB(u >> 1, u & 1, s - 13);
          if constexpr ((s == 9 || s == 10) && u >= 1 && u < 9) { constexpr int t = 8 * (s - 9) + u - 1; asm volatile("v_mov_b32 %0, 0\n\tv_mov_b32 %1, 0\n\tv_mov_b32 %2, 0\n\tv_mov_b32 %3, 0" : "=v"(ac2[t >> 2][t & 3][0]), "=v"(ac2[t >> 2][t & 3][1]), "=v"(ac2[t >> 2][t & 3][2]), "=v"(ac2[t >> 2][t & 3][3])); PIN(ac2[t >> 2][t & 3]); }
          if constexpr (s == 9 && u == 0) { if (ci == 15 && w == 1) stflag(rws, epc, lane, EPC_OFF + cj * 128); }
          if constexpr (s == 14 && u == 5) {
            auto rbad = [&]() AI { PIN2(rgv[0], rgv[1]); return ((rgv[0][1] ^ epr) | (rgv[0][3] ^ epr) | (rgv[1][1] ^ epr) | (rgv[1][3] ^ epr)) != 0; };
            if (__builtin_expect(anyl(rbad()), 0))
              for (unsigned sp = 0; sp < SPIN_CAP; ++sp) { __builtin_amdgcn_s_sleep(2);
                ld128a(rgv[0], rws, tq * 16u, rgb); ld128a(rgv[1], rws, tq * 16u + 4096, rgb);
                waitv<0>(); if (!anyl(rbad())) break; }
          }
          if constexpr (s == 14 && u == 7) {
            wrf(T_ROW + (tq & 1) * 1024 + (tq >> 1) * 4, u2f(rgv[0][0])); wrf(T_ROW + (tq & 1) * 1024 + 512 + (tq >> 1) * 4, u2f(rgv[0][2]));
            wrf2(T_XA + tq * 8, (f32x2){u2f(rgv[1][0]), u2f(rgv[1][2])});
          }
          if constexpr (s == 11 && u == 10) {
#pragma unroll
            for (int q = 0; q < 8; ++q) ld128a(cg[q], rws, cgo, CG_OFF + (cj * 16 + q) * 2048);
            ld128a(rgv[0], rws, tq * 16u, rgb); ld128a(rgv[1], rws, tq * 16u + 4096, rgb);
          }
          if constexpr (s == 14 && u < 4) {
#pragma unroll
            for (int q = 2 * u; q < 2 * u + 2; ++q) { PIN(cg[q]); gd |= (cg[q][1] ^ epc) | (cg[q][3] ^ epc); c0s += u2f(cg[q][0]); c1s += u2f(cg[q][2]); }
            PIN(gd); PIN(c0s); PIN(c1s);
          }
          if constexpr (s == 14 && u == 4) {
            if (__builtin_expect(anyl(gd != 0), 0)) {
              for (unsigned sp = 0; sp < SPIN_CAP; ++sp) { __builtin_amdgcn_s_sleep(2);
#pragma unroll
                for (int q = 0; q < 8; ++q) ld128a(cg[q], rws, cgo, CG_OFF + (cj * 16 + q) * 2048);
                waitv<0>(); if (!anyl(gbad())) break; }
              c0s = 0.f; c1s = 0.f;
#pragma unroll
              for (int q = 0; q < 8; ++q) { c0s += u2f(cg[q][0]); c1s += u2f(cg[q][2]); }
            }
            c0s = sum32(c0s); c1s = sum32(c1s); PIN(c0s); PIN(c1s);
          }
          if constexpr (s == 14 && u == 6) {
            const float v = sel<float>(hl, c1s, c0s), hi = rbf(v), bb = rdf(T_B1 + (2 * cp + hl) * 4);
            *(LAS u32x4*)(size_t)(T_WA + (2 * cp + hl) * 16) = (u32x4){pk2(hi, v - hi), pk2(hi, bb), pk2(bb, 0.f), 0u};
          }
        };
        if constexpr (s < 13) { sfor<11>([&](auto U) AI { G2(IC<s + 3>{}, U); SB(); work(U); SB(); }); if constexpr (s < 12) nexts(); }
        if constexpr (s == 12) {
#pragma unroll
          for (int q = 0; q < 4; ++q) vb[q] = va[q];
          ta = t3a; tb = t3b; sl = 32768; sx = 0; next3(); }
        if constexpr (s >= 13) { sfor<4>([&](auto U) AI { G(IC<U.value + 4>{}); SB(); work(U); SB(); }); sl = nq(sl); next3(); asm volatile("s_nop 4"); }
        if constexpr (s == 14) sfor<4>([&](auto U) AI { G(IC<U.value + 4>{}); SB(); work(IC<U.value + 4>{}); SB(); });
      });
      waitl0(); waitv<8>();
      if (w == 0) check_flags(pv, rws, fvo, flagb + FK_H * 128, lane, 8 * half, 8 * half + 8, epr);
      bar();
    }
    f32x4 (&acc)[4][4] = ac2;
    bf16x8 fs[4][2], fr[2][4][2];
    const int kap = 4 * (g & 1) + q2, hf = p >> 1;
    const unsigned wb = 16384 + (4 * g + q2) * 256 + p * 8, hb = (g >> 1) * 4096 + wm * 2048 + hf * 1024 + (8 * (g & 1) + q2) * 16 + (p & 1) * 8;
    const unsigned cb3[4] = {hb + 64u * hf, hb + 64u * (1 ^ hf), 16384 + wn * 8192u + r15 * 128 + ((g ^ (r15 >> 1)) * 16), 16384 + wn * 8192u + r15 * 128 + (((4 + g) ^ (r15 >> 1)) * 16)};
    const unsigned cb4[5] = {wm * 4096u + lane * 16, wb + (((4 * wn + 0) ^ kap) * 32), wb + (((4 * wn + 1) ^ kap) * 32), wb + (((4 * wn + 2) ^ kap) * 32), wb + (((4 * wn + 3) ^ kap) * 32)};
    unsigned ba[5];
    sl = 32768;
#pragma unroll
    for (int b = 0; b < 4; ++b) { ba[b] = sl + cb3[b]; PIN(ba[b]); }
    auto rd3 = [&](int X, int r) AI {
      if (r >= 4 && r < 12) fr[X][(r - 4) >> 1][r & 1] = rd128(ba[2 + (r & 1)] + ((r - 4) >> 1) * 2048);
      else { const int a = r < 4 ? 0 : (r - 8) >> 2, ks = (r >> 1) & 1, h = r & 1; sethalf(fs[a][ks], h, rtr(ba[h] + ks * 8192 + a * 256)); } };
    auto rd4 = [&](int X, int r) AI {
      if (r >= 4 && r < 12) fr[X][(r - 4) >> 1][r & 1] = rd128(ba[0] + (r & 1) * 8192 + ((r - 4) >> 1) * 1024);
      else { const int b = r < 4 ? 0 : (r - 8) >> 2, ks = (r >> 1) & 1, h = r & 1; sethalf(fs[b][ks], h, rtr(ba[1 + b] + ks * 8192 + h * 4096)); } };
    auto mm3 = [&](int X, int q, int) AI { const int a = q >> 3, b = (q >> 1) & 3, ks = q & 1; MMA(acc[a][b], fr[X][b][ks], fs[a][ks]); };
    auto mm4 = [&](int X, int q, int SL) AI { const int b = q >> 3, a = (q >> 1) & 3, ks = q & 1;
      if (SL < 0) acc[a][b] = MFMA(fs[b][ks], fr[X][a][ks], acc[a][b]); else MMA(acc[a][b], fs[b][ks], fr[X][a][ks]); };
    auto up3 = [&](int b) AI { if (b == 0) { sl = nq(sl); PINS(sl); } else { ba[b - 1] = sl + cb3[b - 1]; PIN(ba[b - 1]); } };
    auto up4 = [&](int b) AI { if (b == 0) { sl = nq(sl); PINS(sl); } else { ba[b - 1] = sl + cb4[b - 1]; PIN(ba[b - 1]); } };
    period<32, 16, 0, true, 24, LASTR, 5>(rd3, mm3, up3); waitv<4>(); bar();
    for (int s = 0; s < 7; ++s) { period<32, 16, 1, false, 24, LASTR, 5>(rd3, mm3, up3); waitv<0>(); bar(); period<32, 16, 0, false, 24, LASTR, 5>(rd3, mm3, up3); bar(); }
    float gr[4];
#pragma unroll
    for (int a = 0; a < 4; ++a) gr[a] = rdf(T_ROW + 1536 + (64 * wm + 16 * a + r15) * 4);
    period<32, 16, 1, false, 24, LASTR, 6>(rd3, mm3, up4); bar();
    {
      auto mmg = [&](int X, int q, int SL) AI { if (SL < 16) mm3(X, q, SL); else mm4(X, q, SL);
        constexpr int ord[16] = {0, 4, 1, 5, 8, 9, 2, 12, 13, 6, 10, 14, 3, 7, 11, 15};
        const int t = ord[SL >> 1], e = 2 * (SL & 1); SB();
        acc[t >> 2][t & 3][e] *= gr[t >> 2]; acc[t >> 2][t & 3][e + 1] *= gr[t >> 2]; PIN(acc[t >> 2][t & 3]); };
      period<32, 16, 0, false, 24, LASTR, 6>(rd4, mmg, up4); bar();
    }
    static_assert(MOVE & 1, "the remaining Q4 periods run in pairs");
    for (int s = 1; s < n4; s += 2) { period<32, 16, 1, false, 24, LASTR, 6>(rd4, mm4, up4); bar(); period<32, 16, 0, false, 24, LASTR, 6>(rd4, mm4, up4); bar(); }
    MPAD();
    if (half) {
      const unsigned pb = ((sl + 65536u + wm * 32768u) & 131071u) + wn * 16384 + g * 256;
      sfor<16>([&](auto M) AI { constexpr int m = M.value, a = m & 3, b = m >> 2;
        if constexpr (m < 8) { mm4(0, 16 + 2 * m, -1); mm4(0, 17 + 2 * m, -1); }
        SB();
        *(LAS f32x4*)(size_t)(pb + (a * 4 + b) * 1024 + ((r15 ^ (4 * b + g)) * 16)) = acc[a][b];
        SB(); });
      waitl0();
    } else {
#pragma unroll
      for (int q = 16; q < 32; ++q) mm4(0, q, -1);
#pragma unroll
      for (int bp = 0; bp < 2; ++bp)
#pragma unroll
        for (int a = 0; a < 4; ++a)
          st128(rws, (u32x4){pk2(acc[a][2 * bp][0], acc[a][2 * bp][1]), pk2(acc[a][2 * bp][2], acc[a][2 * bp][3]), pk2(acc[a][2 * bp + 1][0], acc[a][2 * bp + 1][1]), pk2(acc[a][2 * bp + 1][2], acc[a][2 * bp + 1][3])},
                (a * 2 + bp) * 1024 + g * 256 + ((r15 ^ (8 * wn + 4 * bp + g)) * 16), PART_OFF + (ci * 8 + cnt) * 32768 + wq * 8192);
      waitv<0>();
    }
    bar();
    if (!half && w == 0) stflag(rws, epr, lane, flagb + FK_P * 128 + cnt * 4);
  } else {
    constexpr bool isx = !PROFX; (void)isx;
    unsigned epr, pv, t3a, t3b, sa2h, cb[8], ba[8]; const char *pp0, *pp1;
    f32x4 ac1[28];
    {
      u32x4 xr[2][2], gq[2];
      ra = rx; rb = mk(sel<const u16*>(wq & 1, bim, bre), 1024 * 2048);
#pragma unroll
      for (int q = 0; q < 4; ++q) { va[q] = (32 * (q & 1) + 16 * hl + 4 * (q >> 1) + ((lane >> 3) & 3)) * 2048 + (((lane & 7) ^ ((4 * q + g) & 7)) * 16) - 1024 * q; vb[q] = vnat(q); }
      auto src1 = [&](int s) AI { const int k = s ^ (s < 2 ? 0 : s < 4 ? (rot & 1) : rot); sa = (ci * 128 + 64 * (wq >> 1) + 8 * (wq & 1)) * 2048 + k * 128; sb = (cj * 64 + 32 * (wq >> 1)) * 2048 + k * 128; sl = ((s + 3) & 3) * 32768; };
      src1(0); ival(G, nowork);
      SB();
      setup2(); table3(t3a, t3b);
      {
        const int f = ((q2 >> 1) & 1) | ((g & 1) << 1);
        const unsigned nb = wm * 8192u + r15 * 128, n0 = (g ^ (r15 >> 1)) * 16, n1 = ((4 + g) ^ (r15 >> 1)) * 16, wk = (g >> 1) * 7168u + p * 8;
        cb[0] = nb + wn * 4096 + n0; cb[1] = nb + wn * 4096 + n1; cb[2] = nb + (1 - wn) * 4096 + n0; cb[3] = nb + (1 - wn) * 4096 + n1;
        cb[4] = wk + (8 * (g & 1) + q2) * 128 + (((2 * wn) ^ f) * 32); cb[5] = wk + (8 * (g & 1) + q2) * 128 + (((2 * wn + 1) ^ f) * 32);
        cb[6] = wk + 6144 + (8 * (g & 1) + q2) * 64 + (g & 1) * 32; cb[7] = wk + 6144 + (8 * (g & 1) + q2) * 64 + ((g & 1) ^ 1) * 32;
#pragma unroll
        for (int b = 0; b < 8; ++b) { ba[b] = cb[b] + (b < 4 ? 131072u : 0u); PIN2(cb[b], ba[b]); }
        pp0 = lane < 32 ? (const char*)sel<const u16*>(lane & 16, cim, cre) + (os * 512 + sid) * 2048 + r15 * 128 : (const char*)w2 + (sid * 7 + ((lane >> 3) & 3)) * 2048 + os * 1024 + (lane & 7) * 128;
        pp1 = (const char*)w2 + (sid * 7 + 4 + (lane < 24 ? lane >> 3 : 2)) * 2048 + os * 1024 + (lane & 7) * 128;
        PIN2(pp0, pp1);
#pragma unroll
        for (int m = 0; m < 28; ++m) { ac1[m] = (f32x4){0, 0, 0, 0}; PIN(ac1[m]); }
      }
      src1(1); ival(G, nowork);
      waitv<8>(); bar();
      unsigned pdt, plr, pli, bq, oq; f32x2 lp; float lr, li;
      const int tc = tq < 224 ? tq : 223;
      ld32a(epr, rws, 0u, flagb + FK_EP * 128); ld128g(gq[0], gw, lane * 16u); ld128g(gq[1], gw + 512, lane * 16u);
      ld16p(pdt, ldt + 64 * cj, lane * 2u); ld16p(plr, lre + 64 * cj, lane * 2u); ld16p(pli, lim + 64 * cj, lane * 2u);
      ld16p(bq, b1, (224 * cj + (tc < 192 ? ((cj & 1) ? 32 : 0) + tc : ((cj & 1) ? -192 : 0) + tc)) * 2u);
      ld16p(oq, sel<const u16*>(wq < 2, b2, dsk), (cnt * 128 + (tq & 127)) * 2u);
      PH(1);
      src1(2); ival(G, nowork);
      src1(3); ival(G, nowork);
      float sv[6] = {0, 0, 0, 0, 0, 0}, a1, a2, a3, mean, sd, rstd, qv, gg, kq, rr;
      auto E1 = [](int s) constexpr { return (s == 1 || s == 2) ? 2 : s == 11 ? 1 : 0; };
      sfor<12>([&](auto S) AI { constexpr int s = S.value;
        if constexpr (s == 0) waitv<16 + 8>(); else waitv<16 + E1(s - 2) + E1(s - 1)>();
        bar();
        src1(s + 4); ival(G, [&](auto U) AI { constexpr int u = U.value;
          if constexpr ((s == 1 || s == 2) && (u == 2 || u == 5)) ld128p(xr[s - 1][u / 3], rx, lane * 16u, (ci * 128 + 8 * cj + 2 * wq + s - 1) * 2048 + (u / 3) * 1024);
          if constexpr (s == 1 && u == 0) { PIN2(pdt, plr); PIN(pli); lr = bf2f(plr); li = bf2f(pli); const float dt = rbf(fexp(bf2f(pdt))); lp = (f32x2){fexp(dt * lr), dt * li}; PIN(lp); }
          if constexpr (s == 1 && u == 3) { PIN2(bq, oq); wrf(T_B1 + tq * 4, bf2f(bq)); wrf(T_OUT + tq * 4, bf2f(oq)); PIN(epr); epr = __builtin_amdgcn_readfirstlane(epr) + 1u; }
          if constexpr (s == 1 && u == 6) { kq = __builtin_rintf(lp.y * 0.63661977236f);
            rr = __builtin_fmaf(kq, -1.5707962513f, lp.y); rr = __builtin_fmaf(kq, -7.5497894159e-08f, rr); rr = __builtin_fmaf(kq, -5.3903029534e-15f, rr); PIN2(kq, rr); }
          if constexpr (s == 2 && u == 0) { const float z = rr * rr;
            a1 = __builtin_fmaf(rr * z, __builtin_fmaf(z, __builtin_fmaf(z, __builtin_fmaf(z, 2.7183114939e-06f, -1.9839334836e-04f), 8.3333293858e-03f), -1.6666666642e-01f), rr); PIN(a1); }
          if constexpr (s == 2 && u == 3) { const float z = rr * rr;
            a2 = __builtin_fmaf(z, __builtin_fmaf(z, __builtin_fmaf(z, __builtin_fmaf(z, 2.4390448796e-05f, -1.3886763775e-03f), 4.1666623324e-02f), -4.9999999726e-01f), 1.f); PIN(a2); }
          if constexpr (s == 2 && u == 6) { const int q = (int)kq; const float sa_ = sel<float>(q & 1, a2, a1), ca_ = sel<float>(q & 1, a1, a2);
            lp = (f32x2){lp.x * (((q + 1) & 2) ? -ca_ : ca_), lp.x * ((q & 2) ? -sa_ : sa_)}; PIN(lp); wrf2(T_CON + lane * 8, lp); }
          if constexpr (s == 3 && u == 0) { const float nr = lp.x - 1.f, ni = lp.y, den = 2.f * __builtin_amdgcn_rcpf(lr * lr + li * li);
                                            wrf2(T_CON + 512 + lane * 8, (f32x2){(nr * lr + ni * li) * den, (ni * lr - nr * li) * den}); }
          if constexpr ((s == 3 && u >= 2) || (s == 4 && u >= 4)) { constexpr int q = s == 3 ? u - 2 : u + 2; lp = cmul(lp, lp); PIN(lp);
            if constexpr (q >= 3 && q < 7) wrf2(T_CON + (q - 1) * 512 + lane * 8, lp);
            if constexpr (q == 9) wrf2(T_CON + 3072 + lane * 8, lp); }
          if constexpr ((s == 4 && u < 2) || (s >= 5 && s < 9 && !(u & 1) && 4 * (s - 5) + 2 + (u >> 1) < 16)) {
            constexpr int id = s == 4 ? u : 4 * (s - 5) + 2 + (u >> 1), r = id >> 3, e = id & 7;
            PIN2(xr[r][e >> 2], gq[e >> 2]);
            const unsigned v = xr[r][e >> 2][e & 3], gv = gq[e >> 2][e & 3];
            const float x0 = bf2f(v & 0xffffu), x1 = u2f(v & 0xffff0000u);
            sv[r] += x0 + x1; sv[2 + r] = __builtin_fmaf(x1, x1, __builtin_fmaf(x0, x0, sv[2 + r]));
            sv[4 + r] = __builtin_fmaf(x1, u2f(gv & 0xffff0000u), __builtin_fmaf(x0, bf2f(gv & 0xffffu), sv[4 + r]));
            PIN2(sv[r], sv[2 + r]); PIN(sv[4 + r]);
          }
          if constexpr (s == 9 && u < 4) {
#pragma unroll
            for (int q = 0; q < 6; ++q) sv[q] = u == 0 ? dppadd<0xB1>(sv[q]) : u == 1 ? dppadd<0x4E>(sv[q]) : u == 2 ? dppadd<0x141>(sv[q]) : dppadd<0x140>(sv[q]);
            PIN2(sv[0], sv[1]); PIN2(sv[2], sv[3]); PIN2(sv[4], sv[5]);
          }
          if constexpr (s == 9 && u == 5) { const bool r1 = lane & 4; a1 = sum16(sel<float>(r1, sv[1], sv[0])); a2 = sum16(sel<float>(r1, sv[3], sv[2])); a3 = sum16(sel<float>(r1, sv[5], sv[4])); PIN2(a1, a2); PIN(a3); }
          if constexpr (s == 10 && u == 0) { a1 = sum32(a1); a2 = sum32(a2); a3 = sum32(a3); PIN2(a1, a2); PIN(a3); }
          if constexpr (s == 10 && u == 2) { mean = a1 * (1.f / 1024.f); const float var = __builtin_fmaxf(a2 * (1.f / 1024.f) - mean * mean, 0.f) + 1e-5f; rstd = __builtin_amdgcn_rsqf(var); sd = var * rstd;
                                             PIN2(mean, rstd); PIN(sd); }
          if constexpr (s == 10 && u == 4) { qv = 1.f + fexp(__builtin_fminf(a3, 60.f)); gg = __builtin_amdgcn_rcpf(1.f + fexp(__builtin_fminf(-a3, 80.f))); PIN2(qv, gg); }
          if constexpr (s == 11 && u == 0) { a1 = sel4(lane & 1, lane & 2, -2.302208198f * rstd, -0.1029432397f * rstd * rstd * rstd, qv * sd, gg); PIN(a1); }
          if constexpr (s == 11 && u == 2) { const float mh = rbf(mean), sh = rbf(sd);
                                             a2 = sel4(lane & 1, lane & 2, u2f(pk2(-mh, -mh)), u2f(pk2(mh - mean, sh)), u2f(pk2(sd - sh, 0.f)), 0.f); PIN(a2); }
          if constexpr (s == 11 && u == 5) { if (lane < 16) st64(rws, (u32x2){f2u(sel<float>(lane & 8, a2, a1)), epr}, (lane >> 3) * 4096u + (lane & 7) * 8, rgb + (8 * cj + 2 * wq) * 32); }
        });
      });
      rb = mk((const char*)w1 - 4096, 1024 * 7168 + 4096);
#pragma unroll
      for (int q = 0; q < 4; ++q) va[q] = vb[q];
      sx = 0; nexts();
      const unsigned sb0 = sb, sa0 = sa; nexts();
      const unsigned sb1 = sb, sa1 = sa; nexts();
      const unsigned sa2 = sa;
      waitv<16 + 1>(); bar();
      sa = sa0; sfor<4>([&](auto U) AI { G2(IC<0>{}, U); });
      sa = sa1; sfor<4>([&](auto U) AI { G2(IC<1>{}, U); });
      waitv<16 + 1>(); bar();
      sb = sb0; sfor<7>([&](auto U) AI { G2(IC<0>{}, IC<U.value + 4>{}); });
      sa = sa2; sfor<4>([&](auto U) AI { G2(IC<2>{}, U); });
      waitv<19>(); bar();
      sb = sb1; sfor<7>([&](auto U) AI { G2(IC<1>{}, IC<U.value + 4>{}); });
      ta = t3a; tb = t3b; sx = 2; nexts(); sa2h = sa; next3();
    }
    {
      {
        bf16x8 fa[2][4][2], fb[8][2];
        unsigned xn = 98304, wnx = 32768;
        setm0(T_DUM);
        auto rd = [&](int X, int r) AI {
          if (r >= 4 && r < 12) { const int a = (r - 4) >> 1, ks = r & 1; fa[X][a][ks] = rd128(ba[(a >> 1) * 2 + ks] + (a & 1) * 2048); }
          else { const int b = r < 4 ? 0 : (r - 8) >> 2, ks = (r >> 1) & 1, h = r & 1;
            sethalf(fb[b][ks], h, rtr(b < 6 ? ba[4 + (b & 1)] + (b >> 1) * 2048 + ks * 14336 + h * 512 : ba[b] + ks * 14336 + h * 256)); } };
        auto mm = [&](int X, int q, int) AI { const int b = q < 48 ? q >> 3 : 6 + ((q - 48) >> 2), a = (q >> 1) & (q < 48 ? 3 : 1), ks = q & 1, m = (a < 2 ? 8 * a : 4 + 6 * a) + b;
          MMA(ac1[m], fb[b][ks], fa[X][a][ks]); };
        auto up = [&](int b) AI { if (b < 8) { ba[b] = cb[b] + sel<unsigned>(b < 4, xn, wnx); PIN(ba[b]); }
                                  else if (b == 8) { xn = (xn == 131072u) ? 98304u : xn + 16384u; PINS(xn); }
                                  else if (b == 10) { wnx = (wnx == 65536u) ? 0u : wnx + 32768u; PINS(wnx); } };
        waitl0(); waitv<11>(); bar();
        PH(2);
        period<56, 16, 0, true, 40, 48, 12>(rd, mm, up); waitv<0>(); bar();
        const int sp = (loc + xcd) & 7;
        for (int s = 0; s < 14; s += 2) {
          period<56, 16, 1, false, 40, 48, 12>(rd, mm, up); bar();
          if (s == sp) { pfg(pp0); pfg(pp1); }
          period<56, 16, 0, false, 40, 48, 12>(rd, mm, up); bar();
          if (s + 1 == sp) { pfg(pp0); pfg(pp1); }
        }
        period<56, 16, 1, false, 40, 48, 12>(rd, mm, up); bar();
        PH(3);
        setup3(); sl = 0;
        setm0(98304 + wq * 4096); sfor<4>([&](auto U) AI { dma1<U.value * 1024>(rws, va[0], sa2h); });
        ival(G, [&](auto U) AI { mm(1, 40 + 2 * U.value, 0); mm(1, 41 + 2 * U.value, 0); });
#pragma unroll
        for (int a = 0; a < 4; ++a) fa[0][a][0] = rd128(g ? T_ZERO : T_XA + (64 * wm + 16 * (a ^ (2 * wn)) + r15) * 16);
#pragma unroll
        for (int b = 0; b < 8; ++b) fb[b][0] = rd128(T_WA + ((b < 6 ? (b >> 1) * 64 + wn * 32 + (b & 1) * 16 : 96 + b * 16) + r15) * 16);
        SB();
        sl = nq(sl); next3();
        bar();
        ival(G, [&](auto U) AI { constexpr int u = U.value;
#pragma unroll
          for (int q = 8 * u; q < 8 * u + 8; q += 2) if (q < 56) mm(0, q, 0); });
        sl = nq(sl); next3();
      }
      float k0[4], k1[4], qq[4];
#pragma unroll
      for (int a = 0; a < 4; ++a) { const unsigned ro = T_ROW + (64 * wm + 16 * (a ^ (2 * wn)) + r15) * 4; k0[a] = rdf(ro); k1[a] = rdf(ro + 512); qq[a] = rdf(ro + 1024); }
      u32x4 hw[4]; float t[8];
      auto lv0 = [](int s) constexpr { return s < 1 ? 0 : s < 6 ? 13 * (s - 1) : (64 + 8 * (s - 6) < 224 ? 64 + 8 * (s - 6) : 224); };
      auto st3 = [](int s) constexpr { return (s == 5 || s == 13 || s == 21) ? 4 : s == 25 ? 2 : 0; };
      sfor<36>([&](auto S1) AI { constexpr int s = S1.value + 1;
        if constexpr (s == 7) waitv<8>();
        else waitv<16 + st3(s - 2) + st3(s - 1)>();
        if constexpr (s == 12 || s == 20 || s == 28 || s == 36) {
          if (w == 4) check_flags(pv, rws, fvo, flagb + (FK_M1 + (s - 12) / 8) * 128, lane, s == 36 ? 0 : 8 * half, 8 * half + 8, epr); }
        SB(); bar();
        if constexpr (s == 16) { PH(4); }
        if constexpr (s == 7 || s == 16 || s == 24 || s == 28) { if (w == 5) stflag(rws, epr, lane, flagb + (FK_M1 + (s - 4) / 8) * 128 + cj * 4); }
        if constexpr (s == 10 || s == 18 || s == 26 || s == 34) { if (w == 4) ld32a(pv, rws, fvo, flagb + (FK_M1 + (s - 10) / 8) * 128); }
#define UU(e) ac1[m0 + ((e) >> 2)][(e) & 3]
#define LVL(expr) do { _Pragma("unroll") for (int e = 4 * hh; e < 4 * hh + 4; ++e) t[e] = (expr); PIN2(t[4 * hh], t[4 * hh + 1]); PIN2(t[4 * hh + 2], t[4 * hh + 3]); } while (0)
        auto hlev = [&](auto LL) AI { constexpr int L = LL.value, v = L >> 4, l = (L >> 1) & 7, hh = L & 1, a = v & 3, m0 = (a < 2 ? 8 * a : 4 + 6 * a) + 2 * (v >> 2);
          if constexpr (l == 0) LVL(UU(e) * UU(e));
          if constexpr (l == 1) LVL(__builtin_fmaf(t[e], k1[a], k0[a]));
          if constexpr (l == 2) LVL(UU(e) * t[e]);
          if constexpr (l == 3) LVL(__builtin_amdgcn_exp2f(t[e]));
          if constexpr (l == 4) LVL(__builtin_fmaf(t[e], qq[a], qq[a]));
          if constexpr (l == 5) LVL(__builtin_amdgcn_rcpf(t[e]));
          if constexpr (l == 6) LVL(UU(e) * t[e]);
          if constexpr (l == 7) { hw[a][2 * hh] = pk2(t[4 * hh], t[4 * hh + 1]); hw[a][2 * hh + 1] = pk2(t[4 * hh + 2], t[4 * hh + 3]); PIN(hw[a]); }
        };
        ival(G, [&](auto U) AI { constexpr int u = U.value, n = lv0(s + 1) - lv0(s), i0 = (n * u + 7) / 8, i1 = (n * (u + 1) + 7) / 8;
          if constexpr (i0 < i1) hlev(IC<lv0(s) + i0>{});
          if constexpr (i0 + 1 < i1) hlev(IC<lv0(s) + i0 + 1>{});
        });
#pragma unroll
        for (int q = 0; q < st3(s); ++q) st128(rws, hw[q], lane * 16u + (4 * wm + (q ^ (2 * wn))) * 1024, ci * HID_PANEL + kb_of(cj, s < 25 ? 2 * (s >> 3) + wn : 6) * 8192);
        sl = nq(sl);
        if constexpr (s < 11) next3(); else nexts();
        if constexpr (s == 11) {
          rb = mk(w2, 3584 * 2048);
#pragma unroll
          for (int q = 0; q < 4; ++q) vb[q] = (4 * q + g) * 2048 + ((((r15 >> 1) ^ (4 * (q & 1) + g)) * 2) + (lane & 1)) * 16 - 1024 * q;
        }
      });
    }
    static_assert(MOVE >= 0 && MOVE <= 3, "");
    sfor<3 - MOVE>([&](auto) AI { waitv<16>(); bar(); ival(G, nowork); sl = nq(sl); nexts(); });
    if (half) sfor<2 * MOVE>([&](auto) AI { waitv<16>(); bar(); ival(G, nowork); sl = nq(sl); nexts(); });
    waitv<16>(); bar();
    if (half) {
      sfor<8>([&](auto U) AI { constexpr int u = U.value; if constexpr ((u & 3) == 0) setm0(sl + wq * 8192 + u * 1024);
        dma<(u & 3) * 1024>(rx, (4u * (u & 3) + g) * 2048 + r15 * 16 - (u & 3) * 1024, (ci * 128 + 32 * wq + 16 * (u >> 2)) * 2048 + cnt * 256); });
      waitv<8 + 8>();
    } else waitv<8>();
    bar();
    ld32a(pv, rws, fvo, flagb + FK_P * 128);
    if (half) waitv<9>(); else waitv<0>();
    bar();
    auto getp = [&]() AI {
      sfor<8>([&](auto U) AI { constexpr int u = U.value; if constexpr ((u & 3) == 0) setm0(nq(sl) + wq * 8192 + u * 1024);
        dma1<(u & 3) * 1024>(rws, lane * 16u, PART_OFF + (ci * 8 + cnt) * 32768 + wq * 8192 + (u >> 2) * 4096); }); };
    waitv<0>(); PIN(pv);
    const bool early = half && !anyl(lane == cnt && pv != epr);
    if (early) getp(); else ld32a(pv, rws, fvo, flagb + FK_P * 128);
    bar();
    if (half) {
      if (!early) { waitv<0>(); check_flags(pv, rws, fvo, flagb + FK_P * 128, lane, cnt, cnt + 1, epr); getp(); }
      if (cnt == 0 && w == 4) stflag(rws, epr, lane, flagb + FK_EP * 128);
    }
    PH(5);
    waitv<0>(); bar();
  }
  if (half) {
    const int bg = r31 & 15, sw = (r31 >> 4) * 8 + (bg >> 3) * 4 + (bg & 3);
    const unsigned pa = ((sl + 65536u + (w >> 2) * 32768u) & 131071u) + (r31 >> 4) * 16384 + (wq * 4 + (bg >> 2)) * 1024 + (bg & 3) * 256;
    const unsigned pp = nq(sl) + ((w >> 2) * 2 + (r31 >> 4)) * 8192 + (wq * 2 + (bg >> 3)) * 1024 + (bg & 3) * 256 + ((bg >> 2) & 1) * 8;
    const f32x4 bv = rdf4(T_OUT + r31 * 16), dv = rdf4(T_OUT + 512 + r31 * 16);
    f32x4 av[8]; u32x2 pw[8], xw[8]; float gg[8];
#pragma unroll
    for (int u = 0; u < 8; ++u) {
      const int row = 2 * u + hl;
      av[u] = rdf4(pa + ((row ^ bg) * 16)); pw[u] = *(LAS u32x2*)(size_t)(pp + ((row ^ sw) * 16));
      xw[u] = *(LAS u32x2*)(size_t)(sl + (16 * w + hl) * 256 + r31 * 8 + u * 512); gg[u] = rdf(T_ROW + 1536 + (16 * w + hl) * 4 + u * 8);
    }
    SB();
#pragma unroll
    for (int u = 0; u < 8; ++u) {
      const f32x4 pf = {bf2f(pw[u][0] & 0xffffu), u2f(pw[u][0] & 0xffff0000u), bf2f(pw[u][1] & 0xffffu), u2f(pw[u][1] & 0xffff0000u)};
      const f32x4 xf = {bf2f(xw[u][0] & 0xffffu), u2f(xw[u][0] & 0xffff0000u), bf2f(xw[u][1] & 0xffffu), u2f(xw[u][1] & 0xffff0000u)};
      f32x4 r;
#pragma unroll
      for (int e = 0; e < 4; ++e) r[e] = (av[u][e] + pf[e]) + __builtin_fmaf(gg[u], __builtin_fmaf(xf[e], dv[e], -bv[e]), bv[e]);
      st128<OAUX>(mk(out, 8u << 20), __builtin_bit_cast(u32x4, r), (unsigned)((16 * w + hl + 2 * u) * 4096 + r31 * 16), ci * 128 * 4096 + cnt * 512);
    }
  }
}

extern "C" void kernel_launch(void* const* d_in, const int* in_sizes, int n_in, void* d_out, int out_size, void* d_ws, size_t ws_size, hipStream_t stream) {
  (void)in_sizes; (void)n_in; (void)out_size; (void)ws_size;
  (void)hipFuncSetAttribute((const void*)fused_kernel, hipFuncAttributeMaxDynamicSharedMemorySize, LDS_BYTES);
  fused_kernel<<<256, 512, LDS_BYTES, stream>>>((const u16*)d_in[0], (const u16*)d_in[1], (const u16*)d_in[2], (const u16*)d_in[3], (const u16*)d_in[4],
      (const u16*)d_in[5], (const u16*)d_in[6], (const u16*)d_in[7], (const u16*)d_in[8], (const u16*)d_in[10], (const u16*)d_in[11], (const u16*)d_in[12],
      (const u16*)d_in[13], (const u16*)d_in[14], (char*)d_ws, (float*)d_out);
}
```

```cpp
#define DIAG 0
#include <hip/hip_runtime.h>
#include <utility>

typedef unsigned short u16;
typedef __bf16 bf16x2 __attribute__((ext_vector_type(2)));
typedef __bf16 bf16x4 __attribute__((ext_vector_type(4)));
typedef __bf16 bf16x8 __attribute__((ext_vector_type(8)));
typedef float f32x2 __attribute__((ext_vector_type(2)));
typedef float f32x4 __attribute__((ext_vector_type(4)));
typedef float f32x16 __attribute__((ext_vector_type(16)));
typedef unsigned u32x2 __attribute__((ext_vector_type(2)));
typedef unsigned u32x4 __attribute__((ext_vector_type(4)));
typedef __amdgpu_buffer_rsrc_t rsrc_t;
#define LAS __attribute__((address_space(3)))
#define DI __device__ __forceinline__
#define AI __attribute__((always_inline))
#define SB() __builtin_amdgcn_sched_barrier(0)
#define PIN(x) asm volatile("" : "+v"(x))
#define MFMA(a, b, c) __builtin_amdgcn_mfma_f32_16x16x32_bf16(a, b, c, 0, 0, 0)
#define MMA(c, a, b) asm volatile("v_mfma_f32_16x16x32_bf16 %0, %1, %2, %0" : "+v"(c) : "v"(a), "v"(b))
#define MPAD() asm volatile("s_nop 7")
template <int V> using IC = std::integral_constant<int, V>;

constexpr unsigned HID_PANEL = 112 * 8192, H_OFF = 16 * HID_PANEL, H_PANEL = 32 * 16384;
constexpr unsigned PART_OFF = H_OFF + 16 * H_PANEL;
constexpr unsigned GRAN_OFF = PART_OFF + 128 * 32768;
constexpr unsigned RG_OFF = GRAN_OFF + 256 * 1024;
constexpr unsigned CG_OFF = RG_OFF + 16 * 8192;
constexpr unsigned FLAG_OFF = CG_OFF + 256 * 2048;
constexpr unsigned EPC_OFF = FLAG_OFF + 16 * 1024;
constexpr unsigned WS_BYTES = EPC_OFF + 16 * 128;
constexpr int FK_M1 = 0, FK_M2 = 1, FK_M3 = 2, FK_M4 = 3, FK_H = 4, FK_P = 5, FK_EP = 6;
constexpr unsigned TAB = 147456, T_ROW = TAB, T_XA = TAB + 2048, T_OUT = TAB + 4096, T_B1 = TAB + 5120, T_CS2 = TAB + 6144, T_WA = TAB + 8192, T_ZERO = TAB + 12288, T_AGG = T_WA, T_DUM = TAB + 12544, T_CON = TAB + 12800;
constexpr unsigned LDS_BYTES = 163840;
#define XMAP 1
#define ROTM 3
#define OAUX 2
#define SP0 4
#define SPN 6
#define PTE 0
#define MOVE 3
#define PROFX false
#define WD 8
constexpr int LASTR = 24;
constexpr unsigned SPIN_CAP = 1u << 16;

DI rsrc_t mk(const void* p, unsigned bytes) { return __builtin_amdgcn_make_buffer_rsrc((void*)p, 0, bytes, 0x00020000); }
DI void setm0(unsigned lds) { asm volatile("s_mov_b32 m0, %0\n\ts_nop 0" ::"s"(lds) : "memory"); }
template <int OFF> DI void dma(rsrc_t r, unsigned voff, unsigned soff) {
  asm volatile("buffer_load_dwordx4 %0, %1, %2 offen offset:%3 lds" ::"v"(voff), "s"(r), "s"(soff), "i"(OFF) : "memory");
}
template <int OFF> DI void dma1(rsrc_t r, unsigned voff, unsigned soff) {
  asm volatile("buffer_load_dwordx4 %0, %1, %2 offen offset:%3 sc1 lds" ::"v"(voff), "s"(r), "s"(soff), "i"(OFF) : "memory");
}
#define PIN2(x, y) asm volatile("" : "+v"(x), "+v"(y))
#define PINS(x) asm volatile("" : "+s"(x))
typedef const void* const __attribute__((address_space(4)))* kargs_t;
DI const char* karg(int i) { kargs_t ka = (kargs_t)__builtin_amdgcn_kernarg_segment_ptr(); PINS(ka); return (const char*)ka[i]; }
enum { A_X, A_LRE, A_LIM, A_BRE, A_BIM, A_CRE, A_CIM, A_LDT, A_DSK, A_W1, A_B1, A_W2, A_B2, A_GW, A_WS, A_OUT };
template <int N> DI void waitv() { asm volatile("s_waitcnt vmcnt(%0)" ::"i"(N) : "memory"); }
DI void waitl0() { SB(); __builtin_amdgcn_s_waitcnt(0xC07F); asm volatile("" ::: "memory"); }
#define PH(k) do { SB(); unsigned long long t_; asm volatile("s_memrealtime %0\n\ts_waitcnt lgkmcnt(0)" : "=s"(t_)::"memory"); ph[k] = (unsigned)t_; \
                   asm volatile("s_memtime %0\n\ts_waitcnt lgkmcnt(0)" : "=s"(t_)::"memory"); pc[k] = (unsigned)t_; SB(); } while (0)
DI void bar() { asm volatile("s_barrier" ::: "memory"); SB(); }
DI bf16x8 rd128(unsigned a) { return *(LAS bf16x8*)(size_t)a; }
DI u32x4 rdu4(unsigned a) { return *(LAS u32x4*)(size_t)a; }
DI f32x4 rdf4(unsigned a) { return *(LAS f32x4*)(size_t)a; }
DI f32x2 rdf2(unsigned a) { return *(LAS f32x2*)(size_t)a; }
DI float rdf(unsigned a) { return *(LAS float*)(size_t)a; }
DI void wrf(unsigned a, float v) { *(LAS float*)(size_t)a = v; }
DI void wrf2(unsigned a, f32x2 v) { *(LAS f32x2*)(size_t)a = v; }
DI bf16x4 rtr(unsigned a) { return __builtin_amdgcn_ds_read_tr16_b64_v4bf16((LAS bf16x4*)(size_t)a); }
DI void sethalf(bf16x8& f, int h, bf16x4 v) {
  if (h == 0) f = __builtin_shufflevector(v, v, 0, 1, 2, 3, -1, -1, -1, -1);
  else { bf16x8 t = __builtin_shufflevector(v, v, -1, -1, -1, -1, 0, 1, 2, 3); f = __builtin_shufflevector(f, t, 0, 1, 2, 3, 12, 13, 14, 15); }
}
DI unsigned f2u(float f) { return __builtin_bit_cast(unsigned, f); }
DI float u2f(unsigned u) { return __builtin_bit_cast(float, u); }
DI float shx(int lane, float v, int m) { return u2f(__builtin_amdgcn_ds_bpermute((lane ^ m) * 4, (int)f2u(v))); }
DI float sum16(float v) { const auto r = __builtin_amdgcn_permlane16_swap(f2u(v), f2u(v), false, false); return u2f(r[0]) + u2f(r[1]); }
DI float sum32(float v) { const auto r = __builtin_amdgcn_permlane32_swap(f2u(v), f2u(v), false, false); return u2f(r[0]) + u2f(r[1]); }
DI float fexp(float v) { return __builtin_amdgcn_exp2f(v * 1.4426950408889634f); }
DI float bf2f(unsigned v) { return u2f(v << 16); }
DI unsigned pk2(float lo, float hi) { f32x2 v = {lo, hi}; return __builtin_bit_cast(unsigned, __builtin_convertvector(v, bf16x2)); }
DI float rbf(float f) { return u2f(pk2(f, 0.f) << 16); }
DI float dot2(unsigned a, unsigned b, float c) { return __builtin_amdgcn_fdot2_f32_bf16(__builtin_bit_cast(bf16x2, a), __builtin_bit_cast(bf16x2, b), c, false); }
template <int C> DI float dppadd(float v) { return v + u2f(__builtin_amdgcn_update_dpp(0, (int)f2u(v), C, 0xf, 0xf, true)); }
DI float sel4(bool b0, bool b1, float a, float b, float c, float d) { PIN(a); PIN(b); PIN(c); PIN(d); const float lo = b0 ? b : a, hi = b0 ? d : c; return b1 ? hi : lo; }
DI f32x2 cmul(f32x2 a, f32x2 b) { return (f32x2){a.x * b.x - a.y * b.y, a.x * b.y + a.y * b.x}; }
DI f32x2 cmulc(f32x2 a, f32x2 b) { return (f32x2){__builtin_fmaf(a.x, b.x, -(a.y * b.y)), __builtin_fmaf(-a.x, b.y, -(a.y * b.x))}; }
DI f32x2 cfma(f32x2 a, f32x2 b, f32x2 c) { return (f32x2){__builtin_fmaf(a.x, b.x, __builtin_fmaf(-a.y, b.y, c.x)), __builtin_fmaf(a.x, b.y, __builtin_fmaf(a.y, b.x, c.y))}; }
template <int AUX = 16> DI void st128(rsrc_t r, u32x4 v, unsigned voff, unsigned soff) {
  __builtin_amdgcn_raw_buffer_store_b128(v, r, voff, soff, AUX);
  asm volatile("s_nop 1" ::"v"(v));
}
DI void st64(rsrc_t r, u32x2 v, unsigned voff, unsigned soff) { __builtin_amdgcn_raw_buffer_store_b64(v, r, voff, soff, 16); }
DI void st32(rsrc_t r, unsigned v, unsigned voff, unsigned soff) { __builtin_amdgcn_raw_buffer_store_b32(v, r, voff, soff, 16); }
DI void stflag(rsrc_t r, unsigned v, int lane, unsigned soff) { if (lane == 0) st32(r, v, 0u, soff); }
DI void ld32a(unsigned& d, rsrc_t r, unsigned voff, unsigned soff) { asm volatile("buffer_load_dword %0, %1, %2, %3 offen sc1" : "=v"(d) : "v"(voff), "s"(r), "s"(soff) : "memory"); }
DI void ld16p(unsigned& d, const u16* p, unsigned voff) { asm volatile("global_load_ushort %0, %1, %2" : "=v"(d) : "v"(voff), "s"(p) : "memory"); }
DI void ld64p(u32x2& d, rsrc_t r, unsigned voff, unsigned soff) { asm volatile("buffer_load_dwordx2 %0, %1, %2, %3 offen" : "=v"(d) : "v"(voff), "s"(r), "s"(soff) : "memory"); }
DI void ld64a(u32x2& d, rsrc_t r, unsigned voff, unsigned soff) { asm volatile("buffer_load_dwordx2 %0, %1, %2, %3 offen sc1" : "=v"(d) : "v"(voff), "s"(r), "s"(soff) : "memory"); }
DI void ld128g(u32x4& d, const u16* p, unsigned voff) { asm volatile("global_load_dwordx4 %0, %1, %2" : "=v"(d) : "v"(voff), "s"(p) : "memory"); }
DI void pfl(unsigned& d, rsrc_t r, unsigned voff, unsigned soff) { asm volatile("buffer_load_dword %0, %1, %2, %3 offen" : "+v"(d) : "v"(voff), "s"(r), "s"(soff) : "memory"); }
template <class T> DI T sel(bool c, T a, T b) { return c ? a : b; }
DI void pfg(const char* p) { asm volatile("global_load_lds_dword %0, off" ::"v"(p) : "memory"); }
DI bool anyl(bool b) { return __builtin_amdgcn_ballot_w64(b) != 0; }
DI void ld128p(u32x4& d, rsrc_t r, unsigned voff, unsigned soff) { asm volatile("buffer_load_dwordx4 %0, %1, %2, %3 offen" : "=v"(d) : "v"(voff), "s"(r), "s"(soff) : "memory"); }
DI void sincos_f(float t, float& sn, float& cs) {
  const float k = __builtin_rintf(t * 0.63661977236f);
  float r = __builtin_fmaf(k, -1.5707962513f, t); r = __builtin_fmaf(k, -7.5497894159e-08f, r); r = __builtin_fmaf(k, -5.3903029534e-15f, r);
  const float z = r * r;
  const float sp = __builtin_fmaf(r * z, __builtin_fmaf(z, __builtin_fmaf(z, __builtin_fmaf(z, 2.7183114939e-06f, -1.9839334836e-04f), 8.3333293858e-03f), -1.6666666642e-01f), r);
  const float cp = __builtin_fmaf(z, __builtin_fmaf(z, __builtin_fmaf(z, __builtin_fmaf(z, 2.4390448796e-05f, -1.3886763775e-03f), 4.1666623324e-02f), -4.9999999726e-01f), 1.f);
  const int q = (int)k;
  const float a = (q & 1) ? cp : sp, b = (q & 1) ? sp : cp;
  sn = (q & 2) ? -a : a; cs = ((q + 1) & 2) ? -b : b;
}
DI void ld128a(u32x4& d, rsrc_t r, unsigned voff, unsigned soff) { asm volatile("buffer_load_dwordx4 %0, %1, %2, %3 offen sc1" : "=v"(d) : "v"(voff), "s"(r), "s"(soff) : "memory"); }

DI unsigned x1off(int a) { return a == 0 ? 131072u : (unsigned)(81920 + a * 16384); }
DI unsigned nq(unsigned s) { return (s + 32768u) & 131071u; }

DI int kb_of(int j, int bl) { return 7 * j + ((j & 1) ? (bl == 6 ? 0 : bl + 1) : bl); }

template <int NR, int LAST> constexpr int nread(int sl) {
  constexpr int DENSE = 2 * NR - LAST - 2;
  return sl < 0 ? 0 : sl < DENSE ? sl + 1 : sl >= LAST ? NR : DENSE + (sl - DENSE) / 2 + 1;
}
template <int N, int L, int X, bool FIRST, int NR, int LAST, int NB, int SL, class RD, class MM, class UP>
DI void slot(RD& rd, MM& mm, UP& upd) {
  if constexpr (!FIRST && SL >= L && (SL == L + 4 || (SL - L) % 8 == 0)) {
    __builtin_amdgcn_s_waitcnt(0xC07F | ((nread<NR, LAST>(SL - 1) - nread<NR, LAST>(SL - WD)) << 8)); SB();
  }
  if constexpr (SL >= L) mm(X, SL - L, SL); else if constexpr (!FIRST) mm(X ^ 1, SL + N - L, SL);
  SB();
  if constexpr (nread<NR, LAST>(SL) > nread<NR, LAST>(SL - 1)) rd(X, nread<NR, LAST>(SL) - 1);
  constexpr int per = NB > N - 1 - LAST ? 2 : 1, u0 = (SL - LAST - 1) * per;
  if constexpr (SL > LAST && u0 < NB) { upd(u0); if constexpr (per == 2 && u0 + 1 < NB) upd(u0 + 1); }
  SB();
}
template <int N, int L, int X, bool FIRST, int NR, int LAST, int NB, class RD, class MM, class UP, int... I>
DI void period_(RD& rd, MM& mm, UP& upd, std::integer_sequence<int, I...>) { (slot<N, L, X, FIRST, NR, LAST, NB, I>(rd, mm, upd), ...); }
template <int N, int L, int X, bool FIRST, int NR, int LAST, int NB, class RD, class MM, class UP>
DI void period(RD& rd, MM& mm, UP& upd) {
  static_assert(NR - 1 <= LAST && !(LAST & 1) && NB <= 2 * (N - 1 - LAST), "");
  period_<N, L, X, FIRST, NR, LAST, NB>(rd, mm, upd, std::make_integer_sequence<int, N>{});
  waitl0();
}
template <class G, class W, int... U> DI void ival_(G& g, W& wk, std::integer_sequence<int, U...>) { ((g(IC<U>{}), SB(), wk(IC<U>{}), SB()), ...); }
template <class G, class W> DI void ival(G&& g, W&& wk) { ival_(g, wk, std::make_integer_sequence<int, 8>{}); }
template <int N, class F, int... I> DI void sfor_(F& f, std::integer_sequence<int, I...>) { (f(IC<I>{}), ...); }
template <int N, class F> DI void sfor(F&& f) { sfor_<N>(f, std::make_integer_sequence<int, N>{}); }

DI void check_flags(unsigned& pv, rsrc_t ws, unsigned voff, unsigned soff, int lane, int lo, int hi, unsigned ep) {
  PIN(pv);
  const bool need = lane >= lo && lane < hi;
  if (__builtin_expect(anyl(need && pv != ep), 0)) {
    for (unsigned sp = 0; sp < SPIN_CAP; ++sp) {
      __builtin_amdgcn_s_sleep(2);
      ld32a(pv, ws, voff, soff); asm volatile("s_waitcnt vmcnt(0)" : "+v"(pv)::"memory");
      if (!anyl(need && pv != ep)) break;
    }
  }
}

#if defined(__HIP_DEVICE_COMPILE__) && __HIP_DEVICE_COMPILE__
__attribute__((target("no-packed-fp32-ops")))
#endif
__global__ __launch_bounds__(512, 2) void fused_kernel(const u16* x, const u16* lre, const u16* lim, const u16* bre, const u16* bim, const u16* cre, const u16* cim,
    const u16* ldt, const u16* dsk, const u16* w1, const u16* b1, const u16* w2, const u16* b2, const u16* gw, char* ws, float* out) {
  extern __shared__ __attribute__((aligned(16))) char shm[];
  unsigned long long pc0; asm volatile("s_getpc_b64 %0" : "=s"(pc0));
  const int lane = threadIdx.x & 63, w = __builtin_amdgcn_readfirstlane(threadIdx.x >> 6);
  const int bid = blockIdx.x, xcd = bid & 7, loc = bid >> 3;
  const int ci = (xcd & 3) * 4 + (loc & 3), cj = (loc >> 4) * 8 + (xcd >> 2) * 4 + ((loc >> 2) & 3);
  const int rot = (ci + cj) & ROTM, cnt = cj & 7, half = cj >> 3, wq = w & 3, tq = threadIdx.x & 255;
  const int n4 = half ? 28 + MOVE : 28 - MOVE;
  const rsrc_t rws = mk(ws, WS_BYTES), rx = mk(x, 2048 * 2048);
  const int wm = (w >> 1) & 1, wn = w & 1, g = lane >> 4, r15 = lane & 15, q2 = (lane >> 2) & 3, p = lane & 3, hl = lane >> 5, r31 = lane & 31;
  const unsigned flagb = FLAG_OFF + ci * 1024, fvo = (lane & 15) * 4;
  unsigned rgb = RG_OFF + ci * 8192; PINS(rgb);
  auto nowork = [&](auto) AI {};
  unsigned va[4], vb[4], vw[7], sl, sa, sb, ta, tb; int sx; rsrc_t ra, rb;
  auto vnat = [&](int u) AI { return (8u * u + (lane >> 3)) * 2048 + (((lane & 7) ^ ((4 * u + g) & 7)) * 16) - 1024 * u; };
  auto nexts = [&]() AI { sa = __builtin_amdgcn_readlane(ta, sx); sb = __builtin_amdgcn_readlane(tb, sx); ++sx; PINS(sa); PINS(sb); };
  auto G = [&](auto U) AI { constexpr int u = U.value;
    if constexpr (u == 0) setm0(sl + wq * 4096);
    if constexpr (u == 4) setm0(sl + 16384 + wq * 4096);
    if constexpr (u < 4) dma1<u * 1024>(ra, va[u], sa); else dma<(u - 4) * 1024>(rb, vb[u - 4], sb);
  };
  auto G2 = [&](auto S, auto U) AI { constexpr int a = S.value % 3, u = U.value;
    if constexpr (u == 0) setm0((a == 0 ? 131072u : 81920u + a * 16384u) + wq * 4096);
    if constexpr (u == 4) setm0(a * 32768 + wq * 7168);
    if constexpr (u == 8) setm0(a * 32768 + wq * 7168 + 4096);
    if constexpr (u < 4) dma<u * 1024>(rx, va[u], sa); else dma<(u & 3) * 1024>(rb, vw[u - 4], sb);
  };
  const int c7 = ci & 7;
  auto setup2 = [&]() AI {
#pragma unroll
    for (int q = 0; q < 6; ++q) { const int k = 8 * (q & 1) + (lane >> 3), f = ((k >> 1) & 1) | ((q & 1) << 1);
      vw[q] = 4096 + k * 7168 + ((cj & 1) ? 64 : 0) + (q >> 1) * 128 + (((((lane & 7) >> 1) ^ f) * 2) + (lane & 1)) * 16 - (q & 3) * 1024; }
    vw[6] = 4096 + (lane >> 2) * 7168 + ((cj & 1) ? 0 : 384) + (((((lane & 3) >> 1) ^ hl) * 2) + (lane & 1)) * 16 - 2048;
    int k = lane - 2 + (c7 < 2 ? 3 * c7 : c7 - 2); k = 2 + (k >= 6 ? k - 6 : k);
    k = lane < 2 ? lane : lane < 8 ? k : (lane ^ rot);
    k = (k ^ (ci & 8)) & 15;
    ta = (ci * 128 + 32 * wq) * 2048 + k * 128; tb = (k * 64 + 16 * wq) * 7168 + cj * 448;
    PIN2(ta, tb);
#pragma unroll
    for (int q = 0; q < 7; ++q) PIN(vw[q]);
  };
  auto setup3 = [&]() AI {
    ra = rws;
#pragma unroll
    for (int q = 0; q < 4; ++q) { va[q] = lane * 16; vb[q] = vnat(q); }
  };
  auto table3 = [&](unsigned& ta, unsigned& tb) AI {
    const int ks = 16 * half + ((lane ^ (2 * cnt)) & 15);
    int n = 2 * (lane - 16) + (wq >> 1), hp = half; if (n >= 56) { n -= 2 * MOVE; hp = 0; }
    const int kb = kb_of(hp * 8 + ((n ^ rot) & 7), n >> 3);
    ta = lane < 16 ? H_OFF + ci * H_PANEL + ks * 16384 + wq * 4096 : ci * HID_PANEL + kb * 8192 + (wq & 1) * 4096;
    tb = lane < 16 ? (cnt * 128 + 32 * wq) * 2048 + (ks >> 1) * 128 + (ks & 1) : (kb * 32 + 16 * (wq & 1)) * 2048 + cnt * 256;
    PIN(ta); PIN(tb);
  };
  auto next3 = [&]() AI { nexts(); rb = mk(sel<const u16*>(sb & 1, cim, cre), 1024 * 2048); sb &= ~1u; };
  unsigned ph[8] = {0, 0, 0, 0, 0, 0, 0, 0}, pc[8] = {0, 0, 0, 0, 0, 0, 0, 0};
  PH(0);
  if (w < 4) {
    constexpr bool isx = PROFX; (void)isx;
    unsigned epr, epc;
    const unsigned ca = (32 * wn + r31) * 8;
    const unsigned c0 = (32 * wn + r15) * 8;
    const int seg0 = 4 * wm + 2 * hl;
    f32x2 lam[2], cof[2], cfa, l16, l128, l1k, pw0, pw1; unsigned t3a, t3b;
    f32x4 ac2[4][4];
    {
      setm0(T_DUM);
      const int row = bid * 8 + (lane & 7);
      const char* pp = (const char*)(hl ? (row < 1024 ? bre : bim - 1024 * 1024) : x) + (size_t)row * 2048 + (4 * wq + ((lane >> 3) & 3)) * 128;
      for (int q = 0; q <= wq; ++q) __builtin_amdgcn_s_sleep(6);
      pfg(pp);
      pfg((const char*)pc0 + (((loc * 4 + wq) % 20) * 16 + (lane & 15)) * 128);
      ld32a(epr, rws, 0u, flagb + FK_EP * 128); ld32a(epc, rws, 0u, EPC_OFF + cj * 128);
      setup2(); sx = 2; nexts(); table3(t3a, t3b);
      rb = mk((const char*)w1 - 4096, 1024 * 7168 + 4096);
#pragma unroll
      for (int q = 0; q < 4; ++q) { va[q] = vnat(q); PIN(va[q]); }
      wrf(T_ZERO + (lane & 3) * 4, 0.f);
    }
    {
      f32x4 acc[4][4];
#pragma unroll
      for (int a = 0; a < 16; ++a) { acc[a >> 2][a & 3] = (f32x4){0, 0, 0, 0}; PIN(acc[a >> 2][a & 3]); }
      bf16x8 wv[4][2];
      const int sstar = c7 < 2 ? c7 : 2;
      const int fsw = ((q2 >> 1) & 1) | ((g & 1) << 1);
      unsigned cf = sstar * 32768 + (g >> 1) * 7168 + (8 * (g & 1) + q2) * 128 + ((wq ^ fsw) * 32) + p * 8;
      unsigned ch = sstar * 32768 + (g >> 1) * 7168 + 6144 + (8 * (g & 1) + q2) * 64 + (((wq & 1) ^ (g & 1)) * 32) + p * 8;
      PIN(cf); PIN(ch);
      auto wread = [&](int st) AI {
        if (sstar == st) {
#pragma unroll
          for (int e = 0; e < 4; ++e)
#pragma unroll
            for (int t = 0; t < 4; ++t) sethalf(wv[t][e >> 1], e & 1, rtr((e >> 1) * 14336 + (t < 3 ? cf + t * 2048 + (e & 1) * 512 : ch + (e & 1) * 256)));
        } };
      {
        bf16x8 fa[4][2], fb[2][4][2];
        const unsigned n0 = r15 * 128 + ((g ^ (r15 >> 1)) * 16), n1 = r15 * 128 + (((4 + g) ^ (r15 >> 1)) * 16);
        const unsigned cb[4] = {wm * 8192u + n0, wm * 8192u + n1, 16384 + wn * 8192u + n0, 16384 + wn * 8192u + n1};
        unsigned ba[4], rs = 98304;
#pragma unroll
        for (int b = 0; b < 4; ++b) { ba[b] = rs + cb[b]; PIN(ba[b]); }
        auto rd = [&](int X, int r) AI { const int ks = r & 1, f = r >> 1;
          if (f >= 1 && f < 5) fb[X][f - 1][ks] = rd128(ba[2 + ks] + (f - 1) * 2048); else { const int a = f ? f - 4 : 0; fa[a][ks] = rd128(ba[ks] + a * 2048); } };
        auto mm = [&](int X, int q, int) AI { const int a = q >> 3, b = (q >> 1) & 3, ks = q & 1; MMA(acc[a][b], fa[a][ks], fb[X][b][ks]); };
        auto up = [&](int b) AI { if (b == 0) { rs = nq(rs); PINS(rs); } else { ba[b - 1] = rs + cb[b - 1]; PIN(ba[b - 1]); } };
        waitl0(); bar();
        period<32, 16, 0, true, 16, LASTR, 5>(rd, mm, up); bar();
        for (int s = 0; s < 7; ++s) { period<32, 16, 1, false, 16, LASTR, 5>(rd, mm, up); bar(); period<32, 16, 0, false, 16, LASTR, 5>(rd, mm, up); bar(); }
        period<32, 16, 1, false, 16, LASTR, 5>(rd, mm, up); bar();
        waitv<0>(); PIN(epr); PIN(epc);
        epr = __builtin_amdgcn_readfirstlane(epr) + 1u; epc = __builtin_amdgcn_readfirstlane(epc) + 1u;
        lam[0] = rdf2(T_CON + c0); lam[1] = rdf2(T_CON + c0 + 128);
        wread(0);
        sfor<7>([&](auto U) AI { G2(IC<2>{}, IC<U.value + 4>{}); SB();
#pragma unroll
          for (int q = 16 + (16 * U.value) / 7 / 2 * 2; q < 16 + (16 * (U.value + 1)) / 7 / 2 * 2; ++q) mm(1, q, 0);
          SB(); });
        nexts();
      }
      f32x2 st[2] = {{0, 0}, {0, 0}}, Pf[2] = {{0, 0}, {0, 0}}, Gt = {0, 0}, cr = {0, 0}, ag[8];
      u32x4 lb[8], cg[8], rgv[2]; unsigned lbo[8], lbe[8], pv, gd = 0; unsigned obs[2][2][2][4];
      f32x4 cs[4]; f32x2 so[2]; float c0s = 0.f, c1s = 0.f;
      const rsrc_t rgr = mk(ws + GRAN_OFF + cj * 16384, 16384);
      const int cp = 28 * wq + (r31 < 28 ? r31 : 27);
      const unsigned cgo = hl * 16384 + cp * 16;
#define OB(a, b, c) ((u32x4){obs[a][b][c][0], obs[a][b][c][1], obs[a][b][c][2], obs[a][b][c][3]})
      auto hofs = [&](int hf, int mt) AI { return (2u * wn + mt) * 4096 + wm * 2048 + hf * 1024 + g * 256 + ((r15 ^ (hf << 2)) * 16); };
      auto bad = [&]() AI { unsigned d = 0;
#pragma unroll
        for (int q = 0; q < 8; ++q) { PIN(lb[q]); d |= (int)lbo[q] < 0 ? 0u : ((lb[q][1] ^ epc) | (lb[q][3] ^ epc)); }
        return d != 0; };
      auto gbad = [&]() AI { unsigned d = 0;
#pragma unroll
        for (int q = 0; q < 8; ++q) { PIN(cg[q]); d |= (cg[q][1] ^ epc) | (cg[q][3] ^ epc); }
        return d != 0; };
      auto E2 = [](int s) constexpr { return (s == 2 || s == 3) ? 1 : 0; };
      auto T2 = [](int s) constexpr { return s == 4 ? 8 : (s == 7 || s == 8) ? 4 : s == 11 ? 10 : 0; };
      sfor<15>([&](auto S) AI { constexpr int s = S.value;
        if constexpr (s < 3) waitl0();
        if constexpr (s == 6) waitv<11>();
        else if constexpr (s >= 1) waitv<T2(s - 2) + (s < 14 ? 11 : 4) + E2(s - 1) + T2(s - 1)>();
        bar();
        if constexpr (s < 2) wread(s + 1);
        if constexpr (s == 2) {
#pragma unroll
          for (int sg = 0; sg < 8; ++sg) ag[sg] = rdf2(T_AGG + sg * 512 + ca);
          l16 = rdf2(T_CON + 1024 + ca); cof[0] = rdf2(T_CON + 512 + c0); cof[1] = rdf2(T_CON + 512 + c0 + 128);
        }
        if constexpr (s == 5) { pw1 = rdf2(T_CON + 1536 + ca); pw0 = rdf2(T_CON + 2048 + ca); l128 = rdf2(T_CON + 2560 + ca); l1k = rdf2(T_CON + 3072 + ca); cfa = rdf2(T_CON + 512 + ca); }
        if constexpr (s == 11) { if (w == 0) stflag(rws, epr, lane, flagb + FK_H * 128 + cj * 4); }
        if constexpr (s == 14) { if (w == 0) ld32a(pv, rws, fvo, flagb + FK_H * 128); }
        SB();
        auto work = [&](auto U) AI { constexpr int u = U.value;
          if constexpr (s < 2 && u >= 1 && u < 9) {
            constexpr int t = 8 * s + u - 1;
#pragma unroll
            for (int mt = 0; mt < 2; ++mt) st[mt] = cfma(lam[mt], st[mt], (f32x2){acc[t >> 2][mt][t & 3], acc[t >> 2][2 + mt][t & 3]});
            PIN2(st[0], st[1]);
          }
          if constexpr (s == 1 && u == 9) { wrf2(T_AGG + (4 * wm + g) * 512 + c0, st[0]); wrf2(T_AGG + (4 * wm + g) * 512 + c0 + 128, st[1]); }
          if constexpr (s == 2 && u >= 1 && u < 9) {
            constexpr int sg = u - 1, mt = sg & 1; const bool me = sg == seg0 + mt;
            Pf[mt].x = sel<float>(me, Gt.x, Pf[mt].x); Pf[mt].y = sel<float>(me, Gt.y, Pf[mt].y); Gt = cfma(l16, Gt, ag[sg]);
            PIN2(Gt, Pf[mt]);
          }
          if constexpr (s == 2 && u == 9) st64(rgr, (u32x2){f2u(sel<float>(hl, Gt.y, Gt.x)), epc}, ca * 2 + hl * 8, ci * 1024);
          if constexpr (s == 3 && u < 4) {
            const bf16x8 one = __builtin_bit_cast(bf16x8, (u32x4){0x3F803F80u, 0x3F803F80u, 0x3F803F80u, 0x3F803F80u});
            cs[u] = MFMA(one, wv[u][1], MFMA(one, wv[u][0], ((f32x4){0, 0, 0, 0}))); PIN(cs[u]);
          }
          if constexpr (s == 3 && u == 9)
            st64(rws, (u32x2){f2u(sel4(g & 1, g & 2, cs[0][0], cs[1][0], cs[2][0], cs[3][0])), epc}, (g < 3 ? g * 64 + wq * 16 + r15 : 192 + (wq & 1) * 16 + r15) * 8u, CG_OFF + (cj * 16 + ci) * 2048);
          if constexpr ((s == 3 && u >= 4 && u < 9) || (s == 4 && u < 5) || (s == 5 && u >= 1 && u < 7)) {
            constexpr int t = s == 3 ? u - 4 : s == 4 ? 5 + u : 9 + u;
            f32x2 v0 = cmulc(cof[0], (f32x2){acc[t >> 2][0][t & 3], acc[t >> 2][2][t & 3]}), v1 = cmulc(cof[1], (f32x2){acc[t >> 2][1][t & 3], acc[t >> 2][3][t & 3]}); PIN2(v0, v1);
            acc[t >> 2][0][t & 3] = v0.x; acc[t >> 2][2][t & 3] = v0.y; acc[t >> 2][1][t & 3] = v1.x; acc[t >> 2][3][t & 3] = v1.y;
          }
          if constexpr (s == 4 && (u == 6 || u == 8)) {
#pragma unroll
            for (int q = 2 * u - 12; q < 2 * u - 8; ++q) { lbo[q] = (ci - 8 - 8 * hl + q) * 1024 + ca * 2; PIN(lbo[q]); }
          }
          if constexpr (s == 4 && u == 10) {
#pragma unroll
            for (int q = 0; q < 8; ++q) ld128a(lb[q], rgr, lbo[q], 0u);
          }
          if constexpr (s == 5 && u == 8) {
            if (!wm) pw0 = (f32x2){1.f, 0.f};
            if (ci <= 8) l1k = (f32x2){0.f, 0.f};
            pw0 = cmul(pw0, (f32x2){sel<float>(hl, pw1.x, 1.f), sel<float>(hl, pw1.y, 0.f)}); pw1 = cmul(pw0, l16); PIN2(pw0, pw1);
          }
          if constexpr (s == 5 && (u == 7 || u == 9)) {
#pragma unroll
            for (int q = 2 * u - 14; q < 2 * u - 10; ++q) { lbe[q] = (int)lbo[q] < 0 ? 0u : epc; PIN(lbe[q]); }
          }
          if constexpr (s == 6 && u < 8) {
            PIN(lb[u]); gd |= (lb[u][1] ^ lbe[u]) | (lb[u][3] ^ lbe[u]); cr = cfma(l128, cr, (f32x2){u2f(lb[u][0]), u2f(lb[u][2])});
            PIN2(cr, gd);
          }
          if constexpr (s == 6 && u == 8) {
            if (__builtin_expect(anyl(gd != 0), 0)) {
              for (unsigned sp = 0; sp < SPIN_CAP; ++sp) {
                __builtin_amdgcn_s_sleep(2);
#pragma unroll
                for (int q = 0; q < 8; ++q) ld128a(lb[q], rgr, lbo[q], 0u);
                waitv<0>();
                if (!anyl(bad())) break;
              }
              cr = (f32x2){0, 0};
#pragma unroll
              for (int q = 0; q < 8; ++q) cr = cfma(l128, cr, (f32x2){u2f(lb[q][0]), u2f(lb[q][2])});
            }
            gd = 0;
            const auto sx_ = __builtin_amdgcn_permlane32_swap(f2u(cr.x), f2u(cr.x), false, false), sy_ = __builtin_amdgcn_permlane32_swap(f2u(cr.y), f2u(cr.y), false, false);
            cr = cfma(l1k, (f32x2){u2f(sx_[1]), u2f(sy_[1])}, (f32x2){u2f(sx_[0]), u2f(sy_[0])});
            PIN(cr);
          }
          if constexpr (s == 6 && u == 9) { st[0] = cmulc(cfa, cfma(pw0, cr, Pf[0])); PIN(st[0]); }
          if constexpr (s == 7 && u == 0) { st[1] = cmulc(cfa, cfma(pw1, cr, Pf[1]));
            const auto ex = __builtin_amdgcn_permlane16_swap(f2u(st[0].x), f2u(st[1].x), false, false), ey = __builtin_amdgcn_permlane16_swap(f2u(st[0].y), f2u(st[1].y), false, false);
            st[0] = (f32x2){u2f(ex[0]), u2f(ey[0])}; st[1] = (f32x2){u2f(ex[1]), u2f(ey[1])}; PIN2(st[0], st[1]); }
          if constexpr ((s == 7 && u >= 2 && u < 10) || (s == 8 && u >= 1 && u < 9)) {
            constexpr int t = s == 7 ? u - 2 : 7 + u;
            if constexpr (t & 1) { so[0] = st[0]; so[1] = st[1]; }
#pragma unroll
            for (int mt = 0; mt < 2; ++mt) st[mt] = cfma((f32x2){lam[mt].x, -lam[mt].y}, st[mt], (f32x2){acc[t >> 2][mt][t & 3], acc[t >> 2][2 + mt][t & 3]});
            if constexpr (t & 1) {
#pragma unroll
              for (int mt = 0; mt < 2; ++mt) { obs[t >> 3][mt][0][(t >> 1) & 3] = pk2(so[mt].x, st[mt].x); obs[t >> 3][mt][1][(t >> 1) & 3] = pk2(so[mt].y, st[mt].y); PIN2(obs[t >> 3][mt][0][(t >> 1) & 3], obs[t >> 3][mt][1][(t >> 1) & 3]); }
            }
            PIN2(st[0], st[1]);
          }
          if constexpr ((s == 7 || s == 8) && u == 10) {
#pragma unroll
            for (int q = 0; q < 4; ++q) st128(rws, OB(s - 7, q >> 1, q & 1), hofs(s - 7, q >> 1), H_OFF + ci * H_PANEL + (2 * cj + (q & 1)) * 16384);
          }
          if constexpr (s >= 13 && u < 4) *(LAS u32x4*)(size_t)((s - 12) * 32768 + hofs(u >> 1, u & 1)) = OB(u >> 1, u & 1, s - 13);
          if constexpr ((s == 9 || s == 10) && u >= 1 && u < 9) { constexpr int t = 8 * (s - 9) + u - 1; asm volatile("v_mov_b32 %0, 0\n\tv_mov_b32 %1, 0\n\tv_mov_b32 %2, 0\n\tv_mov_b32 %3, 0" : "=v"(ac2[t >> 2][t & 3][0]), "=v"(ac2[t >> 2][t & 3][1]), "=v"(ac2[t >> 2][t & 3][2]), "=v"(ac2[t >> 2][t & 3][3])); PIN(ac2[t >> 2][t & 3]); }
          if constexpr (s == 9 && u == 0) { if (ci == 15 && w == 1) stflag(rws, epc, lane, EPC_OFF + cj * 128); }
          if constexpr (s == 14 && u == 5) {
            auto rbad = [&]() AI { PIN2(rgv[0], rgv[1]); return ((rgv[0][1] ^ epr) | (rgv[0][3] ^ epr) | (rgv[1][1] ^ epr) | (rgv[1][3] ^ epr)) != 0; };
            if (__builtin_expect(anyl(rbad()), 0))
              for (unsigned sp = 0; sp < SPIN_CAP; ++sp) { __builtin_amdgcn_s_sleep(2);
                ld128a(rgv[0], rws, tq * 16u, rgb); ld128a(rgv[1], rws, tq * 16u + 4096, rgb);
                waitv<0>(); if (!anyl(rbad())) break; }
          }
          if constexpr (s == 14 && u == 7) {
            wrf(T_ROW + (tq & 1) * 1024 + (tq >> 1) * 4, u2f(rgv[0][0])); wrf(T_ROW + (tq & 1) * 1024 + 512 + (tq >> 1) * 4, u2f(rgv[0][2]));
            wrf2(T_XA + tq * 8, (f32x2){u2f(rgv[1][0]), u2f(rgv[1][2])});
          }
          if constexpr (s == 11 && u == 10) {
#pragma unroll
            for (int q = 0; q < 8; ++q) ld128a(cg[q], rws, cgo, CG_OFF + (cj * 16 + q) * 2048);
            ld128a(rgv[0], rws, tq * 16u, rgb); ld128a(rgv[1], rws, tq * 16u + 4096, rgb);
          }
          if constexpr (s == 14 && u < 4) {
#pragma unroll
            for (int q = 2 * u; q < 2 * u + 2; ++q) { PIN(cg[q]); gd |= (cg[q][1] ^ epc) | (cg[q][3] ^ epc); c0s += u2f(cg[q][0]); c1s += u2f(cg[q][2]); }
            PIN(gd); PIN(c0s); PIN(c1s);
          }
          if constexpr (s == 14 && u == 4) {
            if (__builtin_expect(anyl(gd != 0), 0)) {
              for (unsigned sp = 0; sp < SPIN_CAP; ++sp) { __builtin_amdgcn_s_sleep(2);
#pragma unroll
                for (int q = 0; q < 8; ++q) ld128a(cg[q], rws, cgo, CG_OFF + (cj * 16 + q) * 2048);
                waitv<0>(); if (!anyl(gbad())) break; }
              c0s = 0.f; c1s = 0.f;
#pragma unroll
              for (int q = 0; q < 8; ++q) { c0s += u2f(cg[q][0]); c1s += u2f(cg[q][2]); }
            }
            c0s = sum32(c0s); c1s = sum32(c1s); PIN(c0s); PIN(c1s);
          }
          if constexpr (s == 14 && u == 6) {
            const float v = sel<float>(hl, c1s, c0s), hi = rbf(v), bb = rdf(T_B1 + (2 * cp + hl) * 4);
            *(LAS u32x4*)(size_t)(T_WA + (2 * cp + hl) * 16) = (u32x4){pk2(hi, v - hi), pk2(hi, bb), pk2(bb, 0.f), 0u};
          }
        };
        if constexpr (s < 13) { sfor<11>([&](auto U) AI { G2(IC<s + 3>{}, U); SB(); work(U); SB(); }); if constexpr (s < 12) nexts(); }
        if constexpr (s == 12) {
#pragma unroll
          for (int q = 0; q < 4; ++q) vb[q] = va[q];
          ta = t3a; tb = t3b; sl = 32768; sx = 0; next3(); }
        if constexpr (s >= 13) { sfor<4>([&](auto U) AI { G(IC<U.value + 4>{}); SB(); work(U); SB(); }); sl = nq(sl); next3(); asm volatile("s_nop 4"); }
        if constexpr (s == 14) sfor<4>([&](auto U) AI { G(IC<U.value + 4>{}); SB(); work(IC<U.value + 4>{}); SB(); });
      });
      waitl0(); waitv<8>();
      if (w == 0) check_flags(pv, rws, fvo, flagb + FK_H * 128, lane, 8 * half, 8 * half + 8, epr);
      bar();
    }
    f32x4 (&acc)[4][4] = ac2;
    bf16x8 fs[4][2], fr[2][4][2];
    const int kap = 4 * (g & 1) + q2, hf = p >> 1;
    const unsigned wb = 16384 + (4 * g + q2) * 256 + p * 8, hb = (g >> 1) * 4096 + wm * 2048 + hf * 1024 + (8 * (g & 1) + q2) * 16 + (p & 1) * 8;
    const unsigned cb3[4] = {hb + 64u * hf, hb + 64u * (1 ^ hf), 16384 + wn * 8192u + r15 * 128 + ((g ^ (r15 >> 1)) * 16), 16384 + wn * 8192u + r15 * 128 + (((4 + g) ^ (r15 >> 1)) * 16)};
    const unsigned cb4[5] = {wm * 4096u + lane * 16, wb + (((4 * wn + 0) ^ kap) * 32), wb + (((4 * wn + 1) ^ kap) * 32), wb + (((4 * wn + 2) ^ kap) * 32), wb + (((4 * wn + 3) ^ kap) * 32)};
    unsigned ba[5];
    sl = 32768;
#pragma unroll
    for (int b = 0; b < 4; ++b) { ba[b] = sl + cb3[b]; PIN(ba[b]); }
    auto rd3 = [&](int X, int r) AI {
      if (r >= 4 && r < 12) fr[X][(r - 4) >> 1][r & 1] = rd128(ba[2 + (r & 1)] + ((r - 4) >> 1) * 2048);
      else { const int a = r < 4 ? 0 : (r - 8) >> 2, ks = (r >> 1) & 1, h = r & 1; sethalf(fs[a][ks], h, rtr(ba[h] + ks * 8192 + a * 256)); } };
    auto rd4 = [&](int X, int r) AI {
      if (r >= 4 && r < 12) fr[X][(r - 4) >> 1][r & 1] = rd128(ba[0] + (r & 1) * 8192 + ((r - 4) >> 1) * 1024);
      else { const int b = r < 4 ? 0 : (r - 8) >> 2, ks = (r >> 1) & 1, h = r & 1; sethalf(fs[b][ks], h, rtr(ba[1 + b] + ks * 8192 + h * 4096)); } };
    auto mm3 = [&](int X, int q, int) AI { const int a = q >> 3, b = (q >> 1) & 3, ks = q & 1; MMA(acc[a][b], fr[X][b][ks], fs[a][ks]); };
    auto mm4 = [&](int X, int q, int SL) AI { const int b = q >> 3, a = (q >> 1) & 3, ks = q & 1;
      if (SL < 0) acc[a][b] = MFMA(fs[b][ks], fr[X][a][ks], acc[a][b]); else MMA(acc[a][b], fs[b][ks], fr[X][a][ks]); };
    auto up3 = [&](int b) AI { if (b == 0) { sl = nq(sl); PINS(sl); } else { ba[b - 1] = sl + cb3[b - 1]; PIN(ba[b - 1]); } };
    auto up4 = [&](int b) AI { if (b == 0) { sl = nq(sl); PINS(sl); } else { ba[b - 1] = sl + cb4[b - 1]; PIN(ba[b - 1]); } };
    period<32, 16, 0, true, 24, LASTR, 5>(rd3, mm3, up3); waitv<4>(); bar();
    for (int s = 0; s < 7; ++s) { period<32, 16, 1, false, 24, LASTR, 5>(rd3, mm3, up3); waitv<0>(); bar(); period<32, 16, 0, false, 24, LASTR, 5>(rd3, mm3, up3); bar(); }
    float gr[4];
#pragma unroll
    for (int a = 0; a < 4; ++a) gr[a] = rdf(T_ROW + 1536 + (64 * wm + 16 * a + r15) * 4);
    period<32, 16, 1, false, 24, LASTR, 6>(rd3, mm3, up4); bar();
    {
      auto mmg = [&](int X, int q, int SL) AI { if (SL < 16) mm3(X, q, SL); else mm4(X, q, SL);
        constexpr int ord[16] = {0, 4, 1, 5, 8, 9, 2, 12, 13, 6, 10, 14, 3, 7, 11, 15};
        const int t = ord[SL >> 1], e = 2 * (SL & 1); SB();
        acc[t >> 2][t & 3][e] *= gr[t >> 2]; acc[t >> 2][t & 3][e + 1] *= gr[t >> 2]; PIN(acc[t >> 2][t & 3]); };
      period<32, 16, 0, false, 24, LASTR, 6>(rd4, mmg, up4); bar();
    }
    static_assert(MOVE & 1, "the remaining Q4 periods run in pairs");
    for (int s = 1; s < n4; s += 2) { period<32, 16, 1, false, 24, LASTR, 6>(rd4, mm4, up4); bar(); period<32, 16, 0, false, 24, LASTR, 6>(rd4, mm4, up4); bar(); }
    MPAD();
    if (half) {
      const unsigned pb = ((sl + 65536u + wm * 32768u) & 131071u) + wn * 16384 + g * 256;
      sfor<16>([&](auto M) AI { constexpr int m = M.value, a = m & 3, b = m >> 2;
        if constexpr (m < 8) { mm4(0, 16 + 2 * m, -1); mm4(0, 17 + 2 * m, -1); }
        SB();
        *(LAS f32x4*)(size_t)(pb + (a * 4 + b) * 1024 + ((r15 ^ (4 * b + g)) * 16)) = acc[a][b];
        SB(); });
      waitl0();
    } else {
#pragma unroll
      for (int q = 16; q < 32; ++q) mm4(0, q, -1);
#pragma unroll
      for (int bp = 0; bp < 2; ++bp)
#pragma unroll
        for (int a = 0; a < 4; ++a)
          st128(rws, (u32x4){pk2(acc[a][2 * bp][0], acc[a][2 * bp][1]), pk2(acc[a][2 * bp][2], acc[a][2 * bp][3]), pk2(acc[a][2 * bp + 1][0], acc[a][2 * bp + 1][1]), pk2(acc[a][2 * bp + 1][2], acc[a][2 * bp + 1][3])},
                (a * 2 + bp) * 1024 + g * 256 + ((r15 ^ (8 * wn + 4 * bp + g)) * 16), PART_OFF + (ci * 8 + cnt) * 32768 + wq * 8192);
      waitv<0>();
    }
    bar();
    if (!half && w == 0) stflag(rws, epr, lane, flagb + FK_P * 128 + cnt * 4);
  } else {
    constexpr bool isx = !PROFX; (void)isx;
    unsigned epr, pv, t3a, t3b, sa2h, cb[8], ba[8]; const char *pp0, *pp1;
    f32x4 ac1[28];
    {
      u32x4 xr[2][2], gq[2];
      ra = rx; rb = mk(sel<const u16*>(wq & 1, bim, bre), 1024 * 2048);
#pragma unroll
      for (int q = 0; q < 4; ++q) { va[q] = (32 * (q & 1) + 16 * hl + 4 * (q >> 1) + ((lane >> 3) & 3)) * 2048 + (((lane & 7) ^ ((4 * q + g) & 7)) * 16) - 1024 * q; vb[q] = vnat(q); }
      auto src1 = [&](int s) AI { const int k = s ^ (s < 2 ? 0 : s < 4 ? (rot & 1) : rot); sa = (ci * 128 + 64 * (wq >> 1) + 8 * (wq & 1)) * 2048 + k * 128; sb = (cj * 64 + 32 * (wq >> 1)) * 2048 + k * 128; sl = ((s + 3) & 3) * 32768; };
      src1(0); ival(G, nowork);
      SB();
      setup2(); table3(t3a, t3b);
      {
        const int f = ((q2 >> 1) & 1) | ((g & 1) << 1);
        const unsigned nb = wm * 8192u + r15 * 128, n0 = (g ^ (r15 >> 1)) * 16, n1 = ((4 + g) ^ (r15 >> 1)) * 16, wk = (g >> 1) * 7168u + p * 8;
        cb[0] = nb + wn * 4096 + n0; cb[1] = nb + wn * 4096 + n1; cb[2] = nb + (1 - wn) * 4096 + n0; cb[3] = nb + (1 - wn) * 4096 + n1;
        cb[4] = wk + (8 * (g & 1) + q2) * 128 + (((2 * wn) ^ f) * 32); cb[5] = wk + (8 * (g & 1) + q2) * 128 + (((2 * wn + 1) ^ f) * 32);
        cb[6] = wk + 6144 + (8 * (g & 1) + q2) * 64 + (g & 1) * 32; cb[7] = wk + 6144 + (8 * (g & 1) + q2) * 64 + ((g & 1) ^ 1) * 32;
#pragma unroll
        for (int b = 0; b < 8; ++b) { ba[b] = cb[b] + (b < 4 ? 131072u : 0u); PIN2(cb[b], ba[b]); }
        pp0 = lane < 32 ? (const char*)sel<const u16*>(lane & 16, cim, cre) + ((bid * 4 + wq) * 16 + r15) * 128 : (const char*)w2 + ((bid * 4 + wq) * 56 + r15) * 128;
        pp1 = (const char*)w2 + ((bid * 4 + wq) * 56 + (lane < 40 ? 16 + lane : 55)) * 128;
        PIN2(pp0, pp1);
#pragma unroll
        for (int m = 0; m < 28; ++m) { ac1[m] = (f32x4){0, 0, 0, 0}; PIN(ac1[m]); }
      }
      src1(1); ival(G, nowork);
      waitv<8>(); bar();
      unsigned pdt, plr, pli, bq, oq; f32x2 lp; float lr, li;
      const int tc = tq < 224 ? tq : 223;
      ld32a(epr, rws, 0u, flagb + FK_EP * 128); ld128g(gq[0], gw, lane * 16u); ld128g(gq[1], gw + 512, lane * 16u);
      ld16p(pdt, ldt + 64 * cj, lane * 2u); ld16p(plr, lre + 64 * cj, lane * 2u); ld16p(pli, lim + 64 * cj, lane * 2u);
      ld16p(bq, b1, (224 * cj + (tc < 192 ? ((cj & 1) ? 32 : 0) + tc : ((cj & 1) ? -192 : 0) + tc)) * 2u);
      ld16p(oq, sel<const u16*>(wq < 2, b2, dsk), (cnt * 128 + (tq & 127)) * 2u);
      PH(1);
      src1(2); ival(G, nowork);
      src1(3); ival(G, nowork);
      float sv[6] = {0, 0, 0, 0, 0, 0}, a1, a2, a3, mean, sd, rstd, qv, gg, kq, rr;
      auto E1 = [](int s) constexpr { return (s == 1 || s == 2) ? 2 : s == 11 ? 1 : 0; };
      sfor<12>([&](auto S) AI { constexpr int s = S.value;
        if constexpr (s == 0) waitv<16 + 8>(); else waitv<16 + E1(s - 2) + E1(s - 1)>();
        bar();
        src1(s + 4); ival(G, [&](auto U) AI { constexpr int u = U.value;
          if constexpr ((s == 1 || s == 2) && (u == 2 || u == 5)) ld128p(xr[s - 1][u / 3], rx, lane * 16u, (ci * 128 + 8 * cj + 2 * wq + s - 1) * 2048 + (u / 3) * 1024);
          if constexpr (s == 1 && u == 0) { PIN2(pdt, plr); PIN(pli); lr = bf2f(plr); li = bf2f(pli); const float dt = rbf(fexp(bf2f(pdt))); lp = (f32x2){fexp(dt * lr), dt * li}; PIN(lp); }
          if constexpr (s == 1 && u == 3) { PIN2(bq, oq); wrf(T_B1 + tq * 4, bf2f(bq)); wrf(T_OUT + tq * 4, bf2f(oq)); PIN(epr); epr = __builtin_amdgcn_readfirstlane(epr) + 1u; }
          if constexpr (s == 1 && u == 6) { kq = __builtin_rintf(lp.y * 0.63661977236f);
            rr = __builtin_fmaf(kq, -1.5707962513f, lp.y); rr = __builtin_fmaf(kq, -7.5497894159e-08f, rr); rr = __builtin_fmaf(kq, -5.3903029534e-15f, rr); PIN2(kq, rr); }
          if constexpr (s == 2 && u == 0) { const float z = rr * rr;
            a1 = __builtin_fmaf(rr * z, __builtin_fmaf(z, __builtin_fmaf(z, __builtin_fmaf(z, 2.7183114939e-06f, -1.9839334836e-04f), 8.3333293858e-03f), -1.6666666642e-01f), rr); PIN(a1); }
          if constexpr (s == 2 && u == 3) { const float z = rr * rr;
            a2 = __builtin_fmaf(z, __builtin_fmaf(z, __builtin_fmaf(z, __builtin_fmaf(z, 2.4390448796e-05f, -1.3886763775e-03f), 4.1666623324e-02f), -4.9999999726e-01f), 1.f); PIN(a2); }
          if constexpr (s == 2 && u == 6) { const int q = (int)kq; const float sa_ = sel<float>(q & 1, a2, a1), ca_ = sel<float>(q & 1, a1, a2);
            lp = (f32x2){lp.x * (((q + 1) & 2) ? -ca_ : ca_), lp.x * ((q & 2) ? -sa_ : sa_)}; PIN(lp); wrf2(T_CON + lane * 8, lp); }
          if constexpr (s == 3 && u == 0) { const float nr = lp.x - 1.f, ni = lp.y, den = 2.f * __builtin_amdgcn_rcpf(lr * lr + li * li);
                                            wrf2(T_CON + 512 + lane * 8, (f32x2){(nr * lr + ni * li) * den, (ni * lr - nr * li) * den}); }
          if constexpr ((s == 3 && u >= 2) || (s == 4 && u >= 4)) { constexpr int q = s == 3 ? u - 2 : u + 2; lp = cmul(lp, lp); PIN(lp);
            if constexpr (q >= 3 && q < 7) wrf2(T_CON + (q - 1) * 512 + lane * 8, lp);
            if constexpr (q == 9) wrf2(T_CON + 3072 + lane * 8, lp); }
          if constexpr ((s == 4 && u < 2) || (s >= 5 && s < 9 && !(u & 1) && 4 * (s - 5) + 2 + (u >> 1) < 16)) {
            constexpr int id = s == 4 ? u : 4 * (s - 5) + 2 + (u >> 1), r = id >> 3, e = id & 7;
            PIN2(xr[r][e >> 2], gq[e >> 2]);
            const unsigned v = xr[r][e >> 2][e & 3], gv = gq[e >> 2][e & 3];
            const float x0 = bf2f(v & 0xffffu), x1 = u2f(v & 0xffff0000u);
            sv[r] += x0 + x1; sv[2 + r] = __builtin_fmaf(x1, x1, __builtin_fmaf(x0, x0, sv[2 + r]));
            sv[4 + r] = __builtin_fmaf(x1, u2f(gv & 0xffff0000u), __builtin_fmaf(x0, bf2f(gv & 0xffffu), sv[4 + r]));
            PIN2(sv[r], sv[2 + r]); PIN(sv[4 + r]);
          }
          if constexpr (s == 9 && u < 4) {
#pragma unroll
            for (int q = 0; q < 6; ++q) sv[q] = u == 0 ? dppadd<0xB1>(sv[q]) : u == 1 ? dppadd<0x4E>(sv[q]) : u == 2 ? dppadd<0x141>(sv[q]) : dppadd<0x140>(sv[q]);
            PIN2(sv[0], sv[1]); PIN2(sv[2], sv[3]); PIN2(sv[4], sv[5]);
          }
          if constexpr (s == 9 && u == 5) { const bool r1 = lane & 4; a1 = sum16(sel<float>(r1, sv[1], sv[0])); a2 = sum16(sel<float>(r1, sv[3], sv[2])); a3 = sum16(sel<float>(r1, sv[5], sv[4])); PIN2(a1, a2); PIN(a3); }
          if constexpr (s == 10 && u == 0) { a1 = sum32(a1); a2 = sum32(a2); a3 = sum32(a3); PIN2(a1, a2); PIN(a3); }
          if constexpr (s == 10 && u == 2) { mean = a1 * (1.f / 1024.f); const float var = __builtin_fmaxf(a2 * (1.f / 1024.f) - mean * mean, 0.f) + 1e-5f; rstd = __builtin_amdgcn_rsqf(var); sd = var * rstd;
                                             PIN2(mean, rstd); PIN(sd); }
          if constexpr (s == 10 && u == 4) { qv = 1.f + fexp(__builtin_fminf(a3, 60.f)); gg = __builtin_amdgcn_rcpf(1.f + fexp(__builtin_fminf(-a3, 80.f))); PIN2(qv, gg); }
          if constexpr (s == 11 && u == 0) { a1 = sel4(lane & 1, lane & 2, -2.302208198f * rstd, -0.1029432397f * rstd * rstd * rstd, qv * sd, gg); PIN(a1); }
          if constexpr (s == 11 && u == 2) { const float mh = rbf(mean), sh = rbf(sd);
                                             a2 = sel4(lane & 1, lane & 2, u2f(pk2(-mh, -mh)), u2f(pk2(mh - mean, sh)), u2f(pk2(sd - sh, 0.f)), 0.f); PIN(a2); }
          if constexpr (s == 11 && u == 5) { if (lane < 16) st64(rws, (u32x2){f2u(sel<float>(lane & 8, a2, a1)), epr}, (lane >> 3) * 4096u + (lane & 7) * 8, rgb + (8 * cj + 2 * wq) * 32); }
        });
      });
      rb = mk((const char*)w1 - 4096, 1024 * 7168 + 4096);
#pragma unroll
      for (int q = 0; q < 4; ++q) va[q] = vb[q];
      sx = 0; nexts();
      const unsigned sb0 = sb, sa0 = sa; nexts();
      const unsigned sb1 = sb, sa1 = sa; nexts();
      const unsigned sa2 = sa;
      waitv<16 + 1>(); bar();
      sa = sa0; sfor<4>([&](auto U) AI { G2(IC<0>{}, U); });
      sa = sa1; sfor<4>([&](auto U) AI { G2(IC<1>{}, U); });
      waitv<16 + 1>(); bar();
      sb = sb0; sfor<7>([&](auto U) AI { G2(IC<0>{}, IC<U.value + 4>{}); });
      sa = sa2; sfor<4>([&](auto U) AI { G2(IC<2>{}, U); });
      waitv<19>(); bar();
      sb = sb1; sfor<7>([&](auto U) AI { G2(IC<1>{}, IC<U.value + 4>{}); });
      ta = t3a; tb = t3b; sx = 2; nexts(); sa2h = sa; next3();
    }
    {
      {
        bf16x8 fa[2][4][2], fb[8][2];
        unsigned xn = 98304, wnx = 32768;
        setm0(T_DUM);
        auto rd = [&](int X, int r) AI {
          if (r >= 4 && r < 12) { const int a = (r - 4) >> 1, ks = r & 1; fa[X][a][ks] = rd128(ba[(a >> 1) * 2 + ks] + (a & 1) * 2048); }
          else { const int b = r < 4 ? 0 : (r - 8) >> 2, ks = (r >> 1) & 1, h = r & 1;
            sethalf(fb[b][ks], h, rtr(b < 6 ? ba[4 + (b & 1)] + (b >> 1) * 2048 + ks * 14336 + h * 512 : ba[b] + ks * 14336 + h * 256)); } };
        auto mm = [&](int X, int q, int) AI { const int b = q < 48 ? q >> 3 : 6 + ((q - 48) >> 2), a = (q >> 1) & (q < 48 ? 3 : 1), ks = q & 1, m = (a < 2 ? 8 * a : 4 + 6 * a) + b;
          MMA(ac1[m], fb[b][ks], fa[X][a][ks]); };
        auto up = [&](int b) AI { if (b < 8) { ba[b] = cb[b] + sel<unsigned>(b < 4, xn, wnx); PIN(ba[b]); }
                                  else if (b == 8) { xn = (xn == 131072u) ? 98304u : xn + 16384u; PINS(xn); }
                                  else if (b == 10) { wnx = (wnx == 65536u) ? 0u : wnx + 32768u; PINS(wnx); } };
        waitl0(); waitv<11>(); bar();
        PH(2);
        period<56, 16, 0, true, 40, 48, 12>(rd, mm, up); waitv<0>(); bar();
        const int sp = (loc + xcd) & 7;
        for (int s = 0; s < 14; s += 2) {
          period<56, 16, 1, false, 40, 48, 12>(rd, mm, up); bar();
          if (s == sp) { pfg(pp0); pfg(pp1); }
          period<56, 16, 0, false, 40, 48, 12>(rd, mm, up); bar();
          if (s + 1 == sp) { pfg(pp0); pfg(pp1); }
        }
        period<56, 16, 1, false, 40, 48, 12>(rd, mm, up); bar();
        PH(3);
        setup3(); sl = 0;
        setm0(98304 + wq * 4096); sfor<4>([&](auto U) AI { dma1<U.value * 1024>(rws, va[0], sa2h); });
        ival(G, [&](auto U) AI { mm(1, 40 + 2 * U.value, 0); mm(1, 41 + 2 * U.value, 0); });
#pragma unroll
        for (int a = 0; a < 4; ++a) fa[0][a][0] = rd128(g ? T_ZERO : T_XA + (64 * wm + 16 * (a ^ (2 * wn)) + r15) * 16);
#pragma unroll
        for (int b = 0; b < 8; ++b) fb[b][0] = rd128(T_WA + ((b < 6 ? (b >> 1) * 64 + wn * 32 + (b & 1) * 16 : 96 + b * 16) + r15) * 16);
        SB();
        sl = nq(sl); next3();
        bar();
        ival(G, [&](auto U) AI { constexpr int u = U.value;
#pragma unroll
          for (int q = 8 * u; q < 8 * u + 8; q += 2) if (q < 56) mm(0, q, 0); });
        sl = nq(sl); next3();
      }
      float k0[4], k1[4], qq[4];
#pragma unroll
      for (int a = 0; a < 4; ++a) { const unsigned ro = T_ROW + (64 * wm + 16 * (a ^ (2 * wn)) + r15) * 4; k0[a] = rdf(ro); k1[a] = rdf(ro + 512); qq[a] = rdf(ro + 1024); }
      u32x4 hw[4]; float t[8];
      auto lv0 = [](int s) constexpr { return s < 1 ? 0 : s < 6 ? 13 * (s - 1) : (64 + 8 * (s - 6) < 224 ? 64 + 8 * (s - 6) : 224); };
      auto st3 = [](int s) constexpr { return (s == 5 || s == 13 || s == 21) ? 4 : s == 25 ? 2 : 0; };
      sfor<36>([&](auto S1) AI { constexpr int s = S1.value + 1;
        if constexpr (s == 7) waitv<8>();
        else waitv<16 + st3(s - 2) + st3(s - 1)>();
        if constexpr (s == 12 || s == 20 || s == 28 || s == 36) {
          if (w == 4) check_flags(pv, rws, fvo, flagb + (FK_M1 + (s - 12) / 8) * 128, lane, s == 36 ? 0 : 8 * half, 8 * half + 8, epr); }
        SB(); bar();
        if constexpr (s == 16) { PH(4); }
        if constexpr (s == 7 || s == 16 || s == 24 || s == 28) { if (w == 5) stflag(rws, epr, lane, flagb + (FK_M1 + (s - 4) / 8) * 128 + cj * 4); }
        if constexpr (s == 10 || s == 18 || s == 26 || s == 34) { if (w == 4) ld32a(pv, rws, fvo, flagb + (FK_M1 + (s - 10) / 8) * 128); }
#define UU(e) ac1[m0 + ((e) >> 2)][(e) & 3]
#define LVL(expr) do { _Pragma("unroll") for (int e = 4 * hh; e < 4 * hh + 4; ++e) t[e] = (expr); PIN2(t[4 * hh], t[4 * hh + 1]); PIN2(t[4 * hh + 2], t[4 * hh + 3]); } while (0)
        auto hlev = [&](auto LL) AI { constexpr int L = LL.value, v = L >> 4, l = (L >> 1) & 7, hh = L & 1, a = v & 3, m0 = (a < 2 ? 8 * a : 4 + 6 * a) + 2 * (v >> 2);
          if constexpr (l == 0) LVL(UU(e) * UU(e));
          if constexpr (l == 1) LVL(__builtin_fmaf(t[e], k1[a], k0[a]));
          if constexpr (l == 2) LVL(UU(e) * t[e]);
          if constexpr (l == 3) LVL(__builtin_amdgcn_exp2f(t[e]));
          if constexpr (l == 4) LVL(__builtin_fmaf(t[e], qq[a], qq[a]));
          if constexpr (l == 5) LVL(__builtin_amdgcn_rcpf(t[e]));
          if constexpr (l == 6) LVL(UU(e) * t[e]);
          if constexpr (l == 7) { hw[a][2 * hh] = pk2(t[4 * hh], t[4 * hh + 1]); hw[a][2 * hh + 1] = pk2(t[4 * hh + 2], t[4 * hh + 3]); PIN(hw[a]); }
        };
        ival(G, [&](auto U) AI { constexpr int u = U.value, n = lv0(s + 1) - lv0(s), i0 = (n * u + 7) / 8, i1 = (n * (u + 1) + 7) / 8;
          if constexpr (i0 < i1) hlev(IC<lv0(s) + i0>{});
          if constexpr (i0 + 1 < i1) hlev(IC<lv0(s) + i0 + 1>{});
        });
#pragma unroll
        for (int q = 0; q < st3(s); ++q) st128(rws, hw[q], lane * 16u + (4 * wm + (q ^ (2 * wn))) * 1024, ci * HID_PANEL + kb_of(cj, s < 25 ? 2 * (s >> 3) + wn : 6) * 8192);
        sl = nq(sl);
        if constexpr (s < 11) next3(); else nexts();
        if constexpr (s == 11) {
          rb = mk(w2, 3584 * 2048);
#pragma unroll
          for (int q = 0; q < 4; ++q) vb[q] = (4 * q + g) * 2048 + ((((r15 >> 1) ^ (4 * (q & 1) + g)) * 2) + (lane & 1)) * 16 - 1024 * q;
        }
      });
    }
    static_assert(MOVE >= 0 && MOVE <= 3, "");
    sfor<3 - MOVE>([&](auto) AI { waitv<16>(); bar(); ival(G, nowork); sl = nq(sl); nexts(); });
    if (half) sfor<2 * MOVE>([&](auto) AI { waitv<16>(); bar(); ival(G, nowork); sl = nq(sl); nexts(); });
    waitv<16>(); bar();
    if (half) {
      sfor<8>([&](auto U) AI { constexpr int u = U.value; if constexpr ((u & 3) == 0) setm0(sl + wq * 8192 + u * 1024);
        dma<(u & 3) * 1024>(rx, (4u * (u & 3) + g) * 2048 + r15 * 16 - (u & 3) * 1024, (ci * 128 + 32 * wq + 16 * (u >> 2)) * 2048 + cnt * 256); });
      waitv<8 + 8>();
    } else waitv<8>();
    bar();
    ld32a(pv, rws, fvo, flagb + FK_P * 128);
    if (half) waitv<9>(); else waitv<0>();
    bar();
    auto getp = [&]() AI {
      sfor<8>([&](auto U) AI { constexpr int u = U.value; if constexpr ((u & 3) == 0) setm0(nq(sl) + wq * 8192 + u * 1024);
        dma1<(u & 3) * 1024>(rws, lane * 16u, PART_OFF + (ci * 8 + cnt) * 32768 + wq * 8192 + (u >> 2) * 4096); }); };
    waitv<0>(); PIN(pv);
    const bool early = half && !anyl(lane == cnt && pv != epr);
    if (early) getp(); else ld32a(pv, rws, fvo, flagb + FK_P * 128);
    bar();
    if (half) {
      if (!early) { waitv<0>(); check_flags(pv, rws, fvo, flagb + FK_P * 128, lane, cnt, cnt + 1, epr); getp(); }
      if (cnt == 0 && w == 4) stflag(rws, epr, lane, flagb + FK_EP * 128);
    }
    PH(5);
    waitv<0>(); bar();
  }
  if (half) {
    const int bg = r31 & 15, sw = (r31 >> 4) * 8 + (bg >> 3) * 4 + (bg & 3);
    const unsigned pa = ((sl + 65536u + (w >> 2) * 32768u) & 131071u) + (r31 >> 4) * 16384 + (wq * 4 + (bg >> 2)) * 1024 + (bg & 3) * 256;
    const unsigned pp = nq(sl) + ((w >> 2) * 2 + (r31 >> 4)) * 8192 + (wq * 2 + (bg >> 3)) * 1024 + (bg & 3) * 256 + ((bg >> 2) & 1) * 8;
    const f32x4 bv = rdf4(T_OUT + r31 * 16), dv = rdf4(T_OUT + 512 + r31 * 16);
    f32x4 av[8]; u32x2 pw[8], xw[8]; float gg[8];
#pragma unroll
    for (int u = 0; u < 8; ++u) {
      const int row = 2 * u + hl;
      av[u] = rdf4(pa + ((row ^ bg) * 16)); pw[u] = *(LAS u32x2*)(size_t)(pp + ((row ^ sw) * 16));
      xw[u] = *(LAS u32x2*)(size_t)(sl + (16 * w + hl) * 256 + r31 * 8 + u * 512); gg[u] = rdf(T_ROW + 1536 + (16 * w + hl) * 4 + u * 8);
    }
    SB();
#pragma unroll
    for (int u = 0; u < 8; ++u) {
      const f32x4 pf = {bf2f(pw[u][0] & 0xffffu), u2f(pw[u][0] & 0xffff0000u), bf2f(pw[u][1] & 0xffffu), u2f(pw[u][1] & 0xffff0000u)};
      const f32x4 xf = {bf2f(xw[u][0] & 0xffffu), u2f(xw[u][0] & 0xffff0000u), bf2f(xw[u][1] & 0xffffu), u2f(xw[u][1] & 0xffff0000u)};
      f32x4 r;
#pragma unroll
      for (int e = 0; e < 4; ++e) r[e] = (av[u][e] + pf[e]) + __builtin_fmaf(gg[u], __builtin_fmaf(xf[e], dv[e], -bv[e]), bv[e]);
      st128<OAUX>(mk(out, 8u << 20), __builtin_bit_cast(u32x4, r), (unsigned)((16 * w + hl + 2 * u) * 4096 + r31 * 16), ci * 128 * 4096 + cnt * 512);
    }
  }
  if (w == 4 && half) { waitv<0>(); PH(6); const unsigned ex = DIAG;
    while ((unsigned)__builtin_amdgcn_s_memrealtime() - ph[6] < ex) __builtin_amdgcn_s_sleep(1); }
}

extern "C" void kernel_launch(void* const* d_in, const int* in_sizes, int n_in, void* d_out, int out_size, void* d_ws, size_t ws_size, hipStream_t stream) {
  (void)in_sizes; (void)n_in; (void)out_size; (void)ws_size;
  (void)hipFuncSetAttribute((const void*)fused_kernel, hipFuncAttributeMaxDynamicSharedMemorySize, LDS_BYTES);
  fused_kernel<<<256, 512, LDS_BYTES, stream>>>((const u16*)d_in[0], (const u16*)d_in[1], (const u16*)d_in[2], (const u16*)d_in[3], (const u16*)d_in[4],
      (const u16*)d_in[5], (const u16*)d_in[6], (const u16*)d_in[7], (const u16*)d_in[8], (const u16*)d_in[10], (const u16*)d_in[11], (const u16*)d_in[12],
      (const u16*)d_in[13], (const u16*)d_in[14], (char*)d_ws, (float*)d_out);
}
```

```cpp
#include <hip/hip_runtime.h>
#include <utility>

typedef unsigned short u16;
typedef __bf16 bf16x2 __attribute__((ext_vector_type(2)));
typedef __bf16 bf16x4 __attribute__((ext_vector_type(4)));
typedef __bf16 bf16x8 __attribute__((ext_vector_type(8)));
typedef float f32x2 __attribute__((ext_vector_type(2)));
typedef float f32x4 __attribute__((ext_vector_type(4)));
typedef float f32x16 __attribute__((ext_vector_type(16)));
typedef unsigned u32x2 __attribute__((ext_vector_type(2)));
typedef unsigned u32x4 __attribute__((ext_vector_type(4)));
typedef __amdgpu_buffer_rsrc_t rsrc_t;
#define LAS __attribute__((address_space(3)))
#define DI __device__ __forceinline__
#define AI __attribute__((always_inline))
#define SB() __builtin_amdgcn_sched_barrier(0)
#define PIN(x) asm volatile("" : "+v"(x))
#define MFMA(a, b, c) __builtin_amdgcn_mfma_f32_16x16x32_bf16(a, b, c, 0, 0, 0)
#define MMA(c, a, b) asm volatile("v_mfma_f32_16x16x32_bf16 %0, %1, %2, %0" : "+v"(c) : "v"(a), "v"(b))
#define MMZ(c, a, b) asm volatile("v_mfma_f32_16x16x32_bf16 %0, %1, %2, 0" : "=&v"(c) : "v"(a), "v"(b))
#define MPAD() asm volatile("s_nop 7")
template <int V> using IC = std::integral_constant<int, V>;

constexpr unsigned HID_PANEL = 112 * 8192, H_OFF = 16 * HID_PANEL, H_PANEL = 32 * 16384;
constexpr unsigned PART_OFF = H_OFF + 16 * H_PANEL;
constexpr unsigned GRAN_OFF = PART_OFF + 256 * 49152;
constexpr unsigned RG_OFF = GRAN_OFF + 256 * 1024;
constexpr unsigned CG_OFF = RG_OFF + 16 * 8192;
constexpr unsigned FLAG_OFF = CG_OFF + 256 * 2048;
constexpr unsigned EPC_OFF = FLAG_OFF + 16 * 1024;
constexpr unsigned PF_OFF = EPC_OFF + 16 * 128;
constexpr unsigned WS_BYTES = PF_OFF + 256 * 256;
constexpr int FK_M1 = 0, FK_M2 = 1, FK_M3 = 2, FK_M4 = 3, FK_H = 4, FK_P = 5, FK_EP = 6;
constexpr unsigned TAB = 147456, T_ROW = TAB, T_XA = TAB + 2048, T_OUT = TAB + 4096, T_B1 = TAB + 5120, T_CS2 = TAB + 6144, T_WA = TAB + 8192, T_ZERO = TAB + 12288, T_AGG = T_WA, T_DUM = TAB + 12544, T_CON = TAB + 12800;
constexpr unsigned LDS_BYTES = 163840;
#define XMAP 1
#define ROTM 3
#define OAUX 2
#define SP0 4
#define SPN 6
#define PTE 0
#define BW0 0
#define BWN 8
#define MOVE 3
#define PROFX false
#define WD 8
constexpr int LASTR = 24;
constexpr unsigned SPIN_CAP = 1u << 16;

DI rsrc_t mk(const void* p, unsigned bytes) { return __builtin_amdgcn_make_buffer_rsrc((void*)p, 0, bytes, 0x00020000); }
DI void setm0(unsigned lds) { asm volatile("s_mov_b32 m0, %0\n\ts_nop 0" ::"s"(lds) : "memory"); }
template <int OFF> DI void dma(rsrc_t r, unsigned voff, unsigned soff) {
  asm volatile("buffer_load_dwordx4 %0, %1, %2 offen offset:%3 lds" ::"v"(voff), "s"(r), "s"(soff), "i"(OFF) : "memory");
}
template <int OFF> DI void dma1(rsrc_t r, unsigned voff, unsigned soff) {
  asm volatile("buffer_load_dwordx4 %0, %1, %2 offen offset:%3 sc1 lds" ::"v"(voff), "s"(r), "s"(soff), "i"(OFF) : "memory");
}
#define PIN2(x, y) asm volatile("" : "+v"(x), "+v"(y))
#define PINS(x) asm volatile("" : "+s"(x))
typedef const void* const __attribute__((address_space(4)))* kargs_t;
DI const char* karg(int i) { kargs_t ka = (kargs_t)__builtin_amdgcn_kernarg_segment_ptr(); PINS(ka); return (const char*)ka[i]; }
enum { A_X, A_LRE, A_LIM, A_BRE, A_BIM, A_CRE, A_CIM, A_LDT, A_DSK, A_W1, A_B1, A_W2, A_B2, A_GW, A_WS, A_OUT };
template <int N> DI void waitv() { asm volatile("s_waitcnt vmcnt(%0)" ::"i"(N) : "memory"); }
DI void waitl0() { SB(); __builtin_amdgcn_s_waitcnt(0xC07F); asm volatile("" ::: "memory"); }
#define PH(k)
DI void bar() { asm volatile("s_barrier" ::: "memory"); SB(); }
DI bf16x8 rd128(unsigned a) { return *(LAS bf16x8*)(size_t)a; }
DI u32x4 rdu4(unsigned a) { return *(LAS u32x4*)(size_t)a; }
DI f32x4 rdf4(unsigned a) { return *(LAS f32x4*)(size_t)a; }
DI f32x2 rdf2(unsigned a) { return *(LAS f32x2*)(size_t)a; }
DI float rdf(unsigned a) { return *(LAS float*)(size_t)a; }
DI void wrf(unsigned a, float v) { *(LAS float*)(size_t)a = v; }
DI void wrf2(unsigned a, f32x2 v) { *(LAS f32x2*)(size_t)a = v; }
DI bf16x4 rtr(unsigned a) { return __builtin_amdgcn_ds_read_tr16_b64_v4bf16((LAS bf16x4*)(size_t)a); }
DI void sethalf(bf16x8& f, int h, bf16x4 v) {
  if (h == 0) f = __builtin_shufflevector(v, v, 0, 1, 2, 3, -1, -1, -1, -1);
  else { bf16x8 t = __builtin_shufflevector(v, v, -1, -1, -1, -1, 0, 1, 2, 3); f = __builtin_shufflevector(f, t, 0, 1, 2, 3, 12, 13, 14, 15); }
}
DI unsigned f2u(float f) { return __builtin_bit_cast(unsigned, f); }
DI float u2f(unsigned u) { return __builtin_bit_cast(float, u); }
DI float shx(int lane, float v, int m) { return u2f(__builtin_amdgcn_ds_bpermute((lane ^ m) * 4, (int)f2u(v))); }
DI float sum16(float v) { const auto r = __builtin_amdgcn_permlane16_swap(f2u(v), f2u(v), false, false); return u2f(r[0]) + u2f(r[1]); }
DI float sum32(float v) { const auto r = __builtin_amdgcn_permlane32_swap(f2u(v), f2u(v), false, false); return u2f(r[0]) + u2f(r[1]); }
DI float fexp(float v) { return __builtin_amdgcn_exp2f(v * 1.4426950408889634f); }
DI float bf2f(unsigned v) { return u2f(v << 16); }
DI unsigned pk2(float lo, float hi) { f32x2 v = {lo, hi}; return __builtin_bit_cast(unsigned, __builtin_convertvector(v, bf16x2)); }
DI float rbf(float f) { return u2f(pk2(f, 0.f) << 16); }
DI float dot2(unsigned a, unsigned b, float c) { return __builtin_amdgcn_fdot2_f32_bf16(__builtin_bit_cast(bf16x2, a), __builtin_bit_cast(bf16x2, b), c, false); }
template <int C> DI float dppadd(float v) { return v + u2f(__builtin_amdgcn_update_dpp(0, (int)f2u(v), C, 0xf, 0xf, true)); }
DI float sel4(bool b0, bool b1, float a, float b, float c, float d) { PIN(a); PIN(b); PIN(c); PIN(d); const float lo = b0 ? b : a, hi = b0 ? d : c; return b1 ? hi : lo; }
DI f32x2 cmul(f32x2 a, f32x2 b) { return (f32x2){a.x * b.x - a.y * b.y, a.x * b.y + a.y * b.x}; }
DI f32x2 cmulc(f32x2 a, f32x2 b) { return (f32x2){__builtin_fmaf(a.x, b.x, -(a.y * b.y)), __builtin_fmaf(-a.x, b.y, -(a.y * b.x))}; }
DI f32x2 cfma(f32x2 a, f32x2 b, f32x2 c) { return (f32x2){__builtin_fmaf(a.x, b.x, __builtin_fmaf(-a.y, b.y, c.x)), __builtin_fmaf(a.x, b.y, __builtin_fmaf(a.y, b.x, c.y))}; }
template <int AUX = 16> DI void st128(rsrc_t r, u32x4 v, unsigned voff, unsigned soff) {
  __builtin_amdgcn_raw_buffer_store_b128(v, r, voff, soff, AUX);
  asm volatile("s_nop 1" ::"v"(v));
}
DI void st64(rsrc_t r, u32x2 v, unsigned voff, unsigned soff) { __builtin_amdgcn_raw_buffer_store_b64(v, r, voff, soff, 16); }
DI void st32(rsrc_t r, unsigned v, unsigned voff, unsigned soff) { __builtin_amdgcn_raw_buffer_store_b32(v, r, voff, soff, 16); }
DI void stflag(rsrc_t r, unsigned v, int lane, unsigned soff) { if (lane == 0) st32(r, v, 0u, soff); }
DI void ld32a(unsigned& d, rsrc_t r, unsigned voff, unsigned soff) { asm volatile("buffer_load_dword %0, %1, %2, %3 offen sc1" : "=v"(d) : "v"(voff), "s"(r), "s"(soff) : "memory"); }
DI void ld16p(unsigned& d, const u16* p, unsigned voff) { asm volatile("global_load_ushort %0, %1, %2" : "=v"(d) : "v"(voff), "s"(p) : "memory"); }
DI void ld64p(u32x2& d, rsrc_t r, unsigned voff, unsigned soff) { asm volatile("buffer_load_dwordx2 %0, %1, %2, %3 offen" : "=v"(d) : "v"(voff), "s"(r), "s"(soff) : "memory"); }
DI void ld64a(u32x2& d, rsrc_t r, unsigned voff, unsigned soff) { asm volatile("buffer_load_dwordx2 %0, %1, %2, %3 offen sc1" : "=v"(d) : "v"(voff), "s"(r), "s"(soff) : "memory"); }
DI void ld128g(u32x4& d, const u16* p, unsigned voff) { asm volatile("global_load_dwordx4 %0, %1, %2" : "=v"(d) : "v"(voff), "s"(p) : "memory"); }
DI void pfl(unsigned& d, rsrc_t r, unsigned voff, unsigned soff) { asm volatile("buffer_load_dword %0, %1, %2, %3 offen" : "+v"(d) : "v"(voff), "s"(r), "s"(soff) : "memory"); }
template <class T> DI T sel(bool c, T a, T b) { return c ? a : b; }
DI void pfg(const char* p) { asm volatile("global_load_lds_dword %0, off" ::"v"(p) : "memory"); }
DI bool anyl(bool b) { return __builtin_amdgcn_ballot_w64(b) != 0; }
DI void ld128p(u32x4& d, rsrc_t r, unsigned voff, unsigned soff) { asm volatile("buffer_load_dwordx4 %0, %1, %2, %3 offen" : "=v"(d) : "v"(voff), "s"(r), "s"(soff) : "memory"); }
DI void sincos_f(float t, float& sn, float& cs) {
  const float k = __builtin_rintf(t * 0.63661977236f);
  float r = __builtin_fmaf(k, -1.5707962513f, t); r = __builtin_fmaf(k, -7.5497894159e-08f, r); r = __builtin_fmaf(k, -5.3903029534e-15f, r);
  const float z = r * r;
  const float sp = __builtin_fmaf(r * z, __builtin_fmaf(z, __builtin_fmaf(z, __builtin_fmaf(z, 2.7183114939e-06f, -1.9839334836e-04f), 8.3333293858e-03f), -1.6666666642e-01f), r);
  const float cp = __builtin_fmaf(z, __builtin_fmaf(z, __builtin_fmaf(z, __builtin_fmaf(z, 2.4390448796e-05f, -1.3886763775e-03f), 4.1666623324e-02f), -4.9999999726e-01f), 1.f);
  const int q = (int)k;
  const float a = (q & 1) ? cp : sp, b = (q & 1) ? sp : cp;
  sn = (q & 2) ? -a : a; cs = ((q + 1) & 2) ? -b : b;
}
DI void ld128a(u32x4& d, rsrc_t r, unsigned voff, unsigned soff) { asm volatile("buffer_load_dwordx4 %0, %1, %2, %3 offen sc1" : "=v"(d) : "v"(voff), "s"(r), "s"(soff) : "memory"); }

DI unsigned x1off(int a) { return a == 0 ? 131072u : (unsigned)(81920 + a * 16384); }
DI unsigned nq(unsigned s) { return (s + 32768u) & 131071u; }

DI int kb_of(int j, int bl) { return 7 * j + ((j & 1) ? (bl == 6 ? 0 : bl + 1) : bl); }

template <int NR, int LAST> constexpr int nread(int sl) {
  constexpr int DENSE = 2 * NR - LAST - 2;
  return sl < 0 ? 0 : sl < DENSE ? sl + 1 : sl >= LAST ? NR : DENSE + (sl - DENSE) / 2 + 1;
}
template <int N, int L, int X, bool FIRST, int NR, int LAST, int NB, int SL, class RD, class MM, class UP>
DI void slot(RD& rd, MM& mm, UP& upd) {
  if constexpr (!FIRST && SL >= L && (SL == L + 4 || (SL - L) % 8 == 0)) {
    __builtin_amdgcn_s_waitcnt(0xC07F | ((nread<NR, LAST>(SL - 1) - nread<NR, LAST>(SL - WD)) << 8)); SB();
  }
  if constexpr (SL >= L) mm(X, SL - L, SL); else if constexpr (!FIRST) mm(X ^ 1, SL + N - L, SL);
  SB();
  if constexpr (nread<NR, LAST>(SL) > nread<NR, LAST>(SL - 1)) rd(X, nread<NR, LAST>(SL) - 1);
  constexpr int per = NB > N - 1 - LAST ? 2 : 1, u0 = (SL - LAST - 1) * per;
  if constexpr (SL > LAST && u0 < NB) { upd(u0); if constexpr (per == 2 && u0 + 1 < NB) upd(u0 + 1); }
  SB();
}
template <int N, int L, int X, bool FIRST, int NR, int LAST, int NB, class RD, class MM, class UP, int... I>
DI void period_(RD& rd, MM& mm, UP& upd, std::integer_sequence<int, I...>) { (slot<N, L, X, FIRST, NR, LAST, NB, I>(rd, mm, upd), ...); }
template <int N, int L, int X, bool FIRST, int NR, int LAST, int NB, class RD, class MM, class UP>
DI void period(RD& rd, MM& mm, UP& upd) {
  static_assert(NR - 1 <= LAST && !(LAST & 1) && NB <= 2 * (N - 1 - LAST), "");
  period_<N, L, X, FIRST, NR, LAST, NB>(rd, mm, upd, std::make_integer_sequence<int, N>{});
  waitl0();
}
template <class G, class W, int... U> DI void ival_(G& g, W& wk, std::integer_sequence<int, U...>) { ((g(IC<U>{}), SB(), wk(IC<U>{}), SB()), ...); }
template <class G, class W> DI void ival(G&& g, W&& wk) { ival_(g, wk, std::make_integer_sequence<int, 8>{}); }
template <int N, class F, int... I> DI void sfor_(F& f, std::integer_sequence<int, I...>) { (f(IC<I>{}), ...); }
template <int N, class F> DI void sfor(F&& f) { sfor_<N>(f, std::make_integer_sequence<int, N>{}); }

DI void check_flags(unsigned& pv, rsrc_t ws, unsigned voff, unsigned soff, int lane, int lo, int hi, unsigned ep) {
  PIN(pv);
  const bool need = lane >= lo && lane < hi;
  if (__builtin_expect(anyl(need && pv != ep), 0)) {
    for (unsigned sp = 0; sp < SPIN_CAP; ++sp) {
      __builtin_amdgcn_s_sleep(2);
      ld32a(pv, ws, voff, soff); asm volatile("s_waitcnt vmcnt(0)" : "+v"(pv)::"memory");
      if (!anyl(need && pv != ep)) break;
    }
  }
}

#if defined(__HIP_DEVICE_COMPILE__) && __HIP_DEVICE_COMPILE__
__attribute__((target("no-packed-fp32-ops")))
#endif
__global__ __launch_bounds__(512, 2) void fused_kernel(const u16* x, const u16* lre, const u16* lim, const u16* bre, const u16* bim, const u16* cre, const u16* cim,
    const u16* ldt, const u16* dsk, const u16* w1, const u16* b1, const u16* w2, const u16* b2, const u16* gw, char* ws, float* out) {
  extern __shared__ __attribute__((aligned(16))) char shm[];
  unsigned long long pc0; asm volatile("s_getpc_b64 %0" : "=s"(pc0));
  const int lane = threadIdx.x & 63, w = __builtin_amdgcn_readfirstlane(threadIdx.x >> 6);
  const int bid = blockIdx.x, xcd = bid & 7, loc = bid >> 3;
  const int ci = (xcd & 3) * 4 + (loc & 3), cj = (loc >> 4) * 8 + (xcd >> 2) * 4 + ((loc >> 2) & 3);
  const int rot = (ci + cj) & ROTM, ntp = cj & 3, kq = cj >> 2, r2 = kq ^ 3, wq = w & 3, tq = threadIdx.x & 255;
  const rsrc_t rws = mk(ws, WS_BYTES), rx = mk(x, 2048 * 2048);
  const int wm = (w >> 1) & 1, wn = w & 1, g = lane >> 4, r15 = lane & 15, q2 = (lane >> 2) & 3, p = lane & 3, hl = lane >> 5, r31 = lane & 31;
  const unsigned flagb = FLAG_OFF + ci * 1024, fvo = (lane & 15) * 4;
  unsigned rgb = RG_OFF + ci * 8192, osof = ci * 128 * 4096 + ntp * 1024 + kq * 128; PINS(rgb); PINS(osof);
  auto nowork = [&](auto) AI {};
  unsigned va[4], vb[4], vw[7], sl, sa, sb, sc, ta, tb, tc; int sx; rsrc_t ra, rb;
  auto vnat = [&](int u) AI { return (8u * u + (lane >> 3)) * 2048 + (((lane & 7) ^ ((4 * u + g) & 7)) * 16) - 1024 * u; };
  auto nexts = [&]() AI { sa = __builtin_amdgcn_readlane(ta, sx); sb = __builtin_amdgcn_readlane(tb, sx); ++sx; PINS(sa); PINS(sb); };
  auto G = [&](auto U) AI { constexpr int u = U.value;
    if constexpr (u == 0) setm0(sl + wq * 4096);
    if constexpr (u == 4) setm0(sl + 16384 + wq * 4096);
    if constexpr (u < 4) dma1<u * 1024>(ra, va[u], sa); else dma<(u - 4) * 1024>(rb, vb[u - 4], sb);
  };
  auto G2 = [&](auto S, auto U) AI { constexpr int a = S.value % 3, u = U.value;
    if constexpr (u == 0) setm0((a == 0 ? 131072u : 81920u + a * 16384u) + wq * 4096);
    if constexpr (u == 4) setm0(a * 32768 + wq * 7168);
    if constexpr (u == 8) setm0(a * 32768 + wq * 7168 + 4096);
    if constexpr (u < 4) dma<u * 1024>(rx, va[u], sa); else dma<(u & 3) * 1024>(rb, vw[u - 4], sb);
  };
  const int c7 = ci & 7;
  auto setup2 = [&]() AI {
#pragma unroll
    for (int q = 0; q < 6; ++q) { const int k = 8 * (q & 1) + (lane >> 3), f = ((k >> 1) & 1) | ((q & 1) << 1);
      vw[q] = 4096 + k * 7168 + ((cj & 1) ? 64 : 0) + (q >> 1) * 128 + (((((lane & 7) >> 1) ^ f) * 2) + (lane & 1)) * 16 - (q & 3) * 1024; }
    vw[6] = 4096 + (lane >> 2) * 7168 + ((cj & 1) ? 0 : 384) + (((((lane & 3) >> 1) ^ hl) * 2) + (lane & 1)) * 16 - 2048;
    int k = lane - 2 + (c7 < 2 ? 3 * c7 : c7 - 2); k = 2 + (k >= 6 ? k - 6 : k);
    k = lane < 2 ? lane : lane < 8 ? k : (lane ^ rot);
    k = (k ^ (ci & 8)) & 15;
    ta = (ci * 128 + 32 * wq) * 2048 + k * 128; tb = (k * 64 + 16 * wq) * 7168 + cj * 448;
    PIN2(ta, tb);
#pragma unroll
    for (int q = 0; q < 7; ++q) PIN(vw[q]);
  };
  auto G3 = [&](auto S, auto U) AI { constexpr int a = (S.value + 1) % 3, u = U.value;
    if constexpr (u == 0) setm0((a == 0 ? 131072u : 81920u + a * 16384u) + wq * 4096);
    if constexpr (u == 4 || u == 8) setm0(a * 32768 + wq * 8192 + (u - 4) * 1024);
    if constexpr (u < 4) dma1<u * 1024>(rws, va[0], sa); else dma<(u & 3) * 1024>(rb, vb[u & 3], u < 8 ? sb : sc);
  };
  auto table3 = [&](unsigned& ta, unsigned& tb, unsigned& tc) AI {
    const int ks = 8 * kq + ((lane ^ (2 * ntp)) & 7);
    const int t = lane - 8, lc = t >> 2;
    const int kb0 = lc < 3 ? kb_of(4 * kq + ((t ^ ntp) & 3), 2 * lc) : kb_of(4 * kq + (((2 * t) ^ ntp) & 3), 6);
    const int kb1 = lc < 3 ? kb0 + 1 : kb_of(4 * kq + (((2 * t + 1) ^ ntp) & 3), 6);
    const int crow = ntp * 256 + (wq >> 1) * 128, wcol = ntp * 512 + ((wq & 2) + ((wq ^ (r2 >> 1)) & 1)) * 128;
    ta = lane < 8 ? H_OFF + ci * H_PANEL + ks * 16384 + wq * 4096 : ci * HID_PANEL + (wq < 2 ? kb0 : kb1) * 8192 + (wq & 1) * 4096;
    tb = lane < 8 ? (crow + ((2 * (wq & 1)) ^ r2) * 32) * 2048 + (ks >> 1) * 128 + (ks & 1) : kb0 * 65536 + wcol;
    tc = lane < 8 ? (crow + ((2 * (wq & 1) + 1) ^ r2) * 32) * 2048 + (ks >> 1) * 128 : kb1 * 65536 + wcol;
    PIN2(ta, tb); PIN(tc);
  };
  auto next4 = [&]() AI { sc = __builtin_amdgcn_readlane(tc, sx); PINS(sc); nexts(); };
  auto next3 = [&]() AI { next4(); rb = mk(sel<const u16*>(sb & 1, cim, cre), 1024 * 2048); sb &= ~1u; };
  f32x4 fv[4]; unsigned fpp[4], fo;
  auto unp = [&](u32x2 v, int e) AI { return (e & 1) ? u2f(v[e >> 1] & 0xffff0000u) : bf2f(v[e >> 1] & 0xffffu); };
  auto presum = [&]() AI {
    const int c8 = lane & 7, sg = (lane >> 3) & 1, xw_ = (w >> 2) * 2 + sg;
    const f32x4 bv = rdf4(T_OUT + r15 * 16), dv = rdf4(T_OUT + 256 + r15 * 16);
#pragma unroll
    for (int u = 0; u < 4; ++u) {
      const int row = 4 * u + g;
      const f32x4 av = rdf4(65536 + xw_ * 8192 + (wq * 2 + (c8 >> 2)) * 1024 + (c8 & 3) * 256 + ((row ^ (8 * sg + c8)) * 16));
      const u32x2 xw = *(LAS u32x2*)(size_t)(114688 + (16 * w + row) * 128 + r15 * 8); const float gg = rdf(T_ROW + 1536 + (16 * w + row) * 4);
#pragma unroll
      for (int e = 0; e < 4; ++e) fv[u][e] = av[e] + __builtin_fmaf(gg, __builtin_fmaf(unp(xw, e), dv[e], -bv[e]), bv[e]);
      fpp[u] = xw_ * 4096 + wq * 1024 + (c8 & 3) * 256 + (c8 >> 2) * 8 + ((row ^ (8 * sg + 2 * (c8 & 3))) * 16);
      PIN2(fv[u], fpp[u]);
    }
    fo = (16 * w + g) * 4096 + sg * 512 + c8 * 16; PIN(fo);
  };

  if (w < 4) {
    constexpr bool isx = PROFX; (void)isx;
    unsigned epr, epc;
    const unsigned ca = (32 * wn + r31) * 8;
    const unsigned c0 = (32 * wn + r15) * 8;
    const int seg0 = 4 * wm + 2 * hl;
    f32x2 lam[2], cof[2], cfa, l16, l128, l1k, pw0, pw1; unsigned t3a, t3b;
    {
      setm0(T_DUM);
      const int row = bid * 8 + (lane & 7);
      const char* pp = (const char*)(hl ? (row < 1024 ? bre : bim - 1024 * 1024) : x) + (size_t)row * 2048 + (4 * wq + ((lane >> 3) & 3)) * 128;
      for (int q = 0; q <= wq; ++q) __builtin_amdgcn_s_sleep(6);
      pfg(pp);
      pfg((const char*)pc0 + (((loc * 4 + wq) % 20) * 16 + (lane & 15)) * 128);
      ld32a(epr, rws, 0u, flagb + FK_EP * 128); ld32a(epc, rws, 0u, EPC_OFF + cj * 128);
      setup2(); sx = 2; nexts(); table3(t3a, t3b, tc);
      rb = mk((const char*)w1 - 4096, 1024 * 7168 + 4096);
#pragma unroll
      for (int q = 0; q < 4; ++q) { va[q] = vnat(q); PIN(va[q]); }
      wrf(T_ZERO + (lane & 3) * 4, 0.f);
    }
    {
      f32x4 acc[4][4];
#pragma unroll
      for (int a = 0; a < 16; ++a) { acc[a >> 2][a & 3] = (f32x4){0, 0, 0, 0}; PIN(acc[a >> 2][a & 3]); }
      bf16x8 wv[4][2];
      const int sstar = c7 < 2 ? c7 : 2;
      const int fsw = ((q2 >> 1) & 1) | ((g & 1) << 1);
      unsigned cf = sstar * 32768 + (g >> 1) * 7168 + (8 * (g & 1) + q2) * 128 + ((wq ^ fsw) * 32) + p * 8;
      unsigned ch = sstar * 32768 + (g >> 1) * 7168 + 6144 + (8 * (g & 1) + q2) * 64 + (((wq & 1) ^ (g & 1)) * 32) + p * 8;
      PIN(cf); PIN(ch);
      auto wread = [&](int st) AI {
        if (sstar == st) {
#pragma unroll
          for (int e = 0; e < 4; ++e)
#pragma unroll
            for (int t = 0; t < 4; ++t) sethalf(wv[t][e >> 1], e & 1, rtr((e >> 1) * 14336 + (t < 3 ? cf + t * 2048 + (e & 1) * 512 : ch + (e & 1) * 256)));
        } };
      {
        bf16x8 fa[4][2], fb[2][4][2];
        const unsigned n0 = r15 * 128 + ((g ^ (r15 >> 1)) * 16), n1 = r15 * 128 + (((4 + g) ^ (r15 >> 1)) * 16);
        const unsigned cb[4] = {wm * 8192u + n0, wm * 8192u + n1, 16384 + wn * 8192u + n0, 16384 + wn * 8192u + n1};
        unsigned ba[4], rs = 98304;
#pragma unroll
        for (int b = 0; b < 4; ++b) { ba[b] = rs + cb[b]; PIN(ba[b]); }
        auto rd = [&](int X, int r) AI { const int ks = r & 1, f = r >> 1;
          if (f >= 1 && f < 5) fb[X][f - 1][ks] = rd128(ba[2 + ks] + (f - 1) * 2048); else { const int a = f ? f - 4 : 0; fa[a][ks] = rd128(ba[ks] + a * 2048); } };
        auto mm = [&](int X, int q, int) AI { const int a = q >> 3, b = (q >> 1) & 3, ks = q & 1; MMA(acc[a][b], fa[a][ks], fb[X][b][ks]); };
        auto up = [&](int b) AI { if (b == 0) { rs = nq(rs); PINS(rs); } else { ba[b - 1] = rs + cb[b - 1]; PIN(ba[b - 1]); } };
        waitl0(); bar();
        period<32, 16, 0, true, 16, LASTR, 5>(rd, mm, up); bar();
        for (int s = 0; s < 7; ++s) { period<32, 16, 1, false, 16, LASTR, 5>(rd, mm, up); bar(); period<32, 16, 0, false, 16, LASTR, 5>(rd, mm, up); bar(); }
        period<32, 16, 1, false, 16, LASTR, 5>(rd, mm, up); bar();
        waitv<0>(); PIN(epr); PIN(epc);
        epr = __builtin_amdgcn_readfirstlane(epr) + 1u; epc = __builtin_amdgcn_readfirstlane(epc) + 1u;
        lam[0] = rdf2(T_CON + c0); lam[1] = rdf2(T_CON + c0 + 128);
        wread(0);
        sfor<7>([&](auto U) AI { G2(IC<2>{}, IC<U.value + 4>{}); SB();
#pragma unroll
          for (int q = 16 + (16 * U.value) / 7 / 2 * 2; q < 16 + (16 * (U.value + 1)) / 7 / 2 * 2; ++q) mm(1, q, 0);
          SB(); });
        nexts();
      }
      f32x2 st[2] = {{0, 0}, {0, 0}}, Pf[2] = {{0, 0}, {0, 0}}, Gt = {0, 0}, cr = {0, 0}, ag[8];
      u32x4 lb[8], cg[8], rgv[2]; unsigned lbo[8], lbe[8], pv, gd = 0; unsigned obs[2][2][2][4];
      f32x4 cs[4]; f32x2 so[2]; float c0s = 0.f, c1s = 0.f;
      const rsrc_t rgr = mk(ws + GRAN_OFF + cj * 16384, 16384);
      const int cp = 28 * wq + (r31 < 28 ? r31 : 27);
      const unsigned cgo = hl * 16384 + cp * 16;
#define OB(a, b, c) ((u32x4){obs[a][b][c][0], obs[a][b][c][1], obs[a][b][c][2], obs[a][b][c][3]})
      auto hofs = [&](int hf, int mt) AI { return (2u * wn + mt) * 4096 + wm * 2048 + hf * 1024 + g * 256 + ((r15 ^ (hf << 2)) * 16); };
      auto bad = [&]() AI { unsigned d = 0;
#pragma unroll
        for (int q = 0; q < 8; ++q) { PIN(lb[q]); d |= (int)lbo[q] < 0 ? 0u : ((lb[q][1] ^ epc) | (lb[q][3] ^ epc)); }
        return d != 0; };
      auto gbad = [&]() AI { unsigned d = 0;
#pragma unroll
        for (int q = 0; q < 8; ++q) { PIN(cg[q]); d |= (cg[q][1] ^ epc) | (cg[q][3] ^ epc); }
        return d != 0; };
      auto E2 = [](int s) constexpr { return (s == 2 || s == 3) ? 1 : 0; };
      auto T2 = [](int s) constexpr { return s == 4 ? 8 : (s == 7 || s == 8) ? 4 : s == 11 ? 10 : 0; };
      sfor<15>([&](auto S) AI { constexpr int s = S.value;
        if constexpr (s < 3) waitl0();
        if constexpr (s == 6) waitv<11>();
        else if constexpr (s >= 1) waitv<T2(s - 2) + (s < 14 ? 11 : 8) + E2(s - 1) + T2(s - 1)>();
        bar();
        if constexpr (s < 2) wread(s + 1);
        if constexpr (s == 2) {
#pragma unroll
          for (int sg = 0; sg < 8; ++sg) ag[sg] = rdf2(T_AGG + sg * 512 + ca);
          l16 = rdf2(T_CON + 1024 + ca); cof[0] = rdf2(T_CON + 512 + c0); cof[1] = rdf2(T_CON + 512 + c0 + 128);
        }
        if constexpr (s == 5) { pw1 = rdf2(T_CON + 1536 + ca); pw0 = rdf2(T_CON + 2048 + ca); l128 = rdf2(T_CON + 2560 + ca); l1k = rdf2(T_CON + 3072 + ca); cfa = rdf2(T_CON + 512 + ca); }
        if constexpr (s == 11) { if (w == 0) stflag(rws, epr, lane, flagb + FK_H * 128 + cj * 4); }
        if constexpr (s == 14) { if (w == 0) ld32a(pv, rws, fvo, flagb + FK_H * 128); }
        SB();
        auto work = [&](auto U) AI { constexpr int u = U.value;
          if constexpr (s < 2 && u >= 1 && u < 9) {
            constexpr int t = 8 * s + u - 1;
#pragma unroll
            for (int mt = 0; mt < 2; ++mt) st[mt] = cfma(lam[mt], st[mt], (f32x2){acc[t >> 2][mt][t & 3], acc[t >> 2][2 + mt][t & 3]});
            PIN2(st[0], st[1]);
          }
          if constexpr (s == 1 && u == 9) { wrf2(T_AGG + (4 * wm + g) * 512 + c0, st[0]); wrf2(T_AGG + (4 * wm + g) * 512 + c0 + 128, st[1]); }
          if constexpr (s == 2 && u >= 1 && u < 9) {
            constexpr int sg = u - 1, mt = sg & 1; const bool me = sg == seg0 + mt;
            Pf[mt].x = sel<float>(me, Gt.x, Pf[mt].x); Pf[mt].y = sel<float>(me, Gt.y, Pf[mt].y); Gt = cfma(l16, Gt, ag[sg]);
            PIN2(Gt, Pf[mt]);
          }
          if constexpr (s == 2 && u == 9) st64(rgr, (u32x2){f2u(sel<float>(hl, Gt.y, Gt.x)), epc}, ca * 2 + hl * 8, ci * 1024);
          if constexpr (s == 3 && u < 4) {
            const bf16x8 one = __builtin_bit_cast(bf16x8, (u32x4){0x3F803F80u, 0x3F803F80u, 0x3F803F80u, 0x3F803F80u});
            cs[u] = MFMA(one, wv[u][1], MFMA(one, wv[u][0], ((f32x4){0, 0, 0, 0}))); PIN(cs[u]);
          }
          if constexpr (s == 3 && u == 8) { PIN2(cs[0], cs[1]); PIN2(cs[2], cs[3]); }
          if constexpr (s == 3 && u == 9)
            st64(rws, (u32x2){f2u(sel4(g & 1, g & 2, cs[0][0], cs[1][0], cs[2][0], cs[3][0])), epc}, (g < 3 ? g * 64 + wq * 16 + r15 : 192 + (wq & 1) * 16 + r15) * 8u, CG_OFF + (cj * 16 + ci) * 2048);
          if constexpr ((s == 3 && u >= 4 && u < 9) || (s == 4 && u < 5) || (s == 5 && u >= 1 && u < 7)) {
            constexpr int t = s == 3 ? u - 4 : s == 4 ? 5 + u : 9 + u;
            f32x2 v0 = cmulc(cof[0], (f32x2){acc[t >> 2][0][t & 3], acc[t >> 2][2][t & 3]}), v1 = cmulc(cof[1], (f32x2){acc[t >> 2][1][t & 3], acc[t >> 2][3][t & 3]}); PIN2(v0, v1);
            acc[t >> 2][0][t & 3] = v0.x; acc[t >> 2][2][t & 3] = v0.y; acc[t >> 2][1][t & 3] = v1.x; acc[t >> 2][3][t & 3] = v1.y;
          }
          if constexpr (s == 4 && (u == 6 || u == 8)) {
#pragma unroll
            for (int q = 2 * u - 12; q < 2 * u - 8; ++q) { lbo[q] = (ci - 8 - 8 * hl + q) * 1024 + ca * 2; PIN(lbo[q]); }
          }
          if constexpr (s == 4 && u == 10) {
#pragma unroll
            for (int q = 0; q < 8; ++q) ld128a(lb[q], rgr, lbo[q], 0u);
          }
          if constexpr (s == 5 && u == 8) {
            if (!wm) pw0 = (f32x2){1.f, 0.f};
            if (ci <= 8) l1k = (f32x2){0.f, 0.f};
            pw0 = cmul(pw0, (f32x2){sel<float>(hl, pw1.x, 1.f), sel<float>(hl, pw1.y, 0.f)}); pw1 = cmul(pw0, l16); PIN2(pw0, pw1);
          }
          if constexpr (s == 5 && (u == 7 || u == 9)) {
#pragma unroll
            for (int q = 2 * u - 14; q < 2 * u - 10; ++q) { lbe[q] = (int)lbo[q] < 0 ? 0u : epc; PIN(lbe[q]); }
          }
          if constexpr (s == 6 && u < 8) {
            PIN(lb[u]); gd |= (lb[u][1] ^ lbe[u]) | (lb[u][3] ^ lbe[u]); cr = cfma(l128, cr, (f32x2){u2f(lb[u][0]), u2f(lb[u][2])});
            PIN2(cr, gd);
          }
          if constexpr (s == 6 && u == 8) {
            if (__builtin_expect(anyl(gd != 0), 0)) {
              for (unsigned sp = 0; sp < SPIN_CAP; ++sp) {
                __builtin_amdgcn_s_sleep(2);
#pragma unroll
                for (int q = 0; q < 8; ++q) ld128a(lb[q], rgr, lbo[q], 0u);
                waitv<0>();
                if (!anyl(bad())) break;
              }
              cr = (f32x2){0, 0};
#pragma unroll
              for (int q = 0; q < 8; ++q) cr = cfma(l128, cr, (f32x2){u2f(lb[q][0]), u2f(lb[q][2])});
            }
            gd = 0;
            const auto sx_ = __builtin_amdgcn_permlane32_swap(f2u(cr.x), f2u(cr.x), false, false), sy_ = __builtin_amdgcn_permlane32_swap(f2u(cr.y), f2u(cr.y), false, false);
            cr = cfma(l1k, (f32x2){u2f(sx_[1]), u2f(sy_[1])}, (f32x2){u2f(sx_[0]), u2f(sy_[0])});
            PIN(cr);
          }
          if constexpr (s == 6 && u == 9) { st[0] = cmulc(cfa, cfma(pw0, cr, Pf[0])); PIN(st[0]); }
          if constexpr (s == 7 && u == 0) { st[1] = cmulc(cfa, cfma(pw1, cr, Pf[1]));
            const auto ex = __builtin_amdgcn_permlane16_swap(f2u(st[0].x), f2u(st[1].x), false, false), ey = __builtin_amdgcn_permlane16_swap(f2u(st[0].y), f2u(st[1].y), false, false);
            st[0] = (f32x2){u2f(ex[0]), u2f(ey[0])}; st[1] = (f32x2){u2f(ex[1]), u2f(ey[1])}; PIN2(st[0], st[1]); }
          if constexpr ((s == 7 && u >= 2 && u < 10) || (s == 8 && u >= 1 && u < 9)) {
            constexpr int t = s == 7 ? u - 2 : 7 + u;
            if constexpr (t & 1) { so[0] = st[0]; so[1] = st[1]; }
#pragma unroll
            for (int mt = 0; mt < 2; ++mt) st[mt] = cfma((f32x2){lam[mt].x, -lam[mt].y}, st[mt], (f32x2){acc[t >> 2][mt][t & 3], acc[t >> 2][2 + mt][t & 3]});
            if constexpr (t & 1) {
#pragma unroll
              for (int mt = 0; mt < 2; ++mt) { obs[t >> 3][mt][0][(t >> 1) & 3] = pk2(so[mt].x, st[mt].x); obs[t >> 3][mt][1][(t >> 1) & 3] = pk2(so[mt].y, st[mt].y); PIN2(obs[t >> 3][mt][0][(t >> 1) & 3], obs[t >> 3][mt][1][(t >> 1) & 3]); }
            }
            PIN2(st[0], st[1]);
          }
          if constexpr ((s == 7 || s == 8) && u == 10) {
#pragma unroll
            for (int q = 0; q < 4; ++q) st128(rws, OB(s - 7, q >> 1, q & 1), hofs(s - 7, q >> 1), H_OFF + ci * H_PANEL + (2 * cj + (q & 1)) * 16384);
          }
          if constexpr (s >= 13 && u < 4) *(LAS u32x4*)(size_t)(98304 + (s - 13) * 16384 + hofs(u >> 1, u & 1)) = OB(u >> 1, u & 1, s - 13);
          if constexpr (s == 9 && u == 0) { if (ci == 15 && w == 1) stflag(rws, epc, lane, EPC_OFF + cj * 128); }
          if constexpr (s == 14 && u == 5) {
            auto rbad = [&]() AI { PIN2(rgv[0], rgv[1]); return ((rgv[0][1] ^ epr) | (rgv[0][3] ^ epr) | (rgv[1][1] ^ epr) | (rgv[1][3] ^ epr)) != 0; };
            if (__builtin_expect(anyl(rbad()), 0))
              for (unsigned sp = 0; sp < SPIN_CAP; ++sp) { __builtin_amdgcn_s_sleep(2);
                ld128a(rgv[0], rws, tq * 16u, rgb); ld128a(rgv[1], rws, tq * 16u + 4096, rgb);
                waitv<0>(); if (!anyl(rbad())) break; }
          }
          if constexpr (s == 14 && u == 7) {
            wrf(T_ROW + (tq & 1) * 1024 + (tq >> 1) * 4, u2f(rgv[0][0])); wrf(T_ROW + (tq & 1) * 1024 + 512 + (tq >> 1) * 4, u2f(rgv[0][2]));
            wrf2(T_XA + tq * 8, (f32x2){u2f(rgv[1][0]), u2f(rgv[1][2])});
          }
          if constexpr (s == 11 && u == 10) {
#pragma unroll
            for (int q = 0; q < 8; ++q) ld128a(cg[q], rws, cgo, CG_OFF + (cj * 16 + q) * 2048);
            ld128a(rgv[0], rws, tq * 16u, rgb); ld128a(rgv[1], rws, tq * 16u + 4096, rgb);
          }
          if constexpr (s == 14 && u < 4) {
#pragma unroll
            for (int q = 2 * u; q < 2 * u + 2; ++q) { PIN(cg[q]); gd |= (cg[q][1] ^ epc) | (cg[q][3] ^ epc); c0s += u2f(cg[q][0]); c1s += u2f(cg[q][2]); }
            PIN(gd); PIN(c0s); PIN(c1s);
          }
          if constexpr (s == 14 && u == 4) {
            if (__builtin_expect(anyl(gd != 0), 0)) {
              for (unsigned sp = 0; sp < SPIN_CAP; ++sp) { __builtin_amdgcn_s_sleep(2);
#pragma unroll
                for (int q = 0; q < 8; ++q) ld128a(cg[q], rws, cgo, CG_OFF + (cj * 16 + q) * 2048);
                waitv<0>(); if (!anyl(gbad())) break; }
              c0s = 0.f; c1s = 0.f;
#pragma unroll
              for (int q = 0; q < 8; ++q) { c0s += u2f(cg[q][0]); c1s += u2f(cg[q][2]); }
            }
            c0s = sum32(c0s); c1s = sum32(c1s); PIN(c0s); PIN(c1s);
          }
          if constexpr (s == 14 && u == 6) {
            const float v = sel<float>(hl, c1s, c0s), hi = rbf(v), bb = rdf(T_B1 + (2 * cp + hl) * 4);
            *(LAS u32x4*)(size_t)(T_WA + (2 * cp + hl) * 16) = (u32x4){pk2(hi, v - hi), pk2(hi, bb), pk2(bb, 0.f), 0u};
          }
        };
        if constexpr (s < 13) { sfor<11>([&](auto U) AI { G2(IC<s + 3>{}, U); SB(); work(U); SB(); }); if constexpr (s < 12) nexts(); }
        if constexpr (s == 12) {
#pragma unroll
          for (int q = 0; q < 4; ++q) vb[q] = va[q];
          ta = t3a; tb = t3b; sx = 0; next3(); }
        if constexpr (s >= 13) { sfor<8>([&](auto U) AI { G3(IC<s - 13>{}, IC<U.value + 4>{}); SB(); work(U); SB(); }); next3(); }
      });
      waitl0(); waitv<8>();
      if (w == 0) check_flags(pv, rws, fvo, flagb + FK_H * 128, lane, 4 * kq, 4 * kq + 4, epr);
      bar();
    }
    f32x4 acc[4][8];
    bf16x8 fs[8][2], fr[2][4][2];
    const int hf = p >> 1, fw = 2 * (g & 1) + (q2 >> 1);
    const unsigned hb = 98304 + (g >> 1) * 4096 + wm * 2048 + hf * 1024 + (8 * (g & 1) + q2) * 16 + (p & 1) * 8, wb = wn * 16384 + (4 * g + q2) * 128 + p * 8;
    unsigned b3[4] = {hb + 64u * hf, hb + 64u * (1 ^ hf), 32768 + wn * 16384u + r15 * 128 + ((g ^ (r15 >> 1)) * 16), 32768 + wn * 16384u + r15 * 128 + (((4 + g) ^ (r15 >> 1)) * 16)};
    unsigned b4[5];
    unsigned a3 = 1, dA, dW;
    auto rd3 = [&](int X, int r) AI {
      if (r >= 2 && r < 18) { const int a = (r - 2) >> 2, ks = ((r - 2) >> 1) & 1, h = r & 1; sethalf(fr[X][a][ks], h, rtr(b3[h] + ks * 8192 + a * 256)); }
      else { const int b = r < 2 ? 0 : (r - 16) >> 1, ks = r & 1; fs[b][ks] = rd128(b3[2 + ks] + b * 2048); } };
    auto rd4 = [&](int X, int r) AI {
      if (r >= 4 && r < 12) fr[X][(r - 4) >> 1][r & 1] = rd128(b4[0] + (r & 1) * 8192 + ((r - 4) >> 1) * 1024);
      else { const int b = r < 4 ? 0 : (r - 8) >> 2, ks = (r >> 1) & 1, h = r & 1; sethalf(fs[b][ks], h, rtr(b4[1 + (b & 3)] + (b >> 2) * 8192 + ks * 4096 + h * 2048)); } };
    auto mm = [&](int X, int q, int SL) AI { const int b = q >> 3, a = (q >> 1) & 3, ks = q & 1;
      if (SL < 0) acc[a][b] = MFMA(fs[b][ks], fr[X][a][ks], acc[a][b]); else MMA(acc[a][b], fs[b][ks], fr[X][a][ks]); };
    auto mmz = [&](int X, int q, int SL) AI { if (X == 0 && !(q & 1)) MMZ(acc[(q >> 1) & 3][q >> 3], fs[q >> 3][0], fr[0][(q >> 1) & 3][0]); else mm(X, q, SL); };
    auto adv = [&]() AI { dA = a3 == 0 ? -32768 : 16384; dW = a3 == 2 ? -65536 : 32768; a3 = a3 == 2 ? 0 : a3 + 1; PINS(dA); PINS(dW); PINS(a3); };
    auto up3 = [&](int b) AI { if (b == 0) adv(); else { b3[b - 1] += sel<unsigned>(b < 3, dA, dW); PIN(b3[b - 1]); } };
    auto up4 = [&](int b) AI { if (b == 0) adv(); else { b4[b - 1] += sel<unsigned>(b < 2, dA, dW); PIN(b4[b - 1]); } };
    constexpr int L3 = 18;
#pragma unroll
    for (int b = 0; b < 4; ++b) PIN(b3[b]);
    period<64, L3, 0, true, 32, 44, 5>(rd3, mmz, up3); waitv<0>(); bar();
    period<64, L3, 1, false, 32, 44, 5>(rd3, mmz, up3); bar();
    for (int s = 0; s < 3; ++s) { period<64, L3, 0, false, 32, 44, 5>(rd3, mm, up3); bar(); period<64, L3, 1, false, 32, 44, 5>(rd3, mm, up3);
      if (s == 2) break;
      bar(); }
    float gr[4];
#pragma unroll
    for (int a = 0; a < 4; ++a) gr[a] = rdf(T_ROW + 1536 + (64 * wm + 16 * a + r15) * 4);
    b4[0] = 131072 + wm * 4096 + lane * 16;
#pragma unroll
    for (int b = 0; b < 4; ++b) b4[1 + b] = wb + ((b ^ fw) * 32);
#pragma unroll
    for (int b = 0; b < 5; ++b) PIN(b4[b]);
    waitl0(); bar();
    {
      auto mmg = [&](int X, int q, int SL) AI { mm(X, q, SL); const int a = (SL >> 1) & 3, b = SL >> 3, e = 2 * (SL & 1); SB();
        acc[a][b][e] *= gr[a]; acc[a][b][e + 1] *= gr[a]; PIN(acc[a][b]); };
      period<64, L3, 0, false, 40, 56, 6>(rd4, mmg, up4); bar();
    }
    for (int s = 0; s < 6; ++s) { period<64, L3, 1, false, 40, 56, 6>(rd4, mm, up4); bar(); period<64, L3, 0, false, 40, 56, 6>(rd4, mm, up4); bar(); }
    const unsigned po = wq * 4096 + g * 256 + ((r15 ^ (8 * wn + 2 * g)) * 16);
    auto send = [&](int qd, int a) AI {
      st128(rws, (u32x4){pk2(acc[a][2 * qd][0], acc[a][2 * qd][1]), pk2(acc[a][2 * qd][2], acc[a][2 * qd][3]), pk2(acc[a][2 * qd + 1][0], acc[a][2 * qd + 1][1]), pk2(acc[a][2 * qd + 1][2], acc[a][2 * qd + 1][3])},
            po + a * 1024, PART_OFF + (ci * 16 + (qd ^ r2) * 4 + ntp) * 49152 + qd * 16384); };
    {
      auto mms = [&](int X, int q, int SL) AI { mm(X, q, SL); const int d = SL - L3 - 20;
        if (d >= 0 && (d & 1) == 1 && (d & 15) < 8) { SB(); send(d >> 4, (d & 15) >> 1); } };
      period<64, L3, 1, false, 40, 56, 0>(rd4, mms, up4); bar();
    }
    MPAD();
    auto sig = [&](int qd) AI { stflag(rws, epr, lane, PF_OFF + (ci * 16 + (qd ^ r2) * 4 + ntp) * 256 + wq * 64 + qd * 4); };
    sfor<11>([&](auto M) AI { constexpr int m = M.value;
      if constexpr (m < 9) { mm(1, 46 + 2 * m, -1); mm(1, 47 + 2 * m, -1); }
      SB();
      if constexpr (m < 4) send(2, m);
      if constexpr (m >= 7) {
#pragma unroll
        for (int q = 2 * (m - 7); q < 2 * (m - 7) + 2; ++q) { const int a = q & 3, bb = q >> 2;
          *(LAS f32x4*)(size_t)(65536 + wq * 8192 + (a * 2 + bb) * 1024 + g * 256 + ((r15 ^ (8 * wn + 4 * bb + g)) * 16)) = acc[a][6 + bb]; }
      }
      if constexpr (m == 5) { waitv<8>(); sig(0); }
      SB(); });
    waitv<5>(); sig(1);
    waitl0(); bar();
    waitv<2>(); sig(2);
    presum();
    bar();
  } else {
    constexpr bool isx = !PROFX; (void)isx;
    unsigned epr, pv, t3a, t3b, cb[8], ba[8]; const char *pp0, *pp1;
    f32x4 ac1[28];
    {
      u32x4 xr[2][2], gq[2];
      ra = rx; rb = mk(sel<const u16*>(wq & 1, bim, bre), 1024 * 2048);
#pragma unroll
      for (int q = 0; q < 4; ++q) { va[q] = (32 * (q & 1) + 16 * hl + 4 * (q >> 1) + ((lane >> 3) & 3)) * 2048 + (((lane & 7) ^ ((4 * q + g) & 7)) * 16) - 1024 * q; vb[q] = vnat(q); }
      auto src1 = [&](int s) AI { const int k = s ^ (s < 2 ? 0 : s < 4 ? (rot & 1) : rot); sa = (ci * 128 + 64 * (wq >> 1) + 8 * (wq & 1)) * 2048 + k * 128; sb = (cj * 64 + 32 * (wq >> 1)) * 2048 + k * 128; sl = ((s + 3) & 3) * 32768; };
      src1(0); ival(G, nowork);
      SB();
      setup2(); table3(t3a, t3b, tc);
      {
        const int f = ((q2 >> 1) & 1) | ((g & 1) << 1);
        const unsigned nb = wm * 8192u + r15 * 128, n0 = (g ^ (r15 >> 1)) * 16, n1 = ((4 + g) ^ (r15 >> 1)) * 16, wk = (g >> 1) * 7168u + p * 8;
        cb[0] = nb + wn * 4096 + n0; cb[1] = nb + wn * 4096 + n1; cb[2] = nb + (1 - wn) * 4096 + n0; cb[3] = nb + (1 - wn) * 4096 + n1;
        cb[4] = wk + (8 * (g & 1) + q2) * 128 + (((2 * wn) ^ f) * 32); cb[5] = wk + (8 * (g & 1) + q2) * 128 + (((2 * wn + 1) ^ f) * 32);
        cb[6] = wk + 6144 + (8 * (g & 1) + q2) * 64 + (g & 1) * 32; cb[7] = wk + 6144 + (8 * (g & 1) + q2) * 64 + ((g & 1) ^ 1) * 32;
#pragma unroll
        for (int b = 0; b < 8; ++b) { ba[b] = cb[b] + (b < 4 ? 131072u : 0u); PIN2(cb[b], ba[b]); }
        pp0 = lane < 32 ? (const char*)sel<const u16*>(lane & 16, cim, cre) + ((bid * 4 + wq) * 16 + r15) * 128 : (const char*)w2 + ((bid * 4 + wq) * 56 + r15) * 128;
        pp1 = (const char*)w2 + ((bid * 4 + wq) * 56 + (lane < 40 ? 16 + lane : 55)) * 128;
        PIN2(pp0, pp1);
#pragma unroll
        for (int m = 0; m < 28; ++m) { ac1[m] = (f32x4){0, 0, 0, 0}; PIN(ac1[m]); }
      }
      src1(1); ival(G, nowork);
      waitv<8>(); bar();
      unsigned pdt, plr, pli, bq, oq; f32x2 lp; float lr, li;
      const int tc = tq < 224 ? tq : 223;
      ld32a(epr, rws, 0u, flagb + FK_EP * 128); ld128g(gq[0], gw, lane * 16u); ld128g(gq[1], gw + 512, lane * 16u);
      ld16p(pdt, ldt + 64 * cj, lane * 2u); ld16p(plr, lre + 64 * cj, lane * 2u); ld16p(pli, lim + 64 * cj, lane * 2u);
      ld16p(bq, b1, (224 * cj + (tc < 192 ? ((cj & 1) ? 32 : 0) + tc : ((cj & 1) ? -192 : 0) + tc)) * 2u);
      ld16p(oq, sel<const u16*>(wq & 1, dsk, b2), (ntp * 256 + hl * 128 + kq * 32 + r31) * 2u);
      PH(1);
      src1(2); ival(G, nowork);
      src1(3); ival(G, nowork);
      float sv[6] = {0, 0, 0, 0, 0, 0}, a1, a2, a3, mean, sd, rstd, qv, gg, kq, rr;
      auto E1 = [](int s) constexpr { return (s == 1 || s == 2) ? 2 : s == 11 ? 1 : 0; };
      sfor<12>([&](auto S) AI { constexpr int s = S.value;
        if constexpr (s == 0) waitv<16 + 8>(); else waitv<16 + E1(s - 2) + E1(s - 1)>();
        bar();
        src1(s + 4); ival(G, [&](auto U) AI { constexpr int u = U.value;
          if constexpr ((s == 1 || s == 2) && (u == 2 || u == 5)) ld128p(xr[s - 1][u / 3], rx, lane * 16u, (ci * 128 + 8 * cj + 2 * wq + s - 1) * 2048 + (u / 3) * 1024);
          if constexpr (s == 1 && u == 0) { PIN2(pdt, plr); PIN(pli); lr = bf2f(plr); li = bf2f(pli); const float dt = rbf(fexp(bf2f(pdt))); lp = (f32x2){fexp(dt * lr), dt * li}; PIN(lp); }
          if constexpr (s == 1 && u == 3) { PIN2(bq, oq); wrf(T_B1 + tq * 4, bf2f(bq)); wrf(T_OUT + tq * 4, bf2f(oq)); PIN(epr); epr = __builtin_amdgcn_readfirstlane(epr) + 1u; }
          if constexpr (s == 1 && u == 6) { kq = __builtin_rintf(lp.y * 0.63661977236f);
            rr = __builtin_fmaf(kq, -1.5707962513f, lp.y); rr = __builtin_fmaf(kq, -7.5497894159e-08f, rr); rr = __builtin_fmaf(kq, -5.3903029534e-15f, rr); PIN2(kq, rr); }
          if constexpr (s == 2 && u == 0) { const float z = rr * rr;
            a1 = __builtin_fmaf(rr * z, __builtin_fmaf(z, __builtin_fmaf(z, __builtin_fmaf(z, 2.7183114939e-06f, -1.9839334836e-04f), 8.3333293858e-03f), -1.6666666642e-01f), rr); PIN(a1); }
          if constexpr (s == 2 && u == 3) { const float z = rr * rr;
            a2 = __builtin_fmaf(z, __builtin_fmaf(z, __builtin_fmaf(z, __builtin_fmaf(z, 2.4390448796e-05f, -1.3886763775e-03f), 4.1666623324e-02f), -4.9999999726e-01f), 1.f); PIN(a2); }
          if constexpr (s == 2 && u == 6) { const int q = (int)kq; const float sa_ = sel<float>(q & 1, a2, a1), ca_ = sel<float>(q & 1, a1, a2);
            lp = (f32x2){lp.x * (((q + 1) & 2) ? -ca_ : ca_), lp.x * ((q & 2) ? -sa_ : sa_)}; PIN(lp); wrf2(T_CON + lane * 8, lp); }
          if constexpr (s == 3 && u == 0) { const float nr = lp.x - 1.f, ni = lp.y, den = 2.f * __builtin_amdgcn_rcpf(lr * lr + li * li);
                                            wrf2(T_CON + 512 + lane * 8, (f32x2){(nr * lr + ni * li) * den, (ni * lr - nr * li) * den}); }
          if constexpr ((s == 3 && u >= 2) || (s == 4 && u >= 4)) { constexpr int q = s == 3 ? u - 2 : u + 2; lp = cmul(lp, lp); PIN(lp);
            if constexpr (q >= 3 && q < 7) wrf2(T_CON + (q - 1) * 512 + lane * 8, lp);
            if constexpr (q == 9) wrf2(T_CON + 3072 + lane * 8, lp); }
          if constexpr ((s == 4 && u < 2) || (s >= 5 && s < 9 && !(u & 1) && 4 * (s - 5) + 2 + (u >> 1) < 16)) {
            constexpr int id = s == 4 ? u : 4 * (s - 5) + 2 + (u >> 1), r = id >> 3, e = id & 7;
            PIN2(xr[r][e >> 2], gq[e >> 2]);
            const unsigned v = xr[r][e >> 2][e & 3], gv = gq[e >> 2][e & 3];
            const float x0 = bf2f(v & 0xffffu), x1 = u2f(v & 0xffff0000u);
            sv[r] += x0 + x1; sv[2 + r] = __builtin_fmaf(x1, x1, __builtin_fmaf(x0, x0, sv[2 + r]));
            sv[4 + r] = __builtin_fmaf(x1, u2f(gv & 0xffff0000u), __builtin_fmaf(x0, bf2f(gv & 0xffffu), sv[4 + r]));
            PIN2(sv[r], sv[2 + r]); PIN(sv[4 + r]);
          }
          if constexpr (s == 9 && u < 4) {
#pragma unroll
            for (int q = 0; q < 6; ++q) sv[q] = u == 0 ? dppadd<0xB1>(sv[q]) : u == 1 ? dppadd<0x4E>(sv[q]) : u == 2 ? dppadd<0x141>(sv[q]) : dppadd<0x140>(sv[q]);
            PIN2(sv[0], sv[1]); PIN2(sv[2], sv[3]); PIN2(sv[4], sv[5]);
          }
          if constexpr (s == 9 && u == 5) { const bool r1 = lane & 4; a1 = sum16(sel<float>(r1, sv[1], sv[0])); a2 = sum16(sel<float>(r1, sv[3], sv[2])); a3 = sum16(sel<float>(r1, sv[5], sv[4])); PIN2(a1, a2); PIN(a3); }
          if constexpr (s == 10 && u == 0) { a1 = sum32(a1); a2 = sum32(a2); a3 = sum32(a3); PIN2(a1, a2); PIN(a3); }
          if constexpr (s == 10 && u == 2) { mean = a1 * (1.f / 1024.f); const float var = __builtin_fmaxf(a2 * (1.f / 1024.f) - mean * mean, 0.f) + 1e-5f; rstd = __builtin_amdgcn_rsqf(var); sd = var * rstd;
                                             PIN2(mean, rstd); PIN(sd); }
          if constexpr (s == 10 && u == 4) { qv = 1.f + fexp(__builtin_fminf(a3, 60.f)); gg = __builtin_amdgcn_rcpf(1.f + fexp(__builtin_fminf(-a3, 80.f))); PIN2(qv, gg); }
          if constexpr (s == 11 && u == 0) { a1 = sel4(lane & 1, lane & 2, -2.302208198f * rstd, -0.1029432397f * rstd * rstd * rstd, qv * sd, gg); PIN(a1); }
          if constexpr (s == 11 && u == 2) { const float mh = rbf(mean), sh = rbf(sd);
                                             a2 = sel4(lane & 1, lane & 2, u2f(pk2(-mh, -mh)), u2f(pk2(mh - mean, sh)), u2f(pk2(sd - sh, 0.f)), 0.f); PIN(a2); }
          if constexpr (s == 11 && u == 5) { if (lane < 16) st64(rws, (u32x2){f2u(sel<float>(lane & 8, a2, a1)), epr}, (lane >> 3) * 4096u + (lane & 7) * 8, rgb + (8 * cj + 2 * wq) * 32); }
        });
      });
      rb = mk((const char*)w1 - 4096, 1024 * 7168 + 4096);
#pragma unroll
      for (int q = 0; q < 4; ++q) va[q] = vb[q];
      sx = 0; nexts();
      const unsigned sb0 = sb, sa0 = sa; nexts();
      const unsigned sb1 = sb, sa1 = sa; nexts();
      const unsigned sa2 = sa;
      waitv<16 + 1>(); bar();
      sa = sa0; sfor<4>([&](auto U) AI { G2(IC<0>{}, U); });
      sa = sa1; sfor<4>([&](auto U) AI { G2(IC<1>{}, U); });
      waitv<16 + 1>(); bar();
      sb = sb0; sfor<7>([&](auto U) AI { G2(IC<0>{}, IC<U.value + 4>{}); });
      sa = sa2; sfor<4>([&](auto U) AI { G2(IC<2>{}, U); });
      waitv<19>(); bar();
      sb = sb1; sfor<7>([&](auto U) AI { G2(IC<1>{}, IC<U.value + 4>{}); });
      ta = t3a; tb = t3b; sx = 2; next3();
    }
    {
      {
        bf16x8 fa[2][4][2], fb[8][2];
        unsigned xn = 98304, wnx = 32768;
        setm0(T_DUM);
        auto rd = [&](int X, int r) AI {
          if (r >= 4 && r < 12) { const int a = (r - 4) >> 1, ks = r & 1; fa[X][a][ks] = rd128(ba[(a >> 1) * 2 + ks] + (a & 1) * 2048); }
          else { const int b = r < 4 ? 0 : (r - 8) >> 2, ks = (r >> 1) & 1, h = r & 1;
            sethalf(fb[b][ks], h, rtr(b < 6 ? ba[4 + (b & 1)] + (b >> 1) * 2048 + ks * 14336 + h * 512 : ba[b] + ks * 14336 + h * 256)); } };
        auto mm = [&](int X, int q, int) AI { const int b = q < 48 ? q >> 3 : 6 + ((q - 48) >> 2), a = (q >> 1) & (q < 48 ? 3 : 1), ks = q & 1, m = (a < 2 ? 8 * a : 4 + 6 * a) + b;
          MMA(ac1[m], fb[b][ks], fa[X][a][ks]); };
        auto up = [&](int b) AI { if (b < 8) { ba[b] = cb[b] + sel<unsigned>(b < 4, xn, wnx); PIN(ba[b]); }
                                  else if (b == 8) { xn = (xn == 131072u) ? 98304u : xn + 16384u; PINS(xn); }
                                  else if (b == 10) { wnx = (wnx == 65536u) ? 0u : wnx + 32768u; PINS(wnx); } };
        waitl0(); waitv<11>(); bar();
        PH(2);
        period<56, 16, 0, true, 40, 48, 12>(rd, mm, up); waitv<0>(); bar();
        const int sp = BW0 + (loc + xcd) % BWN;
        for (int s = 0; s < 14; s += 2) {
          period<56, 16, 1, false, 40, 48, 12>(rd, mm, up); bar();
          if (s == sp) { pfg(pp0); pfg(pp1); }
          period<56, 16, 0, false, 40, 48, 12>(rd, mm, up); bar();
          if (s + 1 == sp) { pfg(pp0); pfg(pp1); }
        }
        period<56, 16, 1, false, 40, 48, 12>(rd, mm, up); bar();
        PH(3);
        va[0] = lane * 16;
#pragma unroll
        for (int q = 0; q < 4; ++q) vb[q] = vnat(q);
#pragma unroll
        for (int a = 0; a < 4; ++a) fa[0][a][0] = rd128(g ? T_ZERO : T_XA + (64 * wm + 16 * (a ^ (2 * wn)) + r15) * 16);
        sfor<12>([&](auto U) AI { constexpr int u = U.value; G3(IC<2>{}, U); SB();
          if constexpr (u < 4) {
#pragma unroll
            for (int q = 40 + 4 * u; q < 44 + 4 * u; ++q) mm(1, q, 0);
          }
          if constexpr (u == 0 || u == 4) {
#pragma unroll
            for (int b = u ? 5 : 0; b < (u ? 8 : 5); ++b) fb[b][0] = rd128(T_WA + ((b < 6 ? (b >> 1) * 64 + wn * 32 + (b & 1) * 16 : 96 + b * 16) + r15) * 16);
          }
          if constexpr (u >= 5) {
#pragma unroll
            for (int q = 8 * (u - 5); q < 8 * (u - 4); q += 2) mm(0, q, 0);
          }
          SB(); });
        next3();
      }
      float k0[4], k1[4], qq[4];
#pragma unroll
      for (int a = 0; a < 4; ++a) { const unsigned ro = T_ROW + (64 * wm + 16 * (a ^ (2 * wn)) + r15) * 4; k0[a] = rdf(ro); k1[a] = rdf(ro + 512); qq[a] = rdf(ro + 1024); }
      u32x4 hw[14]; float t[8]; unsigned sa9 = 0;
      auto lv0 = [](int s) constexpr { return s < 0 ? 0 : s < 6 ? (64 * s + 2) / 3 : s < 10 ? 128 + 16 * (s - 6) : s < 13 ? 192 + (32 * (s - 10) + 2) / 3 : 224; };
      auto st3 = [](int s) constexpr { return (s == 2 || s == 5 || s == 9) ? 4 : s == 12 ? 2 : 0; };
      auto n3 = [](int s) constexpr { return (s == 5 || s == 6 || s == 9 || s == 13) ? 8 : s == 7 ? 16 : s == 19 ? 4 : s > 19 ? 0 : 12; };
      sfor<22>([&](auto S) AI { constexpr int s = S.value;
        if constexpr (s == 6 || s == 10 || s == 14) waitl0();
        if constexpr (s == 8) waitv<12>();
        else if constexpr (s >= 1) waitv<n3(s - 1) + st3(s - 1)>();
        if constexpr (s == 7 || s == 10 || s == 14 || s == 17) {
          if (w == 4) check_flags(pv, rws, fvo, flagb + (FK_M1 + (s - 7) / 3) * 128, lane, 4 * kq, 4 * kq + 4, epr); }
        SB(); bar();
        if constexpr (s == 8) { PH(4); }
        if constexpr (st3(s - 2) > 0) { if (w == 5) stflag(rws, epr, lane, flagb + (FK_M1 + (s - 4) / 3) * 128 + cj * 4); }
        if constexpr (s == 6 || s == 9 || s == 13 || s == 16) { if (w == 4) ld32a(pv, rws, fvo, flagb + (FK_M1 + (s - 6) / 3) * 128); }
        if constexpr (s == 7) { const unsigned keep = sa; sa = sa9; sfor<4>([&](auto U) AI { G3(IC<9>{}, U); }); sa = keep; }
#define UU(e) ac1[m0 + ((e) >> 2)][(e) & 3]
#define LVL(expr) do { _Pragma("unroll") for (int e = 4 * hh; e < 4 * hh + 4; ++e) t[e] = (expr); PIN2(t[4 * hh], t[4 * hh + 1]); PIN2(t[4 * hh + 2], t[4 * hh + 3]); } while (0)
        auto hlev = [&](auto LL) AI { constexpr int L = LL.value, v = L >> 4, l = (L >> 1) & 7, hh = L & 1, a = v & 3, m0 = (a < 2 ? 8 * a : 4 + 6 * a) + 2 * (v >> 2);
          if constexpr (l == 0) LVL(UU(e) * UU(e));
          if constexpr (l == 1) LVL(__builtin_fmaf(t[e], k1[a], k0[a]));
          if constexpr (l == 2) LVL(UU(e) * t[e]);
          if constexpr (l == 3) LVL(__builtin_amdgcn_exp2f(t[e]));
          if constexpr (l == 4) LVL(__builtin_fmaf(t[e], qq[a], qq[a]));
          if constexpr (l == 5) LVL(__builtin_amdgcn_rcpf(t[e]));
          if constexpr (l == 6) LVL(UU(e) * t[e]);
          if constexpr (l == 7) { hw[v][2 * hh] = pk2(t[4 * hh], t[4 * hh + 1]); hw[v][2 * hh + 1] = pk2(t[4 * hh + 2], t[4 * hh + 3]); if constexpr (hh) PIN(hw[v]); else PIN2(hw[v][0], hw[v][1]); }
        };
        if constexpr (s < 19) sfor<12>([&](auto U) AI { constexpr int u = U.value, n = lv0(s + 1) - lv0(s), i0 = (n * u + 11) / 12, i1 = (n * (u + 1) + 11) / 12;
          if constexpr (n3(s) != 8) G3(IC<s + 3>{}, U);
          else if constexpr (u < 8) G3(IC<s + 3>{}, IC<u + 4>{});
          else if constexpr (s != 6)
            *(LAS u32x4*)(size_t)(((s + 1) % 3 == 0 ? 131072u : 81920u + ((s + 1) % 3) * 16384u) + wn * 8192 + (4 * wm + ((u - 8) ^ (2 * wn))) * 1024 + lane * 16) = hw[s - 13 + u];
          SB();
          if constexpr (i0 < i1) hlev(IC<lv0(s) + i0>{});
          if constexpr (i0 + 1 < i1) hlev(IC<lv0(s) + i0 + 1>{});
          SB(); });
        if constexpr (s == 19) {
          setm0(114688 + wq * 4096);
          sfor<4>([&](auto U) AI { constexpr int u = U.value;
            dma<u * 1024>(rx, (8u * u + (lane >> 3)) * 2048 + ((lane >> 2) & 1) * 256 + (lane & 3) * 16 - u * 1024, (ci * 128 + 32 * wq) * 2048 + ntp * 512 + kq * 64); });
        }
        constexpr int gr0 = s == 2 ? 0 : s == 5 ? 1 : s == 9 ? 2 : 3;
#pragma unroll
        for (int q = 0; q < st3(s); ++q) st128(rws, hw[4 * gr0 + q], lane * 16u + (4 * wm + (q ^ (2 * wn))) * 1024, ci * HID_PANEL + kb_of(cj, gr0 < 3 ? 2 * gr0 + wn : 6) * 8192);
        if constexpr (s == 6) sa9 = sa;
        if constexpr (s < 4) next3(); else if constexpr (s < 18) next4();
        if constexpr (s == 4) {
          rb = mk(w2, 3584 * 2048);
#pragma unroll
          for (int q = 0; q < 4; ++q) vb[q] = (8 * q + (lane >> 3)) * 2048 + (((((lane & 7) >> 1) ^ g ^ (2 * (r2 & 1))) * 2) + (lane & 1)) * 16 - 1024 * q;
        }
      });
    }
    PH(5);
    bar();
    presum();
    sfor<3>([&](auto Q) AI { constexpr int q = Q.value;
      if (q == 0 || anyl(lane == q && pv != epr)) {
#pragma unroll 1
        for (unsigned sp = 0; sp < SPIN_CAP; ++sp) {
          ld32a(pv, rws, fvo, PF_OFF + (ci * 16 + cj) * 256 + wq * 64); asm volatile("s_waitcnt vmcnt(0)" : "+v"(pv)::"memory");
          if (!anyl(lane == q && pv != epr)) break;
        }
      }
      setm0(wq * 4096 + q * 16384);
      sfor<4>([&](auto U) AI { dma1<U.value * 1024>(rws, va[0], PART_OFF + (ci * 16 + cj) * 49152 + q * 16384 + wq * 4096); });
    });
    if (cj == 0 && w == 4) stflag(rws, epr, lane, flagb + FK_EP * 128);
    waitv<0>(); bar();
  }
  {
    u32x2 pw[4][3];
#pragma unroll
    for (int u = 0; u < 4; ++u)
#pragma unroll
      for (int q = 0; q < 3; ++q) pw[u][q] = *(LAS u32x2*)(size_t)(fpp[u] + q * 16384);
    SB();
#pragma unroll
    for (int u = 0; u < 4; ++u) {
      f32x4 r;
#pragma unroll
      for (int e = 0; e < 4; ++e) r[e] = (fv[u][e] + unp(pw[u][0], e)) + (unp(pw[u][1], e) + unp(pw[u][2], e));
      st128<OAUX>(mk(out, 8u << 20), __builtin_bit_cast(u32x4, r), fo + u * 16384, osof);
    }
  }
}

extern "C" void kernel_launch(void* const* d_in, const int* in_sizes, int n_in, void* d_out, int out_size, void* d_ws, size_t ws_size, hipStream_t stream) {
  (void)in_sizes; (void)n_in; (void)out_size; (void)ws_size;
  (void)hipFuncSetAttribute((const void*)fused_kernel, hipFuncAttributeMaxDynamicSharedMemorySize, LDS_BYTES);
  fused_kernel<<<256, 512, LDS_BYTES, stream>>>((const u16*)d_in[0], (const u16*)d_in[1], (const u16*)d_in[2], (const u16*)d_in[3], (const u16*)d_in[4],
      (const u16*)d_in[5], (const u16*)d_in[6], (const u16*)d_in[7], (const u16*)d_in[8], (const u16*)d_in[10], (const u16*)d_in[11], (const u16*)d_in[12],
      (const u16*)d_in[13], (const u16*)d_in[14], (char*)d_ws, (float*)d_out);
}
```

```cpp
#include <hip/hip_runtime.h>
#include <utility>

typedef unsigned short u16;
typedef __bf16 bf16x2 __attribute__((ext_vector_type(2)));
typedef __bf16 bf16x4 __attribute__((ext_vector_type(4)));
typedef __bf16 bf16x8 __attribute__((ext_vector_type(8)));
typedef float f32x2 __attribute__((ext_vector_type(2)));
typedef float f32x4 __attribute__((ext_vector_type(4)));
typedef float f32x16 __attribute__((ext_vector_type(16)));
typedef unsigned u32x2 __attribute__((ext_vector_type(2)));
typedef unsigned u32x4 __attribute__((ext_vector_type(4)));
typedef __amdgpu_buffer_rsrc_t rsrc_t;
#define LAS __attribute__((address_space(3)))
#define DI __device__ __forceinline__
#define AI __attribute__((always_inline))
#define SB() __builtin_amdgcn_sched_barrier(0)
#define PIN(x) asm volatile("" : "+v"(x))
#define MFMA(a, b, c) __builtin_amdgcn_mfma_f32_16x16x32_bf16(a, b, c, 0, 0, 0)
#define MMA(c, a, b) asm volatile("v_mfma_f32_16x16x32_bf16 %0, %1, %2, %0" : "+v"(c) : "v"(a), "v"(b))
#define MMZ(c, a, b) asm volatile("v_mfma_f32_16x16x32_bf16 %0, %1, %2, 0" : "=&v"(c) : "v"(a), "v"(b))
#define MPAD() asm volatile("s_nop 7")
template <int V> using IC = std::integral_constant<int, V>;

constexpr unsigned HID_PANEL = 112 * 8192, H_OFF = 16 * HID_PANEL, H_PANEL = 32 * 16384;
constexpr unsigned PART_OFF = H_OFF + 16 * H_PANEL;
constexpr unsigned GRAN_OFF = PART_OFF + 256 * 49152;
constexpr unsigned RG_OFF = GRAN_OFF + 256 * 1024;
constexpr unsigned CG_OFF = RG_OFF + 16 * 8192;
constexpr unsigned FLAG_OFF = CG_OFF + 256 * 2048;
constexpr unsigned EPC_OFF = FLAG_OFF + 16 * 1024;
constexpr unsigned PF_OFF = EPC_OFF + 16 * 128;
constexpr unsigned WS_BYTES = PF_OFF + 256 * 256;
constexpr int FK_M1 = 0, FK_M2 = 1, FK_M3 = 2, FK_M4 = 3, FK_H = 4, FK_P = 5, FK_EP = 6;
constexpr unsigned TAB = 147456, T_ROW = TAB, T_XA = TAB + 2048, T_OUT = TAB + 4096, T_B1 = TAB + 5120, T_CS2 = TAB + 6144, T_WA = TAB + 8192, T_ZERO = TAB + 12288, T_AGG = T_WA, T_DUM = TAB + 12544, T_CON = TAB + 12800;
constexpr unsigned LDS_BYTES = 163840;
#define XMAP 1
#define ROTM 3
#define OAUX 2
#define SP0 4
#define SPN 6
#define PTE 0
#define BW0 0
#define BWN 8
#define CSL 22
#define PGT 0
#define MOVE 3
#define PROFX false
#define WD 8
constexpr int LASTR = 24;
constexpr unsigned SPIN_CAP = 1u << 16;

DI rsrc_t mk(const void* p, unsigned bytes) { return __builtin_amdgcn_make_buffer_rsrc((void*)p, 0, bytes, 0x00020000); }
DI void setm0(unsigned lds) { asm volatile("s_mov_b32 m0, %0\n\ts_nop 0" ::"s"(lds) : "memory"); }
template <int OFF> DI void dma(rsrc_t r, unsigned voff, unsigned soff) {
  asm volatile("buffer_load_dwordx4 %0, %1, %2 offen offset:%3 lds" ::"v"(voff), "s"(r), "s"(soff), "i"(OFF) : "memory");
}
template <int OFF> DI void dma1(rsrc_t r, unsigned voff, unsigned soff) {
  asm volatile("buffer_load_dwordx4 %0, %1, %2 offen offset:%3 sc1 lds" ::"v"(voff), "s"(r), "s"(soff), "i"(OFF) : "memory");
}
#define PIN2(x, y) asm volatile("" : "+v"(x), "+v"(y))
#define PINS(x) asm volatile("" : "+s"(x))
typedef const void* const __attribute__((address_space(4)))* kargs_t;
DI const char* karg(int i) { kargs_t ka = (kargs_t)__builtin_amdgcn_kernarg_segment_ptr(); PINS(ka); return (const char*)ka[i]; }
enum { A_X, A_LRE, A_LIM, A_BRE, A_BIM, A_CRE, A_CIM, A_LDT, A_DSK, A_W1, A_B1, A_W2, A_B2, A_GW, A_WS, A_OUT };
template <int N> DI void waitv() { asm volatile("s_waitcnt vmcnt(%0)" ::"i"(N) : "memory"); }
DI void waitl0() { SB(); __builtin_amdgcn_s_waitcnt(0xC07F); asm volatile("" ::: "memory"); }
#define PH(k)
DI void bar() { asm volatile("s_barrier" ::: "memory"); SB(); }
DI bf16x8 rd128(unsigned a) { return *(LAS bf16x8*)(size_t)a; }
DI u32x4 rdu4(unsigned a) { return *(LAS u32x4*)(size_t)a; }
DI f32x4 rdf4(unsigned a) { return *(LAS f32x4*)(size_t)a; }
DI f32x2 rdf2(unsigned a) { return *(LAS f32x2*)(size_t)a; }
DI float rdf(unsigned a) { return *(LAS float*)(size_t)a; }
DI void wrf(unsigned a, float v) { *(LAS float*)(size_t)a = v; }
DI void wrf2(unsigned a, f32x2 v) { *(LAS f32x2*)(size_t)a = v; }
DI bf16x4 rtr(unsigned a) { return __builtin_amdgcn_ds_read_tr16_b64_v4bf16((LAS bf16x4*)(size_t)a); }
DI void sethalf(bf16x8& f, int h, bf16x4 v) {
  if (h == 0) f = __builtin_shufflevector(v, v, 0, 1, 2, 3, -1, -1, -1, -1);
  else { bf16x8 t = __builtin_shufflevector(v, v, -1, -1, -1, -1, 0, 1, 2, 3); f = __builtin_shufflevector(f, t, 0, 1, 2, 3, 12, 13, 14, 15); }
}
DI unsigned f2u(float f) { return __builtin_bit_cast(unsigned, f); }
DI float u2f(unsigned u) { return __builtin_bit_cast(float, u); }
DI float shx(int lane, float v, int m) { return u2f(__builtin_amdgcn_ds_bpermute((lane ^ m) * 4, (int)f2u(v))); }
DI float sum16(float v) { const auto r = __builtin_amdgcn_permlane16_swap(f2u(v), f2u(v), false, false); return u2f(r[0]) + u2f(r[1]); }
DI float sum32(float v) { const auto r = __builtin_amdgcn_permlane32_swap(f2u(v), f2u(v), false, false); return u2f(r[0]) + u2f(r[1]); }
DI float fexp(float v) { return __builtin_amdgcn_exp2f(v * 1.4426950408889634f); }
DI float bf2f(unsigned v) { return u2f(v << 16); }
DI unsigned pk2(float lo, float hi) { f32x2 v = {lo, hi}; return __builtin_bit_cast(unsigned, __builtin_convertvector(v, bf16x2)); }
DI float rbf(float f) { return u2f(pk2(f, 0.f) << 16); }
DI float dot2(unsigned a, unsigned b, float c) { return __builtin_amdgcn_fdot2_f32_bf16(__builtin_bit_cast(bf16x2, a), __builtin_bit_cast(bf16x2, b), c, false); }
template <int C> DI float dppadd(float v) { return v + u2f(__builtin_amdgcn_update_dpp(0, (int)f2u(v), C, 0xf, 0xf, true)); }
DI float sel4(bool b0, bool b1, float a, float b, float c, float d) { PIN(a); PIN(b); PIN(c); PIN(d); const float lo = b0 ? b : a, hi = b0 ? d : c; return b1 ? hi : lo; }
DI f32x2 cmul(f32x2 a, f32x2 b) { return (f32x2){a.x * b.x - a.y * b.y, a.x * b.y + a.y * b.x}; }
DI f32x2 cmulc(f32x2 a, f32x2 b) { return (f32x2){__builtin_fmaf(a.x, b.x, -(a.y * b.y)), __builtin_fmaf(-a.x, b.y, -(a.y * b.x))}; }
DI f32x2 cfma(f32x2 a, f32x2 b, f32x2 c) { return (f32x2){__builtin_fmaf(a.x, b.x, __builtin_fmaf(-a.y, b.y, c.x)), __builtin_fmaf(a.x, b.y, __builtin_fmaf(a.y, b.x, c.y))}; }
template <int AUX = 16> DI void st128(rsrc_t r, u32x4 v, unsigned voff, unsigned soff) {
  __builtin_amdgcn_raw_buffer_store_b128(v, r, voff, soff, AUX);
  asm volatile("s_nop 1" ::"v"(v));
}
DI void st64(rsrc_t r, u32x2 v, unsigned voff, unsigned soff) { __builtin_amdgcn_raw_buffer_store_b64(v, r, voff, soff, 16); }
DI void st32(rsrc_t r, unsigned v, unsigned voff, unsigned soff) { __builtin_amdgcn_raw_buffer_store_b32(v, r, voff, soff, 16); }
DI void stflag(rsrc_t r, unsigned v, int lane, unsigned soff) { if (lane == 0) st32(r, v, 0u, soff); }
DI void ld32a(unsigned& d, rsrc_t r, unsigned voff, unsigned soff) { asm volatile("buffer_load_dword %0, %1, %2, %3 offen sc1" : "=v"(d) : "v"(voff), "s"(r), "s"(soff) : "memory"); }
DI void ld16p(unsigned& d, const u16* p, unsigned voff) { asm volatile("global_load_ushort %0, %1, %2" : "=v"(d) : "v"(voff), "s"(p) : "memory"); }
DI void ld64p(u32x2& d, rsrc_t r, unsigned voff, unsigned soff) { asm volatile("buffer_load_dwordx2 %0, %1, %2, %3 offen" : "=v"(d) : "v"(voff), "s"(r), "s"(soff) : "memory"); }
DI void ld64a(u32x2& d, rsrc_t r, unsigned voff, unsigned soff) { asm volatile("buffer_load_dwordx2 %0, %1, %2, %3 offen sc1" : "=v"(d) : "v"(voff), "s"(r), "s"(soff) : "memory"); }
DI void ld128g(u32x4& d, const u16* p, unsigned voff) { asm volatile("global_load_dwordx4 %0, %1, %2" : "=v"(d) : "v"(voff), "s"(p) : "memory"); }
DI void pfl(unsigned& d, rsrc_t r, unsigned voff, unsigned soff) { asm volatile("buffer_load_dword %0, %1, %2, %3 offen" : "+v"(d) : "v"(voff), "s"(r), "s"(soff) : "memory"); }
template <class T> DI T sel(bool c, T a, T b) { return c ? a : b; }
DI void pfg(const char* p) { asm volatile("global_load_lds_dword %0, off" ::"v"(p) : "memory"); }
DI bool anyl(bool b) { return __builtin_amdgcn_ballot_w64(b) != 0; }
DI void ld128p(u32x4& d, rsrc_t r, unsigned voff, unsigned soff) { asm volatile("buffer_load_dwordx4 %0, %1, %2, %3 offen" : "=v"(d) : "v"(voff), "s"(r), "s"(soff) : "memory"); }
DI void sincos_f(float t, float& sn, float& cs) {
  const float k = __builtin_rintf(t * 0.63661977236f);
  float r = __builtin_fmaf(k, -1.5707962513f, t); r = __builtin_fmaf(k, -7.5497894159e-08f, r); r = __builtin_fmaf(k, -5.3903029534e-15f, r);
  const float z = r * r;
  const float sp = __builtin_fmaf(r * z, __builtin_fmaf(z, __builtin_fmaf(z, __builtin_fmaf(z, 2.7183114939e-06f, -1.9839334836e-04f), 8.3333293858e-03f), -1.6666666642e-01f), r);
  const float cp = __builtin_fmaf(z, __builtin_fmaf(z, __builtin_fmaf(z, __builtin_fmaf(z, 2.4390448796e-05f, -1.3886763775e-03f), 4.1666623324e-02f), -4.9999999726e-01f), 1.f);
  const int q = (int)k;
  const float a = (q & 1) ? cp : sp, b = (q & 1) ? sp : cp;
  sn = (q & 2) ? -a : a; cs = ((q + 1) & 2) ? -b : b;
}
DI void ld128a(u32x4& d, rsrc_t r, unsigned voff, unsigned soff) { asm volatile("buffer_load_dwordx4 %0, %1, %2, %3 offen sc1" : "=v"(d) : "v"(voff), "s"(r), "s"(soff) : "memory"); }

DI unsigned x1off(int a) { return a == 0 ? 131072u : (unsigned)(81920 + a * 16384); }
DI unsigned nq(unsigned s) { return (s + 32768u) & 131071u; }

DI int kb_of(int j, int bl) { return 7 * j + ((j & 1) ? (bl == 6 ? 0 : bl + 1) : bl); }

template <int NR, int LAST> constexpr int nread(int sl) {
  constexpr int DENSE = 2 * NR - LAST - 2;
  return sl < 0 ? 0 : sl < DENSE ? sl + 1 : sl >= LAST ? NR : DENSE + (sl - DENSE) / 2 + 1;
}
template <int N, int L, int X, bool FIRST, int NR, int LAST, int NB, int SL, class RD, class MM, class UP>
DI void slot(RD& rd, MM& mm, UP& upd) {
  if constexpr (!FIRST && SL >= L && (SL == L + 4 || (SL - L) % 8 == 0)) {
    __builtin_amdgcn_s_waitcnt(0xC07F | ((nread<NR, LAST>(SL - 1) - nread<NR, LAST>(SL - WD)) << 8)); SB();
  }
  if constexpr (SL >= L) mm(X, SL - L, SL); else if constexpr (!FIRST) mm(X ^ 1, SL + N - L, SL);
  SB();
  if constexpr (nread<NR, LAST>(SL) > nread<NR, LAST>(SL - 1)) rd(X, nread<NR, LAST>(SL) - 1);
  constexpr int per = NB > N - 1 - LAST ? 2 : 1, u0 = (SL - LAST - 1) * per;
  if constexpr (SL > LAST && u0 < NB) { upd(u0); if constexpr (per == 2 && u0 + 1 < NB) upd(u0 + 1); }
  SB();
}
template <int N, int L, int X, bool FIRST, int NR, int LAST, int NB, class RD, class MM, class UP, int... I>
DI void period_(RD& rd, MM& mm, UP& upd, std::integer_sequence<int, I...>) { (slot<N, L, X, FIRST, NR, LAST, NB, I>(rd, mm, upd), ...); }
template <int N, int L, int X, bool FIRST, int NR, int LAST, int NB, class RD, class MM, class UP>
DI void period(RD& rd, MM& mm, UP& upd) {
  static_assert(NR - 1 <= LAST && !(LAST & 1) && NB <= 2 * (N - 1 - LAST), "");
  period_<N, L, X, FIRST, NR, LAST, NB>(rd, mm, upd, std::make_integer_sequence<int, N>{});
  waitl0();
}
template <class G, class W, int... U> DI void ival_(G& g, W& wk, std::integer_sequence<int, U...>) { ((g(IC<U>{}), SB(), wk(IC<U>{}), SB()), ...); }
template <class G, class W> DI void ival(G&& g, W&& wk) { ival_(g, wk, std::make_integer_sequence<int, 8>{}); }
template <int N, class F, int... I> DI void sfor_(F& f, std::integer_sequence<int, I...>) { (f(IC<I>{}), ...); }
template <int N, class F> DI void sfor(F&& f) { sfor_<N>(f, std::make_integer_sequence<int, N>{}); }

DI void check_flags(unsigned& pv, rsrc_t ws, unsigned voff, unsigned soff, int lane, int lo, int hi, unsigned ep) {
  PIN(pv);
  const bool need = lane >= lo && lane < hi;
  if (__builtin_expect(anyl(need && pv != ep), 0)) {
    for (unsigned sp = 0; sp < SPIN_CAP; ++sp) {
      __builtin_amdgcn_s_sleep(2);
      ld32a(pv, ws, voff, soff); asm volatile("s_waitcnt vmcnt(0)" : "+v"(pv)::"memory");
      if (!anyl(need && pv != ep)) break;
    }
  }
}

#if defined(__HIP_DEVICE_COMPILE__) && __HIP_DEVICE_COMPILE__
__attribute__((target("no-packed-fp32-ops")))
#endif
__global__ __launch_bounds__(512, 2) void fused_kernel(const u16* x, const u16* lre, const u16* lim, const u16* bre, const u16* bim, const u16* cre, const u16* cim,
    const u16* ldt, const u16* dsk, const u16* w1, const u16* b1, const u16* w2, const u16* b2, const u16* gw, char* ws, float* out) {
  extern __shared__ __attribute__((aligned(16))) char shm[];
  unsigned long long pc0; asm volatile("s_getpc_b64 %0" : "=s"(pc0));
  const int lane = threadIdx.x & 63, w = __builtin_amdgcn_readfirstlane(threadIdx.x >> 6);
  const int bid = blockIdx.x, xcd = bid & 7, loc = bid >> 3;
  const int ci = (xcd & 3) * 4 + (loc & 3), cj = (loc >> 4) * 8 + (xcd >> 2) * 4 + ((loc >> 2) & 3);
  const int rot = (ci + cj) & ROTM, ntp = cj & 3, kq = cj >> 2, r2 = kq ^ 3, wq = w & 3, tq = threadIdx.x & 255;
  const rsrc_t rws = mk(ws, WS_BYTES), rx = mk(x, 2048 * 2048);
  const int wm = (w >> 1) & 1, wn = w & 1, g = lane >> 4, r15 = lane & 15, q2 = (lane >> 2) & 3, p = lane & 3, hl = lane >> 5, r31 = lane & 31;
  const unsigned flagb = FLAG_OFF + ci * 1024, fvo = (lane & 15) * 4;
  unsigned rgb = RG_OFF + ci * 8192, osof = ci * 128 * 4096 + ntp * 1024 + kq * 128; PINS(rgb); PINS(osof);
  auto nowork = [&](auto) AI {};
  unsigned va[4], vb[4], vw[7], sl, sa, sb, sc, ta, tb, tc; int sx; rsrc_t ra, rb;
  auto vnat = [&](int u) AI { return (8u * u + (lane >> 3)) * 2048 + (((lane & 7) ^ ((4 * u + g) & 7)) * 16) - 1024 * u; };
  auto nexts = [&]() AI { sa = __builtin_amdgcn_readlane(ta, sx); sb = __builtin_amdgcn_readlane(tb, sx); ++sx; PINS(sa); PINS(sb); };
  auto G = [&](auto U) AI { constexpr int u = U.value;
    if constexpr (u == 0) setm0(sl + wq * 4096);
    if constexpr (u == 4) setm0(sl + 16384 + wq * 4096);
    if constexpr (u < 4) dma1<u * 1024>(ra, va[u], sa); else dma<(u - 4) * 1024>(rb, vb[u - 4], sb);
  };
  auto G2 = [&](auto S, auto U) AI { constexpr int a = S.value % 3, u = U.value;
    if constexpr (u == 0) setm0((a == 0 ? 131072u : 81920u + a * 16384u) + wq * 4096);
    if constexpr (u == 4) setm0(a * 32768 + wq * 7168);
    if constexpr (u == 8) setm0(a * 32768 + wq * 7168 + 4096);
    if constexpr (u < 4) dma<u * 1024>(rx, va[u], sa); else dma<(u & 3) * 1024>(rb, vw[u - 4], sb);
  };
  const int c7 = ci & 7;
  auto setup2 = [&]() AI {
#pragma unroll
    for (int q = 0; q < 6; ++q) { const int k = 8 * (q & 1) + (lane >> 3), f = ((k >> 1) & 1) | ((q & 1) << 1);
      vw[q] = 4096 + k * 7168 + ((cj & 1) ? 64 : 0) + (q >> 1) * 128 + (((((lane & 7) >> 1) ^ f) * 2) + (lane & 1)) * 16 - (q & 3) * 1024; }
    vw[6] = 4096 + (lane >> 2) * 7168 + ((cj & 1) ? 0 : 384) + (((((lane & 3) >> 1) ^ hl) * 2) + (lane & 1)) * 16 - 2048;
    int k = lane - 2 + (c7 < 2 ? 3 * c7 : c7 - 2); k = 2 + (k >= 6 ? k - 6 : k);
    k = lane < 2 ? lane : lane < 8 ? k : (lane ^ rot);
    k = (k ^ (ci & 8)) & 15;
    ta = (ci * 128 + 32 * wq) * 2048 + k * 128; tb = (k * 64 + 16 * wq) * 7168 + cj * 448;
    PIN2(ta, tb);
#pragma unroll
    for (int q = 0; q < 7; ++q) PIN(vw[q]);
  };
  auto G3 = [&](auto S, auto U) AI { constexpr int a = (S.value + 1) % 3, u = U.value;
    if constexpr (u == 0) setm0((a == 0 ? 131072u : 81920u + a * 16384u) + wq * 4096);
    if constexpr (u == 4 || u == 8) setm0(a * 32768 + wq * 8192 + (u - 4) * 1024);
    if constexpr (u < 4) dma1<u * 1024>(rws, va[0], sa); else dma<(u & 3) * 1024>(rb, vb[u & 3], u < 8 ? sb : sc);
  };
  auto table3 = [&](unsigned& ta, unsigned& tb, unsigned& tc) AI {
    const int ks = 8 * kq + ((lane ^ (2 * ntp)) & 7);
    const int t = lane - 8, lc = t >> 2;
    const int kb0 = lc < 3 ? kb_of(4 * kq + ((t ^ ntp) & 3), 2 * lc) : kb_of(4 * kq + (((2 * t) ^ ntp) & 3), 6);
    const int kb1 = lc < 3 ? kb0 + 1 : kb_of(4 * kq + (((2 * t + 1) ^ ntp) & 3), 6);
    const int crow = ntp * 256 + (wq >> 1) * 128, wcol = ntp * 512 + ((wq & 2) + ((wq ^ (r2 >> 1)) & 1)) * 128;
    ta = lane < 8 ? H_OFF + ci * H_PANEL + ks * 16384 + wq * 4096 : ci * HID_PANEL + (wq < 2 ? kb0 : kb1) * 8192 + (wq & 1) * 4096;
    tb = lane < 8 ? (crow + ((2 * (wq & 1)) ^ r2) * 32) * 2048 + (ks >> 1) * 128 + (ks & 1) : kb0 * 65536 + wcol;
    tc = lane < 8 ? (crow + ((2 * (wq & 1) + 1) ^ r2) * 32) * 2048 + (ks >> 1) * 128 : kb1 * 65536 + wcol;
    PIN2(ta, tb); PIN(tc);
  };
  auto next4 = [&]() AI { sc = __builtin_amdgcn_readlane(tc, sx); PINS(sc); nexts(); };
  auto next3 = [&]() AI { next4(); rb = mk(sel<const u16*>(sb & 1, cim, cre), 1024 * 2048); sb &= ~1u; };
  f32x4 fv[4]; unsigned fpp[4], fo;
  auto unp = [&](u32x2 v, int e) AI { return (e & 1) ? u2f(v[e >> 1] & 0xffff0000u) : bf2f(v[e >> 1] & 0xffffu); };
  auto presum = [&]() AI {
    const int c8 = lane & 7, sg = (lane >> 3) & 1, xw_ = (w >> 2) * 2 + sg;
    const f32x4 bv = rdf4(T_OUT + r15 * 16), dv = rdf4(T_OUT + 256 + r15 * 16);
#pragma unroll
    for (int u = 0; u < 4; ++u) {
      const int row = 4 * u + g;
      const f32x4 av = rdf4(65536 + xw_ * 8192 + (wq * 2 + (c8 >> 2)) * 1024 + (c8 & 3) * 256 + ((row ^ (8 * sg + c8)) * 16));
      const u32x2 xw = *(LAS u32x2*)(size_t)(114688 + (16 * w + row) * 128 + r15 * 8); const float gg = rdf(T_ROW + 1536 + (16 * w + row) * 4);
#pragma unroll
      for (int e = 0; e < 4; ++e) fv[u][e] = av[e] + __builtin_fmaf(gg, __builtin_fmaf(unp(xw, e), dv[e], -bv[e]), bv[e]);
      fpp[u] = xw_ * 4096 + wq * 1024 + (c8 & 3) * 256 + (c8 >> 2) * 8 + ((row ^ (8 * sg + 2 * (c8 & 3))) * 16);
      PIN2(fv[u], fpp[u]);
    }
    fo = (16 * w + g) * 4096 + sg * 512 + c8 * 16; PIN(fo);
  };

  if (w < 4) {
    constexpr bool isx = PROFX; (void)isx;
    unsigned epr, epc;
    const unsigned ca = (32 * wn + r31) * 8;
    const unsigned c0 = (32 * wn + r15) * 8;
    const int seg0 = 4 * wm + 2 * hl;
    f32x2 lam[2], cof[2], cfa, l16, l128, l1k, pw0, pw1; unsigned t3a, t3b;
    {
      setm0(T_DUM);
      const int row = bid * 8 + (lane & 7);
      const char* pp = (const char*)(hl ? (row < 1024 ? bre : bim - 1024 * 1024) : x) + (size_t)row * 2048 + (4 * wq + ((lane >> 3) & 3)) * 128;
      for (int q = 0; q <= wq; ++q) __builtin_amdgcn_s_sleep(6);
      pfg(pp);
      pfg((const char*)pc0 + (((loc * 4 + wq) % CSL) * 16 + (lane & 15)) * 128);
      ld32a(epr, rws, 0u, flagb + FK_EP * 128); ld32a(epc, rws, 0u, EPC_OFF + cj * 128);
      setup2(); sx = 2; nexts(); table3(t3a, t3b, tc);
      rb = mk((const char*)w1 - 4096, 1024 * 7168 + 4096);
#pragma unroll
      for (int q = 0; q < 4; ++q) { va[q] = vnat(q); PIN(va[q]); }
      wrf(T_ZERO + (lane & 3) * 4, 0.f);
    }
    {
      f32x4 acc[4][4];
#pragma unroll
      for (int a = 0; a < 16; ++a) { acc[a >> 2][a & 3] = (f32x4){0, 0, 0, 0}; PIN(acc[a >> 2][a & 3]); }
      bf16x8 wv[4][2];
      const int sstar = c7 < 2 ? c7 : 2;
      const int fsw = ((q2 >> 1) & 1) | ((g & 1) << 1);
      unsigned cf = sstar * 32768 + (g >> 1) * 7168 + (8 * (g & 1) + q2) * 128 + ((wq ^ fsw) * 32) + p * 8;
      unsigned ch = sstar * 32768 + (g >> 1) * 7168 + 6144 + (8 * (g & 1) + q2) * 64 + (((wq & 1) ^ (g & 1)) * 32) + p * 8;
      PIN(cf); PIN(ch);
      auto wread = [&](int st) AI {
        if (sstar == st) {
#pragma unroll
          for (int e = 0; e < 4; ++e)
#pragma unroll
            for (int t = 0; t < 4; ++t) sethalf(wv[t][e >> 1], e & 1, rtr((e >> 1) * 14336 + (t < 3 ? cf + t * 2048 + (e & 1) * 512 : ch + (e & 1) * 256)));
        } };
      {
        bf16x8 fa[4][2], fb[2][4][2];
        const unsigned n0 = r15 * 128 + ((g ^ (r15 >> 1)) * 16), n1 = r15 * 128 + (((4 + g) ^ (r15 >> 1)) * 16);
        const unsigned cb[4] = {wm * 8192u + n0, wm * 8192u + n1, 16384 + wn * 8192u + n0, 16384 + wn * 8192u + n1};
        unsigned ba[4], rs = 98304;
#pragma unroll
        for (int b = 0; b < 4; ++b) { ba[b] = rs + cb[b]; PIN(ba[b]); }
        auto rd = [&](int X, int r) AI { const int ks = r & 1, f = r >> 1;
          if (f >= 1 && f < 5) fb[X][f - 1][ks] = rd128(ba[2 + ks] + (f - 1) * 2048); else { const int a = f ? f - 4 : 0; fa[a][ks] = rd128(ba[ks] + a * 2048); } };
        auto mm = [&](int X, int q, int) AI { const int a = q >> 3, b = (q >> 1) & 3, ks = q & 1; MMA(acc[a][b], fa[a][ks], fb[X][b][ks]); };
        auto up = [&](int b) AI { if (b == 0) { rs = nq(rs); PINS(rs); } else { ba[b - 1] = rs + cb[b - 1]; PIN(ba[b - 1]); } };
        waitl0(); bar();
        period<32, 16, 0, true, 16, LASTR, 5>(rd, mm, up); bar();
        for (int s = 0; s < 7; ++s) { period<32, 16, 1, false, 16, LASTR, 5>(rd, mm, up); bar(); period<32, 16, 0, false, 16, LASTR, 5>(rd, mm, up); bar(); }
        period<32, 16, 1, false, 16, LASTR, 5>(rd, mm, up); bar();
        waitv<0>(); PIN(epr); PIN(epc);
        epr = __builtin_amdgcn_readfirstlane(epr) + 1u; epc = __builtin_amdgcn_readfirstlane(epc) + 1u;
        lam[0] = rdf2(T_CON + c0); lam[1] = rdf2(T_CON + c0 + 128);
        wread(0);
        sfor<7>([&](auto U) AI { G2(IC<2>{}, IC<U.value + 4>{}); SB();
#pragma unroll
          for (int q = 16 + (16 * U.value) / 7 / 2 * 2; q < 16 + (16 * (U.value + 1)) / 7 / 2 * 2; ++q) mm(1, q, 0);
          SB(); });
        nexts();
      }
      f32x2 st[2] = {{0, 0}, {0, 0}}, Pf[2] = {{0, 0}, {0, 0}}, Gt = {0, 0}, cr = {0, 0}, ag[8];
      u32x4 lb[8], cg[8], rgv[2]; unsigned lbo[8], lbe[8], pv, gd = 0; unsigned obs[2][2][2][4];
      f32x4 cs[4]; f32x2 so[2]; float c0s = 0.f, c1s = 0.f;
      const rsrc_t rgr = mk(ws + GRAN_OFF + cj * 16384, 16384);
      const int cp = 28 * wq + (r31 < 28 ? r31 : 27);
      const unsigned cgo = hl * 16384 + cp * 16;
#define OB(a, b, c) ((u32x4){obs[a][b][c][0], obs[a][b][c][1], obs[a][b][c][2], obs[a][b][c][3]})
      auto hofs = [&](int hf, int mt) AI { return (2u * wn + mt) * 4096 + wm * 2048 + hf * 1024 + g * 256 + ((r15 ^ (hf << 2)) * 16); };
      auto bad = [&]() AI { unsigned d = 0;
#pragma unroll
        for (int q = 0; q < 8; ++q) { PIN(lb[q]); d |= (int)lbo[q] < 0 ? 0u : ((lb[q][1] ^ epc) | (lb[q][3] ^ epc)); }
        return d != 0; };
      auto gbad = [&]() AI { unsigned d = 0;
#pragma unroll
        for (int q = 0; q < 8; ++q) { PIN(cg[q]); d |= (cg[q][1] ^ epc) | (cg[q][3] ^ epc); }
        return d != 0; };
      auto E2 = [](int s) constexpr { return (s == 2 || s == 3) ? 1 : 0; };
      auto T2 = [](int s) constexpr { return s == 4 ? 8 : (s == 7 || s == 8) ? 4 : s == 11 ? 10 : 0; };
      sfor<15>([&](auto S) AI { constexpr int s = S.value;
        if constexpr (s < 3) waitl0();
        if constexpr (s == 6) waitv<11>();
        else if constexpr (s >= 1) waitv<T2(s - 2) + (s < 14 ? 11 : 8) + E2(s - 1) + T2(s - 1)>();
        bar();
        if constexpr (s < 2) wread(s + 1);
        if constexpr (s == 2) {
#pragma unroll
          for (int sg = 0; sg < 8; ++sg) ag[sg] = rdf2(T_AGG + sg * 512 + ca);
          l16 = rdf2(T_CON + 1024 + ca); cof[0] = rdf2(T_CON + 512 + c0); cof[1] = rdf2(T_CON + 512 + c0 + 128);
        }
        if constexpr (s == 5) { pw1 = rdf2(T_CON + 1536 + ca); pw0 = rdf2(T_CON + 2048 + ca); l128 = rdf2(T_CON + 2560 + ca); l1k = rdf2(T_CON + 3072 + ca); cfa = rdf2(T_CON + 512 + ca); }
        if constexpr (s == 11) { if (w == 0) stflag(rws, epr, lane, flagb + FK_H * 128 + cj * 4); }
        if constexpr (s == 14) { if (w == 0) ld32a(pv, rws, fvo, flagb + FK_H * 128); }
        SB();
        auto work = [&](auto U) AI { constexpr int u = U.value;
          if constexpr (s < 2 && u >= 1 && u < 9) {
            constexpr int t = 8 * s + u - 1;
#pragma unroll
            for (int mt = 0; mt < 2; ++mt) st[mt] = cfma(lam[mt], st[mt], (f32x2){acc[t >> 2][mt][t & 3], acc[t >> 2][2 + mt][t & 3]});
            PIN2(st[0], st[1]);
          }
          if constexpr (s == 1 && u == 9) { wrf2(T_AGG + (4 * wm + g) * 512 + c0, st[0]); wrf2(T_AGG + (4 * wm + g) * 512 + c0 + 128, st[1]); }
          if constexpr (s == 2 && u >= 1 && u < 9) {
            constexpr int sg = u - 1, mt = sg & 1; const bool me = sg == seg0 + mt;
            Pf[mt].x = sel<float>(me, Gt.x, Pf[mt].x); Pf[mt].y = sel<float>(me, Gt.y, Pf[mt].y); Gt = cfma(l16, Gt, ag[sg]);
            PIN2(Gt, Pf[mt]);
          }
          if constexpr (s == 2 && u == 9) st64(rgr, (u32x2){f2u(sel<float>(hl, Gt.y, Gt.x)), epc}, ca * 2 + hl * 8, ci * 1024);
          if constexpr (s == 3 && u < 4) {
            const bf16x8 one = __builtin_bit_cast(bf16x8, (u32x4){0x3F803F80u, 0x3F803F80u, 0x3F803F80u, 0x3F803F80u});
            cs[u] = MFMA(one, wv[u][1], MFMA(one, wv[u][0], ((f32x4){0, 0, 0, 0}))); PIN(cs[u]);
          }
          if constexpr (s == 3 && u == 8) { PIN2(cs[0], cs[1]); PIN2(cs[2], cs[3]); }
          if constexpr (s == 3 && u == 9)
            st64(rws, (u32x2){f2u(sel4(g & 1, g & 2, cs[0][0], cs[1][0], cs[2][0], cs[3][0])), epc}, (g < 3 ? g * 64 + wq * 16 + r15 : 192 + (wq & 1) * 16 + r15) * 8u, CG_OFF + (cj * 16 + ci) * 2048);
          if constexpr ((s == 3 && u >= 4 && u < 9) || (s == 4 && u < 5) || (s == 5 && u >= 1 && u < 7)) {
            constexpr int t = s == 3 ? u - 4 : s == 4 ? 5 + u : 9 + u;
            f32x2 v0 = cmulc(cof[0], (f32x2){acc[t >> 2][0][t & 3], acc[t >> 2][2][t & 3]}), v1 = cmulc(cof[1], (f32x2){acc[t >> 2][1][t & 3], acc[t >> 2][3][t & 3]}); PIN2(v0, v1);
            acc[t >> 2][0][t & 3] = v0.x; acc[t >> 2][2][t & 3] = v0.y; acc[t >> 2][1][t & 3] = v1.x; acc[t >> 2][3][t & 3] = v1.y;
          }
          if constexpr (s == 4 && (u == 6 || u == 8)) {
#pragma unroll
            for (int q = 2 * u - 12; q < 2 * u - 8; ++q) { lbo[q] = (ci - 8 - 8 * hl + q) * 1024 + ca * 2; PIN(lbo[q]); }
          }
          if constexpr (s == 4 && u == 10) {
#pragma unroll
            for (int q = 0; q < 8; ++q) ld128a(lb[q], rgr, lbo[q], 0u);
          }
          if constexpr (s == 5 && u == 8) {
            if (!wm) pw0 = (f32x2){1.f, 0.f};
            if (ci <= 8) l1k = (f32x2){0.f, 0.f};
            pw0 = cmul(pw0, (f32x2){sel<float>(hl, pw1.x, 1.f), sel<float>(hl, pw1.y, 0.f)}); pw1 = cmul(pw0, l16); PIN2(pw0, pw1);
          }
          if constexpr (s == 5 && (u == 7 || u == 9)) {
#pragma unroll
            for (int q = 2 * u - 14; q < 2 * u - 10; ++q) { lbe[q] = (int)lbo[q] < 0 ? 0u : epc; PIN(lbe[q]); }
          }
          if constexpr (s == 6 && u < 8) {
            PIN(lb[u]); gd |= (lb[u][1] ^ lbe[u]) | (lb[u][3] ^ lbe[u]); cr = cfma(l128, cr, (f32x2){u2f(lb[u][0]), u2f(lb[u][2])});
            PIN2(cr, gd);
          }
          if constexpr (s == 6 && u == 8) {
            if (__builtin_expect(anyl(gd != 0), 0)) {
              for (unsigned sp = 0; sp < SPIN_CAP; ++sp) {
                __builtin_amdgcn_s_sleep(2);
#pragma unroll
                for (int q = 0; q < 8; ++q) ld128a(lb[q], rgr, lbo[q], 0u);
                waitv<0>();
                if (!anyl(bad())) break;
              }
              cr = (f32x2){0, 0};
#pragma unroll
              for (int q = 0; q < 8; ++q) cr = cfma(l128, cr, (f32x2){u2f(lb[q][0]), u2f(lb[q][2])});
            }
            gd = 0;
            const auto sx_ = __builtin_amdgcn_permlane32_swap(f2u(cr.x), f2u(cr.x), false, false), sy_ = __builtin_amdgcn_permlane32_swap(f2u(cr.y), f2u(cr.y), false, false);
            cr = cfma(l1k, (f32x2){u2f(sx_[1]), u2f(sy_[1])}, (f32x2){u2f(sx_[0]), u2f(sy_[0])});
            PIN(cr);
          }
          if constexpr (s == 6 && u == 9) { st[0] = cmulc(cfa, cfma(pw0, cr, Pf[0])); PIN(st[0]); }
          if constexpr (s == 7 && u == 0) { st[1] = cmulc(cfa, cfma(pw1, cr, Pf[1]));
            const auto ex = __builtin_amdgcn_permlane16_swap(f2u(st[0].x), f2u(st[1].x), false, false), ey = __builtin_amdgcn_permlane16_swap(f2u(st[0].y), f2u(st[1].y), false, false);
            st[0] = (f32x2){u2f(ex[0]), u2f(ey[0])}; st[1] = (f32x2){u2f(ex[1]), u2f(ey[1])}; PIN2(st[0], st[1]); }
          if constexpr ((s == 7 && u >= 2 && u < 10) || (s == 8 && u >= 1 && u < 9)) {
            constexpr int t = s == 7 ? u - 2 : 7 + u;
            if constexpr (t & 1) { so[0] = st[0]; so[1] = st[1]; }
#pragma unroll
            for (int mt = 0; mt < 2; ++mt) st[mt] = cfma((f32x2){lam[mt].x, -lam[mt].y}, st[mt], (f32x2){acc[t >> 2][mt][t & 3], acc[t >> 2][2 + mt][t & 3]});
            if constexpr (t & 1) {
#pragma unroll
              for (int mt = 0; mt < 2; ++mt) { obs[t >> 3][mt][0][(t >> 1) & 3] = pk2(so[mt].x, st[mt].x); obs[t >> 3][mt][1][(t >> 1) & 3] = pk2(so[mt].y, st[mt].y); PIN2(obs[t >> 3][mt][0][(t >> 1) & 3], obs[t >> 3][mt][1][(t >> 1) & 3]); }
            }
            PIN2(st[0], st[1]);
          }
          if constexpr ((s == 7 || s == 8) && u == 10) {
#pragma unroll
            for (int q = 0; q < 4; ++q) st128(rws, OB(s - 7, q >> 1, q & 1), hofs(s - 7, q >> 1), H_OFF + ci * H_PANEL + (2 * cj + (q & 1)) * 16384);
          }
          if constexpr (s >= 13 && u < 4) *(LAS u32x4*)(size_t)(98304 + (s - 13) * 16384 + hofs(u >> 1, u & 1)) = OB(u >> 1, u & 1, s - 13);
          if constexpr (s == 9 && u == 0) { if (ci == 15 && w == 1) stflag(rws, epc, lane, EPC_OFF + cj * 128); }
          if constexpr (s == 14 && u == 5) {
            auto rbad = [&]() AI { PIN2(rgv[0], rgv[1]); return ((rgv[0][1] ^ epr) | (rgv[0][3] ^ epr) | (rgv[1][1] ^ epr) | (rgv[1][3] ^ epr)) != 0; };
            if (__builtin_expect(anyl(rbad()), 0))
              for (unsigned sp = 0; sp < SPIN_CAP; ++sp) { __builtin_amdgcn_s_sleep(2);
                ld128a(rgv[0], rws, tq * 16u, rgb); ld128a(rgv[1], rws, tq * 16u + 4096, rgb);
                waitv<0>(); if (!anyl(rbad())) break; }
          }
          if constexpr (s == 14 && u == 7) {
            wrf(T_ROW + (tq & 1) * 1024 + (tq >> 1) * 4, u2f(rgv[0][0])); wrf(T_ROW + (tq & 1) * 1024 + 512 + (tq >> 1) * 4, u2f(rgv[0][2]));
            wrf2(T_XA + tq * 8, (f32x2){u2f(rgv[1][0]), u2f(rgv[1][2])});
          }
          if constexpr (s == 11 && u == 10) {
#pragma unroll
            for (int q = 0; q < 8; ++q) ld128a(cg[q], rws, cgo, CG_OFF + (cj * 16 + q) * 2048);
            ld128a(rgv[0], rws, tq * 16u, rgb); ld128a(rgv[1], rws, tq * 16u + 4096, rgb);
          }
          if constexpr (s == 14 && u < 4) {
#pragma unroll
            for (int q = 2 * u; q < 2 * u + 2; ++q) { PIN(cg[q]); gd |= (cg[q][1] ^ epc) | (cg[q][3] ^ epc); c0s += u2f(cg[q][0]); c1s += u2f(cg[q][2]); }
            PIN(gd); PIN(c0s); PIN(c1s);
          }
          if constexpr (s == 14 && u == 4) {
            if (__builtin_expect(anyl(gd != 0), 0)) {
              for (unsigned sp = 0; sp < SPIN_CAP; ++sp) { __builtin_amdgcn_s_sleep(2);
#pragma unroll
                for (int q = 0; q < 8; ++q) ld128a(cg[q], rws, cgo, CG_OFF + (cj * 16 + q) * 2048);
                waitv<0>(); if (!anyl(gbad())) break; }
              c0s = 0.f; c1s = 0.f;
#pragma unroll
              for (int q = 0; q < 8; ++q) { c0s += u2f(cg[q][0]); c1s += u2f(cg[q][2]); }
            }
            c0s = sum32(c0s); c1s = sum32(c1s); PIN(c0s); PIN(c1s);
          }
          if constexpr (s == 14 && u == 6) {
            const float v = sel<float>(hl, c1s, c0s), hi = rbf(v), bb = rdf(T_B1 + (2 * cp + hl) * 4);
            *(LAS u32x4*)(size_t)(T_WA + (2 * cp + hl) * 16) = (u32x4){pk2(hi, v - hi), pk2(hi, bb), pk2(bb, 0.f), 0u};
          }
        };
        if constexpr (s < 13) { sfor<11>([&](auto U) AI { G2(IC<s + 3>{}, U); SB(); work(U); SB(); }); if constexpr (s < 12) nexts(); }
        if constexpr (s == 12) {
#pragma unroll
          for (int q = 0; q < 4; ++q) vb[q] = va[q];
          ta = t3a; tb = t3b; sx = 0; next3(); }
        if constexpr (s >= 13) { sfor<8>([&](auto U) AI { G3(IC<s - 13>{}, IC<U.value + 4>{}); SB(); work(U); SB(); }); next3(); }
      });
      waitl0(); waitv<8>();
      if (w == 0) check_flags(pv, rws, fvo, flagb + FK_H * 128, lane, 4 * kq, 4 * kq + 4, epr);
      bar();
    }
    f32x4 acc[4][8];
    bf16x8 fs[8][2], fr[2][4][2];
    const int hf = p >> 1, fw = 2 * (g & 1) + (q2 >> 1);
    const unsigned hb = 98304 + (g >> 1) * 4096 + wm * 2048 + hf * 1024 + (8 * (g & 1) + q2) * 16 + (p & 1) * 8, wb = wn * 16384 + (4 * g + q2) * 128 + p * 8;
    unsigned b3[4] = {hb + 64u * hf, hb + 64u * (1 ^ hf), 32768 + wn * 16384u + r15 * 128 + ((g ^ (r15 >> 1)) * 16), 32768 + wn * 16384u + r15 * 128 + (((4 + g) ^ (r15 >> 1)) * 16)};
    unsigned b4[5];
    unsigned a3 = 1, dA, dW;
    auto rd3 = [&](int X, int r) AI {
      if (r >= 2 && r < 18) { const int a = (r - 2) >> 2, ks = ((r - 2) >> 1) & 1, h = r & 1; sethalf(fr[X][a][ks], h, rtr(b3[h] + ks * 8192 + a * 256)); }
      else { const int b = r < 2 ? 0 : (r - 16) >> 1, ks = r & 1; fs[b][ks] = rd128(b3[2 + ks] + b * 2048); } };
    auto rd4 = [&](int X, int r) AI {
      if (r >= 4 && r < 12) fr[X][(r - 4) >> 1][r & 1] = rd128(b4[0] + (r & 1) * 8192 + ((r - 4) >> 1) * 1024);
      else { const int b = r < 4 ? 0 : (r - 8) >> 2, ks = (r >> 1) & 1, h = r & 1; sethalf(fs[b][ks], h, rtr(b4[1 + (b & 3)] + (b >> 2) * 8192 + ks * 4096 + h * 2048)); } };
    auto mm = [&](int X, int q, int SL) AI { const int b = q >> 3, a = (q >> 1) & 3, ks = q & 1;
      if (SL < 0) acc[a][b] = MFMA(fs[b][ks], fr[X][a][ks], acc[a][b]); else MMA(acc[a][b], fs[b][ks], fr[X][a][ks]); };
    auto mmz = [&](int X, int q, int SL) AI { if (X == 0 && !(q & 1)) MMZ(acc[(q >> 1) & 3][q >> 3], fs[q >> 3][0], fr[0][(q >> 1) & 3][0]); else mm(X, q, SL); };
    auto adv = [&]() AI { dA = a3 == 0 ? -32768 : 16384; dW = a3 == 2 ? -65536 : 32768; a3 = a3 == 2 ? 0 : a3 + 1; PINS(dA); PINS(dW); PINS(a3); };
    auto up3 = [&](int b) AI { if (b == 0) adv(); else { b3[b - 1] += sel<unsigned>(b < 3, dA, dW); PIN(b3[b - 1]); } };
    auto up4 = [&](int b) AI { if (b == 0) adv(); else { b4[b - 1] += sel<unsigned>(b < 2, dA, dW); PIN(b4[b - 1]); } };
    constexpr int L3 = 18;
#pragma unroll
    for (int b = 0; b < 4; ++b) PIN(b3[b]);
    period<64, L3, 0, true, 32, 44, 5>(rd3, mmz, up3); waitv<0>(); bar();
    period<64, L3, 1, false, 32, 44, 5>(rd3, mmz, up3); bar();
    for (int s = 0; s < 3; ++s) { period<64, L3, 0, false, 32, 44, 5>(rd3, mm, up3); bar(); period<64, L3, 1, false, 32, 44, 5>(rd3, mm, up3);
      if (s == 2) break;
      bar(); }
    float gr[4];
#pragma unroll
    for (int a = 0; a < 4; ++a) gr[a] = rdf(T_ROW + 1536 + (64 * wm + 16 * a + r15) * 4);
    b4[0] = 131072 + wm * 4096 + lane * 16;
#pragma unroll
    for (int b = 0; b < 4; ++b) b4[1 + b] = wb + ((b ^ fw) * 32);
#pragma unroll
    for (int b = 0; b < 5; ++b) PIN(b4[b]);
    waitl0(); bar();
    {
      auto mmg = [&](int X, int q, int SL) AI { mm(X, q, SL); const int a = (SL >> 1) & 3, b = SL >> 3, e = 2 * (SL & 1); SB();
        acc[a][b][e] *= gr[a]; acc[a][b][e + 1] *= gr[a]; PIN(acc[a][b]); };
      period<64, L3, 0, false, 40, 56, 6>(rd4, mmg, up4); bar();
    }
    for (int s = 0; s < 6; ++s) { period<64, L3, 1, false, 40, 56, 6>(rd4, mm, up4); bar(); period<64, L3, 0, false, 40, 56, 6>(rd4, mm, up4); bar(); }
    const unsigned po = wq * 4096 + g * 256 + ((r15 ^ (8 * wn + 2 * g)) * 16);
    auto send = [&](int qd, int a) AI {
      st128(rws, (u32x4){pk2(acc[a][2 * qd][0], acc[a][2 * qd][1]), pk2(acc[a][2 * qd][2], acc[a][2 * qd][3]), pk2(acc[a][2 * qd + 1][0], acc[a][2 * qd + 1][1]), pk2(acc[a][2 * qd + 1][2], acc[a][2 * qd + 1][3])},
            po + a * 1024, PART_OFF + (ci * 16 + (qd ^ r2) * 4 + ntp) * 49152 + qd * 16384); };
    {
      auto mms = [&](int X, int q, int SL) AI { mm(X, q, SL); const int d = SL - L3 - 20;
        if (d >= 0 && (d & 1) == 1 && (d & 15) < 8) { SB(); send(d >> 4, (d & 15) >> 1); } };
      period<64, L3, 1, false, 40, 56, 0>(rd4, mms, up4); bar();
    }
    MPAD();
    auto sig = [&](int qd) AI { stflag(rws, epr, lane, PF_OFF + (ci * 16 + (qd ^ r2) * 4 + ntp) * 256 + wq * 64 + qd * 4); };
    sfor<11>([&](auto M) AI { constexpr int m = M.value;
      if constexpr (m < 9) { mm(1, 46 + 2 * m, -1); mm(1, 47 + 2 * m, -1); }
      SB();
      if constexpr (m < 4) send(2, m);
      if constexpr (m >= 7) {
#pragma unroll
        for (int q = 2 * (m - 7); q < 2 * (m - 7) + 2; ++q) { const int a = q & 3, bb = q >> 2;
          *(LAS f32x4*)(size_t)(65536 + wq * 8192 + (a * 2 + bb) * 1024 + g * 256 + ((r15 ^ (8 * wn + 4 * bb + g)) * 16)) = acc[a][6 + bb]; }
      }
      if constexpr (m == 5) { waitv<8>(); sig(0); }
      SB(); });
    waitv<5>(); sig(1);
    waitl0(); bar();
    waitv<2>(); sig(2);
    presum();
    bar();
  } else {
    constexpr bool isx = !PROFX; (void)isx;
    unsigned epr, pv, t3a, t3b, cb[8], ba[8]; const char *pp0, *pp1;
    f32x4 ac1[28];
    {
      u32x4 xr[2][2], gq[2];
      ra = rx; rb = mk(sel<const u16*>(wq & 1, bim, bre), 1024 * 2048);
#pragma unroll
      for (int q = 0; q < 4; ++q) { va[q] = (32 * (q & 1) + 16 * hl + 4 * (q >> 1) + ((lane >> 3) & 3)) * 2048 + (((lane & 7) ^ ((4 * q + g) & 7)) * 16) - 1024 * q; vb[q] = vnat(q); }
      auto src1 = [&](int s) AI { const int k = s ^ (s < 2 ? 0 : s < 4 ? (rot & 1) : rot); sa = (ci * 128 + 64 * (wq >> 1) + 8 * (wq & 1)) * 2048 + k * 128; sb = (cj * 64 + 32 * (wq >> 1)) * 2048 + k * 128; sl = ((s + 3) & 3) * 32768; };
      src1(0); ival(G, nowork);
      SB();
      setup2(); table3(t3a, t3b, tc);
      {
        const int f = ((q2 >> 1) & 1) | ((g & 1) << 1);
        const unsigned nb = wm * 8192u + r15 * 128, n0 = (g ^ (r15 >> 1)) * 16, n1 = ((4 + g) ^ (r15 >> 1)) * 16, wk = (g >> 1) * 7168u + p * 8;
        cb[0] = nb + wn * 4096 + n0; cb[1] = nb + wn * 4096 + n1; cb[2] = nb + (1 - wn) * 4096 + n0; cb[3] = nb + (1 - wn) * 4096 + n1;
        cb[4] = wk + (8 * (g & 1) + q2) * 128 + (((2 * wn) ^ f) * 32); cb[5] = wk + (8 * (g & 1) + q2) * 128 + (((2 * wn + 1) ^ f) * 32);
        cb[6] = wk + 6144 + (8 * (g & 1) + q2) * 64 + (g & 1) * 32; cb[7] = wk + 6144 + (8 * (g & 1) + q2) * 64 + ((g & 1) ^ 1) * 32;
#pragma unroll
        for (int b = 0; b < 8; ++b) { ba[b] = cb[b] + (b < 4 ? 131072u : 0u); PIN2(cb[b], ba[b]); }
        pp0 = lane < 32 ? (const char*)sel<const u16*>(lane & 16, cim, cre) + ((bid * 4 + wq) * 16 + r15) * 128 : (const char*)w2 + ((bid * 4 + wq) * 56 + r15) * 128;
        pp1 = (const char*)w2 + ((bid * 4 + wq) * 56 + (lane < 40 ? 16 + lane : 55)) * 128;
        if (PGT && lane == 62) pp1 = (const char*)out + osof + wq * 131072;
        if (PGT && lane == 63) pp1 = ws + PART_OFF + (ci * 16 + wq * 4 + ntp) * 49152;
        PIN2(pp0, pp1);
#pragma unroll
        for (int m = 0; m < 28; ++m) { ac1[m] = (f32x4){0, 0, 0, 0}; PIN(ac1[m]); }
      }
      src1(1); ival(G, nowork);
      waitv<8>(); bar();
      unsigned pdt, plr, pli, bq, oq; f32x2 lp; float lr, li;
      const int tc = tq < 224 ? tq : 223;
      ld32a(epr, rws, 0u, flagb + FK_EP * 128); ld128g(gq[0], gw, lane * 16u); ld128g(gq[1], gw + 512, lane * 16u);
      ld16p(pdt, ldt + 64 * cj, lane * 2u); ld16p(plr, lre + 64 * cj, lane * 2u); ld16p(pli, lim + 64 * cj, lane * 2u);
      ld16p(bq, b1, (224 * cj + (tc < 192 ? ((cj & 1) ? 32 : 0) + tc : ((cj & 1) ? -192 : 0) + tc)) * 2u);
      ld16p(oq, sel<const u16*>(wq & 1, dsk, b2), (ntp * 256 + hl * 128 + kq * 32 + r31) * 2u);
      PH(1);
      src1(2); ival(G, nowork);
      src1(3); ival(G, nowork);
      float sv[6] = {0, 0, 0, 0, 0, 0}, a1, a2, a3, mean, sd, rstd, qv, gg, kq, rr;
      auto E1 = [](int s) constexpr { return (s == 1 || s == 2) ? 2 : s == 11 ? 1 : 0; };
      sfor<12>([&](auto S) AI { constexpr int s = S.value;
        if constexpr (s == 0) waitv<16 + 8>(); else waitv<16 + E1(s - 2) + E1(s - 1)>();
        bar();
        src1(s + 4); ival(G, [&](auto U) AI { constexpr int u = U.value;
          if constexpr ((s == 1 || s == 2) && (u == 2 || u == 5)) ld128p(xr[s - 1][u / 3], rx, lane * 16u, (ci * 128 + 8 * cj + 2 * wq + s - 1) * 2048 + (u / 3) * 1024);
          if constexpr (s == 1 && u == 0) { PIN2(pdt, plr); PIN(pli); lr = bf2f(plr); li = bf2f(pli); const float dt = rbf(fexp(bf2f(pdt))); lp = (f32x2){fexp(dt * lr), dt * li}; PIN(lp); }
          if constexpr (s == 1 && u == 3) { PIN2(bq, oq); wrf(T_B1 + tq * 4, bf2f(bq)); wrf(T_OUT + tq * 4, bf2f(oq)); PIN(epr); epr = __builtin_amdgcn_readfirstlane(epr) + 1u; }
          if constexpr (s == 1 && u == 6) { kq = __builtin_rintf(lp.y * 0.63661977236f);
            rr = __builtin_fmaf(kq, -1.5707962513f, lp.y); rr = __builtin_fmaf(kq, -7.5497894159e-08f, rr); rr = __builtin_fmaf(kq, -5.3903029534e-15f, rr); PIN2(kq, rr); }
          if constexpr (s == 2 && u == 0) { const float z = rr * rr;
            a1 = __builtin_fmaf(rr * z, __builtin_fmaf(z, __builtin_fmaf(z, __builtin_fmaf(z, 2.7183114939e-06f, -1.9839334836e-04f), 8.3333293858e-03f), -1.6666666642e-01f), rr); PIN(a1); }
          if constexpr (s == 2 && u == 3) { const float z = rr * rr;
            a2 = __builtin_fmaf(z, __builtin_fmaf(z, __builtin_fmaf(z, __builtin_fmaf(z, 2.4390448796e-05f, -1.3886763775e-03f), 4.1666623324e-02f), -4.9999999726e-01f), 1.f); PIN(a2); }
          if constexpr (s == 2 && u == 6) { const int q = (int)kq; const float sa_ = sel<float>(q & 1, a2, a1), ca_ = sel<float>(q & 1, a1, a2);
            lp = (f32x2){lp.x * (((q + 1) & 2) ? -ca_ : ca_), lp.x * ((q & 2) ? -sa_ : sa_)}; PIN(lp); wrf2(T_CON + lane * 8, lp); }
          if constexpr (s == 3 && u == 0) { const float nr = lp.x - 1.f, ni = lp.y, den = 2.f * __builtin_amdgcn_rcpf(lr * lr + li * li);
                                            wrf2(T_CON + 512 + lane * 8, (f32x2){(nr * lr + ni * li) * den, (ni * lr - nr * li) * den}); }
          if constexpr ((s == 3 && u >= 2) || (s == 4 && u >= 4)) { constexpr int q = s == 3 ? u - 2 : u + 2; lp = cmul(lp, lp); PIN(lp);
            if constexpr (q >= 3 && q < 7) wrf2(T_CON + (q - 1) * 512 + lane * 8, lp);
            if constexpr (q == 9) wrf2(T_CON + 3072 + lane * 8, lp); }
          if constexpr ((s == 4 && u < 2) || (s >= 5 && s < 9 && !(u & 1) && 4 * (s - 5) + 2 + (u >> 1) < 16)) {
            constexpr int id = s == 4 ? u : 4 * (s - 5) + 2 + (u >> 1), r = id >> 3, e = id & 7;
            PIN2(xr[r][e >> 2], gq[e >> 2]);
            const unsigned v = xr[r][e >> 2][e & 3], gv = gq[e >> 2][e & 3];
            const float x0 = bf2f(v & 0xffffu), x1 = u2f(v & 0xffff0000u);
            sv[r] += x0 + x1; sv[2 + r] = __builtin_fmaf(x1, x1, __builtin_fmaf(x0, x0, sv[2 + r]));
            sv[4 + r] = __builtin_fmaf(x1, u2f(gv & 0xffff0000u), __builtin_fmaf(x0, bf2f(gv & 0xffffu), sv[4 + r]));
            PIN2(sv[r], sv[2 + r]); PIN(sv[4 + r]);
          }
          if constexpr (s == 9 && u < 4) {
#pragma unroll
            for (int q = 0; q < 6; ++q) sv[q] = u == 0 ? dppadd<0xB1>(sv[q]) : u == 1 ? dppadd<0x4E>(sv[q]) : u == 2 ? dppadd<0x141>(sv[q]) : dppadd<0x140>(sv[q]);
            PIN2(sv[0], sv[1]); PIN2(sv[2], sv[3]); PIN2(sv[4], sv[5]);
          }
          if constexpr (s == 9 && u == 5) { const bool r1 = lane & 4; a1 = sum16(sel<float>(r1, sv[1], sv[0])); a2 = sum16(sel<float>(r1, sv[3], sv[2])); a3 = sum16(sel<float>(r1, sv[5], sv[4])); PIN2(a1, a2); PIN(a3); }
          if constexpr (s == 10 && u == 0) { a1 = sum32(a1); a2 = sum32(a2); a3 = sum32(a3); PIN2(a1, a2); PIN(a3); }
          if constexpr (s == 10 && u == 2) { mean = a1 * (1.f / 1024.f); const float var = __builtin_fmaxf(a2 * (1.f / 1024.f) - mean * mean, 0.f) + 1e-5f; rstd = __builtin_amdgcn_rsqf(var); sd = var * rstd;
                                             PIN2(mean, rstd); PIN(sd); }
          if constexpr (s == 10 && u == 4) { qv = 1.f + fexp(__builtin_fminf(a3, 60.f)); gg = __builtin_amdgcn_rcpf(1.f + fexp(__builtin_fminf(-a3, 80.f))); PIN2(qv, gg); }
          if constexpr (s == 11 && u == 0) { a1 = sel4(lane & 1, lane & 2, -2.302208198f * rstd, -0.1029432397f * rstd * rstd * rstd, qv * sd, gg); PIN(a1); }
          if constexpr (s == 11 && u == 2) { const float mh = rbf(mean), sh = rbf(sd);
                                             a2 = sel4(lane & 1, lane & 2, u2f(pk2(-mh, -mh)), u2f(pk2(mh - mean, sh)), u2f(pk2(sd - sh, 0.f)), 0.f); PIN(a2); }
          if constexpr (s == 11 && u == 5) { if (lane < 16) st64(rws, (u32x2){f2u(sel<float>(lane & 8, a2, a1)), epr}, (lane >> 3) * 4096u + (lane & 7) * 8, rgb + (8 * cj + 2 * wq) * 32); }
        });
      });
      rb = mk((const char*)w1 - 4096, 1024 * 7168 + 4096);
#pragma unroll
      for (int q = 0; q < 4; ++q) va[q] = vb[q];
      sx = 0; nexts();
      const unsigned sb0 = sb, sa0 = sa; nexts();
      const unsigned sb1 = sb, sa1 = sa; nexts();
      const unsigned sa2 = sa;
      waitv<16 + 1>(); bar();
      sa = sa0; sfor<4>([&](auto U) AI { G2(IC<0>{}, U); });
      sa = sa1; sfor<4>([&](auto U) AI { G2(IC<1>{}, U); });
      waitv<16 + 1>(); bar();
      sb = sb0; sfor<7>([&](auto U) AI { G2(IC<0>{}, IC<U.value + 4>{}); });
      sa = sa2; sfor<4>([&](auto U) AI { G2(IC<2>{}, U); });
      waitv<19>(); bar();
      sb = sb1; sfor<7>([&](auto U) AI { G2(IC<1>{}, IC<U.value + 4>{}); });
      ta = t3a; tb = t3b; sx = 2; next3();
    }
    {
      {
        bf16x8 fa[2][4][2], fb[8][2];
        unsigned xn = 98304, wnx = 32768;
        setm0(T_DUM);
        auto rd = [&](int X, int r) AI {
          if (r >= 4 && r < 12) { const int a = (r - 4) >> 1, ks = r & 1; fa[X][a][ks] = rd128(ba[(a >> 1) * 2 + ks] + (a & 1) * 2048); }
          else { const int b = r < 4 ? 0 : (r - 8) >> 2, ks = (r >> 1) & 1, h = r & 1;
            sethalf(fb[b][ks], h, rtr(b < 6 ? ba[4 + (b & 1)] + (b >> 1) * 2048 + ks * 14336 + h * 512 : ba[b] + ks * 14336 + h * 256)); } };
        auto mm = [&](int X, int q, int) AI { const int b = q < 48 ? q >> 3 : 6 + ((q - 48) >> 2), a = (q >> 1) & (q < 48 ? 3 : 1), ks = q & 1, m = (a < 2 ? 8 * a : 4 + 6 * a) + b;
          MMA(ac1[m], fb[b][ks], fa[X][a][ks]); };
        auto up = [&](int b) AI { if (b < 8) { ba[b] = cb[b] + sel<unsigned>(b < 4, xn, wnx); PIN(ba[b]); }
                                  else if (b == 8) { xn = (xn == 131072u) ? 98304u : xn + 16384u; PINS(xn); }
                                  else if (b == 10) { wnx = (wnx == 65536u) ? 0u : wnx + 32768u; PINS(wnx); } };
        waitl0(); waitv<11>(); bar();
        PH(2);
        period<56, 16, 0, true, 40, 48, 12>(rd, mm, up); waitv<0>(); bar();
        const int sp = BW0 + (loc + xcd) % BWN;
        for (int s = 0; s < 14; s += 2) {
          period<56, 16, 1, false, 40, 48, 12>(rd, mm, up); bar();
          if (s == sp) { pfg(pp0); pfg(pp1); }
          period<56, 16, 0, false, 40, 48, 12>(rd, mm, up); bar();
          if (s + 1 == sp) { pfg(pp0); pfg(pp1); }
        }
        period<56, 16, 1, false, 40, 48, 12>(rd, mm, up); bar();
        PH(3);
        va[0] = lane * 16;
#pragma unroll
        for (int q = 0; q < 4; ++q) vb[q] = vnat(q);
#pragma unroll
        for (int a = 0; a < 4; ++a) fa[0][a][0] = rd128(g ? T_ZERO : T_XA + (64 * wm + 16 * (a ^ (2 * wn)) + r15) * 16);
        sfor<12>([&](auto U) AI { constexpr int u = U.value; G3(IC<2>{}, U); SB();
          if constexpr (u < 4) {
#pragma unroll
            for (int q = 40 + 4 * u; q < 44 + 4 * u; ++q) mm(1, q, 0);
          }
          if constexpr (u == 0 || u == 4) {
#pragma unroll
            for (int b = u ? 5 : 0; b < (u ? 8 : 5); ++b) fb[b][0] = rd128(T_WA + ((b < 6 ? (b >> 1) * 64 + wn * 32 + (b & 1) * 16 : 96 + b * 16) + r15) * 16);
          }
          if constexpr (u >= 5) {
#pragma unroll
            for (int q = 8 * (u - 5); q < 8 * (u - 4); q += 2) mm(0, q, 0);
          }
          SB(); });
        next3();
      }
      float k0[4], k1[4], qq[4];
#pragma unroll
      for (int a = 0; a < 4; ++a) { const unsigned ro = T_ROW + (64 * wm + 16 * (a ^ (2 * wn)) + r15) * 4; k0[a] = rdf(ro); k1[a] = rdf(ro + 512); qq[a] = rdf(ro + 1024); }
      u32x4 hw[14]; float t[8]; unsigned sa9 = 0;
      auto lv0 = [](int s) constexpr { return s < 0 ? 0 : s < 6 ? (64 * s + 2) / 3 : s < 10 ? 128 + 16 * (s - 6) : s < 13 ? 192 + (32 * (s - 10) + 2) / 3 : 224; };
      auto st3 = [](int s) constexpr { return (s == 2 || s == 5 || s == 9) ? 4 : s == 12 ? 2 : 0; };
      auto n3 = [](int s) constexpr { return (s == 5 || s == 6 || s == 9 || s == 13) ? 8 : s == 7 ? 16 : s == 19 ? 4 : s > 19 ? 0 : 12; };
      sfor<22>([&](auto S) AI { constexpr int s = S.value;
        if constexpr (s == 6 || s == 10 || s == 14) waitl0();
        if constexpr (s == 8) waitv<12>();
        else if constexpr (s >= 1) waitv<n3(s - 1) + st3(s - 1)>();
        if constexpr (s == 7 || s == 10 || s == 14 || s == 17) {
          if (w == 4) check_flags(pv, rws, fvo, flagb + (FK_M1 + (s - 7) / 3) * 128, lane, 4 * kq, 4 * kq + 4, epr); }
        SB(); bar();
        if constexpr (s == 8) { PH(4); }
        if constexpr (st3(s - 2) > 0) { if (w == 5) stflag(rws, epr, lane, flagb + (FK_M1 + (s - 4) / 3) * 128 + cj * 4); }
        if constexpr (s == 6 || s == 9 || s == 13 || s == 16) { if (w == 4) ld32a(pv, rws, fvo, flagb + (FK_M1 + (s - 6) / 3) * 128); }
        if constexpr (s == 7) { const unsigned keep = sa; sa = sa9; sfor<4>([&](auto U) AI { G3(IC<9>{}, U); }); sa = keep; }
#define UU(e) ac1[m0 + ((e) >> 2)][(e) & 3]
#define LVL(expr) do { _Pragma("unroll") for (int e = 4 * hh; e < 4 * hh + 4; ++e) t[e] = (expr); PIN2(t[4 * hh], t[4 * hh + 1]); PIN2(t[4 * hh + 2], t[4 * hh + 3]); } while (0)
        auto hlev = [&](auto LL) AI { constexpr int L = LL.value, v = L >> 4, l = (L >> 1) & 7, hh = L & 1, a = v & 3, m0 = (a < 2 ? 8 * a : 4 + 6 * a) + 2 * (v >> 2);
          if constexpr (l == 0) LVL(UU(e) * UU(e));
          if constexpr (l == 1) LVL(__builtin_fmaf(t[e], k1[a], k0[a]));
          if constexpr (l == 2) LVL(UU(e) * t[e]);
          if constexpr (l == 3) LVL(__builtin_amdgcn_exp2f(t[e]));
          if constexpr (l == 4) LVL(__builtin_fmaf(t[e], qq[a], qq[a]));
          if constexpr (l == 5) LVL(__builtin_amdgcn_rcpf(t[e]));
          if constexpr (l == 6) LVL(UU(e) * t[e]);
          if constexpr (l == 7) { hw[v][2 * hh] = pk2(t[4 * hh], t[4 * hh + 1]); hw[v][2 * hh + 1] = pk2(t[4 * hh + 2], t[4 * hh + 3]); if constexpr (hh) PIN(hw[v]); else PIN2(hw[v][0], hw[v][1]); }
        };
        if constexpr (s < 19) sfor<12>([&](auto U) AI { constexpr int u = U.value, n = lv0(s + 1) - lv0(s), i0 = (n * u + 11) / 12, i1 = (n * (u + 1) + 11) / 12;
          if constexpr (n3(s) != 8) G3(IC<s + 3>{}, U);
          else if constexpr (u < 8) G3(IC<s + 3>{}, IC<u + 4>{});
          else if constexpr (s != 6)
            *(LAS u32x4*)(size_t)(((s + 1) % 3 == 0 ? 131072u : 81920u + ((s + 1) % 3) * 16384u) + wn * 8192 + (4 * wm + ((u - 8) ^ (2 * wn))) * 1024 + lane * 16) = hw[s - 13 + u];
          SB();
          if constexpr (i0 < i1) hlev(IC<lv0(s) + i0>{});
          if constexpr (i0 + 1 < i1) hlev(IC<lv0(s) + i0 + 1>{});
          SB(); });
        if constexpr (s == 19) {
          setm0(114688 + wq * 4096);
          sfor<4>([&](auto U) AI { constexpr int u = U.value;
            dma<u * 1024>(rx, (8u * u + (lane >> 3)) * 2048 + ((lane >> 2) & 1) * 256 + (lane & 3) * 16 - u * 1024, (ci * 128 + 32 * wq) * 2048 + ntp * 512 + kq * 64); });
        }
        constexpr int gr0 = s == 2 ? 0 : s == 5 ? 1 : s == 9 ? 2 : 3;
#pragma unroll
        for (int q = 0; q < st3(s); ++q) st128(rws, hw[4 * gr0 + q], lane * 16u + (4 * wm + (q ^ (2 * wn))) * 1024, ci * HID_PANEL + kb_of(cj, gr0 < 3 ? 2 * gr0 + wn : 6) * 8192);
        if constexpr (s == 6) sa9 = sa;
        if constexpr (s < 4) next3(); else if constexpr (s < 18) next4();
        if constexpr (s == 4) {
          rb = mk(w2, 3584 * 2048);
#pragma unroll
          for (int q = 0; q < 4; ++q) vb[q] = (8 * q + (lane >> 3)) * 2048 + (((((lane & 7) >> 1) ^ g ^ (2 * (r2 & 1))) * 2) + (lane & 1)) * 16 - 1024 * q;
        }
      });
    }
    PH(5);
    bar();
    presum();
    sfor<3>([&](auto Q) AI { constexpr int q = Q.value;
      if (q == 0 || anyl(lane == q && pv != epr)) {
#pragma unroll 1
        for (unsigned sp = 0; sp < SPIN_CAP; ++sp) {
          ld32a(pv, rws, fvo, PF_OFF + (ci * 16 + cj) * 256 + wq * 64); asm volatile("s_waitcnt vmcnt(0)" : "+v"(pv)::"memory");
          if (!anyl(lane == q && pv != epr)) break;
        }
      }
      setm0(wq * 4096 + q * 16384);
      sfor<4>([&](auto U) AI { dma1<U.value * 1024>(rws, va[0], PART_OFF + (ci * 16 + cj) * 49152 + q * 16384 + wq * 4096); });
    });
    if (cj == 0 && w == 4) stflag(rws, epr, lane, flagb + FK_EP * 128);
    waitv<0>(); bar();
  }
  {
    u32x2 pw[4][3];
#pragma unroll
    for (int u = 0; u < 4; ++u)
#pragma unroll
      for (int q = 0; q < 3; ++q) pw[u][q] = *(LAS u32x2*)(size_t)(fpp[u] + q * 16384);
    SB();
#pragma unroll
    for (int u = 0; u < 4; ++u) {
      f32x4 r;
#pragma unroll
      for (int e = 0; e < 4; ++e) r[e] = (fv[u][e] + unp(pw[u][0], e)) + (unp(pw[u][1], e) + unp(pw[u][2], e));
      st128<OAUX>(mk(out, 8u << 20), __builtin_bit_cast(u32x4, r), fo + u * 16384, osof);
    }
  }
}

extern "C" void kernel_launch(void* const* d_in, const int* in_sizes, int n_in, void* d_out, int out_size, void* d_ws, size_t ws_size, hipStream_t stream) {
  (void)in_sizes; (void)n_in; (void)out_size; (void)ws_size;
  (void)hipFuncSetAttribute((const void*)fused_kernel, hipFuncAttributeMaxDynamicSharedMemorySize, LDS_BYTES);
  fused_kernel<<<256, 512, LDS_BYTES, stream>>>((const u16*)d_in[0], (const u16*)d_in[1], (const u16*)d_in[2], (const u16*)d_in[3], (const u16*)d_in[4],
      (const u16*)d_in[5], (const u16*)d_in[6], (const u16*)d_in[7], (const u16*)d_in[8], (const u16*)d_in[10], (const u16*)d_in[11], (const u16*)d_in[12],
      (const u16*)d_in[13], (const u16*)d_in[14], (char*)d_ws, (float*)d_out);
}
```

```cpp
#include <hip/hip_runtime.h>
#include <utility>

typedef unsigned short u16;
typedef __bf16 bf16x2 __attribute__((ext_vector_type(2)));
typedef __bf16 bf16x4 __attribute__((ext_vector_type(4)));
typedef __bf16 bf16x8 __attribute__((ext_vector_type(8)));
typedef float f32x2 __attribute__((ext_vector_type(2)));
typedef float f32x4 __attribute__((ext_vector_type(4)));
typedef float f32x16 __attribute__((ext_vector_type(16)));
typedef unsigned u32x2 __attribute__((ext_vector_type(2)));
typedef unsigned u32x4 __attribute__((ext_vector_type(4)));
typedef __amdgpu_buffer_rsrc_t rsrc_t;
#define LAS __attribute__((address_space(3)))
#define DI __device__ __forceinline__
#define AI __attribute__((always_inline))
#define SB() __builtin_amdgcn_sched_barrier(0)
#define PIN(x) asm volatile("" : "+v"(x))
#define MFMA(a, b, c) __builtin_amdgcn_mfma_f32_16x16x32_bf16(a, b, c, 0, 0, 0)
#define MMA(c, a, b) asm volatile("v_mfma_f32_16x16x32_bf16 %0, %1, %2, %0" : "+v"(c) : "v"(a), "v"(b))
#define MMZ(c, a, b) asm volatile("v_mfma_f32_16x16x32_bf16 %0, %1, %2, 0" : "=&v"(c) : "v"(a), "v"(b))
#define MPAD() asm volatile("s_nop 7")
template <int V> using IC = std::integral_constant<int, V>;

constexpr unsigned HID_PANEL = 112 * 8192, H_OFF = 16 * HID_PANEL, H_PANEL = 32 * 16384;
constexpr unsigned PART_OFF = H_OFF + 16 * H_PANEL;
constexpr unsigned GRAN_OFF = PART_OFF + 256 * 49152;
constexpr unsigned RG_OFF = GRAN_OFF + 256 * 1024;
constexpr unsigned CG_OFF = RG_OFF + 16 * 8192;
constexpr unsigned FLAG_OFF = CG_OFF + 256 * 2048;
constexpr unsigned EPC_OFF = FLAG_OFF + 16 * 1024;
constexpr unsigned PF_OFF = EPC_OFF + 16 * 128;
constexpr unsigned WS_BYTES = PF_OFF + 256 * 256;
constexpr int FK_M1 = 0, FK_M2 = 1, FK_M3 = 2, FK_M4 = 3, FK_H = 4, FK_P = 5, FK_EP = 6;
constexpr unsigned TAB = 147456, T_ROW = TAB, T_XA = TAB + 2048, T_OUT = TAB + 4096, T_B1 = TAB + 5120, T_CS2 = TAB + 6144, T_WA = TAB + 8192, T_ZERO = TAB + 12288, T_AGG = T_WA, T_DUM = TAB + 12544, T_CON = TAB + 12800;
constexpr unsigned LDS_BYTES = 163840;
#define XMAP 0
#define ROTM 1
#define OAUX 2
#define SP0 4
#define SPN 6
#define PTE 0
#define BW0 0
#define BWN 8
#define CSL 22
#define PGT 0
#define WBI 13
#define WA 0
#define PSL 4
#define MOVE 3
#define PROFX false
#define WD 8
constexpr int LASTR = 24;
constexpr unsigned SPIN_CAP = 1u << 16;

DI rsrc_t mk(const void* p, unsigned bytes) { return __builtin_amdgcn_make_buffer_rsrc((void*)p, 0, bytes, 0x00020000); }
DI void setm0(unsigned lds) { asm volatile("s_mov_b32 m0, %0\n\ts_nop 0" ::"s"(lds) : "memory"); }
template <int OFF> DI void dma(rsrc_t r, unsigned voff, unsigned soff) {
  asm volatile("buffer_load_dwordx4 %0, %1, %2 offen offset:%3 lds" ::"v"(voff), "s"(r), "s"(soff), "i"(OFF) : "memory");
}
template <int OFF> DI void dma1(rsrc_t r, unsigned voff, unsigned soff) {
  asm volatile("buffer_load_dwordx4 %0, %1, %2 offen offset:%3 sc1 lds" ::"v"(voff), "s"(r), "s"(soff), "i"(OFF) : "memory");
}
#define PIN2(x, y) asm volatile("" : "+v"(x), "+v"(y))
#define PINS(x) asm volatile("" : "+s"(x))
typedef const void* const __attribute__((address_space(4)))* kargs_t;
DI const char* karg(int i) { kargs_t ka = (kargs_t)__builtin_amdgcn_kernarg_segment_ptr(); PINS(ka); return (const char*)ka[i]; }
enum { A_X, A_LRE, A_LIM, A_BRE, A_BIM, A_CRE, A_CIM, A_LDT, A_DSK, A_W1, A_B1, A_W2, A_B2, A_GW, A_WS, A_OUT };
template <int N> DI void waitv() { asm volatile("s_waitcnt vmcnt(%0)" ::"i"(N) : "memory"); }
DI void waitl0() { SB(); __builtin_amdgcn_s_waitcnt(0xC07F); asm volatile("" ::: "memory"); }
#define PH(k)
DI void bar() { asm volatile("s_barrier" ::: "memory"); SB(); }
DI bf16x8 rd128(unsigned a) { return *(LAS bf16x8*)(size_t)a; }
DI u32x4 rdu4(unsigned a) { return *(LAS u32x4*)(size_t)a; }
DI f32x4 rdf4(unsigned a) { return *(LAS f32x4*)(size_t)a; }
DI f32x2 rdf2(unsigned a) { return *(LAS f32x2*)(size_t)a; }
DI float rdf(unsigned a) { return *(LAS float*)(size_t)a; }
DI void wrf(unsigned a, float v) { *(LAS float*)(size_t)a = v; }
DI void wrf2(unsigned a, f32x2 v) { *(LAS f32x2*)(size_t)a = v; }
DI bf16x4 rtr(unsigned a) { return __builtin_amdgcn_ds_read_tr16_b64_v4bf16((LAS bf16x4*)(size_t)a); }
DI void sethalf(bf16x8& f, int h, bf16x4 v) {
  if (h == 0) f = __builtin_shufflevector(v, v, 0, 1, 2, 3, -1, -1, -1, -1);
  else { bf16x8 t = __builtin_shufflevector(v, v, -1, -1, -1, -1, 0, 1, 2, 3); f = __builtin_shufflevector(f, t, 0, 1, 2, 3, 12, 13, 14, 15); }
}
DI unsigned f2u(float f) { return __builtin_bit_cast(unsigned, f); }
DI float u2f(unsigned u) { return __builtin_bit_cast(float, u); }
DI float shx(int lane, float v, int m) { return u2f(__builtin_amdgcn_ds_bpermute((lane ^ m) * 4, (int)f2u(v))); }
DI float sum16(float v) { const auto r = __builtin_amdgcn_permlane16_swap(f2u(v), f2u(v), false, false); return u2f(r[0]) + u2f(r[1]); }
DI float sum32(float v) { const auto r = __builtin_amdgcn_permlane32_swap(f2u(v), f2u(v), false, false); return u2f(r[0]) + u2f(r[1]); }
DI float fexp(float v) { return __builtin_amdgcn_exp2f(v * 1.4426950408889634f); }
DI float bf2f(unsigned v) { return u2f(v << 16); }
DI unsigned pk2(float lo, float hi) { f32x2 v = {lo, hi}; return __builtin_bit_cast(unsigned, __builtin_convertvector(v, bf16x2)); }
DI float rbf(float f) { return u2f(pk2(f, 0.f) << 16); }
DI float dot2(unsigned a, unsigned b, float c) { return __builtin_amdgcn_fdot2_f32_bf16(__builtin_bit_cast(bf16x2, a), __builtin_bit_cast(bf16x2, b), c, false); }
template <int C> DI float dppadd(float v) { return v + u2f(__builtin_amdgcn_update_dpp(0, (int)f2u(v), C, 0xf, 0xf, true)); }
DI float sel4(bool b0, bool b1, float a, float b, float c, float d) { PIN(a); PIN(b); PIN(c); PIN(d); const float lo = b0 ? b : a, hi = b0 ? d : c; return b1 ? hi : lo; }
DI f32x2 cmul(f32x2 a, f32x2 b) { return (f32x2){a.x * b.x - a.y * b.y, a.x * b.y + a.y * b.x}; }
DI f32x2 cmulc(f32x2 a, f32x2 b) { return (f32x2){__builtin_fmaf(a.x, b.x, -(a.y * b.y)), __builtin_fmaf(-a.x, b.y, -(a.y * b.x))}; }
DI f32x2 cfma(f32x2 a, f32x2 b, f32x2 c) { return (f32x2){__builtin_fmaf(a.x, b.x, __builtin_fmaf(-a.y, b.y, c.x)), __builtin_fmaf(a.x, b.y, __builtin_fmaf(a.y, b.x, c.y))}; }
template <int AUX = 16> DI void st128(rsrc_t r, u32x4 v, unsigned voff, unsigned soff) {
  __builtin_amdgcn_raw_buffer_store_b128(v, r, voff, soff, AUX);
  asm volatile("s_nop 1" ::"v"(v));
}
DI void st64(rsrc_t r, u32x2 v, unsigned voff, unsigned soff) { __builtin_amdgcn_raw_buffer_store_b64(v, r, voff, soff, 16); }
DI void st32(rsrc_t r, unsigned v, unsigned voff, unsigned soff) { __builtin_amdgcn_raw_buffer_store_b32(v, r, voff, soff, 16); }
DI void stflag(rsrc_t r, unsigned v, int lane, unsigned soff) { if (lane == 0) st32(r, v, 0u, soff); }
DI void ld32a(unsigned& d, rsrc_t r, unsigned voff, unsigned soff) { asm volatile("buffer_load_dword %0, %1, %2, %3 offen sc1" : "=v"(d) : "v"(voff), "s"(r), "s"(soff) : "memory"); }
DI void ld16p(unsigned& d, const u16* p, unsigned voff) { asm volatile("global_load_ushort %0, %1, %2" : "=v"(d) : "v"(voff), "s"(p) : "memory"); }
DI void ld64p(u32x2& d, rsrc_t r, unsigned voff, unsigned soff) { asm volatile("buffer_load_dwordx2 %0, %1, %2, %3 offen" : "=v"(d) : "v"(voff), "s"(r), "s"(soff) : "memory"); }
DI void ld64a(u32x2& d, rsrc_t r, unsigned voff, unsigned soff) { asm volatile("buffer_load_dwordx2 %0, %1, %2, %3 offen sc1" : "=v"(d) : "v"(voff), "s"(r), "s"(soff) : "memory"); }
DI void ld128g(u32x4& d, const u16* p, unsigned voff) { asm volatile("global_load_dwordx4 %0, %1, %2" : "=v"(d) : "v"(voff), "s"(p) : "memory"); }
DI void pfl(unsigned& d, rsrc_t r, unsigned voff, unsigned soff) { asm volatile("buffer_load_dword %0, %1, %2, %3 offen" : "+v"(d) : "v"(voff), "s"(r), "s"(soff) : "memory"); }
template <class T> DI T sel(bool c, T a, T b) { return c ? a : b; }
DI void pfg(const char* p) { asm volatile("global_load_lds_dword %0, off" ::"v"(p) : "memory"); }
DI bool anyl(bool b) { return __builtin_amdgcn_ballot_w64(b) != 0; }
DI void ld128p(u32x4& d, rsrc_t r, unsigned voff, unsigned soff) { asm volatile("buffer_load_dwordx4 %0, %1, %2, %3 offen" : "=v"(d) : "v"(voff), "s"(r), "s"(soff) : "memory"); }
DI void sincos_f(float t, float& sn, float& cs) {
  const float k = __builtin_rintf(t * 0.63661977236f);
  float r = __builtin_fmaf(k, -1.5707962513f, t); r = __builtin_fmaf(k, -7.5497894159e-08f, r); r = __builtin_fmaf(k, -5.3903029534e-15f, r);
  const float z = r * r;
  const float sp = __builtin_fmaf(r * z, __builtin_fmaf(z, __builtin_fmaf(z, __builtin_fmaf(z, 2.7183114939e-06f, -1.9839334836e-04f), 8.3333293858e-03f), -1.6666666642e-01f), r);
  const float cp = __builtin_fmaf(z, __builtin_fmaf(z, __builtin_fmaf(z, __builtin_fmaf(z, 2.4390448796e-05f, -1.3886763775e-03f), 4.1666623324e-02f), -4.9999999726e-01f), 1.f);
  const int q = (int)k;
  const float a = (q & 1) ? cp : sp, b = (q & 1) ? sp : cp;
  sn = (q & 2) ? -a : a; cs = ((q + 1) & 2) ? -b : b;
}
DI void ld128a(u32x4& d, rsrc_t r, unsigned voff, unsigned soff) { asm volatile("buffer_load_dwordx4 %0, %1, %2, %3 offen sc1" : "=v"(d) : "v"(voff), "s"(r), "s"(soff) : "memory"); }

DI unsigned x1off(int a) { return a == 0 ? 131072u : (unsigned)(81920 + a * 16384); }
DI unsigned nq(unsigned s) { return (s + 32768u) & 131071u; }

DI int kb_of(int j, int bl) { return 7 * j + ((j & 1) ? (bl == 6 ? 0 : bl + 1) : bl); }

template <int NR, int LAST> constexpr int nread(int sl) {
  constexpr int DENSE = 2 * NR - LAST - 2;
  return sl < 0 ? 0 : sl < DENSE ? sl + 1 : sl >= LAST ? NR : DENSE + (sl - DENSE) / 2 + 1;
}
template <int N, int L, int X, bool FIRST, int NR, int LAST, int NB, int SL, class RD, class MM, class UP>
DI void slot(RD& rd, MM& mm, UP& upd) {
  if constexpr (!FIRST && SL >= L && (SL == L + 4 || (SL - L) % 8 == 0)) {
    __builtin_amdgcn_s_waitcnt(0xC07F | ((nread<NR, LAST>(SL - 1) - nread<NR, LAST>(SL - WD)) << 8)); SB();
  }
  if constexpr (SL >= L) mm(X, SL - L, SL); else if constexpr (!FIRST) mm(X ^ 1, SL + N - L, SL);
  SB();
  if constexpr (nread<NR, LAST>(SL) > nread<NR, LAST>(SL - 1)) rd(X, nread<NR, LAST>(SL) - 1);
  constexpr int per = NB > N - 1 - LAST ? 2 : 1, u0 = (SL - LAST - 1) * per;
  if constexpr (SL > LAST && u0 < NB) { upd(u0); if constexpr (per == 2 && u0 + 1 < NB) upd(u0 + 1); }
  SB();
}
template <int N, int L, int X, bool FIRST, int NR, int LAST, int NB, class RD, class MM, class UP, int... I>
DI void period_(RD& rd, MM& mm, UP& upd, std::integer_sequence<int, I...>) { (slot<N, L, X, FIRST, NR, LAST, NB, I>(rd, mm, upd), ...); }
template <int N, int L, int X, bool FIRST, int NR, int LAST, int NB, class RD, class MM, class UP>
DI void period(RD& rd, MM& mm, UP& upd) {
  static_assert(NR - 1 <= LAST && !(LAST & 1) && NB <= 2 * (N - 1 - LAST), "");
  period_<N, L, X, FIRST, NR, LAST, NB>(rd, mm, upd, std::make_integer_sequence<int, N>{});
  waitl0();
}
template <class G, class W, int... U> DI void ival_(G& g, W& wk, std::integer_sequence<int, U...>) { ((g(IC<U>{}), SB(), wk(IC<U>{}), SB()), ...); }
template <class G, class W> DI void ival(G&& g, W&& wk) { ival_(g, wk, std::make_integer_sequence<int, 8>{}); }
template <int N, class F, int... I> DI void sfor_(F& f, std::integer_sequence<int, I...>) { (f(IC<I>{}), ...); }
template <int N, class F> DI void sfor(F&& f) { sfor_<N>(f, std::make_integer_sequence<int, N>{}); }

DI void check_flags(unsigned& pv, rsrc_t ws, unsigned voff, unsigned soff, int lane, int lo, int hi, unsigned ep) {
  PIN(pv);
  const bool need = lane >= lo && lane < hi;
  if (__builtin_expect(anyl(need && pv != ep), 0)) {
    for (unsigned sp = 0; sp < SPIN_CAP; ++sp) {
      __builtin_amdgcn_s_sleep(2);
      ld32a(pv, ws, voff, soff); asm volatile("s_waitcnt vmcnt(0)" : "+v"(pv)::"memory");
      if (!anyl(need && pv != ep)) break;
    }
  }
}

#if defined(__HIP_DEVICE_COMPILE__) && __HIP_DEVICE_COMPILE__
__attribute__((target("no-packed-fp32-ops")))
#endif
__global__ __launch_bounds__(512, 2) void fused_kernel(const u16* x, const u16* lre, const u16* lim, const u16* bre, const u16* bim, const u16* cre, const u16* cim,
    const u16* ldt, const u16* dsk, const u16* w1, const u16* b1, const u16* w2, const u16* b2, const u16* gw, char* ws, float* out) {
  extern __shared__ __attribute__((aligned(16))) char shm[];
  unsigned long long pc0; asm volatile("s_getpc_b64 %0" : "=s"(pc0));
  const int lane = threadIdx.x & 63, w = __builtin_amdgcn_readfirstlane(threadIdx.x >> 6);
  const int bid = blockIdx.x, xcd = bid & 7, loc = bid >> 3;
  const int ci = (xcd & 1) * 8 + (loc & 7), cj = (xcd >> 1) * 4 + (loc >> 3);
  const int rot = (ci + cj) & ROTM, ntp = cj & 3, kq = cj >> 2, r2 = kq ^ 3, wq = w & 3, tq = threadIdx.x & 255;
  const rsrc_t rws = mk(ws, WS_BYTES), rx = mk(x, 2048 * 2048);
  const int wm = (w >> 1) & 1, wn = w & 1, g = lane >> 4, r15 = lane & 15, q2 = (lane >> 2) & 3, p = lane & 3, hl = lane >> 5, r31 = lane & 31;
  const unsigned flagb = FLAG_OFF + ci * 1024, fvo = (lane & 15) * 4;
  unsigned rgb = RG_OFF + ci * 8192; PINS(rgb);
  auto nowork = [&](auto) AI {};
  unsigned va[4], vb[4], vw[7], sl, sa, sb, sc, ta, tb, tc; int sx; rsrc_t ra, rb;
  auto vnat = [&](int u) AI { return (8u * u + (lane >> 3)) * 2048 + (((lane & 7) ^ ((4 * u + g) & 7)) * 16) - 1024 * u; };
  auto nexts = [&]() AI { sa = __builtin_amdgcn_readlane(ta, sx); sb = __builtin_amdgcn_readlane(tb, sx); ++sx; PINS(sa); PINS(sb); };
  auto G = [&](auto U) AI { constexpr int u = U.value;
    if constexpr (u == 0) setm0(sl + wq * 4096);
    if constexpr (u == 4) setm0(sl + 16384 + wq * 4096);
    if constexpr (u < 4) dma1<u * 1024>(ra, va[u], sa); else dma<(u - 4) * 1024>(rb, vb[u - 4], sb);
  };
  auto G2 = [&](auto S, auto U) AI { constexpr int a = S.value % 3, u = U.value;
    if constexpr (u == 0) setm0((a == 0 ? 131072u : 81920u + a * 16384u) + wq * 4096);
    if constexpr (u == 4) setm0(a * 32768 + wq * 7168);
    if constexpr (u == 8) setm0(a * 32768 + wq * 7168 + 4096);
    if constexpr (u < 4) dma<u * 1024>(rx, va[u], sa); else dma<(u & 3) * 1024>(rb, vw[u - 4], sb);
  };
  const int c7 = ci & 7;
  auto setup2 = [&]() AI {
#pragma unroll
    for (int q = 0; q < 6; ++q) { const int k = 8 * (q & 1) + (lane >> 3), f = ((k >> 1) & 1) | ((q & 1) << 1);
      vw[q] = 4096 + k * 7168 + ((cj & 1) ? 64 : 0) + (q >> 1) * 128 + (((((lane & 7) >> 1) ^ f) * 2) + (lane & 1)) * 16 - (q & 3) * 1024; }
    vw[6] = 4096 + (lane >> 2) * 7168 + ((cj & 1) ? 0 : 384) + (((((lane & 3) >> 1) ^ hl) * 2) + (lane & 1)) * 16 - 2048;
    int k = lane - 2 + (c7 < 2 ? 3 * c7 : c7 - 2); k = 2 + (k >= 6 ? k - 6 : k);
    k = lane < 2 ? lane : lane < 8 ? k : (lane ^ rot);
    k = (k ^ (ci & 8)) & 15;
    ta = (ci * 128 + 32 * wq) * 2048 + k * 128; tb = (k * 64 + 16 * wq) * 7168 + cj * 448;
    PIN2(ta, tb);
#pragma unroll
    for (int q = 0; q < 7; ++q) PIN(vw[q]);
  };
  auto G3 = [&](auto S, auto U) AI { constexpr int a = (S.value + 1) % 3, u = U.value;
    if constexpr (u == 0) setm0((a == 0 ? 131072u : 81920u + a * 16384u) + wq * 4096);
    if constexpr (u == 4 || u == 8) setm0(a * 32768 + wq * 8192 + (u - 4) * 1024);
    if constexpr (u < 4) dma1<u * 1024>(rws, va[0], sa); else dma<(u & 3) * 1024>(rb, vb[u & 3], u < 8 ? sb : sc);
  };
  auto table3 = [&](unsigned& ta, unsigned& tb, unsigned& tc) AI {
    const int ks = 8 * kq + ((lane ^ (2 * ntp)) & 7);
    const int t = lane - 8, lc = t >> 2;
    const int kb0 = lc < 3 ? kb_of(4 * kq + ((t ^ ntp) & 3), 2 * lc) : kb_of(4 * kq + (((2 * t) ^ ntp) & 3), 6);
    const int kb1 = lc < 3 ? kb0 + 1 : kb_of(4 * kq + (((2 * t + 1) ^ ntp) & 3), 6);
    const int crow = ntp * 256 + (wq >> 1) * 128, wcol = ntp * 512 + ((wq & 2) + ((wq ^ (r2 >> 1)) & 1)) * 128;
    ta = lane < 8 ? H_OFF + ci * H_PANEL + ks * 16384 + wq * 4096 : ci * HID_PANEL + (wq < 2 ? kb0 : kb1) * 8192 + (wq & 1) * 4096;
    tb = lane < 8 ? (crow + ((2 * (wq & 1)) ^ r2) * 32) * 2048 + (ks >> 1) * 128 + (ks & 1) : kb0 * 65536 + wcol;
    tc = lane < 8 ? (crow + ((2 * (wq & 1) + 1) ^ r2) * 32) * 2048 + (ks >> 1) * 128 : kb1 * 65536 + wcol;
    PIN2(ta, tb); PIN(tc);
  };
  auto next4 = [&]() AI { sc = __builtin_amdgcn_readlane(tc, sx); PINS(sc); nexts(); };
  auto next3 = [&]() AI { next4(); rb = mk(sel<const u16*>(sb & 1, cim, cre), 1024 * 2048); sb &= ~1u; };
  f32x4 fv[4]; unsigned fpp[4], fo; const char* outp;
  auto unp = [&](u32x2 v, int e) AI { return (e & 1) ? u2f(v[e >> 1] & 0xffff0000u) : bf2f(v[e >> 1] & 0xffffu); };
  auto presum = [&]() AI {
    const int c8 = lane & 7, sg = (lane >> 3) & 1, xw_ = (w >> 2) * 2 + sg;
    const f32x4 bv = rdf4(T_OUT + r15 * 16), dv = rdf4(T_OUT + 256 + r15 * 16);
#pragma unroll
    for (int u = 0; u < 4; ++u) {
      const int row = 4 * u + g;
      const f32x4 av = rdf4(65536 + xw_ * 8192 + (wq * 2 + (c8 >> 2)) * 1024 + (c8 & 3) * 256 + ((row ^ (8 * sg + c8)) * 16));
      const u32x2 xw = *(LAS u32x2*)(size_t)(114688 + (16 * w + row) * 128 + r15 * 8); const float gg = rdf(T_ROW + 1536 + (16 * w + row) * 4);
#pragma unroll
      for (int e = 0; e < 4; ++e) fv[u][e] = av[e] + __builtin_fmaf(gg, __builtin_fmaf(unp(xw, e), dv[e], -bv[e]), bv[e]);
      fpp[u] = xw_ * 4096 + wq * 1024 + (c8 & 3) * 256 + (c8 >> 2) * 8 + ((row ^ (8 * sg + 2 * (c8 & 3))) * 16);
      PIN2(fv[u], fpp[u]);
    }
    fo = (ci * 128 + 16 * w + g) * 4096 + ntp * 1024 + kq * 128 + sg * 512 + c8 * 16; PIN(fo);
    outp = karg(15);
  };

  if (w < 4) {
    constexpr bool isx = PROFX; (void)isx;
    unsigned epr, epc;
    const unsigned ca = (32 * wn + r31) * 8;
    const unsigned c0 = (32 * wn + r15) * 8;
    const int seg0 = 4 * wm + 2 * hl;
    f32x2 lam[2], cof[2], cfa, l16, l128, l1k, pw0, pw1; unsigned t3a, t3b;
    {
      setm0(T_DUM);
      const int row = bid * 8 + (lane & 7);
      const char* pp = (const char*)(hl ? (row < 1024 ? bre : bim - 1024 * 1024) : x) + (size_t)row * 2048 + (4 * wq + ((lane >> 3) & 3)) * 128;
      for (int q = 0; q <= wq; ++q) __builtin_amdgcn_s_sleep(6);
      pfg(pp);
      pfg((const char*)pc0 + (((loc * 4 + wq) % CSL) * 16 + (lane & 15)) * 128);
      ld32a(epr, rws, 0u, flagb + FK_EP * 128); ld32a(epc, rws, 0u, EPC_OFF + cj * 128);
      setup2(); sx = 2; nexts(); table3(t3a, t3b, tc);
      rb = mk((const char*)w1 - 4096, 1024 * 7168 + 4096);
#pragma unroll
      for (int q = 0; q < 4; ++q) { va[q] = vnat(q); PIN(va[q]); }
      wrf(T_ZERO + (lane & 3) * 4, 0.f);
    }
    {
      f32x4 acc[4][4];
#pragma unroll
      for (int a = 0; a < 16; ++a) { acc[a >> 2][a & 3] = (f32x4){0, 0, 0, 0}; PIN(acc[a >> 2][a & 3]); }
      bf16x8 wv[4][2];
      const int sstar = c7 < 2 ? c7 : 2;
      const int fsw = ((q2 >> 1) & 1) | ((g & 1) << 1);
      unsigned cf = sstar * 32768 + (g >> 1) * 7168 + (8 * (g & 1) + q2) * 128 + ((wq ^ fsw) * 32) + p * 8;
      unsigned ch = sstar * 32768 + (g >> 1) * 7168 + 6144 + (8 * (g & 1) + q2) * 64 + (((wq & 1) ^ (g & 1)) * 32) + p * 8;
      PIN(cf); PIN(ch);
      auto wread = [&](int st) AI {
        if (sstar == st) {
#pragma unroll
          for (int e = 0; e < 4; ++e)
#pragma unroll
            for (int t = 0; t < 4; ++t) sethalf(wv[t][e >> 1], e & 1, rtr((e >> 1) * 14336 + (t < 3 ? cf + t * 2048 + (e & 1) * 512 : ch + (e & 1) * 256)));
        } };
      {
        bf16x8 fa[4][2], fb[2][4][2];
        const unsigned n0 = r15 * 128 + ((g ^ (r15 >> 1)) * 16), n1 = r15 * 128 + (((4 + g) ^ (r15 >> 1)) * 16);
        const unsigned cb[4] = {wm * 8192u + n0, wm * 8192u + n1, 16384 + wn * 8192u + n0, 16384 + wn * 8192u + n1};
        unsigned ba[4], rs = 98304;
#pragma unroll
        for (int b = 0; b < 4; ++b) { ba[b] = rs + cb[b]; PIN(ba[b]); }
        auto rd = [&](int X, int r) AI { const int ks = r & 1, f = r >> 1;
          if (f >= 1 && f < 5) fb[X][f - 1][ks] = rd128(ba[2 + ks] + (f - 1) * 2048); else { const int a = f ? f - 4 : 0; fa[a][ks] = rd128(ba[ks] + a * 2048); } };
        auto mm = [&](int X, int q, int) AI { const int a = q >> 3, b = (q >> 1) & 3, ks = q & 1; MMA(acc[a][b], fa[a][ks], fb[X][b][ks]); };
        auto up = [&](int b) AI { if (b == 0) { rs = nq(rs); PINS(rs); } else { ba[b - 1] = rs + cb[b - 1]; PIN(ba[b - 1]); } };
        waitl0(); bar();
        period<32, 16, 0, true, 16, LASTR, 5>(rd, mm, up); bar();
        for (int s = 0; s < 7; ++s) { period<32, 16, 1, false, 16, LASTR, 5>(rd, mm, up); bar(); period<32, 16, 0, false, 16, LASTR, 5>(rd, mm, up); bar(); }
        period<32, 16, 1, false, 16, LASTR, 5>(rd, mm, up); bar();
        waitv<0>(); PIN(epr); PIN(epc);
        epr = __builtin_amdgcn_readfirstlane(epr) + 1u; epc = __builtin_amdgcn_readfirstlane(epc) + 1u;
        lam[0] = rdf2(T_CON + c0); lam[1] = rdf2(T_CON + c0 + 128);
        wread(0);
        sfor<7>([&](auto U) AI { G2(IC<2>{}, IC<U.value + 4>{}); SB();
#pragma unroll
          for (int q = 16 + (16 * U.value) / 7 / 2 * 2; q < 16 + (16 * (U.value + 1)) / 7 / 2 * 2; ++q) mm(1, q, 0);
          SB(); });
        nexts();
      }
      f32x2 st[2] = {{0, 0}, {0, 0}}, Pf[2] = {{0, 0}, {0, 0}}, Gt = {0, 0}, cr = {0, 0}, ag[8];
      u32x4 lb[8], cg[8], rgv[2]; unsigned lbo[8], lbe[8], pv, gd = 0; unsigned obs[2][2][2][4];
      f32x4 cs[4]; f32x2 so[2]; float c0s = 0.f, c1s = 0.f;
      const rsrc_t rgr = mk(ws + GRAN_OFF + cj * 16384, 16384);
      const int cp = 28 * wq + (r31 < 28 ? r31 : 27);
      const unsigned cgo = hl * 16384 + cp * 16;
#define OB(a, b, c) ((u32x4){obs[a][b][c][0], obs[a][b][c][1], obs[a][b][c][2], obs[a][b][c][3]})
      auto hofs = [&](int hf, int mt) AI { return (2u * wn + mt) * 4096 + wm * 2048 + hf * 1024 + g * 256 + ((r15 ^ (hf << 2)) * 16); };
      auto bad = [&]() AI { unsigned d = 0;
#pragma unroll
        for (int q = 0; q < 8; ++q) { PIN(lb[q]); d |= (int)lbo[q] < 0 ? 0u : ((lb[q][1] ^ epc) | (lb[q][3] ^ epc)); }
        return d != 0; };
      auto gbad = [&]() AI { unsigned d = 0;
#pragma unroll
        for (int q = 0; q < 8; ++q) { PIN(cg[q]); d |= (cg[q][1] ^ epc) | (cg[q][3] ^ epc); }
        return d != 0; };
      auto E2 = [](int s) constexpr { return (s == 2 || s == 3 || s == WBI) ? 1 : 0; };
      auto T2 = [](int s) constexpr { return s == 4 ? 8 : (s == 7 || s == 8) ? 4 : s == 11 ? 10 : 0; };
      sfor<15>([&](auto S) AI { constexpr int s = S.value;
        if constexpr (s < 3) waitl0();
        if constexpr (s == 6) waitv<11>();
        else if constexpr (s >= 1) waitv<T2(s - 2) + (s < 14 ? 11 : 8) + E2(s - 1) + T2(s - 1)>();
        bar();
        if constexpr (s < 2) wread(s + 1);
        if constexpr (s == 2) {
#pragma unroll
          for (int sg = 0; sg < 8; ++sg) ag[sg] = rdf2(T_AGG + sg * 512 + ca);
          l16 = rdf2(T_CON + 1024 + ca); cof[0] = rdf2(T_CON + 512 + c0); cof[1] = rdf2(T_CON + 512 + c0 + 128);
        }
        if constexpr (s == 5) { pw1 = rdf2(T_CON + 1536 + ca); pw0 = rdf2(T_CON + 2048 + ca); l128 = rdf2(T_CON + 2560 + ca); l1k = rdf2(T_CON + 3072 + ca); cfa = rdf2(T_CON + 512 + ca); }
        if constexpr (s == 11) { if (w == 0) stflag(rws, epr, lane, flagb + FK_H * 128 + cj * 4); }
        if constexpr (s == WBI) { setm0(T_DUM); pfg((const char*)w2 + ((bid * 4 + wq) * 56 + (lane + WA < 56 ? lane + WA : 55)) * 128); }
        if constexpr (s == 14) { if (w == 0) ld32a(pv, rws, fvo, flagb + FK_H * 128); }
        SB();
        auto work = [&](auto U) AI { constexpr int u = U.value;
          if constexpr (s < 2 && u >= 1 && u < 9) {
            constexpr int t = 8 * s + u - 1;
#pragma unroll
            for (int mt = 0; mt < 2; ++mt) st[mt] = cfma(lam[mt], st[mt], (f32x2){acc[t >> 2][mt][t & 3], acc[t >> 2][2 + mt][t & 3]});
            PIN2(st[0], st[1]);
          }
          if constexpr (s == 1 && u == 9) { wrf2(T_AGG + (4 * wm + g) * 512 + c0, st[0]); wrf2(T_AGG + (4 * wm + g) * 512 + c0 + 128, st[1]); }
          if constexpr (s == 2 && u >= 1 && u < 9) {
            constexpr int sg = u - 1, mt = sg & 1; const bool me = sg == seg0 + mt;
            Pf[mt].x = sel<float>(me, Gt.x, Pf[mt].x); Pf[mt].y = sel<float>(me, Gt.y, Pf[mt].y); Gt = cfma(l16, Gt, ag[sg]);
            PIN2(Gt, Pf[mt]);
          }
          if constexpr (s == 2 && u == 9) st64(rgr, (u32x2){f2u(sel<float>(hl, Gt.y, Gt.x)), epc}, ca * 2 + hl * 8, ci * 1024);
          if constexpr (s == 3 && u < 4) {
            const bf16x8 one = __builtin_bit_cast(bf16x8, (u32x4){0x3F803F80u, 0x3F803F80u, 0x3F803F80u, 0x3F803F80u});
            cs[u] = MFMA(one, wv[u][1], MFMA(one, wv[u][0], ((f32x4){0, 0, 0, 0}))); PIN(cs[u]);
          }
          if constexpr (s == 3 && u == 8) { PIN2(cs[0], cs[1]); PIN2(cs[2], cs[3]); }
          if constexpr (s == 3 && u == 9)
            st64(rws, (u32x2){f2u(sel4(g & 1, g & 2, cs[0][0], cs[1][0], cs[2][0], cs[3][0])), epc}, (g < 3 ? g * 64 + wq * 16 + r15 : 192 + (wq & 1) * 16 + r15) * 8u, CG_OFF + (cj * 16 + ci) * 2048);
          if constexpr ((s == 3 && u >= 4 && u < 9) || (s == 4 && u < 5) || (s == 5 && u >= 1 && u < 7)) {
            constexpr int t = s == 3 ? u - 4 : s == 4 ? 5 + u : 9 + u;
            f32x2 v0 = cmulc(cof[0], (f32x2){acc[t >> 2][0][t & 3], acc[t >> 2][2][t & 3]}), v1 = cmulc(cof[1], (f32x2){acc[t >> 2][1][t & 3], acc[t >> 2][3][t & 3]}); PIN2(v0, v1);
            acc[t >> 2][0][t & 3] = v0.x; acc[t >> 2][2][t & 3] = v0.y; acc[t >> 2][1][t & 3] = v1.x; acc[t >> 2][3][t & 3] = v1.y;
          }
          if constexpr (s == 4 && (u == 6 || u == 8)) {
#pragma unroll
            for (int q = 2 * u - 12; q < 2 * u - 8; ++q) { lbo[q] = (ci - 8 - 8 * hl + q) * 1024 + ca * 2; PIN(lbo[q]); }
          }
          if constexpr (s == 4 && u == 10) {
#pragma unroll
            for (int q = 0; q < 8; ++q) ld128a(lb[q], rgr, lbo[q], 0u);
          }
          if constexpr (s == 5 && u == 8) {
            if (!wm) pw0 = (f32x2){1.f, 0.f};
            if (ci <= 8) l1k = (f32x2){0.f, 0.f};
            pw0 = cmul(pw0, (f32x2){sel<float>(hl, pw1.x, 1.f), sel<float>(hl, pw1.y, 0.f)}); pw1 = cmul(pw0, l16); PIN2(pw0, pw1);
          }
          if constexpr (s == 5 && (u == 7 || u == 9)) {
#pragma unroll
            for (int q = 2 * u - 14; q < 2 * u - 10; ++q) { lbe[q] = (int)lbo[q] < 0 ? 0u : epc; PIN(lbe[q]); }
          }
          if constexpr (s == 6 && u < 8) {
            PIN(lb[u]); gd |= (lb[u][1] ^ lbe[u]) | (lb[u][3] ^ lbe[u]); cr = cfma(l128, cr, (f32x2){u2f(lb[u][0]), u2f(lb[u][2])});
            PIN2(cr, gd);
          }
          if constexpr (s == 6 && u == 8) {
            if (__builtin_expect(anyl(gd != 0), 0)) {
              for (unsigned sp = 0; sp < SPIN_CAP; ++sp) {
                __builtin_amdgcn_s_sleep(2);
#pragma unroll
                for (int q = 0; q < 8; ++q) ld128a(lb[q], rgr, lbo[q], 0u);
                waitv<0>();
                if (!anyl(bad())) break;
              }
              cr = (f32x2){0, 0};
#pragma unroll
              for (int q = 0; q < 8; ++q) cr = cfma(l128, cr, (f32x2){u2f(lb[q][0]), u2f(lb[q][2])});
            }
            gd = 0;
            const auto sx_ = __builtin_amdgcn_permlane32_swap(f2u(cr.x), f2u(cr.x), false, false), sy_ = __builtin_amdgcn_permlane32_swap(f2u(cr.y), f2u(cr.y), false, false);
            cr = cfma(l1k, (f32x2){u2f(sx_[1]), u2f(sy_[1])}, (f32x2){u2f(sx_[0]), u2f(sy_[0])});
            PIN(cr);
          }
          if constexpr (s == 6 && u == 9) { st[0] = cmulc(cfa, cfma(pw0, cr, Pf[0])); PIN(st[0]); }
          if constexpr (s == 7 && u == 0) { st[1] = cmulc(cfa, cfma(pw1, cr, Pf[1]));
            const auto ex = __builtin_amdgcn_permlane16_swap(f2u(st[0].x), f2u(st[1].x), false, false), ey = __builtin_amdgcn_permlane16_swap(f2u(st[0].y), f2u(st[1].y), false, false);
            st[0] = (f32x2){u2f(ex[0]), u2f(ey[0])}; st[1] = (f32x2){u2f(ex[1]), u2f(ey[1])}; PIN2(st[0], st[1]); }
          if constexpr ((s == 7 && u >= 2 && u < 10) || (s == 8 && u >= 1 && u < 9)) {
            constexpr int t = s == 7 ? u - 2 : 7 + u;
            if constexpr (t & 1) { so[0] = st[0]; so[1] = st[1]; }
#pragma unroll
            for (int mt = 0; mt < 2; ++mt) st[mt] = cfma((f32x2){lam[mt].x, -lam[mt].y}, st[mt], (f32x2){acc[t >> 2][mt][t & 3], acc[t >> 2][2 + mt][t & 3]});
            if constexpr (t & 1) {
#pragma unroll
              for (int mt = 0; mt < 2; ++mt) { obs[t >> 3][mt][0][(t >> 1) & 3] = pk2(so[mt].x, st[mt].x); obs[t >> 3][mt][1][(t >> 1) & 3] = pk2(so[mt].y, st[mt].y); PIN2(obs[t >> 3][mt][0][(t >> 1) & 3], obs[t >> 3][mt][1][(t >> 1) & 3]); }
            }
            PIN2(st[0], st[1]);
          }
          if constexpr ((s == 7 || s == 8) && u == 10) {
#pragma unroll
            for (int q = 0; q < 4; ++q) st128(rws, OB(s - 7, q >> 1, q & 1), hofs(s - 7, q >> 1), H_OFF + ci * H_PANEL + (2 * cj + (q & 1)) * 16384);
          }
          if constexpr (s >= 13 && u < 4) *(LAS u32x4*)(size_t)(98304 + (s - 13) * 16384 + hofs(u >> 1, u & 1)) = OB(u >> 1, u & 1, s - 13);
          if constexpr (s == 9 && u == 0) { if (ci == 15 && w == 1) stflag(rws, epc, lane, EPC_OFF + cj * 128); }
          if constexpr (s == 14 && u == 5) {
            auto rbad = [&]() AI { PIN2(rgv[0], rgv[1]); return ((rgv[0][1] ^ epr) | (rgv[0][3] ^ epr) | (rgv[1][1] ^ epr) | (rgv[1][3] ^ epr)) != 0; };
            if (__builtin_expect(anyl(rbad()), 0))
              for (unsigned sp = 0; sp < SPIN_CAP; ++sp) { __builtin_amdgcn_s_sleep(2);
                ld128a(rgv[0], rws, tq * 16u, rgb); ld128a(rgv[1], rws, tq * 16u + 4096, rgb);
                waitv<0>(); if (!anyl(rbad())) break; }
          }
          if constexpr (s == 14 && u == 7) {
            wrf(T_ROW + (tq & 1) * 1024 + (tq >> 1) * 4, u2f(rgv[0][0])); wrf(T_ROW + (tq & 1) * 1024 + 512 + (tq >> 1) * 4, u2f(rgv[0][2]));
            wrf2(T_XA + tq * 8, (f32x2){u2f(rgv[1][0]), u2f(rgv[1][2])});
          }
          if constexpr (s == 11 && u == 10) {
#pragma unroll
            for (int q = 0; q < 8; ++q) ld128a(cg[q], rws, cgo, CG_OFF + (cj * 16 + q) * 2048);
            ld128a(rgv[0], rws, tq * 16u, rgb); ld128a(rgv[1], rws, tq * 16u + 4096, rgb);
          }
          if constexpr (s == 14 && u < 4) {
#pragma unroll
            for (int q = 2 * u; q < 2 * u + 2; ++q) { PIN(cg[q]); gd |= (cg[q][1] ^ epc) | (cg[q][3] ^ epc); c0s += u2f(cg[q][0]); c1s += u2f(cg[q][2]); }
            PIN(gd); PIN(c0s); PIN(c1s);
          }
          if constexpr (s == 14 && u == 4) {
            if (__builtin_expect(anyl(gd != 0), 0)) {
              for (unsigned sp = 0; sp < SPIN_CAP; ++sp) { __builtin_amdgcn_s_sleep(2);
#pragma unroll
                for (int q = 0; q < 8; ++q) ld128a(cg[q], rws, cgo, CG_OFF + (cj * 16 + q) * 2048);
                waitv<0>(); if (!anyl(gbad())) break; }
              c0s = 0.f; c1s = 0.f;
#pragma unroll
              for (int q = 0; q < 8; ++q) { c0s += u2f(cg[q][0]); c1s += u2f(cg[q][2]); }
            }
            c0s = sum32(c0s); c1s = sum32(c1s); PIN(c0s); PIN(c1s);
          }
          if constexpr (s == 14 && u == 6) {
            const float v = sel<float>(hl, c1s, c0s), hi = rbf(v), bb = rdf(T_B1 + (2 * cp + hl) * 4);
            *(LAS u32x4*)(size_t)(T_WA + (2 * cp + hl) * 16) = (u32x4){pk2(hi, v - hi), pk2(hi, bb), pk2(bb, 0.f), 0u};
          }
        };
        if constexpr (s < 13) { sfor<11>([&](auto U) AI { G2(IC<s + 3>{}, U); SB(); work(U); SB(); }); if constexpr (s < 12) nexts(); }
        if constexpr (s == 12) {
#pragma unroll
          for (int q = 0; q < 4; ++q) vb[q] = va[q];
          ta = t3a; tb = t3b; sx = 0; next3(); }
        if constexpr (s >= 13) { sfor<8>([&](auto U) AI { G3(IC<s - 13>{}, IC<U.value + 4>{}); SB(); work(U); SB(); }); next3(); }
      });
      waitl0(); waitv<8>();
      if (w == 0) check_flags(pv, rws, fvo, flagb + FK_H * 128, lane, 4 * kq, 4 * kq + 4, epr);
      bar();
    }
    f32x4 acc[4][8];
    bf16x8 fs[8][2], fr[2][4][2];
    const int hf = p >> 1, fw = 2 * (g & 1) + (q2 >> 1);
    const unsigned hb = 98304 + (g >> 1) * 4096 + wm * 2048 + hf * 1024 + (8 * (g & 1) + q2) * 16 + (p & 1) * 8, wb = wn * 16384 + (4 * g + q2) * 128 + p * 8;
    unsigned b3[4] = {hb + 64u * hf, hb + 64u * (1 ^ hf), 32768 + wn * 16384u + r15 * 128 + ((g ^ (r15 >> 1)) * 16), 32768 + wn * 16384u + r15 * 128 + (((4 + g) ^ (r15 >> 1)) * 16)};
    unsigned b4[5];
    unsigned a3 = 1, dA, dW;
    auto rd3 = [&](int X, int r) AI {
      if (r >= 2 && r < 18) { const int a = (r - 2) >> 2, ks = ((r - 2) >> 1) & 1, h = r & 1; sethalf(fr[X][a][ks], h, rtr(b3[h] + ks * 8192 + a * 256)); }
      else { const int b = r < 2 ? 0 : (r - 16) >> 1, ks = r & 1; fs[b][ks] = rd128(b3[2 + ks] + b * 2048); } };
    auto rd4 = [&](int X, int r) AI {
      if (r >= 4 && r < 12) fr[X][(r - 4) >> 1][r & 1] = rd128(b4[0] + (r & 1) * 8192 + ((r - 4) >> 1) * 1024);
      else { const int b = r < 4 ? 0 : (r - 8) >> 2, ks = (r >> 1) & 1, h = r & 1; sethalf(fs[b][ks], h, rtr(b4[1 + (b & 3)] + (b >> 2) * 8192 + ks * 4096 + h * 2048)); } };
    auto mm = [&](int X, int q, int SL) AI { const int b = q >> 3, a = (q >> 1) & 3, ks = q & 1;
      if (SL < 0) acc[a][b] = MFMA(fs[b][ks], fr[X][a][ks], acc[a][b]); else MMA(acc[a][b], fs[b][ks], fr[X][a][ks]); };
    auto mmz = [&](int X, int q, int SL) AI { if (X == 0 && !(q & 1)) MMZ(acc[(q >> 1) & 3][q >> 3], fs[q >> 3][0], fr[0][(q >> 1) & 3][0]); else mm(X, q, SL); };
    auto adv = [&]() AI { dA = a3 == 0 ? -32768 : 16384; dW = a3 == 2 ? -65536 : 32768; a3 = a3 == 2 ? 0 : a3 + 1; PINS(dA); PINS(dW); PINS(a3); };
    auto up3 = [&](int b) AI { if (b == 0) adv(); else { b3[b - 1] += sel<unsigned>(b < 3, dA, dW); PIN(b3[b - 1]); } };
    auto up4 = [&](int b) AI { if (b == 0) adv(); else { b4[b - 1] += sel<unsigned>(b < 2, dA, dW); PIN(b4[b - 1]); } };
    constexpr int L3 = 18;
#pragma unroll
    for (int b = 0; b < 4; ++b) PIN(b3[b]);
    period<64, L3, 0, true, 32, 44, 5>(rd3, mmz, up3); waitv<0>(); bar();
    period<64, L3, 1, false, 32, 44, 5>(rd3, mmz, up3); bar();
    for (int s = 0; s < 3; ++s) { period<64, L3, 0, false, 32, 44, 5>(rd3, mm, up3); bar(); period<64, L3, 1, false, 32, 44, 5>(rd3, mm, up3);
      if (s == 2) break;
      bar(); }
    float gr[4];
#pragma unroll
    for (int a = 0; a < 4; ++a) gr[a] = rdf(T_ROW + 1536 + (64 * wm + 16 * a + r15) * 4);
    b4[0] = 131072 + wm * 4096 + lane * 16;
#pragma unroll
    for (int b = 0; b < 4; ++b) b4[1 + b] = wb + ((b ^ fw) * 32);
#pragma unroll
    for (int b = 0; b < 5; ++b) PIN(b4[b]);
    waitl0(); bar();
    {
      auto mmg = [&](int X, int q, int SL) AI { mm(X, q, SL); const int a = (SL >> 1) & 3, b = SL >> 3, e = 2 * (SL & 1); SB();
        acc[a][b][e] *= gr[a]; acc[a][b][e + 1] *= gr[a]; PIN(acc[a][b]); };
      period<64, L3, 0, false, 40, 56, 6>(rd4, mmg, up4); bar();
    }
    for (int s = 0; s < 6; ++s) { period<64, L3, 1, false, 40, 56, 6>(rd4, mm, up4); bar(); period<64, L3, 0, false, 40, 56, 6>(rd4, mm, up4); bar(); }
    const unsigned po = wq * 4096 + g * 256 + ((r15 ^ (8 * wn + 2 * g)) * 16);
    auto send = [&](int qd, int a) AI {
      st128(rws, (u32x4){pk2(acc[a][2 * qd][0], acc[a][2 * qd][1]), pk2(acc[a][2 * qd][2], acc[a][2 * qd][3]), pk2(acc[a][2 * qd + 1][0], acc[a][2 * qd + 1][1]), pk2(acc[a][2 * qd + 1][2], acc[a][2 * qd + 1][3])},
            po + a * 1024, PART_OFF + (ci * 16 + (qd ^ r2) * 4 + ntp) * 49152 + qd * 16384); };
    {
      auto mms = [&](int X, int q, int SL) AI { mm(X, q, SL); const int d = SL - L3 - 20;
        if (d >= 0 && (d & 1) == 1 && (d & 15) < 8) { SB(); send(d >> 4, (d & 15) >> 1); } };
      period<64, L3, 1, false, 40, 56, 0>(rd4, mms, up4); bar();
    }
    MPAD();
    auto sig = [&](int qd) AI { stflag(rws, epr, lane, PF_OFF + (ci * 16 + (qd ^ r2) * 4 + ntp) * 256 + wq * 64 + qd * 4); };
    sfor<11>([&](auto M) AI { constexpr int m = M.value;
      if constexpr (m < 9) { mm(1, 46 + 2 * m, -1); mm(1, 47 + 2 * m, -1); }
      SB();
      if constexpr (m < 4) send(2, m);
      if constexpr (m >= 7) {
#pragma unroll
        for (int q = 2 * (m - 7); q < 2 * (m - 7) + 2; ++q) { const int a = q & 3, bb = q >> 2;
          *(LAS f32x4*)(size_t)(65536 + wq * 8192 + (a * 2 + bb) * 1024 + g * 256 + ((r15 ^ (8 * wn + 4 * bb + g)) * 16)) = acc[a][6 + bb]; }
      }
      if constexpr (m == 5) { waitv<8>(); sig(0); }
      SB(); });
    waitv<5>(); sig(1);
    waitl0(); bar();
    waitv<2>(); sig(2);
    presum();
    bar();
  } else {
    constexpr bool isx = !PROFX; (void)isx;
    unsigned epr, pv, t3a, t3b, cb[8], ba[8]; const char* pp0;
    f32x4 ac1[28];
    {
      u32x4 xr[2][2], gq[2];
      ra = rx; rb = mk(sel<const u16*>(wq & 1, bim, bre), 1024 * 2048);
#pragma unroll
      for (int q = 0; q < 4; ++q) { va[q] = (32 * (q & 1) + 16 * hl + 4 * (q >> 1) + ((lane >> 3) & 3)) * 2048 + (((lane & 7) ^ ((4 * q + g) & 7)) * 16) - 1024 * q; vb[q] = vnat(q); }
      auto src1 = [&](int s) AI { const int k = s ^ (s < 2 ? 0 : s < 4 ? (rot & 1) : rot); sa = (ci * 128 + 64 * (wq >> 1) + 8 * (wq & 1)) * 2048 + k * 128; sb = (cj * 64 + 32 * (wq >> 1)) * 2048 + k * 128; sl = ((s + 3) & 3) * 32768; };
      src1(0); ival(G, nowork);
      SB();
      setup2(); table3(t3a, t3b, tc);
      {
        const int f = ((q2 >> 1) & 1) | ((g & 1) << 1);
        const unsigned nb = wm * 8192u + r15 * 128, n0 = (g ^ (r15 >> 1)) * 16, n1 = ((4 + g) ^ (r15 >> 1)) * 16, wk = (g >> 1) * 7168u + p * 8;
        cb[0] = nb + wn * 4096 + n0; cb[1] = nb + wn * 4096 + n1; cb[2] = nb + (1 - wn) * 4096 + n0; cb[3] = nb + (1 - wn) * 4096 + n1;
        cb[4] = wk + (8 * (g & 1) + q2) * 128 + (((2 * wn) ^ f) * 32); cb[5] = wk + (8 * (g & 1) + q2) * 128 + (((2 * wn + 1) ^ f) * 32);
        cb[6] = wk + 6144 + (8 * (g & 1) + q2) * 64 + (g & 1) * 32; cb[7] = wk + 6144 + (8 * (g & 1) + q2) * 64 + ((g & 1) ^ 1) * 32;
#pragma unroll
        for (int b = 0; b < 8; ++b) { ba[b] = cb[b] + (b < 4 ? 131072u : 0u); PIN2(cb[b], ba[b]); }
        pp0 = lane < 32 ? (const char*)sel<const u16*>(lane & 16, cim, cre) + ((bid * 4 + wq) * 16 + r15) * 128 : (const char*)w2 + ((bid * 4 + wq) * 56 + (r31 < WA ? r31 : 0)) * 128;
        PIN(pp0);
#pragma unroll
        for (int m = 0; m < 28; ++m) { ac1[m] = (f32x4){0, 0, 0, 0}; PIN(ac1[m]); }
      }
      src1(1); ival(G, nowork);
      waitv<8>(); bar();
      unsigned pdt, plr, pli, bq, oq; f32x2 lp; float lr, li;
      const int tc = tq < 224 ? tq : 223;
      ld32a(epr, rws, 0u, flagb + FK_EP * 128); ld128g(gq[0], gw, lane * 16u); ld128g(gq[1], gw + 512, lane * 16u);
      ld16p(pdt, ldt + 64 * cj, lane * 2u); ld16p(plr, lre + 64 * cj, lane * 2u); ld16p(pli, lim + 64 * cj, lane * 2u);
      ld16p(bq, b1, (224 * cj + (tc < 192 ? ((cj & 1) ? 32 : 0) + tc : ((cj & 1) ? -192 : 0) + tc)) * 2u);
      ld16p(oq, sel<const u16*>(wq & 1, dsk, b2), (ntp * 256 + hl * 128 + kq * 32 + r31) * 2u);
      PH(1);
      src1(2); ival(G, nowork);
      src1(3); ival(G, nowork);
      float sv[6] = {0, 0, 0, 0, 0, 0}, a1, a2, a3, mean, sd, rstd, qv, gg, kq, rr;
      auto E1 = [](int s) constexpr { return (s == 1 || s == 2) ? 2 : s == 11 ? 1 : 0; };
      sfor<12>([&](auto S) AI { constexpr int s = S.value;
        if constexpr (s == 0) waitv<16 + 8>(); else waitv<16 + E1(s - 2) + E1(s - 1)>();
        bar();
        src1(s + 4); ival(G, [&](auto U) AI { constexpr int u = U.value;
          if constexpr ((s == 1 || s == 2) && (u == 2 || u == 5)) ld128p(xr[s - 1][u / 3], rx, lane * 16u, (ci * 128 + 8 * cj + 2 * wq + s - 1) * 2048 + (u / 3) * 1024);
          if constexpr (s == 1 && u == 0) { PIN2(pdt, plr); PIN(pli); lr = bf2f(plr); li = bf2f(pli); const float dt = rbf(fexp(bf2f(pdt))); lp = (f32x2){fexp(dt * lr), dt * li}; PIN(lp); }
          if constexpr (s == 1 && u == 3) { PIN2(bq, oq); wrf(T_B1 + tq * 4, bf2f(bq)); wrf(T_OUT + tq * 4, bf2f(oq)); PIN(epr); epr = __builtin_amdgcn_readfirstlane(epr) + 1u; }
          if constexpr (s == 1 && u == 6) { kq = __builtin_rintf(lp.y * 0.63661977236f);
            rr = __builtin_fmaf(kq, -1.5707962513f, lp.y); rr = __builtin_fmaf(kq, -7.5497894159e-08f, rr); rr = __builtin_fmaf(kq, -5.3903029534e-15f, rr); PIN2(kq, rr); }
          if constexpr (s == 2 && u == 0) { const float z = rr * rr;
            a1 = __builtin_fmaf(rr * z, __builtin_fmaf(z, __builtin_fmaf(z, __builtin_fmaf(z, 2.7183114939e-06f, -1.9839334836e-04f), 8.3333293858e-03f), -1.6666666642e-01f), rr); PIN(a1); }
          if constexpr (s == 2 && u == 3) { const float z = rr * rr;
            a2 = __builtin_fmaf(z, __builtin_fmaf(z, __builtin_fmaf(z, __builtin_fmaf(z, 2.4390448796e-05f, -1.3886763775e-03f), 4.1666623324e-02f), -4.9999999726e-01f), 1.f); PIN(a2); }
          if constexpr (s == 2 && u == 6) { const int q = (int)kq; const float sa_ = sel<float>(q & 1, a2, a1), ca_ = sel<float>(q & 1, a1, a2);
            lp = (f32x2){lp.x * (((q + 1) & 2) ? -ca_ : ca_), lp.x * ((q & 2) ? -sa_ : sa_)}; PIN(lp); wrf2(T_CON + lane * 8, lp); }
          if constexpr (s == 3 && u == 0) { const float nr = lp.x - 1.f, ni = lp.y, den = 2.f * __builtin_amdgcn_rcpf(lr * lr + li * li);
                                            wrf2(T_CON + 512 + lane * 8, (f32x2){(nr * lr + ni * li) * den, (ni * lr - nr * li) * den}); }
          if constexpr ((s == 3 && u >= 2) || (s == 4 && u >= 4)) { constexpr int q = s == 3 ? u - 2 : u + 2; lp = cmul(lp, lp); PIN(lp);
            if constexpr (q >= 3 && q < 7) wrf2(T_CON + (q - 1) * 512 + lane * 8, lp);
            if constexpr (q == 9) wrf2(T_CON + 3072 + lane * 8, lp); }
          if constexpr ((s == 4 && u < 2) || (s >= 5 && s < 9 && !(u & 1) && 4 * (s - 5) + 2 + (u >> 1) < 16)) {
            constexpr int id = s == 4 ? u : 4 * (s - 5) + 2 + (u >> 1), r = id >> 3, e = id & 7;
            PIN2(xr[r][e >> 2], gq[e >> 2]);
            const unsigned v = xr[r][e >> 2][e & 3], gv = gq[e >> 2][e & 3];
            const float x0 = bf2f(v & 0xffffu), x1 = u2f(v & 0xffff0000u);
            sv[r] += x0 + x1; sv[2 + r] = __builtin_fmaf(x1, x1, __builtin_fmaf(x0, x0, sv[2 + r]));
            sv[4 + r] = __builtin_fmaf(x1, u2f(gv & 0xffff0000u), __builtin_fmaf(x0, bf2f(gv & 0xffffu), sv[4 + r]));
            PIN2(sv[r], sv[2 + r]); PIN(sv[4 + r]);
          }
          if constexpr (s == 9 && u < 4) {
#pragma unroll
            for (int q = 0; q < 6; ++q) sv[q] = u == 0 ? dppadd<0xB1>(sv[q]) : u == 1 ? dppadd<0x4E>(sv[q]) : u == 2 ? dppadd<0x141>(sv[q]) : dppadd<0x140>(sv[q]);
            PIN2(sv[0], sv[1]); PIN2(sv[2], sv[3]); PIN2(sv[4], sv[5]);
          }
          if constexpr (s == 9 && u == 5) { const bool r1 = lane & 4; a1 = sum16(sel<float>(r1, sv[1], sv[0])); a2 = sum16(sel<float>(r1, sv[3], sv[2])); a3 = sum16(sel<float>(r1, sv[5], sv[4])); PIN2(a1, a2); PIN(a3); }
          if constexpr (s == 10 && u == 0) { a1 = sum32(a1); a2 = sum32(a2); a3 = sum32(a3); PIN2(a1, a2); PIN(a3); }
          if constexpr (s == 10 && u == 2) { mean = a1 * (1.f / 1024.f); const float var = __builtin_fmaxf(a2 * (1.f / 1024.f) - mean * mean, 0.f) + 1e-5f; rstd = __builtin_amdgcn_rsqf(var); sd = var * rstd;
                                             PIN2(mean, rstd); PIN(sd); }
          if constexpr (s == 10 && u == 4) { qv = 1.f + fexp(__builtin_fminf(a3, 60.f)); gg = __builtin_amdgcn_rcpf(1.f + fexp(__builtin_fminf(-a3, 80.f))); PIN2(qv, gg); }
          if constexpr (s == 11 && u == 0) { a1 = sel4(lane & 1, lane & 2, -2.302208198f * rstd, -0.1029432397f * rstd * rstd * rstd, qv * sd, gg); PIN(a1); }
          if constexpr (s == 11 && u == 2) { const float mh = rbf(mean), sh = rbf(sd);
                                             a2 = sel4(lane & 1, lane & 2, u2f(pk2(-mh, -mh)), u2f(pk2(mh - mean, sh)), u2f(pk2(sd - sh, 0.f)), 0.f); PIN(a2); }
          if constexpr (s == 11 && u == 5) { if (lane < 16) st64(rws, (u32x2){f2u(sel<float>(lane & 8, a2, a1)), epr}, (lane >> 3) * 4096u + (lane & 7) * 8, rgb + (8 * cj + 2 * wq) * 32); }
        });
      });
      rb = mk((const char*)w1 - 4096, 1024 * 7168 + 4096);
#pragma unroll
      for (int q = 0; q < 4; ++q) va[q] = vb[q];
      sx = 0; nexts();
      const unsigned sb0 = sb, sa0 = sa; nexts();
      const unsigned sb1 = sb, sa1 = sa; nexts();
      const unsigned sa2 = sa;
      waitv<16 + 1>(); bar();
      setm0(T_DUM); pfg(pp0);
      sa = sa0; sfor<4>([&](auto U) AI { G2(IC<0>{}, U); });
      sa = sa1; sfor<4>([&](auto U) AI { G2(IC<1>{}, U); });
      waitv<16 + 2>(); bar();
      sb = sb0; sfor<7>([&](auto U) AI { G2(IC<0>{}, IC<U.value + 4>{}); });
      sa = sa2; sfor<4>([&](auto U) AI { G2(IC<2>{}, U); });
      waitv<20>(); bar();
      sb = sb1; sfor<7>([&](auto U) AI { G2(IC<1>{}, IC<U.value + 4>{}); });
      ta = t3a; tb = t3b; sx = 2; next3();
    }
    {
      {
        bf16x8 fa[2][4][2], fb[8][2];
        unsigned xn = 98304, wnx = 32768;
        setm0(T_DUM);
        auto rd = [&](int X, int r) AI {
          if (r >= 4 && r < 12) { const int a = (r - 4) >> 1, ks = r & 1; fa[X][a][ks] = rd128(ba[(a >> 1) * 2 + ks] + (a & 1) * 2048); }
          else { const int b = r < 4 ? 0 : (r - 8) >> 2, ks = (r >> 1) & 1, h = r & 1;
            sethalf(fb[b][ks], h, rtr(b < 6 ? ba[4 + (b & 1)] + (b >> 1) * 2048 + ks * 14336 + h * 512 : ba[b] + ks * 14336 + h * 256)); } };
        auto mm = [&](int X, int q, int) AI { const int b = q < 48 ? q >> 3 : 6 + ((q - 48) >> 2), a = (q >> 1) & (q < 48 ? 3 : 1), ks = q & 1, m = (a < 2 ? 8 * a : 4 + 6 * a) + b;
          MMA(ac1[m], fb[b][ks], fa[X][a][ks]); };
        auto up = [&](int b) AI { if (b < 8) { ba[b] = cb[b] + sel<unsigned>(b < 4, xn, wnx); PIN(ba[b]); }
                                  else if (b == 8) { xn = (xn == 131072u) ? 98304u : xn + 16384u; PINS(xn); }
                                  else if (b == 10) { wnx = (wnx == 65536u) ? 0u : wnx + 32768u; PINS(wnx); } };
        waitl0(); waitv<11>(); bar();
        PH(2);
        period<56, 16, 0, true, 40, 48, 12>(rd, mm, up); waitv<0>(); bar();
        for (int s = 0; s < 7; ++s) { period<56, 16, 1, false, 40, 48, 12>(rd, mm, up); bar(); period<56, 16, 0, false, 40, 48, 12>(rd, mm, up); bar(); }
        period<56, 16, 1, false, 40, 48, 12>(rd, mm, up); bar();
        PH(3);
        __builtin_amdgcn_s_setprio(1);
        va[0] = lane * 16;
#pragma unroll
        for (int q = 0; q < 4; ++q) vb[q] = vnat(q);
#pragma unroll
        for (int a = 0; a < 4; ++a) fa[0][a][0] = rd128(g ? T_ZERO : T_XA + (64 * wm + 16 * (a ^ (2 * wn)) + r15) * 16);
        sfor<12>([&](auto U) AI { constexpr int u = U.value; G3(IC<2>{}, U); SB();
          if constexpr (u < 4) {
#pragma unroll
            for (int q = 40 + 4 * u; q < 44 + 4 * u; ++q) mm(1, q, 0);
          }
          if constexpr (u == 0 || u == 4) {
#pragma unroll
            for (int b = u ? 5 : 0; b < (u ? 8 : 5); ++b) fb[b][0] = rd128(T_WA + ((b < 6 ? (b >> 1) * 64 + wn * 32 + (b & 1) * 16 : 96 + b * 16) + r15) * 16);
          }
          if constexpr (u >= 5) {
#pragma unroll
            for (int q = 8 * (u - 5); q < 8 * (u - 4); q += 2) mm(0, q, 0);
          }
          SB(); });
        next3();
      }
      float k0[4], k1[4], qq[4];
#pragma unroll
      for (int a = 0; a < 4; ++a) { const unsigned ro = T_ROW + (64 * wm + 16 * (a ^ (2 * wn)) + r15) * 4; k0[a] = rdf(ro); k1[a] = rdf(ro + 512); qq[a] = rdf(ro + 1024); }
      u32x4 hw[14]; float t[8]; unsigned sa9 = 0;
      auto lv0 = [](int s) constexpr { return s < 0 ? 0 : s < 6 ? (64 * s + 2) / 3 : s < 10 ? 128 + 16 * (s - 6) : s < 13 ? 192 + (32 * (s - 10) + 2) / 3 : 224; };
      auto st3 = [](int s) constexpr { return (s == 2 || s == 5 || s == 9) ? 4 : s == 12 ? 2 : 0; };
      auto n3 = [](int s) constexpr { return (s == 5 || s == 6 || s == 9 || s == 13) ? 8 : s == 7 ? 16 : s == 19 ? 4 : s > 19 ? 0 : 12; };
      sfor<22>([&](auto S) AI { constexpr int s = S.value;
        if constexpr (s == 6 || s == 10 || s == 14) waitl0();
        if constexpr (s == 8) waitv<12>();
        else if constexpr (s >= 1) waitv<n3(s - 1) + st3(s - 1)>();
        if constexpr (s == 7 || s == 10 || s == 14 || s == 17) {
          if (w == 4) check_flags(pv, rws, fvo, flagb + (FK_M1 + (s - 7) / 3) * 128, lane, 4 * kq, 4 * kq + 4, epr); }
        SB(); bar();
        if constexpr (s == 8) { PH(4); }
        if constexpr (st3(s - 2) > 0) { if (w == 5) stflag(rws, epr, lane, flagb + (FK_M1 + (s - 4) / 3) * 128 + cj * 4); }
        if constexpr (s == 6 || s == 9 || s == 13 || s == 16) { if (w == 4) ld32a(pv, rws, fvo, flagb + (FK_M1 + (s - 6) / 3) * 128); }
        if constexpr (s == 7) { const unsigned keep = sa; sa = sa9; sfor<4>([&](auto U) AI { G3(IC<9>{}, U); }); sa = keep; }
#define UU(e) ac1[m0 + ((e) >> 2)][(e) & 3]
#define LVL(expr) do { _Pragma("unroll") for (int e = 4 * hh; e < 4 * hh + 4; ++e) t[e] = (expr); PIN2(t[4 * hh], t[4 * hh + 1]); PIN2(t[4 * hh + 2], t[4 * hh + 3]); } while (0)
        auto hlev = [&](auto LL) AI { constexpr int L = LL.value, v = L >> 4, l = (L >> 1) & 7, hh = L & 1, a = v & 3, m0 = (a < 2 ? 8 * a : 4 + 6 * a) + 2 * (v >> 2);
          if constexpr (l == 0) LVL(UU(e) * UU(e));
          if constexpr (l == 1) LVL(__builtin_fmaf(t[e], k1[a], k0[a]));
          if constexpr (l == 2) LVL(UU(e) * t[e]);
          if constexpr (l == 3) LVL(__builtin_amdgcn_exp2f(t[e]));
          if constexpr (l == 4) LVL(__builtin_fmaf(t[e], qq[a], qq[a]));
          if constexpr (l == 5) LVL(__builtin_amdgcn_rcpf(t[e]));
          if constexpr (l == 6) LVL(UU(e) * t[e]);
          if constexpr (l == 7) { hw[v][2 * hh] = pk2(t[4 * hh], t[4 * hh + 1]); hw[v][2 * hh + 1] = pk2(t[4 * hh + 2], t[4 * hh + 3]); if constexpr (hh) PIN(hw[v]); else PIN2(hw[v][0], hw[v][1]); }
        };
        if constexpr (s < 19) sfor<12>([&](auto U) AI { constexpr int u = U.value, n = lv0(s + 1) - lv0(s), i0 = (n * u + 11) / 12, i1 = (n * (u + 1) + 11) / 12;
          if constexpr (n3(s) != 8) G3(IC<s + 3>{}, U);
          else if constexpr (u < 8) G3(IC<s + 3>{}, IC<u + 4>{});
          else if constexpr (s != 6)
            *(LAS u32x4*)(size_t)(((s + 1) % 3 == 0 ? 131072u : 81920u + ((s + 1) % 3) * 16384u) + wn * 8192 + (4 * wm + ((u - 8) ^ (2 * wn))) * 1024 + lane * 16) = hw[s - 13 + u];
          SB();
          if constexpr (i0 < i1) hlev(IC<lv0(s) + i0>{});
          if constexpr (i0 + 1 < i1) hlev(IC<lv0(s) + i0 + 1>{});
          SB(); });
        if constexpr (s == 19) {
          setm0(114688 + wq * 4096);
          sfor<4>([&](auto U) AI { constexpr int u = U.value;
            dma<u * 1024>(rx, (8u * u + (lane >> 3)) * 2048 + ((lane >> 2) & 1) * 256 + (lane & 3) * 16 - u * 1024, (ci * 128 + 32 * wq) * 2048 + ntp * 512 + kq * 64); });
        }
        constexpr int gr0 = s == 2 ? 0 : s == 5 ? 1 : s == 9 ? 2 : 3;
#pragma unroll
        for (int q = 0; q < st3(s); ++q) st128(rws, hw[4 * gr0 + q], lane * 16u + (4 * wm + (q ^ (2 * wn))) * 1024, ci * HID_PANEL + kb_of(cj, gr0 < 3 ? 2 * gr0 + wn : 6) * 8192);
        if constexpr (s == 6) sa9 = sa;
        if constexpr (s < 4) next3(); else if constexpr (s < 18) next4();
        if constexpr (s == 4) {
          rb = mk(w2, 3584 * 2048);
#pragma unroll
          for (int q = 0; q < 4; ++q) vb[q] = (8 * q + (lane >> 3)) * 2048 + (((((lane & 7) >> 1) ^ g ^ (2 * (r2 & 1))) * 2) + (lane & 1)) * 16 - 1024 * q;
        }
      });
    }
    PH(5);
    unsigned got = 0;
    auto fetch = [&]() AI {
      ld32a(pv, rws, fvo, PF_OFF + (ci * 16 + cj) * 256 + wq * 64); asm volatile("s_waitcnt vmcnt(0)" : "+v"(pv)::"memory");
      const unsigned rdy = (unsigned)__builtin_amdgcn_ballot_w64(pv == epr) & 7u & ~got; got |= rdy;
      sfor<3>([&](auto Q) AI { constexpr int q = Q.value;
        if (rdy & (1u << q)) { setm0(wq * 4096 + q * 16384);
          sfor<4>([&](auto U) AI { dma1<U.value * 1024>(rws, va[0], PART_OFF + (ci * 16 + cj) * 49152 + q * 16384 + wq * 4096); }); } });
    };
    fetch();
    bar();
    if (got != 7u) fetch();
    presum();
#pragma unroll 1
    for (unsigned sp = 0; sp < SPIN_CAP && got != 7u; ++sp) fetch();
    if (cj == 0 && w == 4) stflag(rws, epr, lane, flagb + FK_EP * 128);
    waitv<0>(); bar();
  }
  {
    u32x2 pw[4][3];
#pragma unroll
    for (int u = 0; u < 4; ++u)
#pragma unroll
      for (int q = 0; q < 3; ++q) pw[u][q] = *(LAS u32x2*)(size_t)(fpp[u] + q * 16384);
    SB();
#pragma unroll
    for (int u = 0; u < 4; ++u) {
      f32x4 r;
#pragma unroll
      for (int e = 0; e < 4; ++e) r[e] = (fv[u][e] + unp(pw[u][0], e)) + (unp(pw[u][1], e) + unp(pw[u][2], e));
      st128<OAUX>(mk(outp, 8u << 20), __builtin_bit_cast(u32x4, r), fo + u * 16384, 0);
    }
  }
}

extern "C" void kernel_launch(void* const* d_in, const int* in_sizes, int n_in, void* d_out, int out_size, void* d_ws, size_t ws_size, hipStream_t stream) {
  (void)in_sizes; (void)n_in; (void)out_size; (void)ws_size;
  (void)hipFuncSetAttribute((const void*)fused_kernel, hipFuncAttributeMaxDynamicSharedMemorySize, LDS_BYTES);
  fused_kernel<<<256, 512, LDS_BYTES, stream>>>((const u16*)d_in[0], (const u16*)d_in[1], (const u16*)d_in[2], (const u16*)d_in[3], (const u16*)d_in[4],
      (const u16*)d_in[5], (const u16*)d_in[6], (const u16*)d_in[7], (const u16*)d_in[8], (const u16*)d_in[10], (const u16*)d_in[11], (const u16*)d_in[12],
      (const u16*)d_in[13], (const u16*)d_in[14], (char*)d_ws, (float*)d_out);
}
```

```cpp
#include <hip/hip_runtime.h>
#include <utility>

typedef unsigned short u16;
typedef __bf16 bf16x2 __attribute__((ext_vector_type(2)));
typedef __bf16 bf16x4 __attribute__((ext_vector_type(4)));
typedef __bf16 bf16x8 __attribute__((ext_vector_type(8)));
typedef float f32x2 __attribute__((ext_vector_type(2)));
typedef float f32x4 __attribute__((ext_vector_type(4)));
typedef unsigned u32x2 __attribute__((ext_vector_type(2)));
typedef unsigned u32x4 __attribute__((ext_vector_type(4)));
typedef __amdgpu_buffer_rsrc_t rsrc_t;
#define LAS __attribute__((address_space(3)))
#define DI __device__ __forceinline__
#define AI __attribute__((always_inline))
#define SB() __builtin_amdgcn_sched_barrier(0)
#define PIN(x) asm volatile("" : "+v"(x))
#define MFMA(a, b, c) __builtin_amdgcn_mfma_f32_16x16x32_bf16(a, b, c, 0, 0, 0)
#define MMA(c, a, b) asm volatile("v_mfma_f32_16x16x32_bf16 %0, %1, %2, %0" : "+v"(c) : "v"(a), "v"(b))
#define MMZ(c, a, b) asm volatile("v_mfma_f32_16x16x32_bf16 %0, %1, %2, 0" : "=&v"(c) : "v"(a), "v"(b))
#define MPAD() asm volatile("s_nop 7")
template <int V> using IC = std::integral_constant<int, V>;

constexpr unsigned HID_PANEL = 112 * 8192, H_OFF = 16 * HID_PANEL, H_PANEL = 32 * 16384;
constexpr unsigned PART_OFF = H_OFF + 16 * H_PANEL;
constexpr unsigned GRAN_OFF = PART_OFF + 256 * 49152;
constexpr unsigned RG_OFF = GRAN_OFF + 256 * 1024;
constexpr unsigned CG_OFF = RG_OFF + 16 * 8192;
constexpr unsigned FLAG_OFF = CG_OFF + 256 * 2048;
constexpr unsigned EPC_OFF = FLAG_OFF + 16 * 1024;
constexpr unsigned PF_OFF = EPC_OFF + 16 * 128;
constexpr unsigned WS_BYTES = PF_OFF + 256 * 256;
constexpr int FK_M1 = 0, FK_M2 = 1, FK_M3 = 2, FK_M4 = 3, FK_H = 4, FK_EP = 6;
constexpr unsigned TAB = 147456, T_ROW = TAB, T_XA = TAB + 2048, T_OUT = TAB + 4096, T_B1 = TAB + 5120, T_WA = TAB + 8192, T_ZERO = TAB + 12288, T_AGG = T_WA, T_DUM = TAB + 12544, T_CON = TAB + 12800;
constexpr unsigned LDS_BYTES = 163840;
constexpr int LASTR = 24, ROTM = 1, OAUX = 2, CSL = 22, WBI = 13, WA = 0, WD = 8;
constexpr unsigned SPIN_CAP = 1u << 16;

DI rsrc_t mk(const void* p, unsigned bytes) { return __builtin_amdgcn_make_buffer_rsrc((void*)p, 0, bytes, 0x00020000); }
DI void setm0(unsigned lds) { asm volatile("s_mov_b32 m0, %0\n\ts_nop 0" ::"s"(lds) : "memory"); }
template <int OFF> DI void dma(rsrc_t r, unsigned voff, unsigned soff) {
  asm volatile("buffer_load_dwordx4 %0, %1, %2 offen offset:%3 lds" ::"v"(voff), "s"(r), "s"(soff), "i"(OFF) : "memory");
}
template <int OFF> DI void dma1(rsrc_t r, unsigned voff, unsigned soff) {
  asm volatile("buffer_load_dwordx4 %0, %1, %2 offen offset:%3 sc1 lds" ::"v"(voff), "s"(r), "s"(soff), "i"(OFF) : "memory");
}
#define PIN2(x, y) asm volatile("" : "+v"(x), "+v"(y))
#define PINS(x) asm volatile("" : "+s"(x))
typedef const void* const __attribute__((address_space(4)))* kargs_t;
DI const char* karg(int i) { kargs_t ka = (kargs_t)__builtin_amdgcn_kernarg_segment_ptr(); PINS(ka); return (const char*)ka[i]; }
template <int N> DI void waitv() { asm volatile("s_waitcnt vmcnt(%0)" ::"i"(N) : "memory"); }
DI void waitl0() { SB(); __builtin_amdgcn_s_waitcnt(0xC07F); asm volatile("" ::: "memory"); }
DI void bar() { asm volatile("s_barrier" ::: "memory"); SB(); }
DI bf16x8 rd128(unsigned a) { return *(LAS bf16x8*)(size_t)a; }
DI f32x4 rdf4(unsigned a) { return *(LAS f32x4*)(size_t)a; }
DI f32x2 rdf2(unsigned a) { return *(LAS f32x2*)(size_t)a; }
DI float rdf(unsigned a) { return *(LAS float*)(size_t)a; }
DI void wrf(unsigned a, float v) { *(LAS float*)(size_t)a = v; }
DI void wrf2(unsigned a, f32x2 v) { *(LAS f32x2*)(size_t)a = v; }
DI bf16x4 rtr(unsigned a) { return __builtin_amdgcn_ds_read_tr16_b64_v4bf16((LAS bf16x4*)(size_t)a); }
DI void sethalf(bf16x8& f, int h, bf16x4 v) {
  if (h == 0) f = __builtin_shufflevector(v, v, 0, 1, 2, 3, -1, -1, -1, -1);
  else { bf16x8 t = __builtin_shufflevector(v, v, -1, -1, -1, -1, 0, 1, 2, 3); f = __builtin_shufflevector(f, t, 0, 1, 2, 3, 12, 13, 14, 15); }
}
DI unsigned f2u(float f) { return __builtin_bit_cast(unsigned, f); }
DI float u2f(unsigned u) { return __builtin_bit_cast(float, u); }
DI float sum16(float v) { const auto r = __builtin_amdgcn_permlane16_swap(f2u(v), f2u(v), false, false); return u2f(r[0]) + u2f(r[1]); }
DI float sum32(float v) { const auto r = __builtin_amdgcn_permlane32_swap(f2u(v), f2u(v), false, false); return u2f(r[0]) + u2f(r[1]); }
DI float fexp(float v) { return __builtin_amdgcn_exp2f(v * 1.4426950408889634f); }
DI float bf2f(unsigned v) { return u2f(v << 16); }
DI unsigned pk2(float lo, float hi) { f32x2 v = {lo, hi}; return __builtin_bit_cast(unsigned, __builtin_convertvector(v, bf16x2)); }
DI float rbf(float f) { return u2f(pk2(f, 0.f) << 16); }
template <int C> DI float dppadd(float v) { return v + u2f(__builtin_amdgcn_update_dpp(0, (int)f2u(v), C, 0xf, 0xf, true)); }
DI float sel4(bool b0, bool b1, float a, float b, float c, float d) { PIN(a); PIN(b); PIN(c); PIN(d); const float lo = b0 ? b : a, hi = b0 ? d : c; return b1 ? hi : lo; }
DI f32x2 cmul(f32x2 a, f32x2 b) { return (f32x2){a.x * b.x - a.y * b.y, a.x * b.y + a.y * b.x}; }
DI f32x2 cmulc(f32x2 a, f32x2 b) { return (f32x2){__builtin_fmaf(a.x, b.x, -(a.y * b.y)), __builtin_fmaf(-a.x, b.y, -(a.y * b.x))}; }
DI f32x2 cfma(f32x2 a, f32x2 b, f32x2 c) { return (f32x2){__builtin_fmaf(a.x, b.x, __builtin_fmaf(-a.y, b.y, c.x)), __builtin_fmaf(a.x, b.y, __builtin_fmaf(a.y, b.x, c.y))}; }
template <int AUX = 16> DI void st128(rsrc_t r, u32x4 v, unsigned voff, unsigned soff) {
  __builtin_amdgcn_raw_buffer_store_b128(v, r, voff, soff, AUX);
  asm volatile("s_nop 1" ::"v"(v));
}
DI void st64(rsrc_t r, u32x2 v, unsigned voff, unsigned soff) { __builtin_amdgcn_raw_buffer_store_b64(v, r, voff, soff, 16); }
DI void st32(rsrc_t r, unsigned v, unsigned voff, unsigned soff) { __builtin_amdgcn_raw_buffer_store_b32(v, r, voff, soff, 16); }
DI void stflag(rsrc_t r, unsigned v, int lane, unsigned soff) { if (lane == 0) st32(r, v, 0u, soff); }
DI void ld32a(unsigned& d, rsrc_t r, unsigned voff, unsigned soff) { asm volatile("buffer_load_dword %0, %1, %2, %3 offen sc1" : "=v"(d) : "v"(voff), "s"(r), "s"(soff) : "memory"); }
DI void ld16p(unsigned& d, const u16* p, unsigned voff) { asm volatile("global_load_ushort %0, %1, %2" : "=v"(d) : "v"(voff), "s"(p) : "memory"); }
DI void ld128g(u32x4& d, const u16* p, unsigned voff) { asm volatile("global_load_dwordx4 %0, %1, %2" : "=v"(d) : "v"(voff), "s"(p) : "memory"); }
template <class T> DI T sel(bool c, T a, T b) { return c ? a : b; }
DI void pfg(const char* p) { asm volatile("global_load_lds_dword %0, off" ::"v"(p) : "memory"); }
DI bool anyl(bool b) { return __builtin_amdgcn_ballot_w64(b) != 0; }
DI void ld128p(u32x4& d, rsrc_t r, unsigned voff, unsigned soff) { asm volatile("buffer_load_dwordx4 %0, %1, %2, %3 offen" : "=v"(d) : "v"(voff), "s"(r), "s"(soff) : "memory"); }
DI void ld128a(u32x4& d, rsrc_t r, unsigned voff, unsigned soff) { asm volatile("buffer_load_dwordx4 %0, %1, %2, %3 offen sc1" : "=v"(d) : "v"(voff), "s"(r), "s"(soff) : "memory"); }

DI unsigned nq(unsigned s) { return (s + 32768u) & 131071u; }

DI int kb_of(int j, int bl) { return 7 * j + ((j & 1) ? (bl == 6 ? 0 : bl + 1) : bl); }

template <int NR, int LAST> constexpr int nread(int sl) {
  constexpr int DENSE = 2 * NR - LAST - 2;
  return sl < 0 ? 0 : sl < DENSE ? sl + 1 : sl >= LAST ? NR : DENSE + (sl - DENSE) / 2 + 1;
}
template <int N, int L, int X, bool FIRST, int NR, int LAST, int NB, int SL, class RD, class MM, class UP>
DI void slot(RD& rd, MM& mm, UP& upd) {
  if constexpr (!FIRST && SL >= L && (SL == L + 4 || (SL - L) % 8 == 0)) {
    __builtin_amdgcn_s_waitcnt(0xC07F | ((nread<NR, LAST>(SL - 1) - nread<NR, LAST>(SL - WD)) << 8)); SB();
  }
  if constexpr (SL >= L) mm(X, SL - L, SL); else if constexpr (!FIRST) mm(X ^ 1, SL + N - L, SL);
  SB();
  if constexpr (nread<NR, LAST>(SL) > nread<NR, LAST>(SL - 1)) rd(X, nread<NR, LAST>(SL) - 1);
  constexpr int per = NB > N - 1 - LAST ? 2 : 1, u0 = (SL - LAST - 1) * per;
  if constexpr (SL > LAST && u0 < NB) { upd(u0); if constexpr (per == 2 && u0 + 1 < NB) upd(u0 + 1); }
  SB();
}
template <int N, int L, int X, bool FIRST, int NR, int LAST, int NB, class RD, class MM, class UP, int... I>
DI void period_(RD& rd, MM& mm, UP& upd, std::integer_sequence<int, I...>) { (slot<N, L, X, FIRST, NR, LAST, NB, I>(rd, mm, upd), ...); }
template <int N, int L, int X, bool FIRST, int NR, int LAST, int NB, class RD, class MM, class UP>
DI void period(RD& rd, MM& mm, UP& upd) {
  static_assert(NR - 1 <= LAST && !(LAST & 1) && NB <= 2 * (N - 1 - LAST), "");
  period_<N, L, X, FIRST, NR, LAST, NB>(rd, mm, upd, std::make_integer_sequence<int, N>{});
  waitl0();
}
template <class G, class W, int... U> DI void ival_(G& g, W& wk, std::integer_sequence<int, U...>) { ((g(IC<U>{}), SB(), wk(IC<U>{}), SB()), ...); }
template <class G, class W> DI void ival(G&& g, W&& wk) { ival_(g, wk, std::make_integer_sequence<int, 8>{}); }
template <int N, class F, int... I> DI void sfor_(F& f, std::integer_sequence<int, I...>) { (f(IC<I>{}), ...); }
template <int N, class F> DI void sfor(F&& f) { sfor_<N>(f, std::make_integer_sequence<int, N>{}); }

DI void check_flags(unsigned& pv, rsrc_t ws, unsigned voff, unsigned soff, int lane, int lo, int hi, unsigned ep) {
  PIN(pv);
  const bool need = lane >= lo && lane < hi;
  if (__builtin_expect(anyl(need && pv != ep), 0)) {
    for (unsigned sp = 0; sp < SPIN_CAP; ++sp) {
      __builtin_amdgcn_s_sleep(2);
      ld32a(pv, ws, voff, soff); asm volatile("s_waitcnt vmcnt(0)" : "+v"(pv)::"memory");
      if (!anyl(need && pv != ep)) break;
    }
  }
}

#if defined(__HIP_DEVICE_COMPILE__) && __HIP_DEVICE_COMPILE__
__attribute__((target("no-packed-fp32-ops")))
#endif
__global__ __launch_bounds__(512, 2) void fused_kernel(const u16* x, const u16* lre, const u16* lim, const u16* bre, const u16* bim, const u16* cre, const u16* cim,
    const u16* ldt, const u16* dsk, const u16* w1, const u16* b1, const u16* w2, const u16* b2, const u16* gw, char* ws, float* out) {
  extern __shared__ __attribute__((aligned(16))) char shm[];
  unsigned long long pc0; asm volatile("s_getpc_b64 %0" : "=s"(pc0));
  const int lane = threadIdx.x & 63, w = __builtin_amdgcn_readfirstlane(threadIdx.x >> 6);
  const int bid = blockIdx.x, xcd = bid & 7, loc = bid >> 3;
  const int ci = (xcd & 1) * 8 + (loc & 7), cj = (xcd >> 1) * 4 + (loc >> 3);
  const int rot = (ci + cj) & ROTM, ntp = cj & 3, kq = cj >> 2, r2 = kq ^ 3, wq = w & 3, tq = threadIdx.x & 255;
  const rsrc_t rws = mk(ws, WS_BYTES), rx = mk(x, 2048 * 2048);
  const int wm = (w >> 1) & 1, wn = w & 1, g = lane >> 4, r15 = lane & 15, q2 = (lane >> 2) & 3, p = lane & 3, hl = lane >> 5, r31 = lane & 31;
  const unsigned flagb = FLAG_OFF + ci * 1024, fvo = (lane & 15) * 4;
  unsigned rgb = RG_OFF + ci * 8192; PINS(rgb);
  auto nowork = [&](auto) AI {};
  unsigned va[4], vb[4], vw[7], sl, sa, sb, sc, ta, tb, tc; int sx; rsrc_t ra, rb;
  auto vnat = [&](int u) AI { return (8u * u + (lane >> 3)) * 2048 + (((lane & 7) ^ ((4 * u + g) & 7)) * 16) - 1024 * u; };
  auto nexts = [&]() AI { sa = __builtin_amdgcn_readlane(ta, sx); sb = __builtin_amdgcn_readlane(tb, sx); ++sx; PINS(sa); PINS(sb); };
  auto G = [&](auto U) AI { constexpr int u = U.value;
    if constexpr (u == 0) setm0(sl + wq * 4096);
    if constexpr (u == 4) setm0(sl + 16384 + wq * 4096);
    if constexpr (u < 4) dma1<u * 1024>(ra, va[u], sa); else dma<(u - 4) * 1024>(rb, vb[u - 4], sb);
  };
  auto G2 = [&](auto S, auto U) AI { constexpr int a = S.value % 3, u = U.value;
    if constexpr (u == 0) setm0((a == 0 ? 131072u : 81920u + a * 16384u) + wq * 4096);
    if constexpr (u == 4) setm0(a * 32768 + wq * 7168);
    if constexpr (u == 8) setm0(a * 32768 + wq * 7168 + 4096);
    if constexpr (u < 4) dma<u * 1024>(rx, va[u], sa); else dma<(u & 3) * 1024>(rb, vw[u - 4], sb);
  };
  const int c7 = ci & 7;
  auto setup2 = [&]() AI {
#pragma unroll
    for (int q = 0; q < 6; ++q) { const int k = 8 * (q & 1) + (lane >> 3), f = ((k >> 1) & 1) | ((q & 1) << 1);
      vw[q] = 4096 + k * 7168 + ((cj & 1) ? 64 : 0) + (q >> 1) * 128 + (((((lane & 7) >> 1) ^ f) * 2) + (lane & 1)) * 16 - (q & 3) * 1024; }
    vw[6] = 4096 + (lane >> 2) * 7168 + ((cj & 1) ? 0 : 384) + (((((lane & 3) >> 1) ^ hl) * 2) + (lane & 1)) * 16 - 2048;
    int k = lane - 2 + (c7 < 2 ? 3 * c7 : c7 - 2); k = 2 + (k >= 6 ? k - 6 : k);
    k = lane < 2 ? lane : lane < 8 ? k : (lane ^ rot);
    k = (k ^ (ci & 8)) & 15;
    ta = (ci * 128 + 32 * wq) * 2048 + k * 128; tb = (k * 64 + 16 * wq) * 7168 + cj * 448;
    PIN2(ta, tb);
#pragma unroll
    for (int q = 0; q < 7; ++q) PIN(vw[q]);
  };
  auto G3 = [&](auto S, auto U) AI { constexpr int a = (S.value + 1) % 3, u = U.value;
    if constexpr (u == 0) setm0((a == 0 ? 131072u : 81920u + a * 16384u) + wq * 4096);
    if constexpr (u == 4 || u == 8) setm0(a * 32768 + wq * 8192 + (u - 4) * 1024);
    if constexpr (u < 4) dma1<u * 1024>(rws, va[0], sa); else dma<(u & 3) * 1024>(rb, vb[u & 3], u < 8 ? sb : sc);
  };
  auto table3 = [&](unsigned& ta, unsigned& tb, unsigned& tc) AI {
    const int ks = 8 * kq + ((lane ^ (2 * ntp)) & 7);
    const int t = lane - 8, lc = t >> 2;
    const int kb0 = lc < 3 ? kb_of(4 * kq + ((t ^ ntp) & 3), 2 * lc) : kb_of(4 * kq + (((2 * t) ^ ntp) & 3), 6);
    const int kb1 = lc < 3 ? kb0 + 1 : kb_of(4 * kq + (((2 * t + 1) ^ ntp) & 3), 6);
    const int crow = ntp * 256 + (wq >> 1) * 128, wcol = ntp * 512 + ((wq & 2) + ((wq ^ (r2 >> 1)) & 1)) * 128;
    ta = lane < 8 ? H_OFF + ci * H_PANEL + ks * 16384 + wq * 4096 : ci * HID_PANEL + (wq < 2 ? kb0 : kb1) * 8192 + (wq & 1) * 4096;
    tb = lane < 8 ? (crow + ((2 * (wq & 1)) ^ r2) * 32) * 2048 + (ks >> 1) * 128 + (ks & 1) : kb0 * 65536 + wcol;
    tc = lane < 8 ? (crow + ((2 * (wq & 1) + 1) ^ r2) * 32) * 2048 + (ks >> 1) * 128 : kb1 * 65536 + wcol;
    PIN2(ta, tb); PIN(tc);
  };
  auto next4 = [&]() AI { sc = __builtin_amdgcn_readlane(tc, sx); PINS(sc); nexts(); };
  auto next3 = [&]() AI { next4(); rb = mk(sel<const u16*>(sb & 1, cim, cre), 1024 * 2048); sb &= ~1u; };
  f32x4 fv[4]; unsigned fpp[4], fo; const char* outp;
  auto unp = [&](u32x2 v, int e) AI { return (e & 1) ? u2f(v[e >> 1] & 0xffff0000u) : bf2f(v[e >> 1] & 0xffffu); };
  auto presum = [&]() AI {
    const int c8 = lane & 7, sg = (lane >> 3) & 1, xw_ = (w >> 2) * 2 + sg;
    const f32x4 bv = rdf4(T_OUT + r15 * 16), dv = rdf4(T_OUT + 256 + r15 * 16);
#pragma unroll
    for (int u = 0; u < 4; ++u) {
      const int row = 4 * u + g;
      const f32x4 av = rdf4(65536 + xw_ * 8192 + (wq * 2 + (c8 >> 2)) * 1024 + (c8 & 3) * 256 + ((row ^ (8 * sg + c8)) * 16));
      const u32x2 xw = *(LAS u32x2*)(size_t)(114688 + (16 * w + row) * 128 + r15 * 8); const float gg = rdf(T_ROW + 1536 + (16 * w + row) * 4);
#pragma unroll
      for (int e = 0; e < 4; ++e) fv[u][e] = av[e] + __builtin_fmaf(gg, __builtin_fmaf(unp(xw, e), dv[e], -bv[e]), bv[e]);
      fpp[u] = xw_ * 4096 + wq * 1024 + (c8 & 3) * 256 + (c8 >> 2) * 8 + ((row ^ (8 * sg + 2 * (c8 & 3))) * 16);
      PIN2(fv[u], fpp[u]);
    }
    fo = (ci * 128 + 16 * w + g) * 4096 + ntp * 1024 + kq * 128 + sg * 512 + c8 * 16; PIN(fo);
    outp = karg(15);
  };

  if (w < 4) {
    unsigned epr, epc;
    const unsigned ca = (32 * wn + r31) * 8;
    const unsigned c0 = (32 * wn + r15) * 8;
    const int seg0 = 4 * wm + 2 * hl;
    f32x2 lam[2], cof[2], cfa, l16, l128, l1k, pw0, pw1; unsigned t3a, t3b;
    {
      setm0(T_DUM);
      const int row = bid * 8 + (lane & 7);
      const char* pp = (const char*)(hl ? (row < 1024 ? bre : bim - 1024 * 1024) : x) + (size_t)row * 2048 + (4 * wq + ((lane >> 3) & 3)) * 128;
      for (int q = 0; q <= wq; ++q) __builtin_amdgcn_s_sleep(6);
      pfg(pp);
      pfg((const char*)pc0 + (((loc * 4 + wq) % CSL) * 16 + (lane & 15)) * 128);
      ld32a(epr, rws, 0u, flagb + FK_EP * 128); ld32a(epc, rws, 0u, EPC_OFF + cj * 128);
      setup2(); sx = 2; nexts(); table3(t3a, t3b, tc);
      rb = mk((const char*)w1 - 4096, 1024 * 7168 + 4096);
#pragma unroll
      for (int q = 0; q < 4; ++q) { va[q] = vnat(q); PIN(va[q]); }
      wrf(T_ZERO + (lane & 3) * 4, 0.f);
    }
    {
      f32x4 acc[4][4];
#pragma unroll
      for (int a = 0; a < 16; ++a) { acc[a >> 2][a & 3] = (f32x4){0, 0, 0, 0}; PIN(acc[a >> 2][a & 3]); }
      bf16x8 wv[4][2];
      const int sstar = c7 < 2 ? c7 : 2;
      const int fsw = ((q2 >> 1) & 1) | ((g & 1) << 1);
      unsigned cf = sstar * 32768 + (g >> 1) * 7168 + (8 * (g & 1) + q2) * 128 + ((wq ^ fsw) * 32) + p * 8;
      unsigned ch = sstar * 32768 + (g >> 1) * 7168 + 6144 + (8 * (g & 1) + q2) * 64 + (((wq & 1) ^ (g & 1)) * 32) + p * 8;
      PIN(cf); PIN(ch);
      auto wread = [&](int st) AI {
        if (sstar == st) {
#pragma unroll
          for (int e = 0; e < 4; ++e)
#pragma unroll
            for (int t = 0; t < 4; ++t) sethalf(wv[t][e >> 1], e & 1, rtr((e >> 1) * 14336 + (t < 3 ? cf + t * 2048 + (e & 1) * 512 : ch + (e & 1) * 256)));
        } };
      {
        bf16x8 fa[4][2], fb[2][4][2];
        const unsigned n0 = r15 * 128 + ((g ^ (r15 >> 1)) * 16), n1 = r15 * 128 + (((4 + g) ^ (r15 >> 1)) * 16);
        const unsigned cb[4] = {wm * 8192u + n0, wm * 8192u + n1, 16384 + wn * 8192u + n0, 16384 + wn * 8192u + n1};
        unsigned ba[4], rs = 98304;
#pragma unroll
        for (int b = 0; b < 4; ++b) { ba[b] = rs + cb[b]; PIN(ba[b]); }
        auto rd = [&](int X, int r) AI { const int ks = r & 1, f = r >> 1;
          if (f >= 1 && f < 5) fb[X][f - 1][ks] = rd128(ba[2 + ks] + (f - 1) * 2048); else { const int a = f ? f - 4 : 0; fa[a][ks] = rd128(ba[ks] + a * 2048); } };
        auto mm = [&](int X, int q, int) AI { const int a = q >> 3, b = (q >> 1) & 3, ks = q & 1; MMA(acc[a][b], fa[a][ks], fb[X][b][ks]); };
        auto up = [&](int b) AI { if (b == 0) { rs = nq(rs); PINS(rs); } else { ba[b - 1] = rs + cb[b - 1]; PIN(ba[b - 1]); } };
        waitl0(); bar();
        period<32, 16, 0, true, 16, LASTR, 5>(rd, mm, up); bar();
        for (int s = 0; s < 7; ++s) { period<32, 16, 1, false, 16, LASTR, 5>(rd, mm, up); bar(); period<32, 16, 0, false, 16, LASTR, 5>(rd, mm, up); bar(); }
        period<32, 16, 1, false, 16, LASTR, 5>(rd, mm, up); bar();
        waitv<0>(); PIN(epr); PIN(epc);
        epr = __builtin_amdgcn_readfirstlane(epr) + 1u; epc = __builtin_amdgcn_readfirstlane(epc) + 1u;
        lam[0] = rdf2(T_CON + c0); lam[1] = rdf2(T_CON + c0 + 128);
        wread(0);
        sfor<7>([&](auto U) AI { G2(IC<2>{}, IC<U.value + 4>{}); SB();
#pragma unroll
          for (int q = 16 + (16 * U.value) / 7 / 2 * 2; q < 16 + (16 * (U.value + 1)) / 7 / 2 * 2; ++q) mm(1, q, 0);
          SB(); });
        nexts();
      }
      f32x2 st[2] = {{0, 0}, {0, 0}}, Pf[2] = {{0, 0}, {0, 0}}, Gt = {0, 0}, cr = {0, 0}, ag[8];
      u32x4 lb[8], cg[8], rgv[2]; unsigned lbo[8], lbe[8], pv, gd = 0; unsigned obs[2][2][2][4];
      f32x4 cs[4]; f32x2 so[2]; float c0s = 0.f, c1s = 0.f;
      const rsrc_t rgr = mk(ws + GRAN_OFF + cj * 16384, 16384);
      const int cp = 28 * wq + (r31 < 28 ? r31 : 27);
      const unsigned cgo = hl * 16384 + cp * 16;
#define OB(a, b, c) ((u32x4){obs[a][b][c][0], obs[a][b][c][1], obs[a][b][c][2], obs[a][b][c][3]})
      auto hofs = [&](int hf, int mt) AI { return (2u * wn + mt) * 4096 + wm * 2048 + hf * 1024 + g * 256 + ((r15 ^ (hf << 2)) * 16); };
      auto bad = [&]() AI { unsigned d = 0;
#pragma unroll
        for (int q = 0; q < 8; ++q) { PIN(lb[q]); d |= (int)lbo[q] < 0 ? 0u : ((lb[q][1] ^ epc) | (lb[q][3] ^ epc)); }
        return d != 0; };
      auto gbad = [&]() AI { unsigned d = 0;
#pragma unroll
        for (int q = 0; q < 8; ++q) { PIN(cg[q]); d |= (cg[q][1] ^ epc) | (cg[q][3] ^ epc); }
        return d != 0; };
      auto E2 = [](int s) constexpr { return (s == 2 || s == 3 || s == WBI) ? 1 : 0; };
      auto T2 = [](int s) constexpr { return s == 4 ? 8 : (s == 7 || s == 8) ? 4 : s == 11 ? 10 : 0; };
      sfor<15>([&](auto S) AI { constexpr int s = S.value;
        if constexpr (s < 3) waitl0();
        if constexpr (s == 6) waitv<11>();
        else if constexpr (s >= 1) waitv<T2(s - 2) + (s < 14 ? 11 : 8) + E2(s - 1) + T2(s - 1)>();
        bar();
        if constexpr (s < 2) wread(s + 1);
        if constexpr (s == 2) {
#pragma unroll
          for (int sg = 0; sg < 8; ++sg) ag[sg] = rdf2(T_AGG + sg * 512 + ca);
          l16 = rdf2(T_CON + 1024 + ca); cof[0] = rdf2(T_CON + 512 + c0); cof[1] = rdf2(T_CON + 512 + c0 + 128);
        }
        if constexpr (s == 5) { pw1 = rdf2(T_CON + 1536 + ca); pw0 = rdf2(T_CON + 2048 + ca); l128 = rdf2(T_CON + 2560 + ca); l1k = rdf2(T_CON + 3072 + ca); cfa = rdf2(T_CON + 512 + ca); }
        if constexpr (s == 11) { if (w == 0) stflag(rws, epr, lane, flagb + FK_H * 128 + cj * 4); }
        if constexpr (s == WBI) { setm0(T_DUM); pfg((const char*)w2 + ((bid * 4 + wq) * 56 + (lane + WA < 56 ? lane + WA : 55)) * 128); }
        if constexpr (s == 14) { if (w == 0) ld32a(pv, rws, fvo, flagb + FK_H * 128); }
        SB();
        auto work = [&](auto U) AI { constexpr int u = U.value;
          if constexpr (s < 2 && u >= 1 && u < 9) {
            constexpr int t = 8 * s + u - 1;
#pragma unroll
            for (int mt = 0; mt < 2; ++mt) st[mt] = cfma(lam[mt], st[mt], (f32x2){acc[t >> 2][mt][t & 3], acc[t >> 2][2 + mt][t & 3]});
            PIN2(st[0], st[1]);
          }
          if constexpr (s == 1 && u == 9) { wrf2(T_AGG + (4 * wm + g) * 512 + c0, st[0]); wrf2(T_AGG + (4 * wm + g) * 512 + c0 + 128, st[1]); }
          if constexpr (s == 2 && u >= 1 && u < 9) {
            constexpr int sg = u - 1, mt = sg & 1; const bool me = sg == seg0 + mt;
            Pf[mt].x = sel<float>(me, Gt.x, Pf[mt].x); Pf[mt].y = sel<float>(me, Gt.y, Pf[mt].y); Gt = cfma(l16, Gt, ag[sg]);
            PIN2(Gt, Pf[mt]);
          }
          if constexpr (s == 2 && u == 9) st64(rgr, (u32x2){f2u(sel<float>(hl, Gt.y, Gt.x)), epc}, ca * 2 + hl * 8, ci * 1024);
          if constexpr (s == 3 && u < 4) {
            const bf16x8 one = __builtin_bit_cast(bf16x8, (u32x4){0x3F803F80u, 0x3F803F80u, 0x3F803F80u, 0x3F803F80u});
            cs[u] = MFMA(one, wv[u][1], MFMA(one, wv[u][0], ((f32x4){0, 0, 0, 0}))); PIN(cs[u]);
          }
          if constexpr (s == 3 && u == 8) { PIN2(cs[0], cs[1]); PIN2(cs[2], cs[3]); }
          if constexpr (s == 3 && u == 9)
            st64(rws, (u32x2){f2u(sel4(g & 1, g & 2, cs[0][0], cs[1][0], cs[2][0], cs[3][0])), epc}, (g < 3 ? g * 64 + wq * 16 + r15 : 192 + (wq & 1) * 16 + r15) * 8u, CG_OFF + (cj * 16 + ci) * 2048);
          if constexpr ((s == 3 && u >= 4 && u < 9) || (s == 4 && u < 5) || (s == 5 && u >= 1 && u < 7)) {
            constexpr int t = s == 3 ? u - 4 : s == 4 ? 5 + u : 9 + u;
            f32x2 v0 = cmulc(cof[0], (f32x2){acc[t >> 2][0][t & 3], acc[t >> 2][2][t & 3]}), v1 = cmulc(cof[1], (f32x2){acc[t >> 2][1][t & 3], acc[t >> 2][3][t & 3]}); PIN2(v0, v1);
            acc[t >> 2][0][t & 3] = v0.x; acc[t >> 2][2][t & 3] = v0.y; acc[t >> 2][1][t & 3] = v1.x; acc[t >> 2][3][t & 3] = v1.y;
          }
          if constexpr (s == 4 && (u == 6 || u == 8)) {
#pragma unroll
            for (int q = 2 * u - 12; q < 2 * u - 8; ++q) { lbo[q] = (ci - 8 - 8 * hl + q) * 1024 + ca * 2; PIN(lbo[q]); }
          }
          if constexpr (s == 4 && u == 10) {
#pragma unroll
            for (int q = 0; q < 8; ++q) ld128a(lb[q], rgr, lbo[q], 0u);
          }
          if constexpr (s == 5 && u == 8) {
            if (!wm) pw0 = (f32x2){1.f, 0.f};
            if (ci <= 8) l1k = (f32x2){0.f, 0.f};
            pw0 = cmul(pw0, (f32x2){sel<float>(hl, pw1.x, 1.f), sel<float>(hl, pw1.y, 0.f)}); pw1 = cmul(pw0, l16); PIN2(pw0, pw1);
          }
          if constexpr (s == 5 && (u == 7 || u == 9)) {
#pragma unroll
            for (int q = 2 * u - 14; q < 2 * u - 10; ++q) { lbe[q] = (int)lbo[q] < 0 ? 0u : epc; PIN(lbe[q]); }
          }
          if constexpr (s == 6 && u < 8) {
            PIN(lb[u]); gd |= (lb[u][1] ^ lbe[u]) | (lb[u][3] ^ lbe[u]); cr = cfma(l128, cr, (f32x2){u2f(lb[u][0]), u2f(lb[u][2])});
            PIN2(cr, gd);
          }
          if constexpr (s == 6 && u == 8) {
            if (__builtin_expect(anyl(gd != 0), 0)) {
              for (unsigned sp = 0; sp < SPIN_CAP; ++sp) {
                __builtin_amdgcn_s_sleep(2);
#pragma unroll
                for (int q = 0; q < 8; ++q) ld128a(lb[q], rgr, lbo[q], 0u);
                waitv<0>();
                if (!anyl(bad())) break;
              }
              cr = (f32x2){0, 0};
#pragma unroll
              for (int q = 0; q < 8; ++q) cr = cfma(l128, cr, (f32x2){u2f(lb[q][0]), u2f(lb[q][2])});
            }
            gd = 0;
            const auto sx_ = __builtin_amdgcn_permlane32_swap(f2u(cr.x), f2u(cr.x), false, false), sy_ = __builtin_amdgcn_permlane32_swap(f2u(cr.y), f2u(cr.y), false, false);
            cr = cfma(l1k, (f32x2){u2f(sx_[1]), u2f(sy_[1])}, (f32x2){u2f(sx_[0]), u2f(sy_[0])});
            PIN(cr);
          }
          if constexpr (s == 6 && u == 9) { st[0] = cmulc(cfa, cfma(pw0, cr, Pf[0])); PIN(st[0]); }
          if constexpr (s == 7 && u == 0) { st[1] = cmulc(cfa, cfma(pw1, cr, Pf[1]));
            const auto ex = __builtin_amdgcn_permlane16_swap(f2u(st[0].x), f2u(st[1].x), false, false), ey = __builtin_amdgcn_permlane16_swap(f2u(st[0].y), f2u(st[1].y), false, false);
            st[0] = (f32x2){u2f(ex[0]), u2f(ey[0])}; st[1] = (f32x2){u2f(ex[1]), u2f(ey[1])}; PIN2(st[0], st[1]); }
          if constexpr ((s == 7 && u >= 2 && u < 10) || (s == 8 && u >= 1 && u < 9)) {
            constexpr int t = s == 7 ? u - 2 : 7 + u;
            if constexpr (t & 1) { so[0] = st[0]; so[1] = st[1]; }
#pragma unroll
            for (int mt = 0; mt < 2; ++mt) st[mt] = cfma((f32x2){lam[mt].x, -lam[mt].y}, st[mt], (f32x2){acc[t >> 2][mt][t & 3], acc[t >> 2][2 + mt][t & 3]});
            if constexpr (t & 1) {
#pragma unroll
              for (int mt = 0; mt < 2; ++mt) { obs[t >> 3][mt][0][(t >> 1) & 3] = pk2(so[mt].x, st[mt].x); obs[t >> 3][mt][1][(t >> 1) & 3] = pk2(so[mt].y, st[mt].y); PIN2(obs[t >> 3][mt][0][(t >> 1) & 3], obs[t >> 3][mt][1][(t >> 1) & 3]); }
            }
            PIN2(st[0], st[1]);
          }
          if constexpr ((s == 7 || s == 8) && u == 10) {
#pragma unroll
            for (int q = 0; q < 4; ++q) st128(rws, OB(s - 7, q >> 1, q & 1), hofs(s - 7, q >> 1), H_OFF + ci * H_PANEL + (2 * cj + (q & 1)) * 16384);
          }
          if constexpr (s >= 13 && u < 4) *(LAS u32x4*)(size_t)(98304 + (s - 13) * 16384 + hofs(u >> 1, u & 1)) = OB(u >> 1, u & 1, s - 13);
          if constexpr (s == 9 && u == 0) { if (ci == 15 && w == 1) stflag(rws, epc, lane, EPC_OFF + cj * 128); }
          if constexpr (s == 14 && u == 5) {
            auto rbad = [&]() AI { PIN2(rgv[0], rgv[1]); return ((rgv[0][1] ^ epr) | (rgv[0][3] ^ epr) | (rgv[1][1] ^ epr) | (rgv[1][3] ^ epr)) != 0; };
            if (__builtin_expect(anyl(rbad()), 0))
              for (unsigned sp = 0; sp < SPIN_CAP; ++sp) { __builtin_amdgcn_s_sleep(2);
                ld128a(rgv[0], rws, tq * 16u, rgb); ld128a(rgv[1], rws, tq * 16u + 4096, rgb);
                waitv<0>(); if (!anyl(rbad())) break; }
          }
          if constexpr (s == 14 && u == 7) {
            wrf(T_ROW + (tq & 1) * 1024 + (tq >> 1) * 4, u2f(rgv[0][0])); wrf(T_ROW + (tq & 1) * 1024 + 512 + (tq >> 1) * 4, u2f(rgv[0][2]));
            wrf2(T_XA + tq * 8, (f32x2){u2f(rgv[1][0]), u2f(rgv[1][2])});
          }
          if constexpr (s == 11 && u == 10) {
#pragma unroll
            for (int q = 0; q < 8; ++q) ld128a(cg[q], rws, cgo, CG_OFF + (cj * 16 + q) * 2048);
            ld128a(rgv[0], rws, tq * 16u, rgb); ld128a(rgv[1], rws, tq * 16u + 4096, rgb);
          }
          if constexpr (s == 14 && u < 4) {
#pragma unroll
            for (int q = 2 * u; q < 2 * u + 2; ++q) { PIN(cg[q]); gd |= (cg[q][1] ^ epc) | (cg[q][3] ^ epc); c0s += u2f(cg[q][0]); c1s += u2f(cg[q][2]); }
            PIN(gd); PIN(c0s); PIN(c1s);
          }
          if constexpr (s == 14 && u == 4) {
            if (__builtin_expect(anyl(gd != 0), 0)) {
              for (unsigned sp = 0; sp < SPIN_CAP; ++sp) { __builtin_amdgcn_s_sleep(2);
#pragma unroll
                for (int q = 0; q < 8; ++q) ld128a(cg[q], rws, cgo, CG_OFF + (cj * 16 + q) * 2048);
                waitv<0>(); if (!anyl(gbad())) break; }
              c0s = 0.f; c1s = 0.f;
#pragma unroll
              for (int q = 0; q < 8; ++q) { c0s += u2f(cg[q][0]); c1s += u2f(cg[q][2]); }
            }
            c0s = sum32(c0s); c1s = sum32(c1s); PIN(c0s); PIN(c1s);
          }
          if constexpr (s == 14 && u == 6) {
            const float v = sel<float>(hl, c1s, c0s), hi = rbf(v), bb = rdf(T_B1 + (2 * cp + hl) * 4);
            *(LAS u32x4*)(size_t)(T_WA + (2 * cp + hl) * 16) = (u32x4){pk2(hi, v - hi), pk2(hi, bb), pk2(bb, 0.f), 0u};
          }
        };
        if constexpr (s < 13) { sfor<11>([&](auto U) AI { G2(IC<s + 3>{}, U); SB(); work(U); SB(); }); if constexpr (s < 12) nexts(); }
        if constexpr (s == 12) {
#pragma unroll
          for (int q = 0; q < 4; ++q) vb[q] = va[q];
          ta = t3a; tb = t3b; sx = 0; next3(); }
        if constexpr (s >= 13) { sfor<8>([&](auto U) AI { G3(IC<s - 13>{}, IC<U.value + 4>{}); SB(); work(U); SB(); }); next3(); }
      });
      waitl0(); waitv<8>();
      if (w == 0) check_flags(pv, rws, fvo, flagb + FK_H * 128, lane, 4 * kq, 4 * kq + 4, epr);
      bar();
    }
    f32x4 acc[4][8];
    bf16x8 fs[8][2], fr[2][4][2];
    const int hf = p >> 1, fw = 2 * (g & 1) + (q2 >> 1);
    const unsigned hb = 98304 + (g >> 1) * 4096 + wm * 2048 + hf * 1024 + (8 * (g & 1) + q2) * 16 + (p & 1) * 8, wb = wn * 16384 + (4 * g + q2) * 128 + p * 8;
    unsigned b3[4] = {hb + 64u * hf, hb + 64u * (1 ^ hf), 32768 + wn * 16384u + r15 * 128 + ((g ^ (r15 >> 1)) * 16), 32768 + wn * 16384u + r15 * 128 + (((4 + g) ^ (r15 >> 1)) * 16)};
    unsigned b4[5];
    unsigned a3 = 1, dA, dW;
    auto rd3 = [&](int X, int r) AI {
      if (r >= 2 && r < 18) { const int a = (r - 2) >> 2, ks = ((r - 2) >> 1) & 1, h = r & 1; sethalf(fr[X][a][ks], h, rtr(b3[h] + ks * 8192 + a * 256)); }
      else { const int b = r < 2 ? 0 : (r - 16) >> 1, ks = r & 1; fs[b][ks] = rd128(b3[2 + ks] + b * 2048); } };
    auto rd4 = [&](int X, int r) AI {
      if (r >= 4 && r < 12) fr[X][(r - 4) >> 1][r & 1] = rd128(b4[0] + (r & 1) * 8192 + ((r - 4) >> 1) * 1024);
      else { const int b = r < 4 ? 0 : (r - 8) >> 2, ks = (r >> 1) & 1, h = r & 1; sethalf(fs[b][ks], h, rtr(b4[1 + (b & 3)] + (b >> 2) * 8192 + ks * 4096 + h * 2048)); } };
    auto mm = [&](int X, int q, int SL) AI { const int b = q >> 3, a = (q >> 1) & 3, ks = q & 1;
      if (SL < 0) acc[a][b] = MFMA(fs[b][ks], fr[X][a][ks], acc[a][b]); else MMA(acc[a][b], fs[b][ks], fr[X][a][ks]); };
    auto mmz = [&](int X, int q, int SL) AI { if (X == 0 && !(q & 1)) MMZ(acc[(q >> 1) & 3][q >> 3], fs[q >> 3][0], fr[0][(q >> 1) & 3][0]); else mm(X, q, SL); };
    auto adv = [&]() AI { dA = a3 == 0 ? -32768 : 16384; dW = a3 == 2 ? -65536 : 32768; a3 = a3 == 2 ? 0 : a3 + 1; PINS(dA); PINS(dW); PINS(a3); };
    auto up3 = [&](int b) AI { if (b == 0) adv(); else { b3[b - 1] += sel<unsigned>(b < 3, dA, dW); PIN(b3[b - 1]); } };
    auto up4 = [&](int b) AI { if (b == 0) adv(); else { b4[b - 1] += sel<unsigned>(b < 2, dA, dW); PIN(b4[b - 1]); } };
    constexpr int L3 = 18;
#pragma unroll
    for (int b = 0; b < 4; ++b) PIN(b3[b]);
    period<64, L3, 0, true, 32, 44, 5>(rd3, mmz, up3); waitv<0>(); bar();
    period<64, L3, 1, false, 32, 44, 5>(rd3, mmz, up3); bar();
    for (int s = 0; s < 3; ++s) { period<64, L3, 0, false, 32, 44, 5>(rd3, mm, up3); bar(); period<64, L3, 1, false, 32, 44, 5>(rd3, mm, up3);
      if (s == 2) break;
      bar(); }
    float gr[4];
#pragma unroll
    for (int a = 0; a < 4; ++a) gr[a] = rdf(T_ROW + 1536 + (64 * wm + 16 * a + r15) * 4);
    b4[0] = 131072 + wm * 4096 + lane * 16;
#pragma unroll
    for (int b = 0; b < 4; ++b) b4[1 + b] = wb + ((b ^ fw) * 32);
#pragma unroll
    for (int b = 0; b < 5; ++b) PIN(b4[b]);
    waitl0(); bar();
    {
      auto mmg = [&](int X, int q, int SL) AI { mm(X, q, SL); const int a = (SL >> 1) & 3, b = SL >> 3, e = 2 * (SL & 1); SB();
        acc[a][b][e] *= gr[a]; acc[a][b][e + 1] *= gr[a]; PIN(acc[a][b]); };
      period<64, L3, 0, false, 40, 56, 6>(rd4, mmg, up4); bar();
    }
    for (int s = 0; s < 6; ++s) { period<64, L3, 1, false, 40, 56, 6>(rd4, mm, up4); bar(); period<64, L3, 0, false, 40, 56, 6>(rd4, mm, up4); bar(); }
    const unsigned po = wq * 4096 + g * 256 + ((r15 ^ (8 * wn + 2 * g)) * 16);
    auto send = [&](int qd, int a) AI {
      st128(rws, (u32x4){pk2(acc[a][2 * qd][0], acc[a][2 * qd][1]), pk2(acc[a][2 * qd][2], acc[a][2 * qd][3]), pk2(acc[a][2 * qd + 1][0], acc[a][2 * qd + 1][1]), pk2(acc[a][2 * qd + 1][2], acc[a][2 * qd + 1][3])},
            po + a * 1024, PART_OFF + (ci * 16 + (qd ^ r2) * 4 + ntp) * 49152 + qd * 16384); };
    {
      auto mms = [&](int X, int q, int SL) AI { mm(X, q, SL); const int d = SL - L3 - 20;
        if (d >= 0 && (d & 1) == 1 && (d & 15) < 8) { SB(); send(d >> 4, (d & 15) >> 1); } };
      period<64, L3, 1, false, 40, 56, 0>(rd4, mms, up4); bar();
    }
    MPAD();
    auto sig = [&](int qd) AI { stflag(rws, epr, lane, PF_OFF + (ci * 16 + (qd ^ r2) * 4 + ntp) * 256 + wq * 64 + qd * 4); };
    sfor<11>([&](auto M) AI { constexpr int m = M.value;
      if constexpr (m < 9) { mm(1, 46 + 2 * m, -1); mm(1, 47 + 2 * m, -1); }
      SB();
      if constexpr (m < 4) send(2, m);
      if constexpr (m >= 7) {
#pragma unroll
        for (int q = 2 * (m - 7); q < 2 * (m - 7) + 2; ++q) { const int a = q & 3, bb = q >> 2;
          *(LAS f32x4*)(size_t)(65536 + wq * 8192 + (a * 2 + bb) * 1024 + g * 256 + ((r15 ^ (8 * wn + 4 * bb + g)) * 16)) = acc[a][6 + bb]; }
      }
      if constexpr (m == 5) { waitv<8>(); sig(0); }
      SB(); });
    waitv<5>(); sig(1);
    waitl0(); bar();
    waitv<2>(); sig(2);
    presum();
    bar();
  } else {
    unsigned epr, pv, t3a, t3b, cb[8], ba[8]; const char* pp0;
    f32x4 ac1[28];
    {
      u32x4 xr[2][2], gq[2];
      ra = rx; rb = mk(sel<const u16*>(wq & 1, bim, bre), 1024 * 2048);
#pragma unroll
      for (int q = 0; q < 4; ++q) { va[q] = (32 * (q & 1) + 16 * hl + 4 * (q >> 1) + ((lane >> 3) & 3)) * 2048 + (((lane & 7) ^ ((4 * q + g) & 7)) * 16) - 1024 * q; vb[q] = vnat(q); }
      auto src1 = [&](int s) AI { const int k = s ^ (s < 2 ? 0 : s < 4 ? (rot & 1) : rot); sa = (ci * 128 + 64 * (wq >> 1) + 8 * (wq & 1)) * 2048 + k * 128; sb = (cj * 64 + 32 * (wq >> 1)) * 2048 + k * 128; sl = ((s + 3) & 3) * 32768; };
      src1(0); ival(G, nowork);
      SB();
      setup2(); table3(t3a, t3b, tc);
      {
        const int f = ((q2 >> 1) & 1) | ((g & 1) << 1);
        const unsigned nb = wm * 8192u + r15 * 128, n0 = (g ^ (r15 >> 1)) * 16, n1 = ((4 + g) ^ (r15 >> 1)) * 16, wk = (g >> 1) * 7168u + p * 8;
        cb[0] = nb + wn * 4096 + n0; cb[1] = nb + wn * 4096 + n1; cb[2] = nb + (1 - wn) * 4096 + n0; cb[3] = nb + (1 - wn) * 4096 + n1;
        cb[4] = wk + (8 * (g & 1) + q2) * 128 + (((2 * wn) ^ f) * 32); cb[5] = wk + (8 * (g & 1) + q2) * 128 + (((2 * wn + 1) ^ f) * 32);
        cb[6] = wk + 6144 + (8 * (g & 1) + q2) * 64 + (g & 1) * 32; cb[7] = wk + 6144 + (8 * (g & 1) + q2) * 64 + ((g & 1) ^ 1) * 32;
#pragma unroll
        for (int b = 0; b < 8; ++b) { ba[b] = cb[b] + (b < 4 ? 131072u : 0u); PIN2(cb[b], ba[b]); }
        pp0 = lane < 32 ? (const char*)sel<const u16*>(lane & 16, cim, cre) + ((bid * 4 + wq) * 16 + r15) * 128 : (const char*)w2 + ((bid * 4 + wq) * 56 + (r31 < WA ? r31 : 0)) * 128;
        PIN(pp0);
#pragma unroll
        for (int m = 0; m < 28; ++m) { ac1[m] = (f32x4){0, 0, 0, 0}; PIN(ac1[m]); }
      }
      src1(1); ival(G, nowork);
      waitv<8>(); bar();
      unsigned pdt, plr, pli, bq, oq; f32x2 lp; float lr, li;
      const int tc = tq < 224 ? tq : 223;
      ld32a(epr, rws, 0u, flagb + FK_EP * 128); ld128g(gq[0], gw, lane * 16u); ld128g(gq[1], gw + 512, lane * 16u);
      ld16p(pdt, ldt + 64 * cj, lane * 2u); ld16p(plr, lre + 64 * cj, lane * 2u); ld16p(pli, lim + 64 * cj, lane * 2u);
      ld16p(bq, b1, (224 * cj + (tc < 192 ? ((cj & 1) ? 32 : 0) + tc : ((cj & 1) ? -192 : 0) + tc)) * 2u);
      ld16p(oq, sel<const u16*>(wq & 1, dsk, b2), (ntp * 256 + hl * 128 + kq * 32 + r31) * 2u);
      src1(2); ival(G, nowork);
      src1(3); ival(G, nowork);
      float sv[6] = {0, 0, 0, 0, 0, 0}, a1, a2, a3, mean, sd, rstd, qv, gg, kq, rr;
      auto E1 = [](int s) constexpr { return (s == 1 || s == 2) ? 2 : s == 11 ? 1 : 0; };
      sfor<12>([&](auto S) AI { constexpr int s = S.value;
        if constexpr (s == 0) waitv<16 + 8>(); else waitv<16 + E1(s - 2) + E1(s - 1)>();
        bar();
        src1(s + 4); ival(G, [&](auto U) AI { constexpr int u = U.value;
          if constexpr ((s == 1 || s == 2) && (u == 2 || u == 5)) ld128p(xr[s - 1][u / 3], rx, lane * 16u, (ci * 128 + 8 * cj + 2 * wq + s - 1) * 2048 + (u / 3) * 1024);
          if constexpr (s == 1 && u == 0) { PIN2(pdt, plr); PIN(pli); lr = bf2f(plr); li = bf2f(pli); const float dt = rbf(fexp(bf2f(pdt))); lp = (f32x2){fexp(dt * lr), dt * li}; PIN(lp); }
          if constexpr (s == 1 && u == 3) { PIN2(bq, oq); wrf(T_B1 + tq * 4, bf2f(bq)); wrf(T_OUT + tq * 4, bf2f(oq)); PIN(epr); epr = __builtin_amdgcn_readfirstlane(epr) + 1u; }
          if constexpr (s == 1 && u == 6) { kq = __builtin_rintf(lp.y * 0.63661977236f);
            rr = __builtin_fmaf(kq, -1.5707962513f, lp.y); rr = __builtin_fmaf(kq, -7.5497894159e-08f, rr); rr = __builtin_fmaf(kq, -5.3903029534e-15f, rr); PIN2(kq, rr); }
          if constexpr (s == 2 && u == 0) { const float z = rr * rr;
            a1 = __builtin_fmaf(rr * z, __builtin_fmaf(z, __builtin_fmaf(z, __builtin_fmaf(z, 2.7183114939e-06f, -1.9839334836e-04f), 8.3333293858e-03f), -1.6666666642e-01f), rr); PIN(a1); }
          if constexpr (s == 2 && u == 3) { const float z = rr * rr;
            a2 = __builtin_fmaf(z, __builtin_fmaf(z, __builtin_fmaf(z, __builtin_fmaf(z, 2.4390448796e-05f, -1.3886763775e-03f), 4.1666623324e-02f), -4.9999999726e-01f), 1.f); PIN(a2); }
          if constexpr (s == 2 && u == 6) { const int q = (int)kq; const float sa_ = sel<float>(q & 1, a2, a1), ca_ = sel<float>(q & 1, a1, a2);
            lp = (f32x2){lp.x * (((q + 1) & 2) ? -ca_ : ca_), lp.x * ((q & 2) ? -sa_ : sa_)}; PIN(lp); wrf2(T_CON + lane * 8, lp); }
          if constexpr (s == 3 && u == 0) { const float nr = lp.x - 1.f, ni = lp.y, den = 2.f * __builtin_amdgcn_rcpf(lr * lr + li * li);
                                            wrf2(T_CON + 512 + lane * 8, (f32x2){(nr * lr + ni * li) * den, (ni * lr - nr * li) * den}); }
          if constexpr ((s == 3 && u >= 2) || (s == 4 && u >= 4)) { constexpr int q = s == 3 ? u - 2 : u + 2; lp = cmul(lp, lp); PIN(lp);
            if constexpr (q >= 3 && q < 7) wrf2(T_CON + (q - 1) * 512 + lane * 8, lp);
            if constexpr (q == 9) wrf2(T_CON + 3072 + lane * 8, lp); }
          if constexpr ((s == 4 && u < 2) || (s >= 5 && s < 9 && !(u & 1) && 4 * (s - 5) + 2 + (u >> 1) < 16)) {
            constexpr int id = s == 4 ? u : 4 * (s - 5) + 2 + (u >> 1), r = id >> 3, e = id & 7;
            PIN2(xr[r][e >> 2], gq[e >> 2]);
            const unsigned v = xr[r][e >> 2][e & 3], gv = gq[e >> 2][e & 3];
            const float x0 = bf2f(v & 0xffffu), x1 = u2f(v & 0xffff0000u);
            sv[r] += x0 + x1; sv[2 + r] = __builtin_fmaf(x1, x1, __builtin_fmaf(x0, x0, sv[2 + r]));
            sv[4 + r] = __builtin_fmaf(x1, u2f(gv & 0xffff0000u), __builtin_fmaf(x0, bf2f(gv & 0xffffu), sv[4 + r]));
            PIN2(sv[r], sv[2 + r]); PIN(sv[4 + r]);
          }
          if constexpr (s == 9 && u < 4) {
#pragma unroll
            for (int q = 0; q < 6; ++q) sv[q] = u == 0 ? dppadd<0xB1>(sv[q]) : u == 1 ? dppadd<0x4E>(sv[q]) : u == 2 ? dppadd<0x141>(sv[q]) : dppadd<0x140>(sv[q]);
            PIN2(sv[0], sv[1]); PIN2(sv[2], sv[3]); PIN2(sv[4], sv[5]);
          }
          if constexpr (s == 9 && u == 5) { const bool r1 = lane & 4; a1 = sum16(sel<float>(r1, sv[1], sv[0])); a2 = sum16(sel<float>(r1, sv[3], sv[2])); a3 = sum16(sel<float>(r1, sv[5], sv[4])); PIN2(a1, a2); PIN(a3); }
          if constexpr (s == 10 && u == 0) { a1 = sum32(a1); a2 = sum32(a2); a3 = sum32(a3); PIN2(a1, a2); PIN(a3); }
          if constexpr (s == 10 && u == 2) { mean = a1 * (1.f / 1024.f); const float var = __builtin_fmaxf(a2 * (1.f / 1024.f) - mean * mean, 0.f) + 1e-5f; rstd = __builtin_amdgcn_rsqf(var); sd = var * rstd;
                                             PIN2(mean, rstd); PIN(sd); }
          if constexpr (s == 10 && u == 4) { qv = 1.f + fexp(__builtin_fminf(a3, 60.f)); gg = __builtin_amdgcn_rcpf(1.f + fexp(__builtin_fminf(-a3, 80.f))); PIN2(qv, gg); }
          if constexpr (s == 11 && u == 0) { a1 = sel4(lane & 1, lane & 2, -2.302208198f * rstd, -0.1029432397f * rstd * rstd * rstd, qv * sd, gg); PIN(a1); }
          if constexpr (s == 11 && u == 2) { const float mh = rbf(mean), sh = rbf(sd);
                                             a2 = sel4(lane & 1, lane & 2, u2f(pk2(-mh, -mh)), u2f(pk2(mh - mean, sh)), u2f(pk2(sd - sh, 0.f)), 0.f); PIN(a2); }
          if constexpr (s == 11 && u == 5) { if (lane < 16) st64(rws, (u32x2){f2u(sel<float>(lane & 8, a2, a1)), epr}, (lane >> 3) * 4096u + (lane & 7) * 8, rgb + (8 * cj + 2 * wq) * 32); }
        });
      });
      rb = mk((const char*)w1 - 4096, 1024 * 7168 + 4096);
#pragma unroll
      for (int q = 0; q < 4; ++q) va[q] = vb[q];
      sx = 0; nexts();
      const unsigned sb0 = sb, sa0 = sa; nexts();
      const unsigned sb1 = sb, sa1 = sa; nexts();
      const unsigned sa2 = sa;
      waitv<16 + 1>(); bar();
      setm0(T_DUM); pfg(pp0);
      sa = sa0; sfor<4>([&](auto U) AI { G2(IC<0>{}, U); });
      sa = sa1; sfor<4>([&](auto U) AI { G2(IC<1>{}, U); });
      waitv<16 + 2>(); bar();
      sb = sb0; sfor<7>([&](auto U) AI { G2(IC<0>{}, IC<U.value + 4>{}); });
      sa = sa2; sfor<4>([&](auto U) AI { G2(IC<2>{}, U); });
      waitv<20>(); bar();
      sb = sb1; sfor<7>([&](auto U) AI { G2(IC<1>{}, IC<U.value + 4>{}); });
      ta = t3a; tb = t3b; sx = 2; next3();
    }
    {
      {
        bf16x8 fa[2][4][2], fb[8][2];
        unsigned xn = 98304, wnx = 32768;
        setm0(T_DUM);
        auto rd = [&](int X, int r) AI {
          if (r >= 4 && r < 12) { const int a = (r - 4) >> 1, ks = r & 1; fa[X][a][ks] = rd128(ba[(a >> 1) * 2 + ks] + (a & 1) * 2048); }
          else { const int b = r < 4 ? 0 : (r - 8) >> 2, ks = (r >> 1) & 1, h = r & 1;
            sethalf(fb[b][ks], h, rtr(b < 6 ? ba[4 + (b & 1)] + (b >> 1) * 2048 + ks * 14336 + h * 512 : ba[b] + ks * 14336 + h * 256)); } };
        auto mm = [&](int X, int q, int) AI { const int b = q < 48 ? q >> 3 : 6 + ((q - 48) >> 2), a = (q >> 1) & (q < 48 ? 3 : 1), ks = q & 1, m = (a < 2 ? 8 * a : 4 + 6 * a) + b;
          MMA(ac1[m], fb[b][ks], fa[X][a][ks]); };
        auto up = [&](int b) AI { if (b < 8) { ba[b] = cb[b] + sel<unsigned>(b < 4, xn, wnx); PIN(ba[b]); }
                                  else if (b == 8) { xn = (xn == 131072u) ? 98304u : xn + 16384u; PINS(xn); }
                                  else if (b == 10) { wnx = (wnx == 65536u) ? 0u : wnx + 32768u; PINS(wnx); } };
        waitl0(); waitv<11>(); bar();
        period<56, 16, 0, true, 40, 48, 12>(rd, mm, up); waitv<0>(); bar();
        for (int s = 0; s < 7; ++s) { period<56, 16, 1, false, 40, 48, 12>(rd, mm, up); bar(); period<56, 16, 0, false, 40, 48, 12>(rd, mm, up); bar(); }
        period<56, 16, 1, false, 40, 48, 12>(rd, mm, up); bar();
        __builtin_amdgcn_s_setprio(1);
        va[0] = lane * 16;
#pragma unroll
        for (int q = 0; q < 4; ++q) vb[q] = vnat(q);
#pragma unroll
        for (int a = 0; a < 4; ++a) fa[0][a][0] = rd128(g ? T_ZERO : T_XA + (64 * wm + 16 * (a ^ (2 * wn)) + r15) * 16);
        sfor<12>([&](auto U) AI { constexpr int u = U.value; G3(IC<2>{}, U); SB();
          if constexpr (u < 4) {
#pragma unroll
            for (int q = 40 + 4 * u; q < 44 + 4 * u; ++q) mm(1, q, 0);
          }
          if constexpr (u == 0 || u == 4) {
#pragma unroll
            for (int b = u ? 5 : 0; b < (u ? 8 : 5); ++b) fb[b][0] = rd128(T_WA + ((b < 6 ? (b >> 1) * 64 + wn * 32 + (b & 1) * 16 : 96 + b * 16) + r15) * 16);
          }
          if constexpr (u >= 5) {
#pragma unroll
            for (int q = 8 * (u - 5); q < 8 * (u - 4); q += 2) mm(0, q, 0);
          }
          SB(); });
        next3();
      }
      float k0[4], k1[4], qq[4];
#pragma unroll
      for (int a = 0; a < 4; ++a) { const unsigned ro = T_ROW + (64 * wm + 16 * (a ^ (2 * wn)) + r15) * 4; k0[a] = rdf(ro); k1[a] = rdf(ro + 512); qq[a] = rdf(ro + 1024); }
      u32x4 hw[14]; float t[8]; unsigned sa9 = 0;
      auto lv0 = [](int s) constexpr { return s < 0 ? 0 : s < 6 ? (64 * s + 2) / 3 : s < 10 ? 128 + 16 * (s - 6) : s < 13 ? 192 + (32 * (s - 10) + 2) / 3 : 224; };
      auto st3 = [](int s) constexpr { return (s == 2 || s == 5 || s == 9) ? 4 : s == 12 ? 2 : 0; };
      auto n3 = [](int s) constexpr { return (s == 5 || s == 6 || s == 9 || s == 13) ? 8 : s == 7 ? 16 : s == 19 ? 4 : s > 19 ? 0 : 12; };
      sfor<22>([&](auto S) AI { constexpr int s = S.value;
        if constexpr (s == 6 || s == 10 || s == 14) waitl0();
        if constexpr (s == 8) waitv<12>();
        else if constexpr (s >= 1) waitv<n3(s - 1) + st3(s - 1)>();
        if constexpr (s == 7 || s == 10 || s == 14 || s == 17) {
          if (w == 4) check_flags(pv, rws, fvo, flagb + (FK_M1 + (s - 7) / 3) * 128, lane, 4 * kq, 4 * kq + 4, epr); }
        SB(); bar();
        if constexpr (st3(s - 2) > 0) { if (w == 5) stflag(rws, epr, lane, flagb + (FK_M1 + (s - 4) / 3) * 128 + cj * 4); }
        if constexpr (s == 6 || s == 9 || s == 13 || s == 16) { if (w == 4) ld32a(pv, rws, fvo, flagb + (FK_M1 + (s - 6) / 3) * 128); }
        if constexpr (s == 7) { const unsigned keep = sa; sa = sa9; sfor<4>([&](auto U) AI { G3(IC<9>{}, U); }); sa = keep; }
#define UU(e) ac1[m0 + ((e) >> 2)][(e) & 3]
#define LVL(expr) do { _Pragma("unroll") for (int e = 4 * hh; e < 4 * hh + 4; ++e) t[e] = (expr); PIN2(t[4 * hh], t[4 * hh + 1]); PIN2(t[4 * hh + 2], t[4 * hh + 3]); } while (0)
        auto hlev = [&](auto LL) AI { constexpr int L = LL.value, v = L >> 4, l = (L >> 1) & 7, hh = L & 1, a = v & 3, m0 = (a < 2 ? 8 * a : 4 + 6 * a) + 2 * (v >> 2);
          if constexpr (l == 0) LVL(UU(e) * UU(e));
          if constexpr (l == 1) LVL(__builtin_fmaf(t[e], k1[a], k0[a]));
          if constexpr (l == 2) LVL(UU(e) * t[e]);
          if constexpr (l == 3) LVL(__builtin_amdgcn_exp2f(t[e]));
          if constexpr (l == 4) LVL(__builtin_fmaf(t[e], qq[a], qq[a]));
          if constexpr (l == 5) LVL(__builtin_amdgcn_rcpf(t[e]));
          if constexpr (l == 6) LVL(UU(e) * t[e]);
          if constexpr (l == 7) { hw[v][2 * hh] = pk2(t[4 * hh], t[4 * hh + 1]); hw[v][2 * hh + 1] = pk2(t[4 * hh + 2], t[4 * hh + 3]); if constexpr (hh) PIN(hw[v]); else PIN2(hw[v][0], hw[v][1]); }
        };
        if constexpr (s < 19) sfor<12>([&](auto U) AI { constexpr int u = U.value, n = lv0(s + 1) - lv0(s), i0 = (n * u + 11) / 12, i1 = (n * (u + 1) + 11) / 12;
          if constexpr (n3(s) != 8) G3(IC<s + 3>{}, U);
          else if constexpr (u < 8) G3(IC<s + 3>{}, IC<u + 4>{});
          else if constexpr (s != 6)
            *(LAS u32x4*)(size_t)(((s + 1) % 3 == 0 ? 131072u : 81920u + ((s + 1) % 3) * 16384u) + wn * 8192 + (4 * wm + ((u - 8) ^ (2 * wn))) * 1024 + lane * 16) = hw[s - 13 + u];
          SB();
          if constexpr (i0 < i1) hlev(IC<lv0(s) + i0>{});
          if constexpr (i0 + 1 < i1) hlev(IC<lv0(s) + i0 + 1>{});
          SB(); });
        if constexpr (s == 19) {
          setm0(114688 + wq * 4096);
          sfor<4>([&](auto U) AI { constexpr int u = U.value;
            dma<u * 1024>(rx, (8u * u + (lane >> 3)) * 2048 + ((lane >> 2) & 1) * 256 + (lane & 3) * 16 - u * 1024, (ci * 128 + 32 * wq) * 2048 + ntp * 512 + kq * 64); });
        }
        constexpr int gr0 = s == 2 ? 0 : s == 5 ? 1 : s == 9 ? 2 : 3;
#pragma unroll
        for (int q = 0; q < st3(s); ++q) st128(rws, hw[4 * gr0 + q], lane * 16u + (4 * wm + (q ^ (2 * wn))) * 1024, ci * HID_PANEL + kb_of(cj, gr0 < 3 ? 2 * gr0 + wn : 6) * 8192);
        if constexpr (s == 6) sa9 = sa;
        if constexpr (s < 4) next3(); else if constexpr (s < 18) next4();
        if constexpr (s == 4) {
          rb = mk(w2, 3584 * 2048);
#pragma unroll
          for (int q = 0; q < 4; ++q) vb[q] = (8 * q + (lane >> 3)) * 2048 + (((((lane & 7) >> 1) ^ g ^ (2 * (r2 & 1))) * 2) + (lane & 1)) * 16 - 1024 * q;
        }
      });
    }
    unsigned got = 0;
    auto fetch = [&]() AI {
      ld32a(pv, rws, fvo, PF_OFF + (ci * 16 + cj) * 256 + wq * 64); asm volatile("s_waitcnt vmcnt(0)" : "+v"(pv)::"memory");
      const unsigned rdy = (unsigned)__builtin_amdgcn_ballot_w64(pv == epr) & 7u & ~got; got |= rdy;
      sfor<3>([&](auto Q) AI { constexpr int q = Q.value;
        if (rdy & (1u << q)) { setm0(wq * 4096 + q * 16384);
          sfor<4>([&](auto U) AI { dma1<U.value * 1024>(rws, va[0], PART_OFF + (ci * 16 + cj) * 49152 + q * 16384 + wq * 4096); }); } });
    };
    bar();
    fetch();
    presum();
#pragma unroll 1
    for (unsigned sp = 0; sp < SPIN_CAP && got != 7u; ++sp) fetch();
    if (cj == 0 && w == 4) stflag(rws, epr, lane, flagb + FK_EP * 128);
    waitv<0>(); bar();
  }
  {
    u32x2 pw[4][3];
#pragma unroll
    for (int u = 0; u < 4; ++u)
#pragma unroll
      for (int q = 0; q < 3; ++q) pw[u][q] = *(LAS u32x2*)(size_t)(fpp[u] + q * 16384);
    SB();
#pragma unroll
    for (int u = 0; u < 4; ++u) {
      f32x4 r;
#pragma unroll
      for (int e = 0; e < 4; ++e) r[e] = (fv[u][e] + unp(pw[u][0], e)) + (unp(pw[u][1], e) + unp(pw[u][2], e));
      st128<OAUX>(mk(outp, 8u << 20), __builtin_bit_cast(u32x4, r), fo + u * 16384, 0);
    }
  }
}

extern "C" void kernel_launch(void* const* d_in, const int* in_sizes, int n_in, void* d_out, int out_size, void* d_ws, size_t ws_size, hipStream_t stream) {
  (void)in_sizes; (void)n_in; (void)out_size; (void)ws_size;
  (void)hipFuncSetAttribute((const void*)fused_kernel, hipFuncAttributeMaxDynamicSharedMemorySize, LDS_BYTES);
  fused_kernel<<<256, 512, LDS_BYTES, stream>>>((const u16*)d_in[0], (const u16*)d_in[1], (const u16*)d_in[2], (const u16*)d_in[3], (const u16*)d_in[4],
      (const u16*)d_in[5], (const u16*)d_in[6], (const u16*)d_in[7], (const u16*)d_in[8], (const u16*)d_in[10], (const u16*)d_in[11], (const u16*)d_in[12],
      (const u16*)d_in[13], (const u16*)d_in[14], (char*)d_ws, (float*)d_out);
}
```
